```python
import torch

def _softmax(x, axis=-1):
    return torch.softmax(x, dim=axis)
def _gelu(x):
    return torch.nn.functional.gelu(x, approximate="tanh")
def _silu(x):
    return torch.nn.functional.silu(x)
def _sigmoid(x):
    return torch.sigmoid(x)
def _layernorm(x, axis=-1, eps=1e-5):
    m = x.mean(dim=axis, keepdim=True)
    v = x.var(dim=axis, keepdim=True, unbiased=False)
    return (x - m) / torch.sqrt(v + eps)
def _scatter_add(tgt, idx, src, axis=0):
    return tgt.clone().index_add_(axis, idx, src)
def _segment_sum(src, seg, n):
    out = torch.zeros((n,) + src.shape[1:], dtype=src.dtype)
    return out.index_add_(0, seg, src)
def _pad(x, pw):
    flat = tuple(v for pair in reversed(pw) for v in pair)
    return torch.nn.functional.pad(x, flat)

kernel_name = 'parallel_sum__sliding_local_dilated__dense_activation_gelu__prompt'

def setup_inputs():

    _w = (lambda a: (lambda t: t.to(torch.bfloat16) if t.is_floating_point() else t)(torch.as_tensor(a)))
    def _A_setup():
        B, S, D = 1, 2048, 512
        return {
            "x": _w((lambda *s: torch.randn(*s, dtype=torch.bfloat16) * 0.05)(B, S, D)),
            "wqkv": _w((lambda *s: torch.randn(*s, dtype=torch.bfloat16) * 0.05)(D, 3 * D)),
            "wo": _w((lambda *s: torch.randn(*s, dtype=torch.bfloat16) * 0.05)(D, D)),
        }
    _A_in = _A_setup()

    def _B_setup():
        B, S, D, F = 1, 2048, 512, 1376
        return {
            "x": _w((lambda *s: torch.randn(*s, dtype=torch.bfloat16) * 0.05)(B, S, D)),
            "w1": _w((lambda *s: torch.randn(*s, dtype=torch.bfloat16) * 0.8)(D, F) * (D ** -0.5)),
            "b1": _w((lambda *s: torch.randn(*s, dtype=torch.bfloat16) * 0.8)(F) * 0.01),
            "w2": _w((lambda *s: torch.randn(*s, dtype=torch.bfloat16) * 0.8)(F, D) * (F ** -0.5)),
            "b2": _w((lambda *s: torch.randn(*s, dtype=torch.bfloat16) * 0.8)(D) * 0.01),
        }
    _B_in = _B_setup()

    return {
        "A_x": _A_in["x"],
        "A_wqkv": _A_in["wqkv"],
        "A_wo": _A_in["wo"],
        "B_x": _B_in["x"],
        "B_w1": _B_in["w1"],
        "B_b1": _B_in["b1"],
        "B_w2": _B_in["w2"],
        "B_b2": _B_in["b2"],
    }

def reference(A_x, A_wqkv, A_wo, B_x, B_w1, B_b1, B_w2, B_b2):
    def _A_ref(x, wqkv, wo):
        B, S, D = x.shape
        H = 32; Dh = D // H
        W, dil = 8, 2
        qkv = (lambda x, *d: x.permute(*d))((x @ wqkv).reshape(B, S, 3, H, Dh), 2, 0, 3, 1, 4)
        q, k, v = qkv[0], qkv[1], qkv[2]
        scale = Dh ** -0.5
        pos = torch.arange(S)
        off = torch.arange(W) * dil
        idx = pos[:, None] - off[None, :]
        mask = idx >= 0
        idx_c = torch.where(mask, idx, 0).reshape(-1)
        kg = k[:, :, idx_c, :].reshape(B, H, S, W, Dh)
        vg = v[:, :, idx_c, :].reshape(B, H, S, W, Dh)
        logits = torch.einsum("bhsd,bhswd->bhsw", q, kg) * scale
        logits = torch.where(mask[None, None, :, :], logits, -1e9)
        attn = _softmax(logits, axis=-1)
        o = torch.einsum("bhsw,bhswd->bhsd", attn, vg)
        return (lambda x, *d: x.permute(*d))(o, 0, 2, 1, 3).reshape(B, S, D) @ wo

    def _B_ref(x, w1, b1, w2, b2):
        h = torch.einsum("bsd,df->bsf", x, w1) + b1
        h = _gelu(h)
        return torch.einsum("bsf,fd->bsd", h, w2) + b2

    x = A_x
    xn = _layernorm(x, axis=-1)
    return x + _A_ref(xn, A_wqkv, A_wo) + _B_ref(xn, B_w1, B_b1, B_w2, B_b2)
```

```cpp
#include <hip/hip_runtime.h>
#include <hip/hip_bf16.h>
#include <stdint.h>

typedef unsigned short u16;
__device__ __forceinline__ float bf2f(u16 b){ return __uint_as_float(((unsigned)b)<<16); }
__device__ __forceinline__ u16 f2bf(float f){ unsigned u=__float_as_uint(f); u += 0x7FFFu + ((u>>16)&1u); return (u16)(u>>16); }

constexpr int S=2048, D=512, F=1376, H=32, DH=16;

__global__ void k_ln(const u16* x, u16* xn){
  int row=blockIdx.x; int t=threadIdx.x;
  float v[8]; float s=0;
  for(int j=0;j<8;++j){ v[j]=bf2f(x[row*D+t*8+j]); s+=v[j]; }
  for(int o=32;o>0;o>>=1) s+=__shfl_xor(s,o);
  float m=s/D; float q=0;
  for(int j=0;j<8;++j){ float d=v[j]-m; q+=d*d; }
  for(int o=32;o>0;o>>=1) q+=__shfl_xor(q,o);
  float r=rsqrtf(q/D+1e-5f);
  for(int j=0;j<8;++j) xn[row*D+t*8+j]=f2bf((v[j]-m)*r);
}
__global__ void k_gemm(const u16* A,int lda,const u16* B,int ldb,u16* C,int ldc,int N,int K,const u16* bias,int mode){
  int n=blockIdx.x*64+threadIdx.x; int m=blockIdx.y;
  if(n>=N) return;
  float acc=0;
  for(int k=0;k<K;++k) acc+=bf2f(A[m*lda+k])*bf2f(B[k*ldb+n]);
  if(mode==1){ acc+=bf2f(bias[n]); float u=0.7978845608028654f*(acc+0.044715f*acc*acc*acc); acc=0.5f*acc*(1.f+tanhf(u)); }
  C[m*ldc+n]=f2bf(acc);
}
__global__ void k_attn(const u16* qkv,u16* o,int ldo){
  int s=blockIdx.x; int h=threadIdx.x;
  float q[16]; for(int d=0;d<16;++d) q[d]=bf2f(qkv[s*1536+h*16+d]);
  float l[8]; float mx=-1e30f;
  for(int w=0;w<8;++w){ int p=s-2*w; if(p>=0){ float a=0; for(int d=0;d<16;++d) a+=q[d]*bf2f(qkv[p*1536+512+h*16+d]); l[w]=a*0.25f; } else l[w]=-1e9f; mx=fmaxf(mx,l[w]); }
  float sum=0; for(int w=0;w<8;++w){ l[w]=expf(l[w]-mx); sum+=l[w]; }
  for(int d=0;d<16;++d){ float a=0; for(int w=0;w<8;++w){ int p=s-2*w; if(p<0)p=0; a+=l[w]/sum*bf2f(qkv[p*1536+1024+h*16+d]); } o[s*ldo+h*16+d]=f2bf(a); }
}
__global__ void k_out(const u16* x,const u16* oh,const u16* wo,const u16* w2,const u16* b2,u16* out){
  int n=blockIdx.x*64+threadIdx.x; int m=blockIdx.y;
  float acc=0;
  for(int k=0;k<D;++k) acc+=bf2f(oh[m*1888+k])*bf2f(wo[k*D+n]);
  for(int k=0;k<F;++k) acc+=bf2f(oh[m*1888+512+k])*bf2f(w2[k*D+n]);
  out[m*D+n]=f2bf(acc+bf2f(b2[n])+bf2f(x[m*D+n]));
}

extern "C" void kernel_launch(void* const* d_in, const int* in_sizes, int n_in,
                              void* d_out, int out_size, void* d_ws, size_t ws_size,
                              hipStream_t stream) {
  const u16* x=(const u16*)d_in[0]; const u16* wqkv=(const u16*)d_in[1]; const u16* wo=(const u16*)d_in[2];
  const u16* w1=(const u16*)d_in[4]; const u16* b1=(const u16*)d_in[5]; const u16* w2=(const u16*)d_in[6]; const u16* b2=(const u16*)d_in[7];
  u16* xn=(u16*)d_ws; u16* qkv=xn+S*D; u16* oh=qkv+S*1536;
  k_ln<<<S,64,0,stream>>>(x,xn);
  k_gemm<<<dim3(1536/64,S),64,0,stream>>>(xn,D,wqkv,1536,qkv,1536,1536,D,nullptr,0);
  k_gemm<<<dim3((F+63)/64,S),64,0,stream>>>(xn,D,w1,F,oh+512,1888,F,D,b1,1);
  k_attn<<<S,32,0,stream>>>(qkv,oh,1888);
  k_out<<<dim3(D/64,S),64,0,stream>>>(x,oh,wo,w2,b2,(u16*)d_out);
}
```

```cpp
#include <hip/hip_runtime.h>
#include <stdint.h>

typedef unsigned u32;
typedef unsigned long long u64;
typedef unsigned short u16;
typedef short s16x4 __attribute__((ext_vector_type(4)));
typedef short s16x8 __attribute__((ext_vector_type(8)));
typedef __bf16 b16x2 __attribute__((ext_vector_type(2)));
typedef float f32x2 __attribute__((ext_vector_type(2)));
typedef float f32x4 __attribute__((ext_vector_type(4)));
typedef float f32x16 __attribute__((ext_vector_type(16)));
typedef u32 u32x2 __attribute__((ext_vector_type(2)));
typedef u32 u32x4 __attribute__((ext_vector_type(4)));
#define LAS __attribute__((address_space(3)))
#define GAS __attribute__((address_space(1)))
#define DEV __device__ __forceinline__

namespace fk {
constexpr int LDX = 1024, LDQKV = 3072, LDW1 = 2752, LDW2 = 1024;
constexpr int NKS = 118;
constexpr int SLOT = 43008, BOFF = 18432;
constexpr int L_MEAN = 3 * SLOT, L_RSTD = L_MEAN + 576, L_CS = L_RSTD + 576, L_B1 = L_CS + 768, L_KV = L_B1 + 768,
              L_JUNK = L_KV + 8192, L_TOTAL = L_JUNK + 2048;
constexpr int WS_A2 = 65536, A2_BYTES = 64 * NKS * 1024;
constexpr int AUX_SC1 = 16;
constexpr u32 SPIN_CAP = 1u << 16;

#define STAMP(i) do {} while (0)
#define STAMP_ARG
#define STAMP_PASS
#define STAMP_OUT() do {} while (0)

DEV void dma16(u32 voff, const void* sbase, u32 lds) {
  asm volatile("s_mov_b32 m0, %2\n\ts_nop 0\n\tglobal_load_lds_dwordx4 %0, %1" ::"v"(voff), "s"(sbase), "s"(lds) : "memory");
}
DEV void dma16_masked(u32 voff, const void* sbase, u32 lds, u32 mlo) {
  asm volatile("s_mov_b32 m0, %2\n\ts_mov_b32 exec_lo, %3\n\tglobal_load_lds_dwordx4 %0, %1\n\ts_mov_b32 exec_lo, -1"
               ::"v"(voff), "s"(sbase), "s"(lds), "s"(mlo) : "memory");
}
DEV void dma16_if(u32 voff, const void* sbase, u32 lds, u32 cond) {
  asm volatile("s_cmp_gt_u32 %3, 5\n\ts_cbranch_scc1 1f\n\ts_mov_b32 m0, %2\n\ts_nop 0\n\tglobal_load_lds_dwordx4 %0, %1\n1:"
               ::"v"(voff), "s"(sbase), "s"(lds), "s"(cond) : "memory", "scc");
}
DEV void dma4(u32 voff, const void* sbase, u32 lds) {
  asm volatile("s_mov_b32 m0, %2\n\ts_nop 0\n\tglobal_load_lds_dword %0, %1" ::"v"(voff), "s"(sbase), "s"(lds) : "memory");
}
template <int N> DEV void wait_bar() {
  asm volatile("s_waitcnt vmcnt(%0) lgkmcnt(0)\n\ts_barrier" ::"i"(N) : "memory");
}
DEV void lds_bar() { asm volatile("s_waitcnt lgkmcnt(0)\n\ts_barrier" ::: "memory"); }
#define SB() __builtin_amdgcn_sched_barrier(0)

DEV u32 pk_bf16(float a, float b) {
  f32x2 p = {a, b};
  return __builtin_bit_cast(u32, __builtin_convertvector(p, b16x2));
}
DEV float bf_lo(u32 v) { return __builtin_bit_cast(float, v << 16); }
DEV float bf_hi(u32 v) { return __builtin_bit_cast(float, v & 0xffff0000u); }
DEV float dot2(u32 a, u32 b, float c) {
  return __builtin_amdgcn_fdot2_f32_bf16(__builtin_bit_cast(b16x2, a), __builtin_bit_cast(b16x2, b), c, false);
}
DEV f32x4 mfma16(u32x4 a, u32x4 b, f32x4 c) {
  return __builtin_amdgcn_mfma_f32_16x16x32_bf16(__builtin_bit_cast(s16x8, a), __builtin_bit_cast(s16x8, b), c, 0, 0, 0);
}
DEV f32x4 mfma16k16(u32x2 a, u32x2 b, f32x4 c) {
  return __builtin_amdgcn_mfma_f32_16x16x16bf16_1k(__builtin_bit_cast(s16x4, a), __builtin_bit_cast(s16x4, b), c, 0, 0, 0);
}
DEV f32x16 mfma32(u32x4 a, u32x4 b, f32x16 c) {
  return __builtin_amdgcn_mfma_f32_32x32x16_bf16(__builtin_bit_cast(s16x8, a), __builtin_bit_cast(s16x8, b), c, 0, 0, 0);
}
DEV u32x4 lds128(LAS char* L, u32 off) { return *(LAS u32x4*)(L + off); }
DEV u32x2 ldstr(LAS char* L, u32 off) {
  return __builtin_bit_cast(u32x2, __builtin_amdgcn_ds_read_tr16_b64_v4i16((LAS s16x4*)(L + off)));
}
template <int CTRL> DEV float dpp_add(float v) {
  int t = __builtin_amdgcn_update_dpp(0, __builtin_bit_cast(int, v), CTRL, 0xf, 0xf, true);
  return v + __builtin_bit_cast(float, t);
}
DEV float red8(float v) {
  v = dpp_add<0xB1>(v);
  v = dpp_add<0x4E>(v);
  v = dpp_add<0x141>(v);
  return v;
}
DEV float max_g(float v) {
  u32 u = __builtin_bit_cast(u32, v);
  auto r = __builtin_amdgcn_permlane32_swap(u, u, false, false);
  v = fmaxf(__builtin_bit_cast(float, r[0]), __builtin_bit_cast(float, r[1]));
  u = __builtin_bit_cast(u32, v);
  auto q = __builtin_amdgcn_permlane16_swap(u, u, false, false);
  return fmaxf(__builtin_bit_cast(float, q[0]), __builtin_bit_cast(float, q[1]));
}
DEV u32x4 pair_rows(u32x2 x, u32x2 y) {
  auto a = __builtin_amdgcn_permlane16_swap(x[0], y[0], false, false);
  auto b = __builtin_amdgcn_permlane16_swap(x[1], y[1], false, false);
  return u32x4{a[0], b[0], a[1], b[1]};
}

DEV f32x4 splat4(float v) {
  f32x2 p = {v, v};
  asm volatile("" : "+v"(p));
  return f32x4{p[0], p[1], p[0], p[1]};
}
template <int V> struct IC { static constexpr int v = V; };
template <int LO, int HI, class F> DEV void sfor(F&& f) {
  if constexpr (LO < HI) { f(IC<LO>{}); sfor<LO + 1, HI>(f); }
}
struct P2W { u32 r[120]; };

template <int TYPE>
DEV void phase1(LAS char* L, const char* x, const char* wq, const char* w1, const u16* b1, const char* wo, const char* w2,
                char* ws, int g, int j, int w, int lane, __amdgpu_buffer_rsrc_t rsA2, u32 w2voff, int ks0, P2W& W STAMP_ARG) {
  constexpr bool FFN = TYPE >= 2;
  constexpr int NT = TYPE == 0 ? 5 : (TYPE == 1 ? 4 : 2);
  constexpr int NC = TYPE == 2 ? 6 : (TYPE == 3 ? 5 : 3);
  constexpr int CB = TYPE == 3 ? 6 : 0;
  constexpr int NSP = TYPE == 1 ? 3 : 2;
  constexpr int ND = TYPE == 1 ? 6 : 5;
  constexpr int NM = NT * NC + (FFN ? 2 : 0);
  const int r8 = lane >> 3, pos = lane & 7, lg = lane >> 4, lc = lane & 15;
  const int rot = (j + g) & 7;
  constexpr int ldw = FFN ? LDW1 : LDQKV;
  const u32 voffA = r8 * LDX + ((pos ^ r8) << 4);
  const int swB = (r8 & 2) | ((w & 1) << 2);
  const u32 voffB = r8 * ldw + ((pos ^ swB) << 4);
  const int maxch = (j == 15) ? 1 : 5;
  const u32 voffB2 = FFN ? (u32)(r8 * ldw + (min(pos ^ swB, maxch) << 4)) : voffB;
  const int rowbase = FFN ? 128 * g : 128 * g - 16;
  const int p0row = (!FFN && g == 0 && w < 2) ? 8 * w : rowbase + 8 * w;
  const char* sA0 = x + (size_t)p0row * LDX;
  const char* sA1 = x + (size_t)(rowbase + 8 * (w + 8)) * LDX;
  const char* sA2 = x + (size_t)(rowbase + 8 * (16 + (w & 1))) * LDX;
  const char* sB0 = FFN ? w1 + (size_t)(8 * w) * LDW1 + (j - 8) * 352 : wq + (size_t)(8 * w) * LDQKV + j * 128;
  constexpr int SEGB = FFN ? 128 : 1024;
  const u32 mlo = (w == 0) ? 0xFFFFFF00u : 0xFFFFFFFFu;
  auto issue = [&](int i, int k) {
    const int kc = (i + rot) & 7;
    const u32 slot = (i % 3) * SLOT;
    const u32 va = voffA + kc * 128, vb = voffB + kc * 64 * ldw, vb2 = voffB2 + kc * 64 * ldw;
    if (k == 0) { if (TYPE == 0) dma16_masked(va, sA0, slot + w * 1024, mlo); else dma16(va, sA0, slot + w * 1024); }
    if (k == 1) dma16(vb, sB0, slot + BOFF + w * 1024);
    if (k == 2) dma16(vb, sB0 + SEGB, slot + BOFF + (8 + w) * 1024);
    if (k == 3) dma16(vb2, sB0 + 2 * SEGB, slot + BOFF + (16 + w) * 1024);
    if (k == 4) dma16(va, sA1, slot + (w + 8) * 1024);
    if (k == 5) dma16_if(va, sA2, slot + (16 + (w & 1)) * 1024, w);
  };
#pragma unroll
  for (int i = 0; i < 3; ++i)
#pragma unroll
    for (int k = 0; k < ND; ++k) issue(i, k);
  u32 b1v = 0;
  if (FFN) b1v = b1[min((j - 8) * 176 + (int)threadIdx.x, 1375)];
  if (TYPE == 0 && w == 0 && lane < 8) {
    const u32x4 one = {0x3F803F80u, 0x3F803F80u, 0x3F803F80u, 0x3F803F80u};
#pragma unroll
    for (int s = 0; s < 3; ++s) *(LAS u32x4*)(L + s * SLOT + lane * 16) = one;
  }
  const int T0 = TYPE == 0 ? 0 : (TYPE == 1 ? 5 : 2 * (w & 3));
  const u32 aA = T0 * 2048 + (lc >> 3) * 1024 + (lc & 7) * 128 + ((lg ^ (lc & 7)) << 4);
  const int tq = lc >> 2, tp = lc & 3;
  const int swR = (tq & 2) | ((lg & 1) << 2);
  const u32 aBl = BOFF + lg * 1024 + tq * 128 + ((tp >> 1) << 4) + (tp & 1) * 8;
  const int crot = min(2 * (w & 3), NC - 2);
  u32 aB[NC];
#pragma unroll
  for (int c = 0; c < NC; ++c) {
    const int cc = c + crot >= NC ? c + crot - NC : c + crot;
    const int ct = FFN ? CB + cc : 4 * c + (w & 3);
    aB[c] = aBl + (ct >> 2) * 8192 + (((2 * (ct & 3)) ^ swR) << 4);
  }
  u32 aS[NSP];
  aS[0] = w * 1024 + lane * 16; aS[1] = (w + 8) * 1024 + lane * 16;
  if (NSP == 3) aS[NSP - 1] = (16 + (w & 1)) * 1024 + lane * 16;

  f32x4 acc[NT][NC], acc1[2];
#pragma unroll
  for (int t = 0; t < NT; ++t)
#pragma unroll
    for (int c = 0; c < NC; ++c) acc[t][c] = f32x4{0.f, 0.f, 0.f, 0.f};
  acc1[0] = acc1[1] = f32x4{0.f, 0.f, 0.f, 0.f};
  float ssum[NSP], ssq[NSP];
#pragma unroll
  for (int i = 0; i < NSP; ++i) ssum[i] = ssq[i] = 0.f;
  u32x4 fa[2][NT], sv[NSP];
  u32x2 fb[2][NC][2];
  auto rd = [&](int t, int k) {
    const u32 slot = ((t >> 1) % 3) * SLOT;
    const int b = t & 1;
    if (k < 2 * NC) fb[b][k >> 1][k & 1] = ldstr(L, aB[k >> 1] + slot + b * 4096 + (k & 1) * 512);
    else if (k < 2 * NC + NT) fa[b][k - 2 * NC] = lds128(L, ((aA ^ (b * 64)) + slot + (k - 2 * NC) * 2048));
    else sv[k - 2 * NC - NT] = lds128(L, aS[k - 2 * NC - NT] + slot);
  };
  auto ldw2 = [&](int s, int lo, int hi) {
    const int ks = ks0 + s;
    const bool isO = ks < 32;
    const char* base = isO ? wo + (size_t)ks * 16 * LDW2 : w2 + (size_t)(ks - 32) * 16 * LDW2;
    const __amdgpu_buffer_rsrc_t rs = __builtin_amdgcn_make_buffer_rsrc((void*)base, 0, ks < NKS ? 16 * LDW2 : 0, 0x00020000);
#pragma unroll
    for (int jj = lo; jj < hi; ++jj)
      W.r[s * 8 + jj] = __builtin_amdgcn_raw_buffer_load_b32(rs, w2voff + (jj & 3) * 1024, (jj >> 2) * 4096, 0);
  };
  const u32x4 ones = {0x3F803F80u, 0x3F803F80u, 0x3F803F80u, 0x3F803F80u};
  STAMP(1);
  wait_bar<10>();
  STAMP(2);
#pragma unroll
  for (int k = 0; k < 2 * NC + NT + NSP; ++k) rd(0, k);
  SB();
  sfor<0, 16>([&](auto tc) {
    constexpr int t = decltype(tc)::v, s = t >> 1, b = t & 1;
    if constexpr (b) { if constexpr (s <= 4) wait_bar<5>(); else if constexpr (s == 5) wait_bar<21>(); else if constexpr (s == 6) wait_bar<32>(); }
    constexpr int nr = t == 15 ? 0 : 2 * NC + NT + (b ? NSP : 0);
    constexpr int g0 = (nr + 1) / 2;
    constexpr int nd = (b && s + 3 < 8) ? ND : 0, nw = t >= 9 ? 8 : 0;
    constexpr int per = (nd + nw + NM - g0 - 1) / (NM - g0);
    constexpr int ndot = b ? 0 : 4 * NSP, dper = (ndot + NM - 1) / NM;
    sfor<0, NM>([&](auto qc) {
      constexpr int q = decltype(qc)::v;
      if constexpr (q < NT * NC) {
        constexpr int c = q / NT, i = q % NT;
        const u32x4 wfr = {fb[b][c][0][0], fb[b][c][0][1], fb[b][c][1][0], fb[b][c][1][1]};
        if constexpr (!FFN && c == 2) acc[i][c] = mfma16(fa[b][i], wfr, acc[i][c]);
        else acc[i][c] = mfma16(wfr, fa[b][i], acc[i][c]);
      } else {
        constexpr int c = q - NT * NC;
        const u32x4 wfr = {fb[b][c][0][0], fb[b][c][0][1], fb[b][c][1][0], fb[b][c][1][1]};
        acc1[c] = mfma16(wfr, ones, acc1[c]);
      }
      if constexpr (2 * q < nr) rd(t + 1, 2 * q);
      if constexpr (2 * q + 1 < nr) rd(t + 1, 2 * q + 1);
      if constexpr (q >= g0) {
        sfor<(q - g0) * per, (q - g0 + 1) * per>([&](auto uc) {
          constexpr int u = decltype(uc)::v;
          if constexpr (u < nd) issue(s + 3, u);
          else if constexpr (u < nd + nw) ldw2(t - 9, u - nd, u - nd + 1);
        });
      }
      sfor<q * dper, (q + 1) * dper>([&](auto uc) {
        constexpr int u = decltype(uc)::v;
        if constexpr (u < ndot) {
          constexpr int i = u >> 2, d = u & 3;
          ssum[i] = dot2(sv[i][d], 0x3F803F80u, ssum[i]);
          ssq[i] = dot2(sv[i][d], sv[i][d], ssq[i]);
        }
      });
      SB();
    });
  });
  if (FFN) asm volatile("" : "+v"(acc1[0]), "+v"(acc1[1]));
#pragma unroll
  for (int i = 0; i < NSP; ++i) asm volatile("" : "+v"(ssum[i]), "+v"(ssq[i]));
  STAMP(3);
#pragma unroll
  for (int i = 0; i < NSP; ++i) {
    const float sm = red8(ssum[i]) * (1.f / 512.f), sq = red8(ssq[i]) * (1.f / 512.f);
    const float var = fmaxf(sq - sm * sm, 0.f);
    const float rs = __builtin_amdgcn_rsqf(var + 1e-5f);
    const int p = i == 0 ? w : (i == 1 ? w + 8 : 16 + (w & 1));
    if (pos == 0) { *(LAS float*)(L + L_MEAN + (p * 8 + r8) * 4) = sm; *(LAS float*)(L + L_RSTD + (p * 8 + r8) * 4) = rs; }
  }
  if (TYPE == 0) {
    const int h = w & 3;
    if (lc == 0) {
      *(LAS f32x4*)(L + L_CS + (h * 16 + lg * 4) * 4) = acc[0][0];
      *(LAS f32x4*)(L + L_CS + (64 + h * 16 + lg * 4) * 4) = acc[0][1];
    }
    if (lg == 0) *(LAS float*)(L + L_CS + (128 + h * 16 + lc) * 4) = acc[0][2][0];
    *(LAS f32x4*)(L + L_KV + h * 2048 + lane * 16) = acc[NT - 1][1];
    *(LAS f32x4*)(L + L_KV + h * 2048 + 1024 + lane * 16) = acc[NT - 1][2];
  }
  if (FFN) {
    if (lc == 0) {
      *(LAS f32x4*)(L + L_CS + ((CB + crot) * 16 + lg * 4) * 4) = acc1[0];
      *(LAS f32x4*)(L + L_CS + ((CB + crot + 1) * 16 + lg * 4) * 4) = acc1[1];
    }
    if (threadIdx.x < 192) *(LAS float*)(L + L_B1 + threadIdx.x * 4) = bf_lo(b1v);
  }
  lds_bar();
  STAMP(4);
  if (!FFN) {
    const int h = w & 3;
    constexpr int NK = 5;
    const int TK = TYPE == 0 ? 0 : 4;
    const f32x4 csq = *(LAS f32x4*)(L + L_CS + (h * 16 + lg * 4) * 4);
    const f32x4 csk = *(LAS f32x4*)(L + L_CS + (64 + h * 16 + lg * 4) * 4);
    const f32x4 csv = splat4(*(LAS float*)(L + L_CS + (128 + h * 16 + lc) * 4));
    u32x2 Qb[4], Kb[NK], Vb[NK];
#pragma unroll
    for (int i = 0; i < NK; ++i) {
      const int T = TK + i;
      f32x4 ak, av;
      if (TYPE == 1 && i == 0) {
        ak = *(LAS f32x4*)(L + L_KV + h * 2048 + lane * 16);
        av = *(LAS f32x4*)(L + L_KV + h * 2048 + 1024 + lane * 16);
      } else {
        ak = acc[TYPE == 0 ? i : (i > 0 ? i - 1 : 0)][1];
        av = acc[TYPE == 0 ? i : (i > 0 ? i - 1 : 0)][2];
      }
      const float rc1 = *(LAS float*)(L + L_RSTD + (T * 16 + lc) * 4);
      const f32x4 mc = splat4(*(LAS float*)(L + L_MEAN + (T * 16 + lc) * 4)), rc = splat4(rc1);
      const f32x4 m4 = *(LAS f32x4*)(L + L_MEAN + (T * 16 + lg * 4) * 4), r4 = *(LAS f32x4*)(L + L_RSTD + (T * 16 + lg * 4) * 4);
      const f32x4 kk = (ak - mc * csk) * rc;
      const f32x4 vv = (av - m4 * csv) * r4;
      Kb[i] = u32x2{pk_bf16(kk[0], kk[1]), pk_bf16(kk[2], kk[3])};
      Vb[i] = u32x2{pk_bf16(vv[0], vv[1]), pk_bf16(vv[2], vv[3])};
      if (i > 0) {
        const f32x4 qq = (acc[TYPE == 0 ? i : i - 1][0] - mc * csq) * splat4(rc1 * (0.25f * 1.4426950408889634f));
        Qb[i - 1] = u32x2{pk_bf16(qq[0], qq[1]), pk_bf16(qq[2], qq[3])};
      }
    }
    SB();
    f32x4 mP, mC;
#pragma unroll
    for (int r = 0; r < 4; ++r) {
      mP[r] = (4 * lg + r >= lc + 2) ? 0.f : -1e30f;
      mC[r] = (4 * lg + r <= lc) ? 0.f : -1e30f;
    }
    const bool odd = lc & 1;
    const u32 sel0 = odd ? 0x01000c0cu : 0x0c0c0100u, sel1 = odd ? 0x03020c0cu : 0x0c0c0302u;
    u32x2 Ob[4];
#pragma unroll
    for (int q = 0; q < 4; ++q) {
      ldw2(7 + 2 * q, 0, 8); ldw2(8 + 2 * q, 0, 8);
      SB();
      f32x4 mp = mP;
      if (TYPE == 0 && q == 0 && g == 0) mp = f32x4{-1e30f, -1e30f, -1e30f, -1e30f};
      const f32x4 sp = mfma16k16(Kb[q], Qb[q], mp), sc = mfma16k16(Kb[q + 1], Qb[q], mC);
      const float a0 = odd ? sp[1] : sp[0], a1 = odd ? sp[3] : sp[2], a2 = odd ? sc[1] : sc[0], a3 = odd ? sc[3] : sc[2];
      const float mx = max_g(fmaxf(fmaxf(a0, a1), fmaxf(a2, a3)));
      const u32 u0 = pk_bf16(__builtin_amdgcn_exp2f(a0 - mx), __builtin_amdgcn_exp2f(a1 - mx));
      const u32 u1 = pk_bf16(__builtin_amdgcn_exp2f(a2 - mx), __builtin_amdgcn_exp2f(a3 - mx));
      const u32x4 pb = {__builtin_amdgcn_perm(0, u0, sel0), __builtin_amdgcn_perm(0, u0, sel1),
                        __builtin_amdgcn_perm(0, u1, sel0), __builtin_amdgcn_perm(0, u1, sel1)};
      const u32x4 vf = {Vb[q][0], Vb[q][1], Vb[q + 1][0], Vb[q + 1][1]};
      const f32x4 z = {0.f, 0.f, 0.f, 0.f};
      const f32x4 o = mfma16(vf, pb, z), l = mfma16(ones, pb, z);
      const f32x4 on = o * splat4(__builtin_amdgcn_rcpf(l[0]));
      Ob[q] = u32x2{pk_bf16(on[0], on[1]), pk_bf16(on[2], on[3])};
      SB();
    }
    const int rt = 4 * g + (TYPE == 0 ? 0 : 2), ks = 4 * j + h;
    __builtin_amdgcn_raw_buffer_store_b128(pair_rows(Ob[0], Ob[1]), rsA2, lane * 16, (rt * NKS + ks) * 1024, AUX_SC1);
    __builtin_amdgcn_raw_buffer_store_b128(pair_rows(Ob[2], Ob[3]), rsA2, lane * 16, ((rt + 1) * NKS + ks) * 1024, AUX_SC1);
    asm volatile("s_waitcnt vmcnt(0)\n\ts_barrier" ::: "memory");
  } else {
    const int m = w & 3;
    f32x4 mc[2], rc[2];
#pragma unroll
    for (int i = 0; i < 2; ++i) {
      mc[i] = splat4(*(LAS float*)(L + L_MEAN + ((2 * m + i) * 16 + lc) * 4));
      rc[i] = splat4(*(LAS float*)(L + L_RSTD + ((2 * m + i) * 16 + lc) * 4));
    }
    const int nct = j == 15 ? 9 : 11;
    const int rt = 4 * g + m, ksb = 32 + 11 * (j - 8);
    const f32x4 C1 = splat4(-2.f * 1.4426950408889634f * 0.7978845608028654f);
    const f32x4 C2 = splat4(-2.f * 1.4426950408889634f * 0.7978845608028654f * 0.044715f);
    const f32x4 ONE = splat4(1.f);
#pragma unroll
    for (int c = 0; c < NC; ++c) {
      if (c < 4) { ldw2(7 + 2 * c, 0, 8); ldw2(8 + 2 * c, 0, 8); }
      SB();
      const int ct = CB + (c + crot >= NC ? c + crot - NC : c + crot);
      const f32x4 cs = *(LAS f32x4*)(L + L_CS + (ct * 16 + lg * 4) * 4);
      const f32x4 bb = *(LAS f32x4*)(L + L_B1 + (ct * 16 + lg * 4) * 4);
      u32x2 hb[2];
#pragma unroll
      for (int i = 0; i < 2; ++i) {
        const f32x4 v = (acc[i][c] - mc[i] * cs) * rc[i] + bb;
        const f32x4 zz = v * (v * v * C2 + C1);
        f32x4 e;
#pragma unroll
        for (int r = 0; r < 4; ++r) e[r] = __builtin_amdgcn_exp2f(zz[r]);
        e += ONE;
#pragma unroll
        for (int r = 0; r < 4; ++r) e[r] = __builtin_amdgcn_rcpf(e[r]);
        const f32x4 y = v * e;
        hb[i] = u32x2{pk_bf16(y[0], y[1]), pk_bf16(y[2], y[3])};
      }
      __builtin_amdgcn_raw_buffer_store_b128(pair_rows(hb[0], hb[1]), rsA2, lane * 16,
                                             ct < nct ? (rt * NKS + ksb + ct) * 1024 : A2_BYTES, AUX_SC1);
      SB();
    }
    asm volatile("s_waitcnt vmcnt(0)\n\ts_barrier" ::: "memory");
  }
}

template <int TYPE>
DEV void body(LAS char* L, const char* __restrict__ x, const char* __restrict__ wq, const char* __restrict__ wo,
              const char* __restrict__ w1, const u16* __restrict__ b1, const char* __restrict__ w2, const char* __restrict__ b2,
              char* __restrict__ out, char* __restrict__ ws, int g, int j, int w, int lane STAMP_ARG) {
  const int mt = 2 * g + (j & 1), nt = j >> 1;
  const __amdgpu_buffer_rsrc_t rsA2 = __builtin_amdgcn_make_buffer_rsrc(ws + WS_A2, 0, A2_BYTES, 0x00020000);
  const int ks0 = w < 6 ? 15 * w : 90 + 14 * (w - 6);
  const u32 w2voff = (lane >> 5) * 8 * LDW2 + nt * 128 + (lane & 31) * 4;
  GAS u64* ctl = (GAS u64*)(ws + g * 4096);
  const u64 tag = __hip_atomic_load(ctl, __ATOMIC_RELAXED, __HIP_MEMORY_SCOPE_AGENT) + 1;
  P2W W;
  phase1<TYPE>(L, x, wq, w1, b1, wo, w2, ws, g, j, w, lane, rsA2, w2voff, ks0, W STAMP_PASS);
  if (threadIdx.x == 0) __hip_atomic_store(ctl + 16 * (j + 1), tag, __ATOMIC_RELAXED, __HIP_MEMORY_SCOPE_AGENT);
  STAMP(5);
  const int orow = mt * 64 + (w >> 2) * 32 + (lane & 31), ocol = nt * 64 + (w & 3) * 16 + (lane >> 5) * 8;
  const u32 ooff = (u32)(orow * LDX + ocol * 2);
  const u32x4 xres = *(const GAS u32x4*)(x + ooff);
  const u32x4 bres = *(const GAS u32x4*)(b2 + (u32)(ocol * 2));
  u32x4 wf[15][2];
#pragma unroll
  for (int s = 0; s < 15; ++s)
#pragma unroll
    for (int d = 0; d < 4; ++d) {
      wf[s][0][d] = __builtin_amdgcn_perm(W.r[s * 8 + 2 * d + 1], W.r[s * 8 + 2 * d], 0x05040100u);
      wf[s][1][d] = __builtin_amdgcn_perm(W.r[s * 8 + 2 * d + 1], W.r[s * 8 + 2 * d], 0x07060302u);
    }
#pragma unroll
  for (int s = 0; s < 15; ++s) asm volatile("" : "+v"(wf[s][0]), "+v"(wf[s][1]));
  SB();
  STAMP(6);
  {
    const int ke = min(ks0 + 14, NKS - 1);
    const int plo = ks0 < 32 ? ks0 >> 2 : 8 + (ks0 - 32) / 11, phi = ke < 32 ? ke >> 2 : 8 + (ke - 32) / 11;
    const int pj = min(plo + (lane & 7), phi);
#pragma nounroll
    for (u32 spins = 0; spins < SPIN_CAP; ++spins) {
      const u64 v = __hip_atomic_load(ctl + 16 * (pj + 1), __ATOMIC_RELAXED, __HIP_MEMORY_SCOPE_AGENT);
      if (__all(v == tag)) break;
      __builtin_amdgcn_s_sleep(1);
    }
    __builtin_amdgcn_fence(__ATOMIC_ACQUIRE, "wavefront");
  }
  STAMP(7);
  const int nks = w < 6 ? 15 : 14;
  u32x4 af[2][15];
#pragma unroll
  for (int r = 0; r < 2; ++r)
#pragma unroll
    for (int s = 0; s < 15; ++s) {
      if (r == 1 && s >= 5) continue;
      const int so = (s < nks) ? ((2 * mt + r) * NKS + ks0 + s) * 1024 : A2_BYTES;
      af[r][s] = __builtin_amdgcn_raw_buffer_load_b128(rsA2, lane * 16, so, AUX_SC1);
    }
  SB();
#pragma unroll
  for (int r = 0; r < 2; ++r) {
    f32x16 a0, a1;
#pragma unroll
    for (int i = 0; i < 16; ++i) a0[i] = a1[i] = 0.f;
#pragma unroll
    for (int s = 0; s < 15; ++s) {
      a0 = mfma32(wf[s][0], af[r][s], a0);
      a1 = mfma32(wf[s][1], af[r][s], a1);
      if (r == 0 && s < 10) {
        const int s2 = s + 5;
        const int so = (s2 < nks) ? ((2 * mt + 1) * NKS + ks0 + s2) * 1024 : A2_BYTES;
        af[1][s2] = __builtin_amdgcn_raw_buffer_load_b128(rsA2, lane * 16, so, AUX_SC1);
      }
      SB();
    }
#pragma unroll
    for (int y = 0; y < 4; ++y) {
      *(LAS f32x4*)(L + ((w * 2 + r) * 8 + y * 2) * 1024 + lane * 16) = f32x4{a0[4 * y], a0[4 * y + 1], a0[4 * y + 2], a0[4 * y + 3]};
      *(LAS f32x4*)(L + ((w * 2 + r) * 8 + y * 2 + 1) * 1024 + lane * 16) = f32x4{a1[4 * y], a1[4 * y + 1], a1[4 * y + 2], a1[4 * y + 3]};
    }
    SB();
  }
  STAMP(8);
  lds_bar();
  if (j == 0 && threadIdx.x == 0) __hip_atomic_store(ctl, tag, __ATOMIC_RELAXED, __HIP_MEMORY_SCOPE_AGENT);
  f32x4 s0 = {0.f, 0.f, 0.f, 0.f}, s1 = s0;
#pragma unroll
  for (int sw = 0; sw < 8; ++sw) {
    s0 += *(LAS f32x4*)(L + ((sw * 2 + (w >> 2)) * 8 + (w & 3) * 2) * 1024 + lane * 16);
    s1 += *(LAS f32x4*)(L + ((sw * 2 + (w >> 2)) * 8 + (w & 3) * 2 + 1) * 1024 + lane * 16);
  }
  u32x4 o;
#pragma unroll
  for (int d = 0; d < 4; ++d)
    o[d] = pk_bf16(s0[d] + bf_lo(xres[d]) + bf_lo(bres[d]), s1[d] + bf_hi(xres[d]) + bf_hi(bres[d]));
  *(GAS u32x4*)(out + ooff) = o;
  STAMP(9);
  STAMP_OUT();
}

__global__ __launch_bounds__(512, 2) void fused(const char* __restrict__ x, const char* __restrict__ wq, const char* __restrict__ wo,
                                                const char* __restrict__ w1, const u16* __restrict__ b1, const char* __restrict__ w2,
                                                const char* __restrict__ b2, char* __restrict__ out, char* __restrict__ ws) {
  extern __shared__ __attribute__((aligned(16))) char smem[];
  LAS char* L = (LAS char*)smem;
  STAMP(0);
  const int lane = threadIdx.x & 63, w = __builtin_amdgcn_readfirstlane(threadIdx.x >> 6);
  const int b = blockIdx.x, g = (b & 7) * 2 + ((b >> 3) & 1), j = b >> 4;
  if (j < 8) {
    if (w < 4) body<0>(L, x, wq, wo, w1, b1, w2, b2, out, ws, g, j, w, lane STAMP_PASS);
    else body<1>(L, x, wq, wo, w1, b1, w2, b2, out, ws, g, j, w, lane STAMP_PASS);
  } else {
    if (w < 4) body<2>(L, x, wq, wo, w1, b1, w2, b2, out, ws, g, j, w, lane STAMP_PASS);
    else body<3>(L, x, wq, wo, w1, b1, w2, b2, out, ws, g, j, w, lane STAMP_PASS);
  }
}
}

extern "C" void kernel_launch(void* const* d_in, const int* in_sizes, int n_in, void* d_out, int out_size, void* d_ws,
                              size_t ws_size, hipStream_t stream) {
  (void)hipFuncSetAttribute((const void*)fk::fused, hipFuncAttributeMaxDynamicSharedMemorySize, fk::L_TOTAL);
  fk::fused<<<256, 512, fk::L_TOTAL, stream>>>((const char*)d_in[0], (const char*)d_in[1], (const char*)d_in[2], (const char*)d_in[4],
                                               (const u16*)d_in[5], (const char*)d_in[6], (const char*)d_in[7], (char*)d_out,
                                               (char*)d_ws);
}
```

```cpp
#define PF_CODE
#define PF_DATA
#define MAP2
#include <hip/hip_runtime.h>
#include <stdint.h>

typedef unsigned u32;
typedef unsigned long long u64;
typedef unsigned short u16;
typedef short s16x4 __attribute__((ext_vector_type(4)));
typedef short s16x8 __attribute__((ext_vector_type(8)));
typedef __bf16 b16x2 __attribute__((ext_vector_type(2)));
typedef float f32x2 __attribute__((ext_vector_type(2)));
typedef float f32x4 __attribute__((ext_vector_type(4)));
typedef float f32x16 __attribute__((ext_vector_type(16)));
typedef u32 u32x2 __attribute__((ext_vector_type(2)));
typedef u32 u32x4 __attribute__((ext_vector_type(4)));
#define LAS __attribute__((address_space(3)))
#define GAS __attribute__((address_space(1)))
#define DEV __device__ __forceinline__

namespace fk {
constexpr int LDX = 1024, LDQKV = 3072, LDW1 = 2752, LDW2 = 1024;
constexpr int NKS = 118;
constexpr int SLOT = 43008, BOFF = 18432;
constexpr int L_MEAN = 3 * SLOT, L_RSTD = L_MEAN + 576, L_CS = L_RSTD + 576, L_B1 = L_CS + 768, L_KV = L_B1 + 768,
              L_JUNK = L_KV + 8192, L_TOTAL = L_JUNK + 2048;
constexpr int WS_A2 = 65536, A2_BYTES = 64 * NKS * 1024;
constexpr int AUX_SC1 = 16;
constexpr u32 SPIN_CAP = 1u << 16;

#define STAMP(i) do {} while (0)
#define STAMP_ARG
#define STAMP_PASS
#define STAMP_OUT() do {} while (0)

DEV void dma16(u32 voff, const void* sbase, u32 lds) {
  asm volatile("s_mov_b32 m0, %2\n\ts_nop 0\n\tglobal_load_lds_dwordx4 %0, %1" ::"v"(voff), "s"(sbase), "s"(lds) : "memory");
}
DEV void dma16_masked(u32 voff, const void* sbase, u32 lds, u32 mlo) {
  asm volatile("s_mov_b32 m0, %2\n\ts_mov_b32 exec_lo, %3\n\tglobal_load_lds_dwordx4 %0, %1\n\ts_mov_b32 exec_lo, -1"
               ::"v"(voff), "s"(sbase), "s"(lds), "s"(mlo) : "memory");
}
DEV void dma16_if(u32 voff, const void* sbase, u32 lds, u32 cond) {
  asm volatile("s_cmp_gt_u32 %3, 5\n\ts_cbranch_scc1 1f\n\ts_mov_b32 m0, %2\n\ts_nop 0\n\tglobal_load_lds_dwordx4 %0, %1\n1:"
               ::"v"(voff), "s"(sbase), "s"(lds), "s"(cond) : "memory", "scc");
}
DEV void dma4(u32 voff, const void* sbase, u32 lds) {
  asm volatile("s_mov_b32 m0, %2\n\ts_nop 0\n\tglobal_load_lds_dword %0, %1" ::"v"(voff), "s"(sbase), "s"(lds) : "memory");
}
template <int N> DEV void wait_bar() {
  asm volatile("s_waitcnt vmcnt(%0) lgkmcnt(0)\n\ts_barrier" ::"i"(N) : "memory");
}
DEV void lds_bar() { asm volatile("s_waitcnt lgkmcnt(0)\n\ts_barrier" ::: "memory"); }
#define SB() __builtin_amdgcn_sched_barrier(0)

DEV u32 pk_bf16(float a, float b) {
  f32x2 p = {a, b};
  return __builtin_bit_cast(u32, __builtin_convertvector(p, b16x2));
}
DEV float bf_lo(u32 v) { return __builtin_bit_cast(float, v << 16); }
DEV float bf_hi(u32 v) { return __builtin_bit_cast(float, v & 0xffff0000u); }
DEV float dot2(u32 a, u32 b, float c) {
  return __builtin_amdgcn_fdot2_f32_bf16(__builtin_bit_cast(b16x2, a), __builtin_bit_cast(b16x2, b), c, false);
}
DEV f32x4 mfma16(u32x4 a, u32x4 b, f32x4 c) {
  return __builtin_amdgcn_mfma_f32_16x16x32_bf16(__builtin_bit_cast(s16x8, a), __builtin_bit_cast(s16x8, b), c, 0, 0, 0);
}
DEV f32x4 mfma16k16(u32x2 a, u32x2 b, f32x4 c) {
  return __builtin_amdgcn_mfma_f32_16x16x16bf16_1k(__builtin_bit_cast(s16x4, a), __builtin_bit_cast(s16x4, b), c, 0, 0, 0);
}
DEV f32x16 mfma32(u32x4 a, u32x4 b, f32x16 c) {
  return __builtin_amdgcn_mfma_f32_32x32x16_bf16(__builtin_bit_cast(s16x8, a), __builtin_bit_cast(s16x8, b), c, 0, 0, 0);
}
DEV u32x4 lds128(LAS char* L, u32 off) { return *(LAS u32x4*)(L + off); }
DEV u32x2 ldstr(LAS char* L, u32 off) {
  return __builtin_bit_cast(u32x2, __builtin_amdgcn_ds_read_tr16_b64_v4i16((LAS s16x4*)(L + off)));
}
template <int CTRL> DEV float dpp_add(float v) {
  int t = __builtin_amdgcn_update_dpp(0, __builtin_bit_cast(int, v), CTRL, 0xf, 0xf, true);
  return v + __builtin_bit_cast(float, t);
}
DEV float red8(float v) {
  v = dpp_add<0xB1>(v);
  v = dpp_add<0x4E>(v);
  v = dpp_add<0x141>(v);
  return v;
}
DEV float max_g(float v) {
  u32 u = __builtin_bit_cast(u32, v);
  auto r = __builtin_amdgcn_permlane32_swap(u, u, false, false);
  v = fmaxf(__builtin_bit_cast(float, r[0]), __builtin_bit_cast(float, r[1]));
  u = __builtin_bit_cast(u32, v);
  auto q = __builtin_amdgcn_permlane16_swap(u, u, false, false);
  return fmaxf(__builtin_bit_cast(float, q[0]), __builtin_bit_cast(float, q[1]));
}
DEV u32x4 pair_rows(u32x2 x, u32x2 y) {
  auto a = __builtin_amdgcn_permlane16_swap(x[0], y[0], false, false);
  auto b = __builtin_amdgcn_permlane16_swap(x[1], y[1], false, false);
  return u32x4{a[0], b[0], a[1], b[1]};
}

DEV f32x4 splat4(float v) {
  f32x2 p = {v, v};
  asm volatile("" : "+v"(p));
  return f32x4{p[0], p[1], p[0], p[1]};
}
template <int V> struct IC { static constexpr int v = V; };
template <int LO, int HI, class F> DEV void sfor(F&& f) {
  if constexpr (LO < HI) { f(IC<LO>{}); sfor<LO + 1, HI>(f); }
}
struct P2W { u32 r[120]; };

template <int TYPE>
DEV void phase1(LAS char* L, const char* x, const char* wq, const char* w1, const u16* b1, const char* wo, const char* w2,
                char* ws, const char* outp, u64 code0, int g, int j, int w, int lane, __amdgpu_buffer_rsrc_t rsA2, u32 w2voff, int ks0, P2W& W STAMP_ARG) {
  constexpr bool FFN = TYPE >= 2;
  constexpr int NT = TYPE == 0 ? 5 : (TYPE == 1 ? 4 : 2);
  constexpr int NC = TYPE == 2 ? 6 : (TYPE == 3 ? 5 : 3);
  constexpr int CB = TYPE == 3 ? 6 : 0;
  constexpr int NSP = TYPE == 1 ? 3 : 2;
  constexpr int ND = TYPE == 1 ? 6 : 5;
  constexpr int NM = NT * NC + (FFN ? 2 : 0);
  const int r8 = lane >> 3, pos = lane & 7, lg = lane >> 4, lc = lane & 15;
  const int rot = (j + g) & 7;
  constexpr int ldw = FFN ? LDW1 : LDQKV;
  const u32 voffA = r8 * LDX + ((pos ^ r8) << 4);
  const int swB = (r8 & 2) | ((w & 1) << 2);
  const u32 voffB = r8 * ldw + ((pos ^ swB) << 4);
  const int maxch = (j == 15) ? 1 : 5;
  const u32 voffB2 = FFN ? (u32)(r8 * ldw + (min(pos ^ swB, maxch) << 4)) : voffB;
  const int rowbase = FFN ? 128 * g : 128 * g - 16;
  const int p0row = (!FFN && g == 0 && w < 2) ? 8 * w : rowbase + 8 * w;
  const char* sA0 = x + (size_t)p0row * LDX;
  const char* sA1 = x + (size_t)(rowbase + 8 * (w + 8)) * LDX;
  const char* sA2 = x + (size_t)(rowbase + 8 * (16 + (w & 1))) * LDX;
  const char* sB0 = FFN ? w1 + (size_t)(8 * w) * LDW1 + (j - 8) * 352 : wq + (size_t)(8 * w) * LDQKV + j * 128;
  constexpr int SEGB = FFN ? 128 : 1024;
  const u32 mlo = (w == 0) ? 0xFFFFFF00u : 0xFFFFFFFFu;
  auto issue = [&](int i, int k) {
    const int kc = (i + rot) & 7;
    const u32 slot = (i % 3) * SLOT;
    const u32 va = voffA + kc * 128, vb = voffB + kc * 64 * ldw, vb2 = voffB2 + kc * 64 * ldw;
    if (k == 0) { if (TYPE == 0) dma16_masked(va, sA0, slot + w * 1024, mlo); else dma16(va, sA0, slot + w * 1024); }
    if (k == 1) dma16(vb, sB0, slot + BOFF + w * 1024);
    if (k == 2) dma16(vb, sB0 + SEGB, slot + BOFF + (8 + w) * 1024);
    if (k == 3) dma16(vb2, sB0 + 2 * SEGB, slot + BOFF + (16 + w) * 1024);
    if (k == 4) dma16(va, sA1, slot + (w + 8) * 1024);
    if (k == 5) dma16_if(va, sA2, slot + (16 + (w & 1)) * 1024, w);
  };
#pragma unroll
  for (int i = 0; i < 3; ++i)
#pragma unroll
    for (int k = 0; k < ND; ++k) issue(i, k);
  {
    const int bi = blockIdx.x >> 3;
    if (w == 7) {
      dma4(min(lane * 128 + (bi & 7) * 8192, 61000), (const void*)code0, L_JUNK);
    }
    if (w == 6) {
      const int ln = blockIdx.x * 59 + min(lane, 58);
      if (blockIdx.x * 59 + 58 < 4096) dma4(ln * 128, wo, L_JUNK + 256);
      else dma4((u32)max(ln - 4096, 0) * 128, w2, L_JUNK + 256);
    }
    if (w == 5) {
      dma4((lane & 3) * (2u << 20) + WS_A2 + (blockIdx.x & 63) * 4096, ws, L_JUNK + 512);
      dma4(blockIdx.x * 8192 + lane * 4, outp, L_JUNK + 768);
    }
  }
  u32 b1v = 0;
  if (FFN) b1v = b1[min((j - 8) * 176 + (int)threadIdx.x, 1375)];
  if (TYPE == 0 && w == 0 && lane < 8) {
    const u32x4 one = {0x3F803F80u, 0x3F803F80u, 0x3F803F80u, 0x3F803F80u};
#pragma unroll
    for (int s = 0; s < 3; ++s) *(LAS u32x4*)(L + s * SLOT + lane * 16) = one;
  }
  const int T0 = TYPE == 0 ? 0 : (TYPE == 1 ? 5 : 2 * (w & 3));
  const u32 aA = T0 * 2048 + (lc >> 3) * 1024 + (lc & 7) * 128 + ((lg ^ (lc & 7)) << 4);
  const int tq = lc >> 2, tp = lc & 3;
  const int swR = (tq & 2) | ((lg & 1) << 2);
  const u32 aBl = BOFF + lg * 1024 + tq * 128 + ((tp >> 1) << 4) + (tp & 1) * 8;
  const int crot = min(2 * (w & 3), NC - 2);
  u32 aB[NC];
#pragma unroll
  for (int c = 0; c < NC; ++c) {
    const int cc = c + crot >= NC ? c + crot - NC : c + crot;
    const int ct = FFN ? CB + cc : 4 * c + (w & 3);
    aB[c] = aBl + (ct >> 2) * 8192 + (((2 * (ct & 3)) ^ swR) << 4);
  }
  u32 aS[NSP];
  aS[0] = w * 1024 + lane * 16; aS[1] = (w + 8) * 1024 + lane * 16;
  if (NSP == 3) aS[NSP - 1] = (16 + (w & 1)) * 1024 + lane * 16;

  f32x4 acc[NT][NC], acc1[2];
#pragma unroll
  for (int t = 0; t < NT; ++t)
#pragma unroll
    for (int c = 0; c < NC; ++c) acc[t][c] = f32x4{0.f, 0.f, 0.f, 0.f};
  acc1[0] = acc1[1] = f32x4{0.f, 0.f, 0.f, 0.f};
  float ssum[NSP], ssq[NSP];
#pragma unroll
  for (int i = 0; i < NSP; ++i) ssum[i] = ssq[i] = 0.f;
  u32x4 fa[2][NT], sv[NSP];
  u32x2 fb[2][NC][2];
  auto rd = [&](int t, int k) {
    const u32 slot = ((t >> 1) % 3) * SLOT;
    const int b = t & 1;
    if (k < 2 * NC) fb[b][k >> 1][k & 1] = ldstr(L, aB[k >> 1] + slot + b * 4096 + (k & 1) * 512);
    else if (k < 2 * NC + NT) fa[b][k - 2 * NC] = lds128(L, ((aA ^ (b * 64)) + slot + (k - 2 * NC) * 2048));
    else sv[k - 2 * NC - NT] = lds128(L, aS[k - 2 * NC - NT] + slot);
  };
  auto ldw2 = [&](int s, int lo, int hi) {
    const int ks = ks0 + s;
    const bool isO = ks < 32;
    const char* base = isO ? wo + (size_t)ks * 16 * LDW2 : w2 + (size_t)(ks - 32) * 16 * LDW2;
    const __amdgpu_buffer_rsrc_t rs = __builtin_amdgcn_make_buffer_rsrc((void*)base, 0, ks < NKS ? 16 * LDW2 : 0, 0x00020000);
#pragma unroll
    for (int jj = lo; jj < hi; ++jj)
      W.r[s * 8 + jj] = __builtin_amdgcn_raw_buffer_load_b32(rs, w2voff + (jj & 3) * 1024, (jj >> 2) * 4096, 0);
  };
  const u32x4 ones = {0x3F803F80u, 0x3F803F80u, 0x3F803F80u, 0x3F803F80u};
  STAMP(1);
  wait_bar<10>();
  STAMP(2);
#pragma unroll
  for (int k = 0; k < 2 * NC + NT + NSP; ++k) rd(0, k);
  SB();
  sfor<0, 16>([&](auto tc) {
    constexpr int t = decltype(tc)::v, s = t >> 1, b = t & 1;
    if constexpr (b) { if constexpr (s <= 4) wait_bar<5>(); else if constexpr (s == 5) wait_bar<21>(); else if constexpr (s == 6) wait_bar<32>(); }
    constexpr int nr = t == 15 ? 0 : 2 * NC + NT + (b ? NSP : 0);
    constexpr int g0 = (nr + 1) / 2;
    constexpr int nd = (b && s + 3 < 8) ? ND : 0, nw = t >= 9 ? 8 : 0;
    constexpr int per = (nd + nw + NM - g0 - 1) / (NM - g0);
    constexpr int ndot = b ? 0 : 4 * NSP, dper = (ndot + NM - 1) / NM;
    sfor<0, NM>([&](auto qc) {
      constexpr int q = decltype(qc)::v;
      if constexpr (q < NT * NC) {
        constexpr int c = q / NT, i = q % NT;
        const u32x4 wfr = {fb[b][c][0][0], fb[b][c][0][1], fb[b][c][1][0], fb[b][c][1][1]};
        if constexpr (!FFN && c == 2) acc[i][c] = mfma16(fa[b][i], wfr, acc[i][c]);
        else acc[i][c] = mfma16(wfr, fa[b][i], acc[i][c]);
      } else {
        constexpr int c = q - NT * NC;
        const u32x4 wfr = {fb[b][c][0][0], fb[b][c][0][1], fb[b][c][1][0], fb[b][c][1][1]};
        acc1[c] = mfma16(wfr, ones, acc1[c]);
      }
      if constexpr (2 * q < nr) rd(t + 1, 2 * q);
      if constexpr (2 * q + 1 < nr) rd(t + 1, 2 * q + 1);
      if constexpr (q >= g0) {
        sfor<(q - g0) * per, (q - g0 + 1) * per>([&](auto uc) {
          constexpr int u = decltype(uc)::v;
          if constexpr (u < nd) issue(s + 3, u);
          else if constexpr (u < nd + nw) ldw2(t - 9, u - nd, u - nd + 1);
        });
      }
      sfor<q * dper, (q + 1) * dper>([&](auto uc) {
        constexpr int u = decltype(uc)::v;
        if constexpr (u < ndot) {
          constexpr int i = u >> 2, d = u & 3;
          ssum[i] = dot2(sv[i][d], 0x3F803F80u, ssum[i]);
          ssq[i] = dot2(sv[i][d], sv[i][d], ssq[i]);
        }
      });
      SB();
    });
  });
  if (FFN) asm volatile("" : "+v"(acc1[0]), "+v"(acc1[1]));
#pragma unroll
  for (int i = 0; i < NSP; ++i) asm volatile("" : "+v"(ssum[i]), "+v"(ssq[i]));
  STAMP(3);
#pragma unroll
  for (int i = 0; i < NSP; ++i) {
    const float sm = red8(ssum[i]) * (1.f / 512.f), sq = red8(ssq[i]) * (1.f / 512.f);
    const float var = fmaxf(sq - sm * sm, 0.f);
    const float rs = __builtin_amdgcn_rsqf(var + 1e-5f);
    const int p = i == 0 ? w : (i == 1 ? w + 8 : 16 + (w & 1));
    if (pos == 0) { *(LAS float*)(L + L_MEAN + (p * 8 + r8) * 4) = sm; *(LAS float*)(L + L_RSTD + (p * 8 + r8) * 4) = rs; }
  }
  if (TYPE == 0) {
    const int h = w & 3;
    if (lc == 0) {
      *(LAS f32x4*)(L + L_CS + (h * 16 + lg * 4) * 4) = acc[0][0];
      *(LAS f32x4*)(L + L_CS + (64 + h * 16 + lg * 4) * 4) = acc[0][1];
    }
    if (lg == 0) *(LAS float*)(L + L_CS + (128 + h * 16 + lc) * 4) = acc[0][2][0];
    *(LAS f32x4*)(L + L_KV + h * 2048 + lane * 16) = acc[NT - 1][1];
    *(LAS f32x4*)(L + L_KV + h * 2048 + 1024 + lane * 16) = acc[NT - 1][2];
  }
  if (FFN) {
    if (lc == 0) {
      *(LAS f32x4*)(L + L_CS + ((CB + crot) * 16 + lg * 4) * 4) = acc1[0];
      *(LAS f32x4*)(L + L_CS + ((CB + crot + 1) * 16 + lg * 4) * 4) = acc1[1];
    }
    if (threadIdx.x < 192) *(LAS float*)(L + L_B1 + threadIdx.x * 4) = bf_lo(b1v);
  }
  lds_bar();
  STAMP(4);
  if (!FFN) {
    const int h = w & 3;
    constexpr int NK = 5;
    const int TK = TYPE == 0 ? 0 : 4;
    const f32x4 csq = *(LAS f32x4*)(L + L_CS + (h * 16 + lg * 4) * 4);
    const f32x4 csk = *(LAS f32x4*)(L + L_CS + (64 + h * 16 + lg * 4) * 4);
    const f32x4 csv = splat4(*(LAS float*)(L + L_CS + (128 + h * 16 + lc) * 4));
    u32x2 Qb[4], Kb[NK], Vb[NK];
#pragma unroll
    for (int i = 0; i < NK; ++i) {
      const int T = TK + i;
      f32x4 ak, av;
      if (TYPE == 1 && i == 0) {
        ak = *(LAS f32x4*)(L + L_KV + h * 2048 + lane * 16);
        av = *(LAS f32x4*)(L + L_KV + h * 2048 + 1024 + lane * 16);
      } else {
        ak = acc[TYPE == 0 ? i : (i > 0 ? i - 1 : 0)][1];
        av = acc[TYPE == 0 ? i : (i > 0 ? i - 1 : 0)][2];
      }
      const float rc1 = *(LAS float*)(L + L_RSTD + (T * 16 + lc) * 4);
      const f32x4 mc = splat4(*(LAS float*)(L + L_MEAN + (T * 16 + lc) * 4)), rc = splat4(rc1);
      const f32x4 m4 = *(LAS f32x4*)(L + L_MEAN + (T * 16 + lg * 4) * 4), r4 = *(LAS f32x4*)(L + L_RSTD + (T * 16 + lg * 4) * 4);
      const f32x4 kk = (ak - mc * csk) * rc;
      const f32x4 vv = (av - m4 * csv) * r4;
      Kb[i] = u32x2{pk_bf16(kk[0], kk[1]), pk_bf16(kk[2], kk[3])};
      Vb[i] = u32x2{pk_bf16(vv[0], vv[1]), pk_bf16(vv[2], vv[3])};
      if (i > 0) {
        const f32x4 qq = (acc[TYPE == 0 ? i : i - 1][0] - mc * csq) * splat4(rc1 * (0.25f * 1.4426950408889634f));
        Qb[i - 1] = u32x2{pk_bf16(qq[0], qq[1]), pk_bf16(qq[2], qq[3])};
      }
    }
    SB();
    f32x4 mP, mC;
#pragma unroll
    for (int r = 0; r < 4; ++r) {
      mP[r] = (4 * lg + r >= lc + 2) ? 0.f : -1e30f;
      mC[r] = (4 * lg + r <= lc) ? 0.f : -1e30f;
    }
    const bool odd = lc & 1;
    const u32 sel0 = odd ? 0x01000c0cu : 0x0c0c0100u, sel1 = odd ? 0x03020c0cu : 0x0c0c0302u;
    u32x2 Ob[4];
#pragma unroll
    for (int q = 0; q < 4; ++q) {
      ldw2(7 + 2 * q, 0, 8); ldw2(8 + 2 * q, 0, 8);
      SB();
      f32x4 mp = mP;
      if (TYPE == 0 && q == 0 && g == 0) mp = f32x4{-1e30f, -1e30f, -1e30f, -1e30f};
      const f32x4 sp = mfma16k16(Kb[q], Qb[q], mp), sc = mfma16k16(Kb[q + 1], Qb[q], mC);
      const float a0 = odd ? sp[1] : sp[0], a1 = odd ? sp[3] : sp[2], a2 = odd ? sc[1] : sc[0], a3 = odd ? sc[3] : sc[2];
      const float mx = max_g(fmaxf(fmaxf(a0, a1), fmaxf(a2, a3)));
      const u32 u0 = pk_bf16(__builtin_amdgcn_exp2f(a0 - mx), __builtin_amdgcn_exp2f(a1 - mx));
      const u32 u1 = pk_bf16(__builtin_amdgcn_exp2f(a2 - mx), __builtin_amdgcn_exp2f(a3 - mx));
      const u32x4 pb = {__builtin_amdgcn_perm(0, u0, sel0), __builtin_amdgcn_perm(0, u0, sel1),
                        __builtin_amdgcn_perm(0, u1, sel0), __builtin_amdgcn_perm(0, u1, sel1)};
      const u32x4 vf = {Vb[q][0], Vb[q][1], Vb[q + 1][0], Vb[q + 1][1]};
      const f32x4 z = {0.f, 0.f, 0.f, 0.f};
      const f32x4 o = mfma16(vf, pb, z), l = mfma16(ones, pb, z);
      const f32x4 on = o * splat4(__builtin_amdgcn_rcpf(l[0]));
      Ob[q] = u32x2{pk_bf16(on[0], on[1]), pk_bf16(on[2], on[3])};
      SB();
    }
    const int rt = 4 * g + (TYPE == 0 ? 0 : 2), ks = 4 * j + h;
    __builtin_amdgcn_raw_buffer_store_b128(pair_rows(Ob[0], Ob[1]), rsA2, lane * 16, (rt * NKS + ks) * 1024, AUX_SC1);
    __builtin_amdgcn_raw_buffer_store_b128(pair_rows(Ob[2], Ob[3]), rsA2, lane * 16, ((rt + 1) * NKS + ks) * 1024, AUX_SC1);
    asm volatile("s_waitcnt vmcnt(0)\n\ts_barrier" ::: "memory");
  } else {
    const int m = w & 3;
    f32x4 mc[2], rc[2];
#pragma unroll
    for (int i = 0; i < 2; ++i) {
      mc[i] = splat4(*(LAS float*)(L + L_MEAN + ((2 * m + i) * 16 + lc) * 4));
      rc[i] = splat4(*(LAS float*)(L + L_RSTD + ((2 * m + i) * 16 + lc) * 4));
    }
    const int nct = j == 15 ? 9 : 11;
    const int rt = 4 * g + m, ksb = 32 + 11 * (j - 8);
    const f32x4 C1 = splat4(-2.f * 1.4426950408889634f * 0.7978845608028654f);
    const f32x4 C2 = splat4(-2.f * 1.4426950408889634f * 0.7978845608028654f * 0.044715f);
    const f32x4 ONE = splat4(1.f);
#pragma unroll
    for (int c = 0; c < NC; ++c) {
      if (c < 4) { ldw2(7 + 2 * c, 0, 8); ldw2(8 + 2 * c, 0, 8); }
      SB();
      const int ct = CB + (c + crot >= NC ? c + crot - NC : c + crot);
      const f32x4 cs = *(LAS f32x4*)(L + L_CS + (ct * 16 + lg * 4) * 4);
      const f32x4 bb = *(LAS f32x4*)(L + L_B1 + (ct * 16 + lg * 4) * 4);
      u32x2 hb[2];
#pragma unroll
      for (int i = 0; i < 2; ++i) {
        const f32x4 v = (acc[i][c] - mc[i] * cs) * rc[i] + bb;
        const f32x4 zz = v * (v * v * C2 + C1);
        f32x4 e;
#pragma unroll
        for (int r = 0; r < 4; ++r) e[r] = __builtin_amdgcn_exp2f(zz[r]);
        e += ONE;
#pragma unroll
        for (int r = 0; r < 4; ++r) e[r] = __builtin_amdgcn_rcpf(e[r]);
        const f32x4 y = v * e;
        hb[i] = u32x2{pk_bf16(y[0], y[1]), pk_bf16(y[2], y[3])};
      }
      __builtin_amdgcn_raw_buffer_store_b128(pair_rows(hb[0], hb[1]), rsA2, lane * 16,
                                             ct < nct ? (rt * NKS + ksb + ct) * 1024 : A2_BYTES, AUX_SC1);
      SB();
    }
    asm volatile("s_waitcnt vmcnt(0)\n\ts_barrier" ::: "memory");
  }
}

template <int TYPE>
DEV void body(LAS char* L, const char* __restrict__ x, const char* __restrict__ wq, const char* __restrict__ wo,
              const char* __restrict__ w1, const u16* __restrict__ b1, const char* __restrict__ w2, const char* __restrict__ b2,
              char* __restrict__ out, char* __restrict__ ws, u64 code0, int g, int j, int w, int lane STAMP_ARG) {
  const int mt = 2 * g + ((j >> 2) & 1), nt = (j & 3) + (j >> 3) * 4;
  const __amdgpu_buffer_rsrc_t rsA2 = __builtin_amdgcn_make_buffer_rsrc(ws + WS_A2, 0, A2_BYTES, 0x00020000);
  const int ks0 = w < 6 ? 15 * w : 90 + 14 * (w - 6);
  const u32 w2voff = (lane >> 5) * 8 * LDW2 + nt * 128 + (lane & 31) * 4;
  GAS u64* ctl = (GAS u64*)(ws + g * 4096);
  const u64 tag = __hip_atomic_load(ctl, __ATOMIC_RELAXED, __HIP_MEMORY_SCOPE_AGENT) + 1;
  P2W W;
  phase1<TYPE>(L, x, wq, w1, b1, wo, w2, ws, out, code0, g, j, w, lane, rsA2, w2voff, ks0, W STAMP_PASS);
  if (threadIdx.x == 0) __hip_atomic_store(ctl + 16 * (j + 1), tag, __ATOMIC_RELAXED, __HIP_MEMORY_SCOPE_AGENT);
  STAMP(5);
  const int orow = mt * 64 + (w >> 2) * 32 + (lane & 31), ocol = nt * 64 + (w & 3) * 16 + (lane >> 5) * 8;
  const u32 ooff = (u32)(orow * LDX + ocol * 2);
  const u32x4 xres = *(const GAS u32x4*)(x + ooff);
  const u32x4 bres = *(const GAS u32x4*)(b2 + (u32)(ocol * 2));
  u32x4 wf[15][2];
#pragma unroll
  for (int s = 0; s < 15; ++s)
#pragma unroll
    for (int d = 0; d < 4; ++d) {
      wf[s][0][d] = __builtin_amdgcn_perm(W.r[s * 8 + 2 * d + 1], W.r[s * 8 + 2 * d], 0x05040100u);
      wf[s][1][d] = __builtin_amdgcn_perm(W.r[s * 8 + 2 * d + 1], W.r[s * 8 + 2 * d], 0x07060302u);
    }
#pragma unroll
  for (int s = 0; s < 15; ++s) asm volatile("" : "+v"(wf[s][0]), "+v"(wf[s][1]));
  SB();
  STAMP(6);
  {
    const int ke = min(ks0 + 14, NKS - 1);
    const int plo = ks0 < 32 ? ks0 >> 2 : 8 + (ks0 - 32) / 11, phi = ke < 32 ? ke >> 2 : 8 + (ke - 32) / 11;
    const int pj = min(plo + (lane & 7), phi);
#pragma nounroll
    for (u32 spins = 0; spins < SPIN_CAP; ++spins) {
      const u64 v = __hip_atomic_load(ctl + 16 * (pj + 1), __ATOMIC_RELAXED, __HIP_MEMORY_SCOPE_AGENT);
      if (__all(v == tag)) break;
      __builtin_amdgcn_s_sleep(1);
    }
    __builtin_amdgcn_fence(__ATOMIC_ACQUIRE, "wavefront");
  }
  STAMP(7);
  const int nks = w < 6 ? 15 : 14;
  u32x4 af[2][15];
#pragma unroll
  for (int r = 0; r < 2; ++r)
#pragma unroll
    for (int s = 0; s < 15; ++s) {
      if (r == 1 && s >= 5) continue;
      const int so = (s < nks) ? ((2 * mt + r) * NKS + ks0 + s) * 1024 : A2_BYTES;
      af[r][s] = __builtin_amdgcn_raw_buffer_load_b128(rsA2, lane * 16, so, AUX_SC1);
    }
  SB();
#pragma unroll
  for (int r = 0; r < 2; ++r) {
    f32x16 a0, a1;
#pragma unroll
    for (int i = 0; i < 16; ++i) a0[i] = a1[i] = 0.f;
#pragma unroll
    for (int s = 0; s < 15; ++s) {
      a0 = mfma32(wf[s][0], af[r][s], a0);
      a1 = mfma32(wf[s][1], af[r][s], a1);
      if (r == 0 && s < 10) {
        const int s2 = s + 5;
        const int so = (s2 < nks) ? ((2 * mt + 1) * NKS + ks0 + s2) * 1024 : A2_BYTES;
        af[1][s2] = __builtin_amdgcn_raw_buffer_load_b128(rsA2, lane * 16, so, AUX_SC1);
      }
      SB();
    }
#pragma unroll
    for (int y = 0; y < 4; ++y) {
      *(LAS f32x4*)(L + ((w * 2 + r) * 8 + y * 2) * 1024 + lane * 16) = f32x4{a0[4 * y], a0[4 * y + 1], a0[4 * y + 2], a0[4 * y + 3]};
      *(LAS f32x4*)(L + ((w * 2 + r) * 8 + y * 2 + 1) * 1024 + lane * 16) = f32x4{a1[4 * y], a1[4 * y + 1], a1[4 * y + 2], a1[4 * y + 3]};
    }
    SB();
  }
  STAMP(8);
  lds_bar();
  if (j == 0 && threadIdx.x == 0) __hip_atomic_store(ctl, tag, __ATOMIC_RELAXED, __HIP_MEMORY_SCOPE_AGENT);
  f32x4 s0 = {0.f, 0.f, 0.f, 0.f}, s1 = s0;
#pragma unroll
  for (int sw = 0; sw < 8; ++sw) {
    s0 += *(LAS f32x4*)(L + ((sw * 2 + (w >> 2)) * 8 + (w & 3) * 2) * 1024 + lane * 16);
    s1 += *(LAS f32x4*)(L + ((sw * 2 + (w >> 2)) * 8 + (w & 3) * 2 + 1) * 1024 + lane * 16);
  }
  u32x4 o;
#pragma unroll
  for (int d = 0; d < 4; ++d)
    o[d] = pk_bf16(s0[d] + bf_lo(xres[d]) + bf_lo(bres[d]), s1[d] + bf_hi(xres[d]) + bf_hi(bres[d]));
  *(GAS u32x4*)(out + ooff) = o;
  STAMP(9);
  STAMP_OUT();
}

__global__ __launch_bounds__(512, 2) void fused(const char* __restrict__ x, const char* __restrict__ wq, const char* __restrict__ wo,
                                                const char* __restrict__ w1, const u16* __restrict__ b1, const char* __restrict__ w2,
                                                const char* __restrict__ b2, char* __restrict__ out, char* __restrict__ ws) {
  extern __shared__ __attribute__((aligned(16))) char smem[];
  LAS char* L = (LAS char*)smem;
  STAMP(0);
  const int lane = threadIdx.x & 63, w = __builtin_amdgcn_readfirstlane(threadIdx.x >> 6);
  u64 code0;
  asm volatile("s_getpc_b64 %0" : "=s"(code0));
  code0 &= ~127ull;
  const int b = blockIdx.x, bi = b >> 3, g = (b & 3) * 4 + (bi & 3), j = ((bi >> 2) & 3) + ((b >> 2) & 1) * 4 + (bi >> 4) * 8;
  if (j < 8) {
    if (w < 4) body<0>(L, x, wq, wo, w1, b1, w2, b2, out, ws, code0, g, j, w, lane STAMP_PASS);
    else body<1>(L, x, wq, wo, w1, b1, w2, b2, out, ws, code0, g, j, w, lane STAMP_PASS);
  } else {
    if (w < 4) body<2>(L, x, wq, wo, w1, b1, w2, b2, out, ws, code0, g, j, w, lane STAMP_PASS);
    else body<3>(L, x, wq, wo, w1, b1, w2, b2, out, ws, code0, g, j, w, lane STAMP_PASS);
  }
}
}

extern "C" void kernel_launch(void* const* d_in, const int* in_sizes, int n_in, void* d_out, int out_size, void* d_ws,
                              size_t ws_size, hipStream_t stream) {
  (void)hipFuncSetAttribute((const void*)fk::fused, hipFuncAttributeMaxDynamicSharedMemorySize, fk::L_TOTAL);
  fk::fused<<<256, 512, fk::L_TOTAL, stream>>>((const char*)d_in[0], (const char*)d_in[1], (const char*)d_in[2], (const char*)d_in[4],
                                               (const u16*)d_in[5], (const char*)d_in[6], (const char*)d_in[7], (char*)d_out,
                                               (char*)d_ws);
}
```

```cpp
#define PF_CODE
#define PF_DATA
#define MAP2
#include <hip/hip_runtime.h>
#include <stdint.h>

typedef unsigned u32;
typedef unsigned long long u64;
typedef unsigned short u16;
typedef short s16x4 __attribute__((ext_vector_type(4)));
typedef short s16x8 __attribute__((ext_vector_type(8)));
typedef __bf16 b16x2 __attribute__((ext_vector_type(2)));
typedef float f32x2 __attribute__((ext_vector_type(2)));
typedef float f32x4 __attribute__((ext_vector_type(4)));
typedef float f32x16 __attribute__((ext_vector_type(16)));
typedef u32 u32x2 __attribute__((ext_vector_type(2)));
typedef u32 u32x4 __attribute__((ext_vector_type(4)));
#define LAS __attribute__((address_space(3)))
#define GAS __attribute__((address_space(1)))
#define DEV __device__ __forceinline__

namespace fk {
constexpr int LDX = 1024, LDQKV = 3072, LDW1 = 2752, LDW2 = 1024;
constexpr int NKS = 118;
constexpr int SLOT = 43008, BOFF = 18432;
constexpr int L_MEAN = 3 * SLOT, L_RSTD = L_MEAN + 576, L_CS = L_RSTD + 576, L_B1 = L_CS + 768, L_KV = L_B1 + 768,
              L_JUNK = L_KV + 8192, L_TOTAL = L_JUNK + 2048;
constexpr int WS_A2 = 65536, A2_BYTES = 64 * NKS * 1024;
constexpr int AUX_SC1 = 16;
constexpr u32 SPIN_CAP = 1u << 16;

#define STAMP(i) do {} while (0)
#define STAMP_ARG
#define STAMP_PASS
#define STAMP_OUT() do {} while (0)

DEV void dma16(u32 voff, const void* sbase, u32 lds) {
  asm volatile("s_mov_b32 m0, %2\n\ts_nop 0\n\tglobal_load_lds_dwordx4 %0, %1" ::"v"(voff), "s"(sbase), "s"(lds) : "memory");
}
DEV void dma16_masked(u32 voff, const void* sbase, u32 lds, u32 mlo) {
  asm volatile("s_mov_b32 m0, %2\n\ts_mov_b32 exec_lo, %3\n\tglobal_load_lds_dwordx4 %0, %1\n\ts_mov_b32 exec_lo, -1"
               ::"v"(voff), "s"(sbase), "s"(lds), "s"(mlo) : "memory");
}
DEV void dma16_if(u32 voff, const void* sbase, u32 lds, u32 cond) {
  asm volatile("s_cmp_gt_u32 %3, 5\n\ts_cbranch_scc1 1f\n\ts_mov_b32 m0, %2\n\ts_nop 0\n\tglobal_load_lds_dwordx4 %0, %1\n1:"
               ::"v"(voff), "s"(sbase), "s"(lds), "s"(cond) : "memory", "scc");
}
DEV void dma4(u32 voff, const void* sbase, u32 lds) {
  asm volatile("s_mov_b32 m0, %2\n\ts_nop 0\n\tglobal_load_lds_dword %0, %1" ::"v"(voff), "s"(sbase), "s"(lds) : "memory");
}
template <int N> DEV void wait_bar() {
  asm volatile("s_waitcnt vmcnt(%0) lgkmcnt(0)\n\ts_barrier" ::"i"(N) : "memory");
}
DEV void lds_bar() { asm volatile("s_waitcnt lgkmcnt(0)\n\ts_barrier" ::: "memory"); }
#define SB() __builtin_amdgcn_sched_barrier(0)

DEV u32 pk_bf16(float a, float b) {
  f32x2 p = {a, b};
  return __builtin_bit_cast(u32, __builtin_convertvector(p, b16x2));
}
DEV float bf_lo(u32 v) { return __builtin_bit_cast(float, v << 16); }
DEV float bf_hi(u32 v) { return __builtin_bit_cast(float, v & 0xffff0000u); }
DEV float dot2(u32 a, u32 b, float c) {
  return __builtin_amdgcn_fdot2_f32_bf16(__builtin_bit_cast(b16x2, a), __builtin_bit_cast(b16x2, b), c, false);
}
DEV f32x4 mfma16(u32x4 a, u32x4 b, f32x4 c) {
  return __builtin_amdgcn_mfma_f32_16x16x32_bf16(__builtin_bit_cast(s16x8, a), __builtin_bit_cast(s16x8, b), c, 0, 0, 0);
}
DEV f32x4 mfma16k16(u32x2 a, u32x2 b, f32x4 c) {
  return __builtin_amdgcn_mfma_f32_16x16x16bf16_1k(__builtin_bit_cast(s16x4, a), __builtin_bit_cast(s16x4, b), c, 0, 0, 0);
}
DEV f32x16 mfma32(u32x4 a, u32x4 b, f32x16 c) {
  return __builtin_amdgcn_mfma_f32_32x32x16_bf16(__builtin_bit_cast(s16x8, a), __builtin_bit_cast(s16x8, b), c, 0, 0, 0);
}
DEV u32x4 lds128(LAS char* L, u32 off) { return *(LAS u32x4*)(L + off); }
DEV u32x2 ldstr(LAS char* L, u32 off) {
  return __builtin_bit_cast(u32x2, __builtin_amdgcn_ds_read_tr16_b64_v4i16((LAS s16x4*)(L + off)));
}
template <int CTRL> DEV float dpp_add(float v) {
  int t = __builtin_amdgcn_update_dpp(0, __builtin_bit_cast(int, v), CTRL, 0xf, 0xf, true);
  return v + __builtin_bit_cast(float, t);
}
DEV float red8(float v) {
  v = dpp_add<0xB1>(v);
  v = dpp_add<0x4E>(v);
  v = dpp_add<0x141>(v);
  return v;
}
DEV float max_g(float v) {
  u32 u = __builtin_bit_cast(u32, v);
  auto r = __builtin_amdgcn_permlane32_swap(u, u, false, false);
  v = fmaxf(__builtin_bit_cast(float, r[0]), __builtin_bit_cast(float, r[1]));
  u = __builtin_bit_cast(u32, v);
  auto q = __builtin_amdgcn_permlane16_swap(u, u, false, false);
  return fmaxf(__builtin_bit_cast(float, q[0]), __builtin_bit_cast(float, q[1]));
}
DEV u32x4 pair_rows(u32x2 x, u32x2 y) {
  auto a = __builtin_amdgcn_permlane16_swap(x[0], y[0], false, false);
  auto b = __builtin_amdgcn_permlane16_swap(x[1], y[1], false, false);
  return u32x4{a[0], b[0], a[1], b[1]};
}

DEV f32x4 splat4(float v) {
  f32x2 p = {v, v};
  asm volatile("" : "+v"(p));
  return f32x4{p[0], p[1], p[0], p[1]};
}
template <int V> struct IC { static constexpr int v = V; };
template <int LO, int HI, class F> DEV void sfor(F&& f) {
  if constexpr (LO < HI) { f(IC<LO>{}); sfor<LO + 1, HI>(f); }
}
struct P2W { u32 r[120]; };

template <int TYPE>
DEV void phase1(LAS char* L, const char* x, const char* wq, const char* w1, const u16* b1, const char* wo, const char* w2,
                char* ws, const char* outp, u64 code0, int g, int j, int w, int lane, __amdgpu_buffer_rsrc_t rsA2, u32 w2voff, int ks0, P2W& W STAMP_ARG) {
  constexpr bool FFN = TYPE >= 2;
  constexpr int NT = TYPE == 0 ? 5 : (TYPE == 1 ? 4 : 2);
  constexpr int NC = TYPE == 2 ? 6 : (TYPE == 3 ? 5 : 3);
  constexpr int CB = TYPE == 3 ? 6 : 0;
  constexpr int NSP = TYPE == 1 ? 3 : 2;
  constexpr int ND = TYPE == 1 ? 6 : 5;
  constexpr int NM = NT * NC + (FFN ? 2 : 0);
  const int r8 = lane >> 3, pos = lane & 7, lg = lane >> 4, lc = lane & 15;
  const int rot = (j + g) & 7;
  constexpr int ldw = FFN ? LDW1 : LDQKV;
  const u32 voffA = r8 * LDX + ((pos ^ r8) << 4);
  const int swB = (r8 & 2) | ((w & 1) << 2);
  const u32 voffB = r8 * ldw + ((pos ^ swB) << 4);
  const int maxch = (j == 15) ? 1 : 5;
  const u32 voffB2 = FFN ? (u32)(r8 * ldw + (min(pos ^ swB, maxch) << 4)) : voffB;
  const int rowbase = FFN ? 128 * g : 128 * g - 16;
  const int p0row = (!FFN && g == 0 && w < 2) ? 8 * w : rowbase + 8 * w;
  const char* sA0 = x + (size_t)p0row * LDX;
  const char* sA1 = x + (size_t)(rowbase + 8 * (w + 8)) * LDX;
  const char* sA2 = x + (size_t)(rowbase + 8 * (16 + (w & 1))) * LDX;
  const char* sB0 = FFN ? w1 + (size_t)(8 * w) * LDW1 + (j - 8) * 352 : wq + (size_t)(8 * w) * LDQKV + j * 128;
  constexpr int SEGB = FFN ? 128 : 1024;
  const u32 mlo = (w == 0) ? 0xFFFFFF00u : 0xFFFFFFFFu;
  auto issue = [&](int i, int k) {
    const int kc = (i + rot) & 7;
    const u32 slot = (i % 3) * SLOT;
    const u32 va = voffA + kc * 128, vb = voffB + kc * 64 * ldw, vb2 = voffB2 + kc * 64 * ldw;
    if (k == 0) { if (TYPE == 0) dma16_masked(va, sA0, slot + w * 1024, mlo); else dma16(va, sA0, slot + w * 1024); }
    if (k == 1) dma16(vb, sB0, slot + BOFF + w * 1024);
    if (k == 2) dma16(vb, sB0 + SEGB, slot + BOFF + (8 + w) * 1024);
    if (k == 3) dma16(vb2, sB0 + 2 * SEGB, slot + BOFF + (16 + w) * 1024);
    if (k == 4) dma16(va, sA1, slot + (w + 8) * 1024);
    if (k == 5) dma16_if(va, sA2, slot + (16 + (w & 1)) * 1024, w);
  };
#pragma unroll
  for (int i = 0; i < 3; ++i)
#pragma unroll
    for (int k = 0; k < ND; ++k) issue(i, k);
  {
    const int bi = blockIdx.x >> 3;
    if (w == 7) {
      dma4(min(lane * 128 + (bi & 7) * 8192, 61000), (const void*)code0, L_JUNK);
    }
    if (w == 6) {
      const int ln = blockIdx.x * 59 + min(lane, 58);
      if (blockIdx.x * 59 + 58 < 4096) dma4(ln * 128, wo, L_JUNK + 256);
      else dma4((u32)max(ln - 4096, 0) * 128, w2, L_JUNK + 256);
    }
    if (w == 5) {
      dma4((lane & 3) * (2u << 20) + WS_A2 + (blockIdx.x & 63) * 4096, ws, L_JUNK + 512);
      dma4(blockIdx.x * 8192 + lane * 4, outp, L_JUNK + 768);
    }
  }
  u32 b1v = 0;
  if (FFN) b1v = b1[min((j - 8) * 176 + (int)threadIdx.x, 1375)];
  if (TYPE == 0 && w == 0 && lane < 8) {
    const u32x4 one = {0x3F803F80u, 0x3F803F80u, 0x3F803F80u, 0x3F803F80u};
#pragma unroll
    for (int s = 0; s < 3; ++s) *(LAS u32x4*)(L + s * SLOT + lane * 16) = one;
  }
  const int T0 = TYPE == 0 ? 0 : (TYPE == 1 ? 5 : 2 * (w & 3));
  const u32 aA = T0 * 2048 + (lc >> 3) * 1024 + (lc & 7) * 128 + ((lg ^ (lc & 7)) << 4);
  const int tq = lc >> 2, tp = lc & 3;
  const int swR = (tq & 2) | ((lg & 1) << 2);
  const u32 aBl = BOFF + lg * 1024 + tq * 128 + ((tp >> 1) << 4) + (tp & 1) * 8;
  const int crot = min(2 * (w & 3), NC - 2);
  u32 aB[NC];
#pragma unroll
  for (int c = 0; c < NC; ++c) {
    const int cc = c + crot >= NC ? c + crot - NC : c + crot;
    const int ct = FFN ? CB + cc : 4 * c + (w & 3);
    aB[c] = aBl + (ct >> 2) * 8192 + (((2 * (ct & 3)) ^ swR) << 4);
  }
  u32 aS[NSP];
  aS[0] = w * 1024 + lane * 16; aS[1] = (w + 8) * 1024 + lane * 16;
  if (NSP == 3) aS[NSP - 1] = (16 + (w & 1)) * 1024 + lane * 16;

  f32x4 acc[NT][NC], acc1[2];
#pragma unroll
  for (int t = 0; t < NT; ++t)
#pragma unroll
    for (int c = 0; c < NC; ++c) acc[t][c] = f32x4{0.f, 0.f, 0.f, 0.f};
  acc1[0] = acc1[1] = f32x4{0.f, 0.f, 0.f, 0.f};
  float ssum[NSP], ssq[NSP];
#pragma unroll
  for (int i = 0; i < NSP; ++i) ssum[i] = ssq[i] = 0.f;
  u32x4 fa[2][NT], sv[NSP];
  u32x2 fb[2][NC][2];
  auto rd = [&](int t, int k) {
    const u32 slot = ((t >> 1) % 3) * SLOT;
    const int b = t & 1;
    if (k < 2 * NC) fb[b][k >> 1][k & 1] = ldstr(L, aB[k >> 1] + slot + b * 4096 + (k & 1) * 512);
    else if (k < 2 * NC + NT) fa[b][k - 2 * NC] = lds128(L, ((aA ^ (b * 64)) + slot + (k - 2 * NC) * 2048));
    else sv[k - 2 * NC - NT] = lds128(L, aS[k - 2 * NC - NT] + slot);
  };
  auto ldw2 = [&](int s, int lo, int hi) {
    const int ks = ks0 + s;
    const bool isO = ks < 32;
    const char* base = isO ? wo + (size_t)ks * 16 * LDW2 : w2 + (size_t)(ks - 32) * 16 * LDW2;
    const __amdgpu_buffer_rsrc_t rs = __builtin_amdgcn_make_buffer_rsrc((void*)base, 0, ks < NKS ? 16 * LDW2 : 0, 0x00020000);
#pragma unroll
    for (int jj = lo; jj < hi; ++jj)
      W.r[s * 8 + jj] = __builtin_amdgcn_raw_buffer_load_b32(rs, w2voff + (jj & 3) * 1024, (jj >> 2) * 4096, 0);
  };
  const u32x4 ones = {0x3F803F80u, 0x3F803F80u, 0x3F803F80u, 0x3F803F80u};
  STAMP(1);
  wait_bar<10>();
  STAMP(2);
#pragma unroll
  for (int k = 0; k < 2 * NC + NT + NSP; ++k) rd(0, k);
  SB();
  sfor<0, 16>([&](auto tc) {
    constexpr int t = decltype(tc)::v, s = t >> 1, b = t & 1;
    if constexpr (b) { if constexpr (s <= 4) wait_bar<5>(); else if constexpr (s == 5) wait_bar<21>(); else if constexpr (s == 6) wait_bar<32>(); }
    constexpr int nr = t == 15 ? 0 : 2 * NC + NT + (b ? NSP : 0);
    constexpr int g0 = (nr + 1) / 2;
    constexpr int nd = (b && s + 3 < 8) ? ND : 0, nw = t >= 9 ? 8 : 0;
    constexpr int per = (nd + nw + NM - g0 - 1) / (NM - g0);
    constexpr int ndot = b ? 0 : 4 * NSP, dper = (ndot + NM - 1) / NM;
    sfor<0, NM>([&](auto qc) {
      constexpr int q = decltype(qc)::v;
      if constexpr (q < NT * NC) {
        constexpr int c = q / NT, i = q % NT;
        const u32x4 wfr = {fb[b][c][0][0], fb[b][c][0][1], fb[b][c][1][0], fb[b][c][1][1]};
        if constexpr (!FFN && c == 2) acc[i][c] = mfma16(fa[b][i], wfr, acc[i][c]);
        else acc[i][c] = mfma16(wfr, fa[b][i], acc[i][c]);
      } else {
        constexpr int c = q - NT * NC;
        const u32x4 wfr = {fb[b][c][0][0], fb[b][c][0][1], fb[b][c][1][0], fb[b][c][1][1]};
        acc1[c] = mfma16(wfr, ones, acc1[c]);
      }
      if constexpr (2 * q < nr) rd(t + 1, 2 * q);
      if constexpr (2 * q + 1 < nr) rd(t + 1, 2 * q + 1);
      if constexpr (q >= g0) {
        sfor<(q - g0) * per, (q - g0 + 1) * per>([&](auto uc) {
          constexpr int u = decltype(uc)::v;
          if constexpr (u < nd) issue(s + 3, u);
          else if constexpr (u < nd + nw) ldw2(t - 9, u - nd, u - nd + 1);
        });
      }
      sfor<q * dper, (q + 1) * dper>([&](auto uc) {
        constexpr int u = decltype(uc)::v;
        if constexpr (u < ndot) {
          constexpr int i = u >> 2, d = u & 3;
          ssum[i] = dot2(sv[i][d], 0x3F803F80u, ssum[i]);
          ssq[i] = dot2(sv[i][d], sv[i][d], ssq[i]);
        }
      });
      SB();
    });
  });
  if (FFN) asm volatile("" : "+v"(acc1[0]), "+v"(acc1[1]));
#pragma unroll
  for (int i = 0; i < NSP; ++i) asm volatile("" : "+v"(ssum[i]), "+v"(ssq[i]));
  STAMP(3);
#pragma unroll
  for (int i = 0; i < NSP; ++i) {
    const float sm = red8(ssum[i]) * (1.f / 512.f), sq = red8(ssq[i]) * (1.f / 512.f);
    const float var = fmaxf(sq - sm * sm, 0.f);
    const float rs = __builtin_amdgcn_rsqf(var + 1e-5f);
    const int p = i == 0 ? w : (i == 1 ? w + 8 : 16 + (w & 1));
    if (pos == 0) { *(LAS float*)(L + L_MEAN + (p * 8 + r8) * 4) = sm; *(LAS float*)(L + L_RSTD + (p * 8 + r8) * 4) = rs; }
  }
  if (TYPE == 0) {
    const int h = w & 3;
    if (lc == 0) {
      *(LAS f32x4*)(L + L_CS + (h * 16 + lg * 4) * 4) = acc[0][0];
      *(LAS f32x4*)(L + L_CS + (64 + h * 16 + lg * 4) * 4) = acc[0][1];
    }
    if (lg == 0) *(LAS float*)(L + L_CS + (128 + h * 16 + lc) * 4) = acc[0][2][0];
    *(LAS f32x4*)(L + L_KV + h * 2048 + lane * 16) = acc[NT - 1][1];
    *(LAS f32x4*)(L + L_KV + h * 2048 + 1024 + lane * 16) = acc[NT - 1][2];
  }
  if (FFN) {
    if (lc == 0) {
      *(LAS f32x4*)(L + L_CS + ((CB + crot) * 16 + lg * 4) * 4) = acc1[0];
      *(LAS f32x4*)(L + L_CS + ((CB + crot + 1) * 16 + lg * 4) * 4) = acc1[1];
    }
    if (threadIdx.x < 192) *(LAS float*)(L + L_B1 + threadIdx.x * 4) = bf_lo(b1v);
  }
  lds_bar();
  STAMP(4);
  if (!FFN) {
    const int h = w & 3;
    constexpr int NK = 5;
    const int TK = TYPE == 0 ? 0 : 4;
    const f32x4 csq = *(LAS f32x4*)(L + L_CS + (h * 16 + lg * 4) * 4);
    const f32x4 csk = *(LAS f32x4*)(L + L_CS + (64 + h * 16 + lg * 4) * 4);
    const f32x4 csv = splat4(*(LAS float*)(L + L_CS + (128 + h * 16 + lc) * 4));
    u32x2 Qb[4], Kb[NK], Vb[NK];
#pragma unroll
    for (int i = 0; i < NK; ++i) {
      const int T = TK + i;
      f32x4 ak, av;
      if (TYPE == 1 && i == 0) {
        ak = *(LAS f32x4*)(L + L_KV + h * 2048 + lane * 16);
        av = *(LAS f32x4*)(L + L_KV + h * 2048 + 1024 + lane * 16);
      } else {
        ak = acc[TYPE == 0 ? i : (i > 0 ? i - 1 : 0)][1];
        av = acc[TYPE == 0 ? i : (i > 0 ? i - 1 : 0)][2];
      }
      const float rc1 = *(LAS float*)(L + L_RSTD + (T * 16 + lc) * 4);
      const f32x4 mc = splat4(*(LAS float*)(L + L_MEAN + (T * 16 + lc) * 4)), rc = splat4(rc1);
      const f32x4 m4 = *(LAS f32x4*)(L + L_MEAN + (T * 16 + lg * 4) * 4), r4 = *(LAS f32x4*)(L + L_RSTD + (T * 16 + lg * 4) * 4);
      const f32x4 kk = (ak - mc * csk) * rc;
      const f32x4 vv = (av - m4 * csv) * r4;
      Kb[i] = u32x2{pk_bf16(kk[0], kk[1]), pk_bf16(kk[2], kk[3])};
      Vb[i] = u32x2{pk_bf16(vv[0], vv[1]), pk_bf16(vv[2], vv[3])};
      if (i > 0) {
        const f32x4 qq = (acc[TYPE == 0 ? i : i - 1][0] - mc * csq) * splat4(rc1 * (0.25f * 1.4426950408889634f));
        Qb[i - 1] = u32x2{pk_bf16(qq[0], qq[1]), pk_bf16(qq[2], qq[3])};
      }
    }
    SB();
    f32x4 mP, mC;
#pragma unroll
    for (int r = 0; r < 4; ++r) {
      mP[r] = (4 * lg + r >= lc + 2) ? 0.f : -1e30f;
      mC[r] = (4 * lg + r <= lc) ? 0.f : -1e30f;
    }
    const bool odd = lc & 1;
    const u32 sel0 = odd ? 0x01000c0cu : 0x0c0c0100u, sel1 = odd ? 0x03020c0cu : 0x0c0c0302u;
    u32x2 Ob[4];
#pragma unroll
    for (int q = 0; q < 4; ++q) {
      ldw2(7 + 2 * q, 0, 8); ldw2(8 + 2 * q, 0, 8);
      SB();
      f32x4 mp = mP;
      if (TYPE == 0 && q == 0 && g == 0) mp = f32x4{-1e30f, -1e30f, -1e30f, -1e30f};
      const f32x4 sp = mfma16k16(Kb[q], Qb[q], mp), sc = mfma16k16(Kb[q + 1], Qb[q], mC);
      const float a0 = odd ? sp[1] : sp[0], a1 = odd ? sp[3] : sp[2], a2 = odd ? sc[1] : sc[0], a3 = odd ? sc[3] : sc[2];
      const float mx = max_g(fmaxf(fmaxf(a0, a1), fmaxf(a2, a3)));
      const u32 u0 = pk_bf16(__builtin_amdgcn_exp2f(a0 - mx), __builtin_amdgcn_exp2f(a1 - mx));
      const u32 u1 = pk_bf16(__builtin_amdgcn_exp2f(a2 - mx), __builtin_amdgcn_exp2f(a3 - mx));
      const u32x4 pb = {__builtin_amdgcn_perm(0, u0, sel0), __builtin_amdgcn_perm(0, u0, sel1),
                        __builtin_amdgcn_perm(0, u1, sel0), __builtin_amdgcn_perm(0, u1, sel1)};
      const u32x4 vf = {Vb[q][0], Vb[q][1], Vb[q + 1][0], Vb[q + 1][1]};
      const f32x4 z = {0.f, 0.f, 0.f, 0.f};
      const f32x4 o = mfma16(vf, pb, z), l = mfma16(ones, pb, z);
      const f32x4 on = o * splat4(__builtin_amdgcn_rcpf(l[0]));
      Ob[q] = u32x2{pk_bf16(on[0], on[1]), pk_bf16(on[2], on[3])};
      SB();
    }
    const int rt = 4 * g + (TYPE == 0 ? 0 : 2), ks = 4 * j + h;
    __builtin_amdgcn_raw_buffer_store_b128(pair_rows(Ob[0], Ob[1]), rsA2, lane * 16, (rt * NKS + ks) * 1024, AUX_SC1);
    __builtin_amdgcn_raw_buffer_store_b128(pair_rows(Ob[2], Ob[3]), rsA2, lane * 16, ((rt + 1) * NKS + ks) * 1024, AUX_SC1);
    asm volatile("s_waitcnt vmcnt(0)\n\ts_barrier" ::: "memory");
  } else {
    const int m = w & 3;
    f32x4 mc[2], rc[2];
#pragma unroll
    for (int i = 0; i < 2; ++i) {
      mc[i] = splat4(*(LAS float*)(L + L_MEAN + ((2 * m + i) * 16 + lc) * 4));
      rc[i] = splat4(*(LAS float*)(L + L_RSTD + ((2 * m + i) * 16 + lc) * 4));
    }
    const int nct = j == 15 ? 9 : 11;
    const int rt = 4 * g + m, ksb = 32 + 11 * (j - 8);
    const f32x4 C1 = splat4(-2.f * 1.4426950408889634f * 0.7978845608028654f);
    const f32x4 C2 = splat4(-2.f * 1.4426950408889634f * 0.7978845608028654f * 0.044715f);
    const f32x4 ONE = splat4(1.f);
#pragma unroll
    for (int c = 0; c < NC; ++c) {
      if (c < 4) { ldw2(7 + 2 * c, 0, 8); ldw2(8 + 2 * c, 0, 8); }
      SB();
      const int ct = CB + (c + crot >= NC ? c + crot - NC : c + crot);
      const f32x4 cs = *(LAS f32x4*)(L + L_CS + (ct * 16 + lg * 4) * 4);
      const f32x4 bb = *(LAS f32x4*)(L + L_B1 + (ct * 16 + lg * 4) * 4);
      u32x2 hb[2];
#pragma unroll
      for (int i = 0; i < 2; ++i) {
        const f32x4 v = (acc[i][c] - mc[i] * cs) * rc[i] + bb;
        const f32x4 zz = v * (v * v * C2 + C1);
        f32x4 e;
#pragma unroll
        for (int r = 0; r < 4; ++r) e[r] = __builtin_amdgcn_exp2f(zz[r]);
        e += ONE;
#pragma unroll
        for (int r = 0; r < 4; ++r) e[r] = __builtin_amdgcn_rcpf(e[r]);
        const f32x4 y = v * e;
        hb[i] = u32x2{pk_bf16(y[0], y[1]), pk_bf16(y[2], y[3])};
      }
      __builtin_amdgcn_raw_buffer_store_b128(pair_rows(hb[0], hb[1]), rsA2, lane * 16,
                                             ct < nct ? (rt * NKS + ksb + ct) * 1024 : A2_BYTES, AUX_SC1);
      SB();
    }
    asm volatile("s_waitcnt vmcnt(0)\n\ts_barrier" ::: "memory");
  }
}

template <int TYPE>
DEV void body(LAS char* L, const char* __restrict__ x, const char* __restrict__ wq, const char* __restrict__ wo,
              const char* __restrict__ w1, const u16* __restrict__ b1, const char* __restrict__ w2, const char* __restrict__ b2,
              char* __restrict__ out, char* __restrict__ ws, u64 code0, int g, int j, int w, int lane STAMP_ARG) {
  const int mt = 2 * g + ((j >> 2) & 1), nt = (j & 3) + (j >> 3) * 4;
  const __amdgpu_buffer_rsrc_t rsA2 = __builtin_amdgcn_make_buffer_rsrc(ws + WS_A2, 0, A2_BYTES, 0x00020000);
  const int ks0 = w < 6 ? 15 * w : 90 + 14 * (w - 6);
  const u32 w2voff = (lane >> 5) * 8 * LDW2 + nt * 128 + (lane & 31) * 4;
  GAS u64* ctl = (GAS u64*)(ws + g * 4096);
  const u64 tag = __hip_atomic_load(ctl, __ATOMIC_RELAXED, __HIP_MEMORY_SCOPE_AGENT) + 1;
  P2W W;
  phase1<TYPE>(L, x, wq, w1, b1, wo, w2, ws, out, code0, g, j, w, lane, rsA2, w2voff, ks0, W STAMP_PASS);
  if (threadIdx.x == 0) __hip_atomic_store(ctl + 16 * (j + 1), tag, __ATOMIC_RELAXED, __HIP_MEMORY_SCOPE_AGENT);
  STAMP(5);
  const int orow = mt * 64 + (w >> 2) * 32 + (lane & 31), ocol = nt * 64 + (w & 3) * 16 + (lane >> 5) * 8;
  const u32 ooff = (u32)(orow * LDX + ocol * 2);
  const u32x4 xres = *(const GAS u32x4*)(x + ooff);
  const u32x4 bres = *(const GAS u32x4*)(b2 + (u32)(ocol * 2));
  u32x4 wf[15][2];
#pragma unroll
  for (int s = 0; s < 15; ++s)
#pragma unroll
    for (int d = 0; d < 4; ++d) {
      wf[s][0][d] = __builtin_amdgcn_perm(W.r[s * 8 + 2 * d + 1], W.r[s * 8 + 2 * d], 0x05040100u);
      wf[s][1][d] = __builtin_amdgcn_perm(W.r[s * 8 + 2 * d + 1], W.r[s * 8 + 2 * d], 0x07060302u);
    }
#pragma unroll
  for (int s = 0; s < 15; ++s) asm volatile("" : "+v"(wf[s][0]), "+v"(wf[s][1]));
  SB();
  STAMP(6);
  {
    const int ke = min(ks0 + 14, NKS - 1);
    const int plo = ks0 < 32 ? ks0 >> 2 : 8 + (ks0 - 32) / 11, phi = ke < 32 ? ke >> 2 : 8 + (ke - 32) / 11;
    const int pj = min(plo + (lane & 7), phi);
#pragma nounroll
    for (u32 spins = 0; spins < SPIN_CAP; ++spins) {
      const u64 v = __hip_atomic_load(ctl + 16 * (pj + 1), __ATOMIC_RELAXED, __HIP_MEMORY_SCOPE_AGENT);
      if (__all(v == tag)) break;
      __builtin_amdgcn_s_sleep(1);
    }
    __builtin_amdgcn_fence(__ATOMIC_ACQUIRE, "wavefront");
  }
  STAMP(7);
  const int nks = w < 6 ? 15 : 14;
  u32x4 af[2][15];
#pragma unroll
  for (int r = 0; r < 2; ++r)
#pragma unroll
    for (int s = 0; s < 15; ++s) {
      if (r == 1 && s >= 5) continue;
      const int so = (s < nks) ? ((2 * mt + r) * NKS + ks0 + s) * 1024 : A2_BYTES;
      af[r][s] = __builtin_amdgcn_raw_buffer_load_b128(rsA2, lane * 16, so, AUX_SC1);
    }
  SB();
#pragma unroll
  for (int r = 0; r < 2; ++r) {
    f32x16 a0, a1;
#pragma unroll
    for (int i = 0; i < 16; ++i) a0[i] = a1[i] = 0.f;
#pragma unroll
    for (int s = 0; s < 15; ++s) {
      a0 = mfma32(wf[s][0], af[r][s], a0);
      a1 = mfma32(wf[s][1], af[r][s], a1);
      if (r == 0 && s < 10) {
        const int s2 = s + 5;
        const int so = (s2 < nks) ? ((2 * mt + 1) * NKS + ks0 + s2) * 1024 : A2_BYTES;
        af[1][s2] = __builtin_amdgcn_raw_buffer_load_b128(rsA2, lane * 16, so, AUX_SC1);
      }
      SB();
    }
#pragma unroll
    for (int y = 0; y < 4; ++y) {
      *(LAS f32x4*)(L + ((w * 2 + r) * 8 + y * 2) * 1024 + lane * 16) = f32x4{a0[4 * y], a0[4 * y + 1], a0[4 * y + 2], a0[4 * y + 3]};
      *(LAS f32x4*)(L + ((w * 2 + r) * 8 + y * 2 + 1) * 1024 + lane * 16) = f32x4{a1[4 * y], a1[4 * y + 1], a1[4 * y + 2], a1[4 * y + 3]};
    }
    SB();
  }
  STAMP(8);
  lds_bar();
  if (j == 0 && threadIdx.x == 0) __hip_atomic_store(ctl, tag, __ATOMIC_RELAXED, __HIP_MEMORY_SCOPE_AGENT);
  f32x4 s0 = {0.f, 0.f, 0.f, 0.f}, s1 = s0;
#pragma unroll
  for (int sw = 0; sw < 8; ++sw) {
    s0 += *(LAS f32x4*)(L + ((sw * 2 + (w >> 2)) * 8 + (w & 3) * 2) * 1024 + lane * 16);
    s1 += *(LAS f32x4*)(L + ((sw * 2 + (w >> 2)) * 8 + (w & 3) * 2 + 1) * 1024 + lane * 16);
  }
  u32x4 o;
#pragma unroll
  for (int d = 0; d < 4; ++d)
    o[d] = pk_bf16(s0[d] + bf_lo(xres[d]) + bf_lo(bres[d]), s1[d] + bf_hi(xres[d]) + bf_hi(bres[d]));
  *(GAS u32x4*)(out + ooff) = o;
  STAMP(9);
  STAMP_OUT();
}

__global__ __launch_bounds__(512, 2) void fused(const char* __restrict__ x, const char* __restrict__ wq, const char* __restrict__ wo,
                                                const char* __restrict__ w1, const u16* __restrict__ b1, const char* __restrict__ w2,
                                                const char* __restrict__ b2, char* __restrict__ out, char* __restrict__ ws) {
  extern __shared__ __attribute__((aligned(16))) char smem[];
  LAS char* L = (LAS char*)smem;
  STAMP(0);
  const int lane = threadIdx.x & 63, w = __builtin_amdgcn_readfirstlane(threadIdx.x >> 6);
  u64 code0;
  asm volatile("s_getpc_b64 %0" : "=s"(code0));
  code0 &= ~127ull;
  const int b = blockIdx.x, bi = b >> 3, g = (b & 3) * 4 + (bi & 3), j = ((bi >> 2) & 3) + ((b >> 2) & 1) * 4 + (bi >> 4) * 8;
  if (j < 8) {
    if (w < 4) body<0>(L, x, wq, wo, w1, b1, w2, b2, out, ws, code0, g, j, w, lane STAMP_PASS);
    else body<1>(L, x, wq, wo, w1, b1, w2, b2, out, ws, code0, g, j, w, lane STAMP_PASS);
  } else {
    if (w < 4) body<2>(L, x, wq, wo, w1, b1, w2, b2, out, ws, code0, g, j, w, lane STAMP_PASS);
    else body<3>(L, x, wq, wo, w1, b1, w2, b2, out, ws, code0, g, j, w, lane STAMP_PASS);
  }
}
}

struct PreArgs { const char* p[10]; unsigned n[10]; };
__global__ void pre_k(const char* p0, const char* p1, const char* p2, const char* p3, const char* p4, const char* p5, const char* p6, int stride, int which, unsigned* sink) {
  const char* ps[7] = {p0, p1, p2, p3, p4, p5, p6};
  const unsigned ns[7] = {2097152u, 1572864u, 524288u, 1409024u, 1409024u, 2097152u, 65536u + 7733248u};
  unsigned a = 0;
  const unsigned tid = blockIdx.x * blockDim.x + threadIdx.x, nth = gridDim.x * blockDim.x;
#pragma unroll
  for (int b = 0; b < 7; ++b) {
    if (!((which >> b) & 1)) continue;
    for (unsigned o = tid * stride; o < ns[b]; o += nth * stride) a ^= *(const unsigned*)(ps[b] + o);
  }
  if (a == 0x12345678) sink[0] = a;
}
extern "C" void kernel_launch(void* const* d_in, const int* in_sizes, int n_in, void* d_out, int out_size, void* d_ws,
                              size_t ws_size, hipStream_t stream) {
  (void)hipFuncSetAttribute((const void*)fk::fused, hipFuncAttributeMaxDynamicSharedMemorySize, fk::L_TOTAL);
  pre_k<<<256, 256, 0, stream>>>((const char*)d_in[0], (const char*)d_in[1], (const char*)d_in[2], (const char*)d_in[4], (const char*)d_in[6], (const char*)d_out, (const char*)d_ws, 4096, 0, (unsigned*)d_ws + (100<<20)/4);
  fk::fused<<<256, 512, fk::L_TOTAL, stream>>>((const char*)d_in[0], (const char*)d_in[1], (const char*)d_in[2], (const char*)d_in[4],
                                               (const u16*)d_in[5], (const char*)d_in[6], (const char*)d_in[7], (char*)d_out,
                                               (char*)d_ws);
}
```

```cpp
#define STAMPS
#define NSTAMP 12
#define AMP 0
#define PF_CODE
#define PF_DATA
#define MAP2
#include <hip/hip_runtime.h>
#include <stdint.h>

typedef unsigned u32;
typedef unsigned long long u64;
typedef unsigned short u16;
typedef short s16x4 __attribute__((ext_vector_type(4)));
typedef short s16x8 __attribute__((ext_vector_type(8)));
typedef __bf16 b16x2 __attribute__((ext_vector_type(2)));
typedef float f32x2 __attribute__((ext_vector_type(2)));
typedef float f32x4 __attribute__((ext_vector_type(4)));
typedef float f32x16 __attribute__((ext_vector_type(16)));
typedef u32 u32x2 __attribute__((ext_vector_type(2)));
typedef u32 u32x4 __attribute__((ext_vector_type(4)));
#define LAS __attribute__((address_space(3)))
#define GAS __attribute__((address_space(1)))
#define DEV __device__ __forceinline__

namespace fk {
constexpr int LDX = 1024, LDQKV = 3072, LDW1 = 2752, LDW2 = 1024;
constexpr int NKS = 118;
constexpr int SLOT = 43008, BOFF = 18432;
constexpr int L_MEAN = 3 * SLOT, L_RSTD = L_MEAN + 576, L_CS = L_RSTD + 576, L_B1 = L_CS + 768, L_KV = L_B1 + 768,
              L_JUNK = L_KV + 8192, L_TOTAL = L_JUNK + 2048;
constexpr int WS_A2 = 65536, A2_BYTES = 64 * NKS * 1024;
constexpr int AUX_SC1 = 16;
constexpr u32 SPIN_CAP = 1u << 16;

struct Stamps { u32 t[12]; };
#define STAMP(i) do { u64 t_; asm volatile("s_memrealtime %0\n\ts_waitcnt lgkmcnt(0)" : "=s"(t_) :: "memory"); ST.t[i] = (u32)t_; } while (0)
#define STAMP_ARG , Stamps& ST
#define STAMP_PASS , ST
#define STAMP_OUT() do { if ((threadIdx.x & 63) == 0) { _Pragma("unroll") for (int i_ = 0; i_ < NSTAMP; ++i_) \
  ((u64*)(ws + (64u << 20)))[(blockIdx.x * 8 + (threadIdx.x >> 6)) * 16 + i_] = ST.t[i_]; } } while (0)

#undef STAMP_OUT
#define STAMP_OUT() do { const u32 lim_ = 4000u + 10u * (ST.t[AMP] - ST.t[0]); \
  while ((u32)__builtin_amdgcn_s_memrealtime() - ST.t[0] < lim_) __builtin_amdgcn_s_sleep(2); } while (0)
DEV void dma16(u32 voff, const void* sbase, u32 lds) {
  asm volatile("s_mov_b32 m0, %2\n\ts_nop 0\n\tglobal_load_lds_dwordx4 %0, %1" ::"v"(voff), "s"(sbase), "s"(lds) : "memory");
}
DEV void dma16_masked(u32 voff, const void* sbase, u32 lds, u32 mlo) {
  asm volatile("s_mov_b32 m0, %2\n\ts_mov_b32 exec_lo, %3\n\tglobal_load_lds_dwordx4 %0, %1\n\ts_mov_b32 exec_lo, -1"
               ::"v"(voff), "s"(sbase), "s"(lds), "s"(mlo) : "memory");
}
DEV void dma16_if(u32 voff, const void* sbase, u32 lds, u32 cond) {
  asm volatile("s_cmp_gt_u32 %3, 5\n\ts_cbranch_scc1 1f\n\ts_mov_b32 m0, %2\n\ts_nop 0\n\tglobal_load_lds_dwordx4 %0, %1\n1:"
               ::"v"(voff), "s"(sbase), "s"(lds), "s"(cond) : "memory", "scc");
}
DEV void dma4(u32 voff, const void* sbase, u32 lds) {
  asm volatile("s_mov_b32 m0, %2\n\ts_nop 0\n\tglobal_load_lds_dword %0, %1" ::"v"(voff), "s"(sbase), "s"(lds) : "memory");
}
template <int N> DEV void wait_bar() {
  asm volatile("s_waitcnt vmcnt(%0) lgkmcnt(0)\n\ts_barrier" ::"i"(N) : "memory");
}
DEV void lds_bar() { asm volatile("s_waitcnt lgkmcnt(0)\n\ts_barrier" ::: "memory"); }
#define SB() __builtin_amdgcn_sched_barrier(0)

DEV u32 pk_bf16(float a, float b) {
  f32x2 p = {a, b};
  return __builtin_bit_cast(u32, __builtin_convertvector(p, b16x2));
}
DEV float bf_lo(u32 v) { return __builtin_bit_cast(float, v << 16); }
DEV float bf_hi(u32 v) { return __builtin_bit_cast(float, v & 0xffff0000u); }
DEV float dot2(u32 a, u32 b, float c) {
  return __builtin_amdgcn_fdot2_f32_bf16(__builtin_bit_cast(b16x2, a), __builtin_bit_cast(b16x2, b), c, false);
}
DEV f32x4 mfma16(u32x4 a, u32x4 b, f32x4 c) {
  return __builtin_amdgcn_mfma_f32_16x16x32_bf16(__builtin_bit_cast(s16x8, a), __builtin_bit_cast(s16x8, b), c, 0, 0, 0);
}
DEV f32x4 mfma16k16(u32x2 a, u32x2 b, f32x4 c) {
  return __builtin_amdgcn_mfma_f32_16x16x16bf16_1k(__builtin_bit_cast(s16x4, a), __builtin_bit_cast(s16x4, b), c, 0, 0, 0);
}
DEV f32x16 mfma32(u32x4 a, u32x4 b, f32x16 c) {
  return __builtin_amdgcn_mfma_f32_32x32x16_bf16(__builtin_bit_cast(s16x8, a), __builtin_bit_cast(s16x8, b), c, 0, 0, 0);
}
DEV u32x4 lds128(LAS char* L, u32 off) { return *(LAS u32x4*)(L + off); }
DEV u32x2 ldstr(LAS char* L, u32 off) {
  return __builtin_bit_cast(u32x2, __builtin_amdgcn_ds_read_tr16_b64_v4i16((LAS s16x4*)(L + off)));
}
template <int CTRL> DEV float dpp_add(float v) {
  int t = __builtin_amdgcn_update_dpp(0, __builtin_bit_cast(int, v), CTRL, 0xf, 0xf, true);
  return v + __builtin_bit_cast(float, t);
}
DEV float red8(float v) {
  v = dpp_add<0xB1>(v);
  v = dpp_add<0x4E>(v);
  v = dpp_add<0x141>(v);
  return v;
}
DEV float max_g(float v) {
  u32 u = __builtin_bit_cast(u32, v);
  auto r = __builtin_amdgcn_permlane32_swap(u, u, false, false);
  v = fmaxf(__builtin_bit_cast(float, r[0]), __builtin_bit_cast(float, r[1]));
  u = __builtin_bit_cast(u32, v);
  auto q = __builtin_amdgcn_permlane16_swap(u, u, false, false);
  return fmaxf(__builtin_bit_cast(float, q[0]), __builtin_bit_cast(float, q[1]));
}
DEV u32x4 pair_rows(u32x2 x, u32x2 y) {
  auto a = __builtin_amdgcn_permlane16_swap(x[0], y[0], false, false);
  auto b = __builtin_amdgcn_permlane16_swap(x[1], y[1], false, false);
  return u32x4{a[0], b[0], a[1], b[1]};
}

DEV f32x4 splat4(float v) {
  f32x2 p = {v, v};
  asm volatile("" : "+v"(p));
  return f32x4{p[0], p[1], p[0], p[1]};
}
template <int V> struct IC { static constexpr int v = V; };
template <int LO, int HI, class F> DEV void sfor(F&& f) {
  if constexpr (LO < HI) { f(IC<LO>{}); sfor<LO + 1, HI>(f); }
}
struct P2W { u32 r[120]; };

template <int TYPE>
DEV void phase1(LAS char* L, const char* x, const char* wq, const char* w1, const u16* b1, const char* wo, const char* w2,
                char* ws, const char* outp, u64 code0, int g, int j, int w, int lane, __amdgpu_buffer_rsrc_t rsA2, u32 w2voff, int ks0, P2W& W STAMP_ARG) {
  constexpr bool FFN = TYPE >= 2;
  constexpr int NT = TYPE == 0 ? 5 : (TYPE == 1 ? 4 : 2);
  constexpr int NC = TYPE == 2 ? 6 : (TYPE == 3 ? 5 : 3);
  constexpr int CB = TYPE == 3 ? 6 : 0;
  constexpr int NSP = TYPE == 1 ? 3 : 2;
  constexpr int ND = TYPE == 1 ? 6 : 5;
  constexpr int NM = NT * NC + (FFN ? 2 : 0);
  const int r8 = lane >> 3, pos = lane & 7, lg = lane >> 4, lc = lane & 15;
  const int rot = (j + g) & 7;
  constexpr int ldw = FFN ? LDW1 : LDQKV;
  const u32 voffA = r8 * LDX + ((pos ^ r8) << 4);
  const int swB = (r8 & 2) | ((w & 1) << 2);
  const u32 voffB = r8 * ldw + ((pos ^ swB) << 4);
  const int maxch = (j == 15) ? 1 : 5;
  const u32 voffB2 = FFN ? (u32)(r8 * ldw + (min(pos ^ swB, maxch) << 4)) : voffB;
  const int rowbase = FFN ? 128 * g : 128 * g - 16;
  const int p0row = (!FFN && g == 0 && w < 2) ? 8 * w : rowbase + 8 * w;
  const char* sA0 = x + (size_t)p0row * LDX;
  const char* sA1 = x + (size_t)(rowbase + 8 * (w + 8)) * LDX;
  const char* sA2 = x + (size_t)(rowbase + 8 * (16 + (w & 1))) * LDX;
  const char* sB0 = FFN ? w1 + (size_t)(8 * w) * LDW1 + (j - 8) * 352 : wq + (size_t)(8 * w) * LDQKV + j * 128;
  constexpr int SEGB = FFN ? 128 : 1024;
  const u32 mlo = (w == 0) ? 0xFFFFFF00u : 0xFFFFFFFFu;
  auto issue = [&](int i, int k) {
    const int kc = (i + rot) & 7;
    const u32 slot = (i % 3) * SLOT;
    const u32 va = voffA + kc * 128, vb = voffB + kc * 64 * ldw, vb2 = voffB2 + kc * 64 * ldw;
    if (k == 0) { if (TYPE == 0) dma16_masked(va, sA0, slot + w * 1024, mlo); else dma16(va, sA0, slot + w * 1024); }
    if (k == 1) dma16(vb, sB0, slot + BOFF + w * 1024);
    if (k == 2) dma16(vb, sB0 + SEGB, slot + BOFF + (8 + w) * 1024);
    if (k == 3) dma16(vb2, sB0 + 2 * SEGB, slot + BOFF + (16 + w) * 1024);
    if (k == 4) dma16(va, sA1, slot + (w + 8) * 1024);
    if (k == 5) dma16_if(va, sA2, slot + (16 + (w & 1)) * 1024, w);
  };
#pragma unroll
  for (int i = 0; i < 3; ++i)
#pragma unroll
    for (int k = 0; k < ND; ++k) issue(i, k);
  {
    const int bi = blockIdx.x >> 3;
    if (w == 7) {
      dma4(min(lane * 128 + (bi & 7) * 8192, 61000), (const void*)code0, L_JUNK);
    }
    if (w == 6) {
      const int ln = blockIdx.x * 59 + min(lane, 58);
      if (blockIdx.x * 59 + 58 < 4096) dma4(ln * 128, wo, L_JUNK + 256);
      else dma4((u32)max(ln - 4096, 0) * 128, w2, L_JUNK + 256);
    }
    if (w == 5) {
      dma4((lane & 3) * (2u << 20) + WS_A2 + (blockIdx.x & 63) * 4096, ws, L_JUNK + 512);
      dma4(blockIdx.x * 8192 + lane * 4, outp, L_JUNK + 768);
    }
  }
  u32 b1v = 0;
  if (FFN) b1v = b1[min((j - 8) * 176 + (int)threadIdx.x, 1375)];
  if (TYPE == 0 && w == 0 && lane < 8) {
    const u32x4 one = {0x3F803F80u, 0x3F803F80u, 0x3F803F80u, 0x3F803F80u};
#pragma unroll
    for (int s = 0; s < 3; ++s) *(LAS u32x4*)(L + s * SLOT + lane * 16) = one;
  }
  const int T0 = TYPE == 0 ? 0 : (TYPE == 1 ? 5 : 2 * (w & 3));
  const u32 aA = T0 * 2048 + (lc >> 3) * 1024 + (lc & 7) * 128 + ((lg ^ (lc & 7)) << 4);
  const int tq = lc >> 2, tp = lc & 3;
  const int swR = (tq & 2) | ((lg & 1) << 2);
  const u32 aBl = BOFF + lg * 1024 + tq * 128 + ((tp >> 1) << 4) + (tp & 1) * 8;
  const int crot = min(2 * (w & 3), NC - 2);
  u32 aB[NC];
#pragma unroll
  for (int c = 0; c < NC; ++c) {
    const int cc = c + crot >= NC ? c + crot - NC : c + crot;
    const int ct = FFN ? CB + cc : 4 * c + (w & 3);
    aB[c] = aBl + (ct >> 2) * 8192 + (((2 * (ct & 3)) ^ swR) << 4);
  }
  u32 aS[NSP];
  aS[0] = w * 1024 + lane * 16; aS[1] = (w + 8) * 1024 + lane * 16;
  if (NSP == 3) aS[NSP - 1] = (16 + (w & 1)) * 1024 + lane * 16;

  f32x4 acc[NT][NC], acc1[2];
#pragma unroll
  for (int t = 0; t < NT; ++t)
#pragma unroll
    for (int c = 0; c < NC; ++c) acc[t][c] = f32x4{0.f, 0.f, 0.f, 0.f};
  acc1[0] = acc1[1] = f32x4{0.f, 0.f, 0.f, 0.f};
  float ssum[NSP], ssq[NSP];
#pragma unroll
  for (int i = 0; i < NSP; ++i) ssum[i] = ssq[i] = 0.f;
  u32x4 fa[2][NT], sv[NSP];
  u32x2 fb[2][NC][2];
  auto rd = [&](int t, int k) {
    const u32 slot = ((t >> 1) % 3) * SLOT;
    const int b = t & 1;
    if (k < 2 * NC) fb[b][k >> 1][k & 1] = ldstr(L, aB[k >> 1] + slot + b * 4096 + (k & 1) * 512);
    else if (k < 2 * NC + NT) fa[b][k - 2 * NC] = lds128(L, ((aA ^ (b * 64)) + slot + (k - 2 * NC) * 2048));
    else sv[k - 2 * NC - NT] = lds128(L, aS[k - 2 * NC - NT] + slot);
  };
  auto ldw2 = [&](int s, int lo, int hi) {
    const int ks = ks0 + s;
    const bool isO = ks < 32;
    const char* base = isO ? wo + (size_t)ks * 16 * LDW2 : w2 + (size_t)(ks - 32) * 16 * LDW2;
    const __amdgpu_buffer_rsrc_t rs = __builtin_amdgcn_make_buffer_rsrc((void*)base, 0, ks < NKS ? 16 * LDW2 : 0, 0x00020000);
#pragma unroll
    for (int jj = lo; jj < hi; ++jj)
      W.r[s * 8 + jj] = __builtin_amdgcn_raw_buffer_load_b32(rs, w2voff + (jj & 3) * 1024, (jj >> 2) * 4096, 0);
  };
  const u32x4 ones = {0x3F803F80u, 0x3F803F80u, 0x3F803F80u, 0x3F803F80u};
  STAMP(1);
  wait_bar<10>();
  STAMP(2);
#pragma unroll
  for (int k = 0; k < 2 * NC + NT + NSP; ++k) rd(0, k);
  SB();
  sfor<0, 16>([&](auto tc) {
    constexpr int t = decltype(tc)::v, s = t >> 1, b = t & 1;
    if constexpr (b) { if constexpr (s <= 4) wait_bar<5>(); else if constexpr (s == 5) wait_bar<21>(); else if constexpr (s == 6) wait_bar<32>(); }
    constexpr int nr = t == 15 ? 0 : 2 * NC + NT + (b ? NSP : 0);
    constexpr int g0 = (nr + 1) / 2;
    constexpr int nd = (b && s + 3 < 8) ? ND : 0, nw = t >= 9 ? 8 : 0;
    constexpr int per = (nd + nw + NM - g0 - 1) / (NM - g0);
    constexpr int ndot = b ? 0 : 4 * NSP, dper = (ndot + NM - 1) / NM;
    sfor<0, NM>([&](auto qc) {
      constexpr int q = decltype(qc)::v;
      if constexpr (q < NT * NC) {
        constexpr int c = q / NT, i = q % NT;
        const u32x4 wfr = {fb[b][c][0][0], fb[b][c][0][1], fb[b][c][1][0], fb[b][c][1][1]};
        if constexpr (!FFN && c == 2) acc[i][c] = mfma16(fa[b][i], wfr, acc[i][c]);
        else acc[i][c] = mfma16(wfr, fa[b][i], acc[i][c]);
      } else {
        constexpr int c = q - NT * NC;
        const u32x4 wfr = {fb[b][c][0][0], fb[b][c][0][1], fb[b][c][1][0], fb[b][c][1][1]};
        acc1[c] = mfma16(wfr, ones, acc1[c]);
      }
      if constexpr (2 * q < nr) rd(t + 1, 2 * q);
      if constexpr (2 * q + 1 < nr) rd(t + 1, 2 * q + 1);
      if constexpr (q >= g0) {
        sfor<(q - g0) * per, (q - g0 + 1) * per>([&](auto uc) {
          constexpr int u = decltype(uc)::v;
          if constexpr (u < nd) issue(s + 3, u);
          else if constexpr (u < nd + nw) ldw2(t - 9, u - nd, u - nd + 1);
        });
      }
      sfor<q * dper, (q + 1) * dper>([&](auto uc) {
        constexpr int u = decltype(uc)::v;
        if constexpr (u < ndot) {
          constexpr int i = u >> 2, d = u & 3;
          ssum[i] = dot2(sv[i][d], 0x3F803F80u, ssum[i]);
          ssq[i] = dot2(sv[i][d], sv[i][d], ssq[i]);
        }
      });
      SB();
    });
  });
  if (FFN) asm volatile("" : "+v"(acc1[0]), "+v"(acc1[1]));
#pragma unroll
  for (int i = 0; i < NSP; ++i) asm volatile("" : "+v"(ssum[i]), "+v"(ssq[i]));
  STAMP(3);
#pragma unroll
  for (int i = 0; i < NSP; ++i) {
    const float sm = red8(ssum[i]) * (1.f / 512.f), sq = red8(ssq[i]) * (1.f / 512.f);
    const float var = fmaxf(sq - sm * sm, 0.f);
    const float rs = __builtin_amdgcn_rsqf(var + 1e-5f);
    const int p = i == 0 ? w : (i == 1 ? w + 8 : 16 + (w & 1));
    if (pos == 0) { *(LAS float*)(L + L_MEAN + (p * 8 + r8) * 4) = sm; *(LAS float*)(L + L_RSTD + (p * 8 + r8) * 4) = rs; }
  }
  if (TYPE == 0) {
    const int h = w & 3;
    if (lc == 0) {
      *(LAS f32x4*)(L + L_CS + (h * 16 + lg * 4) * 4) = acc[0][0];
      *(LAS f32x4*)(L + L_CS + (64 + h * 16 + lg * 4) * 4) = acc[0][1];
    }
    if (lg == 0) *(LAS float*)(L + L_CS + (128 + h * 16 + lc) * 4) = acc[0][2][0];
    *(LAS f32x4*)(L + L_KV + h * 2048 + lane * 16) = acc[NT - 1][1];
    *(LAS f32x4*)(L + L_KV + h * 2048 + 1024 + lane * 16) = acc[NT - 1][2];
  }
  if (FFN) {
    if (lc == 0) {
      *(LAS f32x4*)(L + L_CS + ((CB + crot) * 16 + lg * 4) * 4) = acc1[0];
      *(LAS f32x4*)(L + L_CS + ((CB + crot + 1) * 16 + lg * 4) * 4) = acc1[1];
    }
    if (threadIdx.x < 192) *(LAS float*)(L + L_B1 + threadIdx.x * 4) = bf_lo(b1v);
  }
  lds_bar();
  STAMP(4);
  if (!FFN) {
    const int h = w & 3;
    constexpr int NK = 5;
    const int TK = TYPE == 0 ? 0 : 4;
    const f32x4 csq = *(LAS f32x4*)(L + L_CS + (h * 16 + lg * 4) * 4);
    const f32x4 csk = *(LAS f32x4*)(L + L_CS + (64 + h * 16 + lg * 4) * 4);
    const f32x4 csv = splat4(*(LAS float*)(L + L_CS + (128 + h * 16 + lc) * 4));
    u32x2 Qb[4], Kb[NK], Vb[NK];
#pragma unroll
    for (int i = 0; i < NK; ++i) {
      const int T = TK + i;
      f32x4 ak, av;
      if (TYPE == 1 && i == 0) {
        ak = *(LAS f32x4*)(L + L_KV + h * 2048 + lane * 16);
        av = *(LAS f32x4*)(L + L_KV + h * 2048 + 1024 + lane * 16);
      } else {
        ak = acc[TYPE == 0 ? i : (i > 0 ? i - 1 : 0)][1];
        av = acc[TYPE == 0 ? i : (i > 0 ? i - 1 : 0)][2];
      }
      const float rc1 = *(LAS float*)(L + L_RSTD + (T * 16 + lc) * 4);
      const f32x4 mc = splat4(*(LAS float*)(L + L_MEAN + (T * 16 + lc) * 4)), rc = splat4(rc1);
      const f32x4 m4 = *(LAS f32x4*)(L + L_MEAN + (T * 16 + lg * 4) * 4), r4 = *(LAS f32x4*)(L + L_RSTD + (T * 16 + lg * 4) * 4);
      const f32x4 kk = (ak - mc * csk) * rc;
      const f32x4 vv = (av - m4 * csv) * r4;
      Kb[i] = u32x2{pk_bf16(kk[0], kk[1]), pk_bf16(kk[2], kk[3])};
      Vb[i] = u32x2{pk_bf16(vv[0], vv[1]), pk_bf16(vv[2], vv[3])};
      if (i > 0) {
        const f32x4 qq = (acc[TYPE == 0 ? i : i - 1][0] - mc * csq) * splat4(rc1 * (0.25f * 1.4426950408889634f));
        Qb[i - 1] = u32x2{pk_bf16(qq[0], qq[1]), pk_bf16(qq[2], qq[3])};
      }
    }
    SB();
    f32x4 mP, mC;
#pragma unroll
    for (int r = 0; r < 4; ++r) {
      mP[r] = (4 * lg + r >= lc + 2) ? 0.f : -1e30f;
      mC[r] = (4 * lg + r <= lc) ? 0.f : -1e30f;
    }
    const bool odd = lc & 1;
    const u32 sel0 = odd ? 0x01000c0cu : 0x0c0c0100u, sel1 = odd ? 0x03020c0cu : 0x0c0c0302u;
    u32x2 Ob[4];
#pragma unroll
    for (int q = 0; q < 4; ++q) {
      ldw2(7 + 2 * q, 0, 8); ldw2(8 + 2 * q, 0, 8);
      SB();
      f32x4 mp = mP;
      if (TYPE == 0 && q == 0 && g == 0) mp = f32x4{-1e30f, -1e30f, -1e30f, -1e30f};
      const f32x4 sp = mfma16k16(Kb[q], Qb[q], mp), sc = mfma16k16(Kb[q + 1], Qb[q], mC);
      const float a0 = odd ? sp[1] : sp[0], a1 = odd ? sp[3] : sp[2], a2 = odd ? sc[1] : sc[0], a3 = odd ? sc[3] : sc[2];
      const float mx = max_g(fmaxf(fmaxf(a0, a1), fmaxf(a2, a3)));
      const u32 u0 = pk_bf16(__builtin_amdgcn_exp2f(a0 - mx), __builtin_amdgcn_exp2f(a1 - mx));
      const u32 u1 = pk_bf16(__builtin_amdgcn_exp2f(a2 - mx), __builtin_amdgcn_exp2f(a3 - mx));
      const u32x4 pb = {__builtin_amdgcn_perm(0, u0, sel0), __builtin_amdgcn_perm(0, u0, sel1),
                        __builtin_amdgcn_perm(0, u1, sel0), __builtin_amdgcn_perm(0, u1, sel1)};
      const u32x4 vf = {Vb[q][0], Vb[q][1], Vb[q + 1][0], Vb[q + 1][1]};
      const f32x4 z = {0.f, 0.f, 0.f, 0.f};
      const f32x4 o = mfma16(vf, pb, z), l = mfma16(ones, pb, z);
      const f32x4 on = o * splat4(__builtin_amdgcn_rcpf(l[0]));
      Ob[q] = u32x2{pk_bf16(on[0], on[1]), pk_bf16(on[2], on[3])};
      SB();
    }
    const int rt = 4 * g + (TYPE == 0 ? 0 : 2), ks = 4 * j + h;
    __builtin_amdgcn_raw_buffer_store_b128(pair_rows(Ob[0], Ob[1]), rsA2, lane * 16, (rt * NKS + ks) * 1024, AUX_SC1);
    __builtin_amdgcn_raw_buffer_store_b128(pair_rows(Ob[2], Ob[3]), rsA2, lane * 16, ((rt + 1) * NKS + ks) * 1024, AUX_SC1);
    asm volatile("s_waitcnt vmcnt(0)\n\ts_barrier" ::: "memory");
  } else {
    const int m = w & 3;
    f32x4 mc[2], rc[2];
#pragma unroll
    for (int i = 0; i < 2; ++i) {
      mc[i] = splat4(*(LAS float*)(L + L_MEAN + ((2 * m + i) * 16 + lc) * 4));
      rc[i] = splat4(*(LAS float*)(L + L_RSTD + ((2 * m + i) * 16 + lc) * 4));
    }
    const int nct = j == 15 ? 9 : 11;
    const int rt = 4 * g + m, ksb = 32 + 11 * (j - 8);
    const f32x4 C1 = splat4(-2.f * 1.4426950408889634f * 0.7978845608028654f);
    const f32x4 C2 = splat4(-2.f * 1.4426950408889634f * 0.7978845608028654f * 0.044715f);
    const f32x4 ONE = splat4(1.f);
#pragma unroll
    for (int c = 0; c < NC; ++c) {
      if (c < 4) { ldw2(7 + 2 * c, 0, 8); ldw2(8 + 2 * c, 0, 8); }
      SB();
      const int ct = CB + (c + crot >= NC ? c + crot - NC : c + crot);
      const f32x4 cs = *(LAS f32x4*)(L + L_CS + (ct * 16 + lg * 4) * 4);
      const f32x4 bb = *(LAS f32x4*)(L + L_B1 + (ct * 16 + lg * 4) * 4);
      u32x2 hb[2];
#pragma unroll
      for (int i = 0; i < 2; ++i) {
        const f32x4 v = (acc[i][c] - mc[i] * cs) * rc[i] + bb;
        const f32x4 zz = v * (v * v * C2 + C1);
        f32x4 e;
#pragma unroll
        for (int r = 0; r < 4; ++r) e[r] = __builtin_amdgcn_exp2f(zz[r]);
        e += ONE;
#pragma unroll
        for (int r = 0; r < 4; ++r) e[r] = __builtin_amdgcn_rcpf(e[r]);
        const f32x4 y = v * e;
        hb[i] = u32x2{pk_bf16(y[0], y[1]), pk_bf16(y[2], y[3])};
      }
      __builtin_amdgcn_raw_buffer_store_b128(pair_rows(hb[0], hb[1]), rsA2, lane * 16,
                                             ct < nct ? (rt * NKS + ksb + ct) * 1024 : A2_BYTES, AUX_SC1);
      SB();
    }
    asm volatile("s_waitcnt vmcnt(0)\n\ts_barrier" ::: "memory");
  }
}

template <int TYPE>
DEV void body(LAS char* L, const char* __restrict__ x, const char* __restrict__ wq, const char* __restrict__ wo,
              const char* __restrict__ w1, const u16* __restrict__ b1, const char* __restrict__ w2, const char* __restrict__ b2,
              char* __restrict__ out, char* __restrict__ ws, u64 code0, int g, int j, int w, int lane STAMP_ARG) {
  const int mt = 2 * g + ((j >> 2) & 1), nt = (j & 3) + (j >> 3) * 4;
  const __amdgpu_buffer_rsrc_t rsA2 = __builtin_amdgcn_make_buffer_rsrc(ws + WS_A2, 0, A2_BYTES, 0x00020000);
  const int ks0 = w < 6 ? 15 * w : 90 + 14 * (w - 6);
  const u32 w2voff = (lane >> 5) * 8 * LDW2 + nt * 128 + (lane & 31) * 4;
  GAS u64* ctl = (GAS u64*)(ws + g * 4096);
  const u64 tag = __hip_atomic_load(ctl, __ATOMIC_RELAXED, __HIP_MEMORY_SCOPE_AGENT) + 1;
  P2W W;
  phase1<TYPE>(L, x, wq, w1, b1, wo, w2, ws, out, code0, g, j, w, lane, rsA2, w2voff, ks0, W STAMP_PASS);
  if (threadIdx.x == 0) __hip_atomic_store(ctl + 16 * (j + 1), tag, __ATOMIC_RELAXED, __HIP_MEMORY_SCOPE_AGENT);
  STAMP(5);
  const int orow = mt * 64 + (w >> 2) * 32 + (lane & 31), ocol = nt * 64 + (w & 3) * 16 + (lane >> 5) * 8;
  const u32 ooff = (u32)(orow * LDX + ocol * 2);
  const u32x4 xres = *(const GAS u32x4*)(x + ooff);
  const u32x4 bres = *(const GAS u32x4*)(b2 + (u32)(ocol * 2));
  u32x4 wf[15][2];
#pragma unroll
  for (int s = 0; s < 15; ++s)
#pragma unroll
    for (int d = 0; d < 4; ++d) {
      wf[s][0][d] = __builtin_amdgcn_perm(W.r[s * 8 + 2 * d + 1], W.r[s * 8 + 2 * d], 0x05040100u);
      wf[s][1][d] = __builtin_amdgcn_perm(W.r[s * 8 + 2 * d + 1], W.r[s * 8 + 2 * d], 0x07060302u);
    }
#pragma unroll
  for (int s = 0; s < 15; ++s) asm volatile("" : "+v"(wf[s][0]), "+v"(wf[s][1]));
  SB();
  STAMP(6);
  {
    const int ke = min(ks0 + 14, NKS - 1);
    const int plo = ks0 < 32 ? ks0 >> 2 : 8 + (ks0 - 32) / 11, phi = ke < 32 ? ke >> 2 : 8 + (ke - 32) / 11;
    const int pj = min(plo + (lane & 7), phi);
#pragma nounroll
    for (u32 spins = 0; spins < SPIN_CAP; ++spins) {
      const u64 v = __hip_atomic_load(ctl + 16 * (pj + 1), __ATOMIC_RELAXED, __HIP_MEMORY_SCOPE_AGENT);
      if (__all(v == tag)) break;
      __builtin_amdgcn_s_sleep(1);
    }
    __builtin_amdgcn_fence(__ATOMIC_ACQUIRE, "wavefront");
  }
  STAMP(7);
  const int nks = w < 6 ? 15 : 14;
  u32x4 af[2][15];
#pragma unroll
  for (int r = 0; r < 2; ++r)
#pragma unroll
    for (int s = 0; s < 15; ++s) {
      if (r == 1 && s >= 5) continue;
      const int so = (s < nks) ? ((2 * mt + r) * NKS + ks0 + s) * 1024 : A2_BYTES;
      af[r][s] = __builtin_amdgcn_raw_buffer_load_b128(rsA2, lane * 16, so, AUX_SC1);
    }
  SB();
#pragma unroll
  for (int r = 0; r < 2; ++r) {
    f32x16 a0, a1;
#pragma unroll
    for (int i = 0; i < 16; ++i) a0[i] = a1[i] = 0.f;
#pragma unroll
    for (int s = 0; s < 15; ++s) {
      a0 = mfma32(wf[s][0], af[r][s], a0);
      a1 = mfma32(wf[s][1], af[r][s], a1);
      if (r == 0 && s < 10) {
        const int s2 = s + 5;
        const int so = (s2 < nks) ? ((2 * mt + 1) * NKS + ks0 + s2) * 1024 : A2_BYTES;
        af[1][s2] = __builtin_amdgcn_raw_buffer_load_b128(rsA2, lane * 16, so, AUX_SC1);
      }
      SB();
    }
#pragma unroll
    for (int y = 0; y < 4; ++y) {
      *(LAS f32x4*)(L + ((w * 2 + r) * 8 + y * 2) * 1024 + lane * 16) = f32x4{a0[4 * y], a0[4 * y + 1], a0[4 * y + 2], a0[4 * y + 3]};
      *(LAS f32x4*)(L + ((w * 2 + r) * 8 + y * 2 + 1) * 1024 + lane * 16) = f32x4{a1[4 * y], a1[4 * y + 1], a1[4 * y + 2], a1[4 * y + 3]};
    }
    SB();
  }
  STAMP(8);
  lds_bar();
  if (j == 0 && threadIdx.x == 0) __hip_atomic_store(ctl, tag, __ATOMIC_RELAXED, __HIP_MEMORY_SCOPE_AGENT);
  f32x4 s0 = {0.f, 0.f, 0.f, 0.f}, s1 = s0;
#pragma unroll
  for (int sw = 0; sw < 8; ++sw) {
    s0 += *(LAS f32x4*)(L + ((sw * 2 + (w >> 2)) * 8 + (w & 3) * 2) * 1024 + lane * 16);
    s1 += *(LAS f32x4*)(L + ((sw * 2 + (w >> 2)) * 8 + (w & 3) * 2 + 1) * 1024 + lane * 16);
  }
  u32x4 o;
#pragma unroll
  for (int d = 0; d < 4; ++d)
    o[d] = pk_bf16(s0[d] + bf_lo(xres[d]) + bf_lo(bres[d]), s1[d] + bf_hi(xres[d]) + bf_hi(bres[d]));
  *(GAS u32x4*)(out + ooff) = o;
  STAMP(9);
  STAMP_OUT();
}

__global__ __launch_bounds__(512, 2) void fused(const char* __restrict__ x, const char* __restrict__ wq, const char* __restrict__ wo,
                                                const char* __restrict__ w1, const u16* __restrict__ b1, const char* __restrict__ w2,
                                                const char* __restrict__ b2, char* __restrict__ out, char* __restrict__ ws) {
  extern __shared__ __attribute__((aligned(16))) char smem[];
  LAS char* L = (LAS char*)smem;
  Stamps ST;
  STAMP(0);
  const int lane = threadIdx.x & 63, w = __builtin_amdgcn_readfirstlane(threadIdx.x >> 6);
  u64 code0;
  asm volatile("s_getpc_b64 %0" : "=s"(code0));
  code0 &= ~127ull;
  const int b = blockIdx.x, bi = b >> 3, g = (b & 3) * 4 + (bi & 3), j = ((bi >> 2) & 3) + ((b >> 2) & 1) * 4 + (bi >> 4) * 8;
  if (j < 8) {
    if (w < 4) body<0>(L, x, wq, wo, w1, b1, w2, b2, out, ws, code0, g, j, w, lane STAMP_PASS);
    else body<1>(L, x, wq, wo, w1, b1, w2, b2, out, ws, code0, g, j, w, lane STAMP_PASS);
  } else {
    if (w < 4) body<2>(L, x, wq, wo, w1, b1, w2, b2, out, ws, code0, g, j, w, lane STAMP_PASS);
    else body<3>(L, x, wq, wo, w1, b1, w2, b2, out, ws, code0, g, j, w, lane STAMP_PASS);
  }
}
}

extern "C" void kernel_launch(void* const* d_in, const int* in_sizes, int n_in, void* d_out, int out_size, void* d_ws,
                              size_t ws_size, hipStream_t stream) {
  (void)hipFuncSetAttribute((const void*)fk::fused, hipFuncAttributeMaxDynamicSharedMemorySize, fk::L_TOTAL);
  fk::fused<<<256, 512, fk::L_TOTAL, stream>>>((const char*)d_in[0], (const char*)d_in[1], (const char*)d_in[2], (const char*)d_in[4],
                                               (const u16*)d_in[5], (const char*)d_in[6], (const char*)d_in[7], (char*)d_out,
                                               (char*)d_ws);
}
```

```cpp
#define PF_CODE
#define PF_DATA
#define MAP2
#include <hip/hip_runtime.h>
#include <stdint.h>

typedef unsigned u32;
typedef unsigned long long u64;
typedef unsigned short u16;
typedef short s16x4 __attribute__((ext_vector_type(4)));
typedef short s16x8 __attribute__((ext_vector_type(8)));
typedef __bf16 b16x2 __attribute__((ext_vector_type(2)));
typedef float f32x2 __attribute__((ext_vector_type(2)));
typedef float f32x4 __attribute__((ext_vector_type(4)));
typedef float f32x16 __attribute__((ext_vector_type(16)));
typedef u32 u32x2 __attribute__((ext_vector_type(2)));
typedef u32 u32x4 __attribute__((ext_vector_type(4)));
#define LAS __attribute__((address_space(3)))
#define GAS __attribute__((address_space(1)))
#define DEV __device__ __forceinline__

namespace fk {
constexpr int LDX = 1024, LDQKV = 3072, LDW1 = 2752, LDW2 = 1024;
constexpr int NKS = 118;
constexpr int SLOT = 43008, BOFF = 18432;
constexpr int L_MEAN = 3 * SLOT, L_RSTD = L_MEAN + 576, L_CS = L_RSTD + 576, L_B1 = L_CS + 768, L_KV = L_B1 + 768,
              L_JUNK = L_KV + 8192, L_TOTAL = L_JUNK + 2048;
constexpr int WS_A2 = 65536, A2_BYTES = 64 * NKS * 1024;
constexpr int AUX_SC1 = 16;
constexpr u32 SPIN_CAP = 1u << 16;

#define STAMP(i) do {} while (0)
#define STAMP_ARG
#define STAMP_PASS
#define STAMP_OUT() do {} while (0)

DEV void dma16(u32 voff, const void* sbase, u32 lds) {
  asm volatile("s_mov_b32 m0, %2\n\ts_nop 0\n\tglobal_load_lds_dwordx4 %0, %1" ::"v"(voff), "s"(sbase), "s"(lds) : "memory");
}
DEV void dma16_masked(u32 voff, const void* sbase, u32 lds, u32 mlo) {
  asm volatile("s_mov_b32 m0, %2\n\ts_mov_b32 exec_lo, %3\n\tglobal_load_lds_dwordx4 %0, %1\n\ts_mov_b32 exec_lo, -1"
               ::"v"(voff), "s"(sbase), "s"(lds), "s"(mlo) : "memory");
}
DEV void dma16_if(u32 voff, const void* sbase, u32 lds, u32 cond) {
  asm volatile("s_cmp_gt_u32 %3, 5\n\ts_cbranch_scc1 1f\n\ts_mov_b32 m0, %2\n\ts_nop 0\n\tglobal_load_lds_dwordx4 %0, %1\n1:"
               ::"v"(voff), "s"(sbase), "s"(lds), "s"(cond) : "memory", "scc");
}
DEV void dma4(u32 voff, const void* sbase, u32 lds) {
  asm volatile("s_mov_b32 m0, %2\n\ts_nop 0\n\tglobal_load_lds_dword %0, %1" ::"v"(voff), "s"(sbase), "s"(lds) : "memory");
}
template <int N> DEV void wait_bar() {
  asm volatile("s_waitcnt vmcnt(%0) lgkmcnt(0)\n\ts_barrier" ::"i"(N) : "memory");
}
DEV void lds_bar() { asm volatile("s_waitcnt lgkmcnt(0)\n\ts_barrier" ::: "memory"); }
#define SB() __builtin_amdgcn_sched_barrier(0)

DEV u32 pk_bf16(float a, float b) {
  f32x2 p = {a, b};
  return __builtin_bit_cast(u32, __builtin_convertvector(p, b16x2));
}
DEV float bf_lo(u32 v) { return __builtin_bit_cast(float, v << 16); }
DEV float bf_hi(u32 v) { return __builtin_bit_cast(float, v & 0xffff0000u); }
DEV float dot2(u32 a, u32 b, float c) {
  return __builtin_amdgcn_fdot2_f32_bf16(__builtin_bit_cast(b16x2, a), __builtin_bit_cast(b16x2, b), c, false);
}
DEV f32x4 mfma16(u32x4 a, u32x4 b, f32x4 c) {
  return __builtin_amdgcn_mfma_f32_16x16x32_bf16(__builtin_bit_cast(s16x8, a), __builtin_bit_cast(s16x8, b), c, 0, 0, 0);
}
DEV f32x4 mfma16k16(u32x2 a, u32x2 b, f32x4 c) {
  return __builtin_amdgcn_mfma_f32_16x16x16bf16_1k(__builtin_bit_cast(s16x4, a), __builtin_bit_cast(s16x4, b), c, 0, 0, 0);
}
DEV f32x16 mfma32(u32x4 a, u32x4 b, f32x16 c) {
  return __builtin_amdgcn_mfma_f32_32x32x16_bf16(__builtin_bit_cast(s16x8, a), __builtin_bit_cast(s16x8, b), c, 0, 0, 0);
}
DEV u32x4 lds128(LAS char* L, u32 off) { return *(LAS u32x4*)(L + off); }
DEV u32x2 ldstr(LAS char* L, u32 off) {
  return __builtin_bit_cast(u32x2, __builtin_amdgcn_ds_read_tr16_b64_v4i16((LAS s16x4*)(L + off)));
}
template <int CTRL> DEV float dpp_add(float v) {
  int t = __builtin_amdgcn_update_dpp(0, __builtin_bit_cast(int, v), CTRL, 0xf, 0xf, true);
  return v + __builtin_bit_cast(float, t);
}
DEV float red8(float v) {
  v = dpp_add<0xB1>(v);
  v = dpp_add<0x4E>(v);
  v = dpp_add<0x141>(v);
  return v;
}
DEV float max_g(float v) {
  u32 u = __builtin_bit_cast(u32, v);
  auto r = __builtin_amdgcn_permlane32_swap(u, u, false, false);
  v = fmaxf(__builtin_bit_cast(float, r[0]), __builtin_bit_cast(float, r[1]));
  u = __builtin_bit_cast(u32, v);
  auto q = __builtin_amdgcn_permlane16_swap(u, u, false, false);
  return fmaxf(__builtin_bit_cast(float, q[0]), __builtin_bit_cast(float, q[1]));
}
DEV u32x4 pair_rows(u32x2 x, u32x2 y) {
  auto a = __builtin_amdgcn_permlane16_swap(x[0], y[0], false, false);
  auto b = __builtin_amdgcn_permlane16_swap(x[1], y[1], false, false);
  return u32x4{a[0], b[0], a[1], b[1]};
}

DEV f32x4 splat4(float v) {
  f32x2 p = {v, v};
  asm volatile("" : "+v"(p));
  return f32x4{p[0], p[1], p[0], p[1]};
}
template <int V> struct IC { static constexpr int v = V; };
template <int LO, int HI, class F> DEV void sfor(F&& f) {
  if constexpr (LO < HI) { f(IC<LO>{}); sfor<LO + 1, HI>(f); }
}
struct P2W { u32 r[120]; };

template <int TYPE>
DEV void phase1(LAS char* L, const char* x, const char* wq, const char* w1, const u16* b1, const char* wo, const char* w2,
                char* ws, const char* outp, u64 code0, int g, int j, int w, int lane, __amdgpu_buffer_rsrc_t rsA2, u32 w2voff, int ks0, P2W& W STAMP_ARG) {
  constexpr bool FFN = TYPE >= 2;
  constexpr int NT = TYPE == 0 ? 5 : (TYPE == 1 ? 4 : 2);
  constexpr int NC = TYPE == 2 ? 6 : (TYPE == 3 ? 5 : 3);
  constexpr int CB = TYPE == 3 ? 6 : 0;
  constexpr int NSP = TYPE == 1 ? 3 : 2;
  constexpr int ND = TYPE == 1 ? 6 : 5;
  constexpr int NM = NT * NC + (FFN ? 2 : 0);
  const int r8 = lane >> 3, pos = lane & 7, lg = lane >> 4, lc = lane & 15;
  const int rot = (j + g) & 7;
  constexpr int ldw = FFN ? LDW1 : LDQKV;
  const u32 voffA = r8 * LDX + ((pos ^ r8) << 4);
  const int swB = (r8 & 2) | ((w & 1) << 2);
  const u32 voffB = r8 * ldw + ((pos ^ swB) << 4);
  const int maxch = (j == 15) ? 1 : 5;
  const u32 voffB2 = FFN ? (u32)(r8 * ldw + (min(pos ^ swB, maxch) << 4)) : voffB;
  const int rowbase = FFN ? 128 * g : 128 * g - 16;
  const int p0row = (!FFN && g == 0 && w < 2) ? 8 * w : rowbase + 8 * w;
  const char* sA0 = x + (size_t)p0row * LDX;
  const char* sA1 = x + (size_t)(rowbase + 8 * (w + 8)) * LDX;
  const char* sA2 = x + (size_t)(rowbase + 8 * (16 + (w & 1))) * LDX;
  const char* sB0 = FFN ? w1 + (size_t)(8 * w) * LDW1 + (j - 8) * 352 : wq + (size_t)(8 * w) * LDQKV + j * 128;
  constexpr int SEGB = FFN ? 128 : 1024;
  const u32 mlo = (w == 0) ? 0xFFFFFF00u : 0xFFFFFFFFu;
  auto issue = [&](int i, int k) {
    const int kc = (i + rot) & 7;
    const u32 slot = (i % 3) * SLOT;
    const u32 va = voffA + kc * 128, vb = voffB + kc * 64 * ldw, vb2 = voffB2 + kc * 64 * ldw;
    if (k == 0) { if (TYPE == 0) dma16_masked(va, sA0, slot + w * 1024, mlo); else dma16(va, sA0, slot + w * 1024); }
    if (k == 1) dma16(vb, sB0, slot + BOFF + w * 1024);
    if (k == 2) dma16(vb, sB0 + SEGB, slot + BOFF + (8 + w) * 1024);
    if (k == 3) dma16(vb2, sB0 + 2 * SEGB, slot + BOFF + (16 + w) * 1024);
    if (k == 4) dma16(va, sA1, slot + (w + 8) * 1024);
    if (k == 5) dma16_if(va, sA2, slot + (16 + (w & 1)) * 1024, w);
  };
#pragma unroll
  for (int i = 0; i < 3; ++i)
#pragma unroll
    for (int k = 0; k < ND; ++k) issue(i, k);
  {
    const int bi = blockIdx.x >> 3;
    if (w == 7) {
      dma4(min(lane * 128 + (bi & 7) * 8192, 61000), (const void*)code0, L_JUNK);
    }
    if (w == 6) {
      const int ln = blockIdx.x * 59 + min(lane, 58);
      if (blockIdx.x * 59 + 58 < 4096) dma4(ln * 128, wo, L_JUNK + 256);
      else dma4((u32)max(ln - 4096, 0) * 128, w2, L_JUNK + 256);
    }
    if (w == 5) {
      dma4((lane & 3) * (2u << 20) + WS_A2 + (blockIdx.x & 63) * 4096, ws, L_JUNK + 512);
      dma4(blockIdx.x * 8192 + lane * 4, outp, L_JUNK + 768);
    }
  }
  u32 b1v = 0;
  if (FFN) b1v = b1[min((j - 8) * 176 + (int)threadIdx.x, 1375)];
  if (TYPE == 0 && w == 0 && lane < 8) {
    const u32x4 one = {0x3F803F80u, 0x3F803F80u, 0x3F803F80u, 0x3F803F80u};
#pragma unroll
    for (int s = 0; s < 3; ++s) *(LAS u32x4*)(L + s * SLOT + lane * 16) = one;
  }
  const int T0 = TYPE == 0 ? 0 : (TYPE == 1 ? 5 : 2 * (w & 3));
  const u32 aA = T0 * 2048 + (lc >> 3) * 1024 + (lc & 7) * 128 + ((lg ^ (lc & 7)) << 4);
  const int tq = lc >> 2, tp = lc & 3;
  const int swR = (tq & 2) | ((lg & 1) << 2);
  const u32 aBl = BOFF + lg * 1024 + tq * 128 + ((tp >> 1) << 4) + (tp & 1) * 8;
  const int crot = min(2 * (w & 3), NC - 2);
  u32 aB[NC];
#pragma unroll
  for (int c = 0; c < NC; ++c) {
    const int cc = c + crot >= NC ? c + crot - NC : c + crot;
    const int ct = FFN ? CB + cc : 4 * c + (w & 3);
    aB[c] = aBl + (ct >> 2) * 8192 + (((2 * (ct & 3)) ^ swR) << 4);
  }
  u32 aS[NSP];
  aS[0] = w * 1024 + lane * 16; aS[1] = (w + 8) * 1024 + lane * 16;
  if (NSP == 3) aS[NSP - 1] = (16 + (w & 1)) * 1024 + lane * 16;

  f32x4 acc[NT][NC], acc1[2];
#pragma unroll
  for (int t = 0; t < NT; ++t)
#pragma unroll
    for (int c = 0; c < NC; ++c) acc[t][c] = f32x4{0.f, 0.f, 0.f, 0.f};
  acc1[0] = acc1[1] = f32x4{0.f, 0.f, 0.f, 0.f};
  float ssum[NSP], ssq[NSP];
#pragma unroll
  for (int i = 0; i < NSP; ++i) ssum[i] = ssq[i] = 0.f;
  u32x4 fa[2][NT], sv[NSP];
  u32x2 fb[2][NC][2];
  auto rd = [&](int t, int k) {
    const u32 slot = ((t >> 1) % 3) * SLOT;
    const int b = t & 1;
    if (k < 2 * NC) fb[b][k >> 1][k & 1] = ldstr(L, aB[k >> 1] + slot + b * 4096 + (k & 1) * 512);
    else if (k < 2 * NC + NT) fa[b][k - 2 * NC] = lds128(L, ((aA ^ (b * 64)) + slot + (k - 2 * NC) * 2048));
    else sv[k - 2 * NC - NT] = lds128(L, aS[k - 2 * NC - NT] + slot);
  };
  auto ldw2 = [&](int s, int lo, int hi) {
    const int ks = ks0 + s;
    const bool isO = ks < 32;
    const char* base = isO ? wo + (size_t)ks * 16 * LDW2 : w2 + (size_t)(ks - 32) * 16 * LDW2;
    const __amdgpu_buffer_rsrc_t rs = __builtin_amdgcn_make_buffer_rsrc((void*)base, 0, ks < NKS ? 16 * LDW2 : 0, 0x00020000);
#pragma unroll
    for (int jj = lo; jj < hi; ++jj)
      W.r[s * 8 + jj] = __builtin_amdgcn_raw_buffer_load_b32(rs, w2voff + (jj & 3) * 1024, (jj >> 2) * 4096, 0);
  };
  const u32x4 ones = {0x3F803F80u, 0x3F803F80u, 0x3F803F80u, 0x3F803F80u};
  STAMP(1);
  wait_bar<10>();
  STAMP(2);
#pragma unroll
  for (int k = 0; k < 2 * NC + NT + NSP; ++k) rd(0, k);
  SB();
  sfor<0, 16>([&](auto tc) {
    constexpr int t = decltype(tc)::v, s = t >> 1, b = t & 1;
    if constexpr (b) { if constexpr (s <= 4) wait_bar<5>(); else if constexpr (s == 5) wait_bar<21>(); else if constexpr (s == 6) wait_bar<32>(); }
    constexpr int nr = t == 15 ? 0 : 2 * NC + NT + (b ? NSP : 0);
    constexpr int g0 = (nr + 1) / 2;
    constexpr int nd = (b && s + 3 < 8) ? ND : 0, nw = t >= 9 ? 8 : 0;
    constexpr int per = (nd + nw + NM - g0 - 1) / (NM - g0);
    constexpr int ndot = b ? 0 : 4 * NSP, dper = (ndot + NM - 1) / NM;
    sfor<0, NM>([&](auto qc) {
      constexpr int q = decltype(qc)::v;
      if constexpr (q < NT * NC) {
        constexpr int c = q / NT, i = q % NT;
        const u32x4 wfr = {fb[b][c][0][0], fb[b][c][0][1], fb[b][c][1][0], fb[b][c][1][1]};
        if constexpr (!FFN && c == 2) acc[i][c] = mfma16(fa[b][i], wfr, acc[i][c]);
        else acc[i][c] = mfma16(wfr, fa[b][i], acc[i][c]);
      } else {
        constexpr int c = q - NT * NC;
        const u32x4 wfr = {fb[b][c][0][0], fb[b][c][0][1], fb[b][c][1][0], fb[b][c][1][1]};
        acc1[c] = mfma16(wfr, ones, acc1[c]);
      }
      if constexpr (2 * q < nr) rd(t + 1, 2 * q);
      if constexpr (2 * q + 1 < nr) rd(t + 1, 2 * q + 1);
      if constexpr (q >= g0) {
        sfor<(q - g0) * per, (q - g0 + 1) * per>([&](auto uc) {
          constexpr int u = decltype(uc)::v;
          if constexpr (u < nd) issue(s + 3, u);
          else if constexpr (u < nd + nw) ldw2(t - 9, u - nd, u - nd + 1);
        });
      }
      sfor<q * dper, (q + 1) * dper>([&](auto uc) {
        constexpr int u = decltype(uc)::v;
        if constexpr (u < ndot) {
          constexpr int i = u >> 2, d = u & 3;
          ssum[i] = dot2(sv[i][d], 0x3F803F80u, ssum[i]);
          ssq[i] = dot2(sv[i][d], sv[i][d], ssq[i]);
        }
      });
      SB();
    });
  });
  if (FFN) asm volatile("" : "+v"(acc1[0]), "+v"(acc1[1]));
#pragma unroll
  for (int i = 0; i < NSP; ++i) asm volatile("" : "+v"(ssum[i]), "+v"(ssq[i]));
  STAMP(3);
#pragma unroll
  for (int i = 0; i < NSP; ++i) {
    const float sm = red8(ssum[i]) * (1.f / 512.f), sq = red8(ssq[i]) * (1.f / 512.f);
    const float var = fmaxf(sq - sm * sm, 0.f);
    const float rs = __builtin_amdgcn_rsqf(var + 1e-5f);
    const int p = i == 0 ? w : (i == 1 ? w + 8 : 16 + (w & 1));
    if (pos == 0) { *(LAS float*)(L + L_MEAN + (p * 8 + r8) * 4) = sm; *(LAS float*)(L + L_RSTD + (p * 8 + r8) * 4) = rs; }
  }
  if (TYPE == 0) {
    const int h = w & 3;
    if (lc == 0) {
      *(LAS f32x4*)(L + L_CS + (h * 16 + lg * 4) * 4) = acc[0][0];
      *(LAS f32x4*)(L + L_CS + (64 + h * 16 + lg * 4) * 4) = acc[0][1];
    }
    if (lg == 0) *(LAS float*)(L + L_CS + (128 + h * 16 + lc) * 4) = acc[0][2][0];
    *(LAS f32x4*)(L + L_KV + h * 2048 + lane * 16) = acc[NT - 1][1];
    *(LAS f32x4*)(L + L_KV + h * 2048 + 1024 + lane * 16) = acc[NT - 1][2];
  }
  if (FFN) {
    if (lc == 0) {
      *(LAS f32x4*)(L + L_CS + ((CB + crot) * 16 + lg * 4) * 4) = acc1[0];
      *(LAS f32x4*)(L + L_CS + ((CB + crot + 1) * 16 + lg * 4) * 4) = acc1[1];
    }
    if (threadIdx.x < 192) *(LAS float*)(L + L_B1 + threadIdx.x * 4) = bf_lo(b1v);
  }
  lds_bar();
  STAMP(4);
  if (!FFN) {
    const int h = w & 3;
    constexpr int NK = 5;
    const int TK = TYPE == 0 ? 0 : 4;
    const f32x4 csq = *(LAS f32x4*)(L + L_CS + (h * 16 + lg * 4) * 4);
    const f32x4 csk = *(LAS f32x4*)(L + L_CS + (64 + h * 16 + lg * 4) * 4);
    const f32x4 csv = splat4(*(LAS float*)(L + L_CS + (128 + h * 16 + lc) * 4));
    u32x2 Qb[4], Kb[NK], Vb[NK];
#pragma unroll
    for (int i = 0; i < NK; ++i) {
      const int T = TK + i;
      f32x4 ak, av;
      if (TYPE == 1 && i == 0) {
        ak = *(LAS f32x4*)(L + L_KV + h * 2048 + lane * 16);
        av = *(LAS f32x4*)(L + L_KV + h * 2048 + 1024 + lane * 16);
      } else {
        ak = acc[TYPE == 0 ? i : (i > 0 ? i - 1 : 0)][1];
        av = acc[TYPE == 0 ? i : (i > 0 ? i - 1 : 0)][2];
      }
      const float rc1 = *(LAS float*)(L + L_RSTD + (T * 16 + lc) * 4);
      const f32x4 mc = splat4(*(LAS float*)(L + L_MEAN + (T * 16 + lc) * 4)), rc = splat4(rc1);
      const f32x4 m4 = *(LAS f32x4*)(L + L_MEAN + (T * 16 + lg * 4) * 4), r4 = *(LAS f32x4*)(L + L_RSTD + (T * 16 + lg * 4) * 4);
      const f32x4 kk = (ak - mc * csk) * rc;
      const f32x4 vv = (av - m4 * csv) * r4;
      Kb[i] = u32x2{pk_bf16(kk[0], kk[1]), pk_bf16(kk[2], kk[3])};
      Vb[i] = u32x2{pk_bf16(vv[0], vv[1]), pk_bf16(vv[2], vv[3])};
      if (i > 0) {
        const f32x4 qq = (acc[TYPE == 0 ? i : i - 1][0] - mc * csq) * splat4(rc1 * (0.25f * 1.4426950408889634f));
        Qb[i - 1] = u32x2{pk_bf16(qq[0], qq[1]), pk_bf16(qq[2], qq[3])};
      }
    }
    SB();
    f32x4 mP, mC;
#pragma unroll
    for (int r = 0; r < 4; ++r) {
      mP[r] = (4 * lg + r >= lc + 2) ? 0.f : -1e30f;
      mC[r] = (4 * lg + r <= lc) ? 0.f : -1e30f;
    }
    const bool odd = lc & 1;
    const u32 sel0 = odd ? 0x01000c0cu : 0x0c0c0100u, sel1 = odd ? 0x03020c0cu : 0x0c0c0302u;
    u32x2 Ob[4];
#pragma unroll
    for (int q = 0; q < 4; ++q) {
      ldw2(7 + 2 * q, 0, 8); ldw2(8 + 2 * q, 0, 8);
      SB();
      f32x4 mp = mP;
      if (TYPE == 0 && q == 0 && g == 0) mp = f32x4{-1e30f, -1e30f, -1e30f, -1e30f};
      const f32x4 sp = mfma16k16(Kb[q], Qb[q], mp), sc = mfma16k16(Kb[q + 1], Qb[q], mC);
      const float a0 = odd ? sp[1] : sp[0], a1 = odd ? sp[3] : sp[2], a2 = odd ? sc[1] : sc[0], a3 = odd ? sc[3] : sc[2];
      const float mx = max_g(fmaxf(fmaxf(a0, a1), fmaxf(a2, a3)));
      const u32 u0 = pk_bf16(__builtin_amdgcn_exp2f(a0 - mx), __builtin_amdgcn_exp2f(a1 - mx));
      const u32 u1 = pk_bf16(__builtin_amdgcn_exp2f(a2 - mx), __builtin_amdgcn_exp2f(a3 - mx));
      const u32x4 pb = {__builtin_amdgcn_perm(0, u0, sel0), __builtin_amdgcn_perm(0, u0, sel1),
                        __builtin_amdgcn_perm(0, u1, sel0), __builtin_amdgcn_perm(0, u1, sel1)};
      const u32x4 vf = {Vb[q][0], Vb[q][1], Vb[q + 1][0], Vb[q + 1][1]};
      const f32x4 z = {0.f, 0.f, 0.f, 0.f};
      const f32x4 o = mfma16(vf, pb, z), l = mfma16(ones, pb, z);
      const f32x4 on = o * splat4(__builtin_amdgcn_rcpf(l[0]));
      Ob[q] = u32x2{pk_bf16(on[0], on[1]), pk_bf16(on[2], on[3])};
      SB();
    }
    const int rt = 4 * g + (TYPE == 0 ? 0 : 2), ks = 4 * j + h;
    __builtin_amdgcn_raw_buffer_store_b128(pair_rows(Ob[0], Ob[1]), rsA2, lane * 16, (rt * NKS + ks) * 1024, AUX_SC1);
    __builtin_amdgcn_raw_buffer_store_b128(pair_rows(Ob[2], Ob[3]), rsA2, lane * 16, ((rt + 1) * NKS + ks) * 1024, AUX_SC1);
    asm volatile("s_waitcnt vmcnt(0)\n\ts_barrier" ::: "memory");
  } else {
    const int m = w & 3;
    f32x4 mc[2], rc[2];
#pragma unroll
    for (int i = 0; i < 2; ++i) {
      mc[i] = splat4(*(LAS float*)(L + L_MEAN + ((2 * m + i) * 16 + lc) * 4));
      rc[i] = splat4(*(LAS float*)(L + L_RSTD + ((2 * m + i) * 16 + lc) * 4));
    }
    const int nct = j == 15 ? 9 : 11;
    const int rt = 4 * g + m, ksb = 32 + 11 * (j - 8);
    const f32x4 C1 = splat4(-2.f * 1.4426950408889634f * 0.7978845608028654f);
    const f32x4 C2 = splat4(-2.f * 1.4426950408889634f * 0.7978845608028654f * 0.044715f);
    const f32x4 ONE = splat4(1.f);
#pragma unroll
    for (int c = 0; c < NC; ++c) {
      if (c < 4) { ldw2(7 + 2 * c, 0, 8); ldw2(8 + 2 * c, 0, 8); }
      SB();
      const int ct = CB + (c + crot >= NC ? c + crot - NC : c + crot);
      const f32x4 cs = *(LAS f32x4*)(L + L_CS + (ct * 16 + lg * 4) * 4);
      const f32x4 bb = *(LAS f32x4*)(L + L_B1 + (ct * 16 + lg * 4) * 4);
      u32x2 hb[2];
#pragma unroll
      for (int i = 0; i < 2; ++i) {
        const f32x4 v = (acc[i][c] - mc[i] * cs) * rc[i] + bb;
        const f32x4 zz = v * (v * v * C2 + C1);
        f32x4 e;
#pragma unroll
        for (int r = 0; r < 4; ++r) e[r] = __builtin_amdgcn_exp2f(zz[r]);
        e += ONE;
#pragma unroll
        for (int r = 0; r < 4; ++r) e[r] = __builtin_amdgcn_rcpf(e[r]);
        const f32x4 y = v * e;
        hb[i] = u32x2{pk_bf16(y[0], y[1]), pk_bf16(y[2], y[3])};
      }
      __builtin_amdgcn_raw_buffer_store_b128(pair_rows(hb[0], hb[1]), rsA2, lane * 16,
                                             ct < nct ? (rt * NKS + ksb + ct) * 1024 : A2_BYTES, AUX_SC1);
      SB();
    }
    asm volatile("s_waitcnt vmcnt(0)\n\ts_barrier" ::: "memory");
  }
}

template <int TYPE>
DEV void body(LAS char* L, const char* __restrict__ x, const char* __restrict__ wq, const char* __restrict__ wo,
              const char* __restrict__ w1, const u16* __restrict__ b1, const char* __restrict__ w2, const char* __restrict__ b2,
              char* __restrict__ out, char* __restrict__ ws, u64 code0, int g, int j, int w, int lane STAMP_ARG) {
  const int mt = 2 * g + ((j >> 2) & 1), nt = (j & 3) + (j >> 3) * 4;
  const __amdgpu_buffer_rsrc_t rsA2 = __builtin_amdgcn_make_buffer_rsrc(ws + WS_A2, 0, A2_BYTES, 0x00020000);
  const int ks0 = w < 6 ? 15 * w : 90 + 14 * (w - 6);
  const u32 w2voff = (lane >> 5) * 8 * LDW2 + nt * 128 + (lane & 31) * 4;
  GAS u64* ctl = (GAS u64*)(ws + g * 4096);
  const u64 tag = __hip_atomic_load(ctl, __ATOMIC_RELAXED, __HIP_MEMORY_SCOPE_AGENT) + 1;
  P2W W;
  phase1<TYPE>(L, x, wq, w1, b1, wo, w2, ws, out, code0, g, j, w, lane, rsA2, w2voff, ks0, W STAMP_PASS);
  if (threadIdx.x == 0) __hip_atomic_store(ctl + 16 * (j + 1), tag, __ATOMIC_RELAXED, __HIP_MEMORY_SCOPE_AGENT);
  STAMP(5);
  const int orow = mt * 64 + (w >> 2) * 32 + (lane & 31), ocol = nt * 64 + (w & 3) * 16 + (lane >> 5) * 8;
  const u32 ooff = (u32)(orow * LDX + ocol * 2);
  const u32x4 xres = *(const GAS u32x4*)(x + ooff);
  const u32x4 bres = *(const GAS u32x4*)(b2 + (u32)(ocol * 2));
  u32x4 wf[15][2];
#pragma unroll
  for (int s = 0; s < 15; ++s)
#pragma unroll
    for (int d = 0; d < 4; ++d) {
      wf[s][0][d] = __builtin_amdgcn_perm(W.r[s * 8 + 2 * d + 1], W.r[s * 8 + 2 * d], 0x05040100u);
      wf[s][1][d] = __builtin_amdgcn_perm(W.r[s * 8 + 2 * d + 1], W.r[s * 8 + 2 * d], 0x07060302u);
    }
#pragma unroll
  for (int s = 0; s < 15; ++s) asm volatile("" : "+v"(wf[s][0]), "+v"(wf[s][1]));
  SB();
  STAMP(6);
  {
    const int ke = min(ks0 + 14, NKS - 1);
    const int plo = ks0 < 32 ? ks0 >> 2 : 8 + (ks0 - 32) / 11, phi = ke < 32 ? ke >> 2 : 8 + (ke - 32) / 11;
    const int pj = min(plo + (lane & 7), phi);
#pragma nounroll
    for (u32 spins = 0; spins < SPIN_CAP; ++spins) {
      const u64 v = __hip_atomic_load(ctl + 16 * (pj + 1), __ATOMIC_RELAXED, __HIP_MEMORY_SCOPE_AGENT);
      if (__all(v == tag)) break;
      __builtin_amdgcn_s_sleep(1);
    }
    __builtin_amdgcn_fence(__ATOMIC_ACQUIRE, "wavefront");
  }
  STAMP(7);
  const int nks = w < 6 ? 15 : 14;
  u32x4 af[2][15];
#pragma unroll
  for (int r = 0; r < 2; ++r)
#pragma unroll
    for (int s = 0; s < 15; ++s) {
      if (r == 1 && s >= 5) continue;
      const int so = (s < nks) ? ((2 * mt + r) * NKS + ks0 + s) * 1024 : A2_BYTES;
      af[r][s] = __builtin_amdgcn_raw_buffer_load_b128(rsA2, lane * 16, so, AUX_SC1);
    }
  SB();
#pragma unroll
  for (int r = 0; r < 2; ++r) {
    f32x16 a0, a1;
#pragma unroll
    for (int i = 0; i < 16; ++i) a0[i] = a1[i] = 0.f;
#pragma unroll
    for (int s = 0; s < 15; ++s) {
      a0 = mfma32(wf[s][0], af[r][s], a0);
      a1 = mfma32(wf[s][1], af[r][s], a1);
      if (r == 0 && s < 10) {
        const int s2 = s + 5;
        const int so = (s2 < nks) ? ((2 * mt + 1) * NKS + ks0 + s2) * 1024 : A2_BYTES;
        af[1][s2] = __builtin_amdgcn_raw_buffer_load_b128(rsA2, lane * 16, so, AUX_SC1);
      }
      SB();
    }
#pragma unroll
    for (int y = 0; y < 4; ++y) {
      *(LAS f32x4*)(L + ((w * 2 + r) * 8 + y * 2) * 1024 + lane * 16) = f32x4{a0[4 * y], a0[4 * y + 1], a0[4 * y + 2], a0[4 * y + 3]};
      *(LAS f32x4*)(L + ((w * 2 + r) * 8 + y * 2 + 1) * 1024 + lane * 16) = f32x4{a1[4 * y], a1[4 * y + 1], a1[4 * y + 2], a1[4 * y + 3]};
    }
    SB();
  }
  STAMP(8);
  lds_bar();
  if (j == 0 && threadIdx.x == 0) __hip_atomic_store(ctl, tag, __ATOMIC_RELAXED, __HIP_MEMORY_SCOPE_AGENT);
  f32x4 s0 = {0.f, 0.f, 0.f, 0.f}, s1 = s0;
#pragma unroll
  for (int sw = 0; sw < 8; ++sw) {
    s0 += *(LAS f32x4*)(L + ((sw * 2 + (w >> 2)) * 8 + (w & 3) * 2) * 1024 + lane * 16);
    s1 += *(LAS f32x4*)(L + ((sw * 2 + (w >> 2)) * 8 + (w & 3) * 2 + 1) * 1024 + lane * 16);
  }
  u32x4 o;
#pragma unroll
  for (int d = 0; d < 4; ++d)
    o[d] = pk_bf16(s0[d] + bf_lo(xres[d]) + bf_lo(bres[d]), s1[d] + bf_hi(xres[d]) + bf_hi(bres[d]));
  {
    const __amdgpu_buffer_rsrc_t rsO = __builtin_amdgcn_make_buffer_rsrc(out, 0, 2048 * 1024, 0x00020000);
    __builtin_amdgcn_raw_buffer_store_b128(o, rsO, ooff, 0, 2);
  }
  STAMP(9);
  STAMP_OUT();
}

__global__ __launch_bounds__(512, 2) void fused(const char* __restrict__ x, const char* __restrict__ wq, const char* __restrict__ wo,
                                                const char* __restrict__ w1, const u16* __restrict__ b1, const char* __restrict__ w2,
                                                const char* __restrict__ b2, char* __restrict__ out, char* __restrict__ ws) {
  extern __shared__ __attribute__((aligned(16))) char smem[];
  LAS char* L = (LAS char*)smem;
  STAMP(0);
  const int lane = threadIdx.x & 63, w = __builtin_amdgcn_readfirstlane(threadIdx.x >> 6);
  u64 code0;
  asm volatile("s_getpc_b64 %0" : "=s"(code0));
  code0 &= ~127ull;
  const int b = blockIdx.x, bi = b >> 3, g = (b & 3) * 4 + (bi & 3), j = ((bi >> 2) & 3) + ((b >> 2) & 1) * 4 + (bi >> 4) * 8;
  if (j < 8) {
    if (w < 4) body<0>(L, x, wq, wo, w1, b1, w2, b2, out, ws, code0, g, j, w, lane STAMP_PASS);
    else body<1>(L, x, wq, wo, w1, b1, w2, b2, out, ws, code0, g, j, w, lane STAMP_PASS);
  } else {
    if (w < 4) body<2>(L, x, wq, wo, w1, b1, w2, b2, out, ws, code0, g, j, w, lane STAMP_PASS);
    else body<3>(L, x, wq, wo, w1, b1, w2, b2, out, ws, code0, g, j, w, lane STAMP_PASS);
  }
}
}

extern "C" void kernel_launch(void* const* d_in, const int* in_sizes, int n_in, void* d_out, int out_size, void* d_ws,
                              size_t ws_size, hipStream_t stream) {
  (void)hipFuncSetAttribute((const void*)fk::fused, hipFuncAttributeMaxDynamicSharedMemorySize, fk::L_TOTAL);
  fk::fused<<<256, 512, fk::L_TOTAL, stream>>>((const char*)d_in[0], (const char*)d_in[1], (const char*)d_in[2], (const char*)d_in[4],
                                               (const u16*)d_in[5], (const char*)d_in[6], (const char*)d_in[7], (char*)d_out,
                                               (char*)d_ws);
}
```

```cpp
#define PF_CODE
#define PF_DATA
#define MAP2
#include <hip/hip_runtime.h>
#include <stdint.h>

typedef unsigned u32;
typedef unsigned long long u64;
typedef unsigned short u16;
typedef short s16x4 __attribute__((ext_vector_type(4)));
typedef short s16x8 __attribute__((ext_vector_type(8)));
typedef __bf16 b16x2 __attribute__((ext_vector_type(2)));
typedef float f32x2 __attribute__((ext_vector_type(2)));
typedef float f32x4 __attribute__((ext_vector_type(4)));
typedef float f32x16 __attribute__((ext_vector_type(16)));
typedef u32 u32x2 __attribute__((ext_vector_type(2)));
typedef u32 u32x4 __attribute__((ext_vector_type(4)));
#define LAS __attribute__((address_space(3)))
#define GAS __attribute__((address_space(1)))
#define DEV __device__ __forceinline__

namespace fk {
constexpr int LDX = 1024, LDQKV = 3072, LDW1 = 2752, LDW2 = 1024;
constexpr int NKS = 118;
constexpr int SLOT = 43008, BOFF = 18432;
constexpr int L_MEAN = 3 * SLOT, L_RSTD = L_MEAN + 576, L_CS = L_RSTD + 576, L_B1 = L_CS + 768, L_KV = L_B1 + 768,
              L_JUNK = L_KV + 8192, L_TOTAL = L_JUNK + 2048;
constexpr int WS_A2 = 65536, A2_BYTES = 64 * NKS * 1024;
constexpr int AUX_SC1 = 16;
constexpr u32 SPIN_CAP = 1u << 16;

#define STAMP(i) do {} while (0)
#define STAMP_ARG
#define STAMP_PASS
#define STAMP_OUT() do {} while (0)

DEV void dma16(u32 voff, const void* sbase, u32 lds) {
  asm volatile("s_mov_b32 m0, %2\n\ts_nop 0\n\tglobal_load_lds_dwordx4 %0, %1" ::"v"(voff), "s"(sbase), "s"(lds) : "memory");
}
DEV void dma16_masked(u32 voff, const void* sbase, u32 lds, u32 mlo) {
  asm volatile("s_mov_b32 m0, %2\n\ts_mov_b32 exec_lo, %3\n\tglobal_load_lds_dwordx4 %0, %1\n\ts_mov_b32 exec_lo, -1"
               ::"v"(voff), "s"(sbase), "s"(lds), "s"(mlo) : "memory");
}
DEV void dma16_if(u32 voff, const void* sbase, u32 lds, u32 cond) {
  asm volatile("s_cmp_gt_u32 %3, 5\n\ts_cbranch_scc1 1f\n\ts_mov_b32 m0, %2\n\ts_nop 0\n\tglobal_load_lds_dwordx4 %0, %1\n1:"
               ::"v"(voff), "s"(sbase), "s"(lds), "s"(cond) : "memory", "scc");
}
DEV void dma4(u32 voff, const void* sbase, u32 lds) {
  asm volatile("s_mov_b32 m0, %2\n\ts_nop 0\n\tglobal_load_lds_dword %0, %1" ::"v"(voff), "s"(sbase), "s"(lds) : "memory");
}
template <int N> DEV void wait_bar() {
  asm volatile("s_waitcnt vmcnt(%0) lgkmcnt(0)\n\ts_barrier" ::"i"(N) : "memory");
}
DEV void lds_bar() { asm volatile("s_waitcnt lgkmcnt(0)\n\ts_barrier" ::: "memory"); }
#define SB() __builtin_amdgcn_sched_barrier(0)

DEV u32 pk_bf16(float a, float b) {
  f32x2 p = {a, b};
  return __builtin_bit_cast(u32, __builtin_convertvector(p, b16x2));
}
DEV float bf_lo(u32 v) { return __builtin_bit_cast(float, v << 16); }
DEV float bf_hi(u32 v) { return __builtin_bit_cast(float, v & 0xffff0000u); }
DEV float dot2(u32 a, u32 b, float c) {
  return __builtin_amdgcn_fdot2_f32_bf16(__builtin_bit_cast(b16x2, a), __builtin_bit_cast(b16x2, b), c, false);
}
DEV f32x4 mfma16(u32x4 a, u32x4 b, f32x4 c) {
  return __builtin_amdgcn_mfma_f32_16x16x32_bf16(__builtin_bit_cast(s16x8, a), __builtin_bit_cast(s16x8, b), c, 0, 0, 0);
}
DEV f32x4 mfma16k16(u32x2 a, u32x2 b, f32x4 c) {
  return __builtin_amdgcn_mfma_f32_16x16x16bf16_1k(__builtin_bit_cast(s16x4, a), __builtin_bit_cast(s16x4, b), c, 0, 0, 0);
}
DEV f32x16 mfma32(u32x4 a, u32x4 b, f32x16 c) {
  return __builtin_amdgcn_mfma_f32_32x32x16_bf16(__builtin_bit_cast(s16x8, a), __builtin_bit_cast(s16x8, b), c, 0, 0, 0);
}
DEV u32x4 lds128(LAS char* L, u32 off) { return *(LAS u32x4*)(L + off); }
DEV u32x2 ldstr(LAS char* L, u32 off) {
  return __builtin_bit_cast(u32x2, __builtin_amdgcn_ds_read_tr16_b64_v4i16((LAS s16x4*)(L + off)));
}
template <int CTRL> DEV float dpp_add(float v) {
  int t = __builtin_amdgcn_update_dpp(0, __builtin_bit_cast(int, v), CTRL, 0xf, 0xf, true);
  return v + __builtin_bit_cast(float, t);
}
DEV float red8(float v) {
  v = dpp_add<0xB1>(v);
  v = dpp_add<0x4E>(v);
  v = dpp_add<0x141>(v);
  return v;
}
DEV float max_g(float v) {
  u32 u = __builtin_bit_cast(u32, v);
  auto r = __builtin_amdgcn_permlane32_swap(u, u, false, false);
  v = fmaxf(__builtin_bit_cast(float, r[0]), __builtin_bit_cast(float, r[1]));
  u = __builtin_bit_cast(u32, v);
  auto q = __builtin_amdgcn_permlane16_swap(u, u, false, false);
  return fmaxf(__builtin_bit_cast(float, q[0]), __builtin_bit_cast(float, q[1]));
}
DEV u32x4 pair_rows(u32x2 x, u32x2 y) {
  auto a = __builtin_amdgcn_permlane16_swap(x[0], y[0], false, false);
  auto b = __builtin_amdgcn_permlane16_swap(x[1], y[1], false, false);
  return u32x4{a[0], b[0], a[1], b[1]};
}

DEV f32x4 splat4(float v) {
  f32x2 p = {v, v};
  asm volatile("" : "+v"(p));
  return f32x4{p[0], p[1], p[0], p[1]};
}
template <int V> struct IC { static constexpr int v = V; };
template <int LO, int HI, class F> DEV void sfor(F&& f) {
  if constexpr (LO < HI) { f(IC<LO>{}); sfor<LO + 1, HI>(f); }
}
struct P2W { u32 r[120]; };

template <int TYPE>
DEV void phase1(LAS char* L, const char* x, const char* wq, const char* w1, const u16* b1, const char* wo, const char* w2,
                char* ws, const char* outp, u64 code0, int g, int j, int w, int lane, __amdgpu_buffer_rsrc_t rsA2, u32 w2voff, int ks0, P2W& W STAMP_ARG) {
  constexpr bool FFN = TYPE >= 2;
  constexpr int NT = TYPE == 0 ? 5 : (TYPE == 1 ? 4 : 2);
  constexpr int NC = TYPE == 2 ? 6 : (TYPE == 3 ? 5 : 3);
  constexpr int CB = TYPE == 3 ? 6 : 0;
  constexpr int NSP = TYPE == 1 ? 3 : 2;
  constexpr int ND = TYPE == 1 ? 6 : 5;
  constexpr int NM = NT * NC + (FFN ? 2 : 0);
  const int r8 = lane >> 3, pos = lane & 7, lg = lane >> 4, lc = lane & 15;
  const int rot = (j + g) & 7;
  constexpr int ldw = FFN ? LDW1 : LDQKV;
  const u32 voffA = r8 * LDX + ((pos ^ r8) << 4);
  const int swB = (r8 & 2) | ((w & 1) << 2);
  const u32 voffB = r8 * ldw + ((pos ^ swB) << 4);
  const int maxch = (j == 15) ? 1 : 5;
  const u32 voffB2 = FFN ? (u32)(r8 * ldw + (min(pos ^ swB, maxch) << 4)) : voffB;
  const int rowbase = FFN ? 128 * g : 128 * g - 16;
  const int p0row = (!FFN && g == 0 && w < 2) ? 8 * w : rowbase + 8 * w;
  const char* sA0 = x + (size_t)p0row * LDX;
  const char* sA1 = x + (size_t)(rowbase + 8 * (w + 8)) * LDX;
  const char* sA2 = x + (size_t)(rowbase + 8 * (16 + (w & 1))) * LDX;
  const char* sB0 = FFN ? w1 + (size_t)(8 * w) * LDW1 + (j - 8) * 352 : wq + (size_t)(8 * w) * LDQKV + j * 128;
  constexpr int SEGB = FFN ? 128 : 1024;
  const u32 mlo = (w == 0) ? 0xFFFFFF00u : 0xFFFFFFFFu;
  auto issue = [&](int i, int k) {
    const int kc = (i + rot) & 7;
    const u32 slot = (i % 3) * SLOT;
    const u32 va = voffA + kc * 128, vb = voffB + kc * 64 * ldw, vb2 = voffB2 + kc * 64 * ldw;
    if (k == 0) { if (TYPE == 0) dma16_masked(va, sA0, slot + w * 1024, mlo); else dma16(va, sA0, slot + w * 1024); }
    if (k == 1) dma16(vb, sB0, slot + BOFF + w * 1024);
    if (k == 2) dma16(vb, sB0 + SEGB, slot + BOFF + (8 + w) * 1024);
    if (k == 3) dma16(vb2, sB0 + 2 * SEGB, slot + BOFF + (16 + w) * 1024);
    if (k == 4) dma16(va, sA1, slot + (w + 8) * 1024);
    if (k == 5) dma16_if(va, sA2, slot + (16 + (w & 1)) * 1024, w);
  };
#pragma unroll
  for (int i = 0; i < 3; ++i)
#pragma unroll
    for (int k = 0; k < ND; ++k) issue(i, k);
  {
    const int bi = blockIdx.x >> 3;
    if (w == 7) {
      dma4(min(lane * 128 + (bi & 7) * 8192, 61000), (const void*)code0, L_JUNK);
    }
    if (w == 6) {
      const int ln = blockIdx.x * 59 + min(lane, 58);
      if (blockIdx.x * 59 + 58 < 4096) dma4(ln * 128, wo, L_JUNK + 256);
      else dma4((u32)max(ln - 4096, 0) * 128, w2, L_JUNK + 256);
    }
    if (w == 5) {
      dma4((lane & 3) * (2u << 20) + WS_A2 + (blockIdx.x & 63) * 4096, ws, L_JUNK + 512);
      dma4(blockIdx.x * 8192 + lane * 4, outp, L_JUNK + 768);
    }
  }
  u32 b1v = 0;
  if (FFN) b1v = b1[min((j - 8) * 176 + (int)threadIdx.x, 1375)];
  if (TYPE == 0 && w == 0 && lane < 8) {
    const u32x4 one = {0x3F803F80u, 0x3F803F80u, 0x3F803F80u, 0x3F803F80u};
#pragma unroll
    for (int s = 0; s < 3; ++s) *(LAS u32x4*)(L + s * SLOT + lane * 16) = one;
  }
  const int T0 = TYPE == 0 ? 0 : (TYPE == 1 ? 5 : 2 * (w & 3));
  const u32 aA = T0 * 2048 + (lc >> 3) * 1024 + (lc & 7) * 128 + ((lg ^ (lc & 7)) << 4);
  const int tq = lc >> 2, tp = lc & 3;
  const int swR = (tq & 2) | ((lg & 1) << 2);
  const u32 aBl = BOFF + lg * 1024 + tq * 128 + ((tp >> 1) << 4) + (tp & 1) * 8;
  const int crot = min(2 * (w & 3), NC - 2);
  u32 aB[NC];
#pragma unroll
  for (int c = 0; c < NC; ++c) {
    const int cc = c + crot >= NC ? c + crot - NC : c + crot;
    const int ct = FFN ? CB + cc : 4 * c + (w & 3);
    aB[c] = aBl + (ct >> 2) * 8192 + (((2 * (ct & 3)) ^ swR) << 4);
  }
  u32 aS[NSP];
  aS[0] = w * 1024 + lane * 16; aS[1] = (w + 8) * 1024 + lane * 16;
  if (NSP == 3) aS[NSP - 1] = (16 + (w & 1)) * 1024 + lane * 16;

  f32x4 acc[NT][NC], acc1[2];
#pragma unroll
  for (int t = 0; t < NT; ++t)
#pragma unroll
    for (int c = 0; c < NC; ++c) acc[t][c] = f32x4{0.f, 0.f, 0.f, 0.f};
  acc1[0] = acc1[1] = f32x4{0.f, 0.f, 0.f, 0.f};
  float ssum[NSP], ssq[NSP];
#pragma unroll
  for (int i = 0; i < NSP; ++i) ssum[i] = ssq[i] = 0.f;
  u32x4 fa[2][NT], sv[NSP];
  u32x2 fb[2][NC][2];
  auto rd = [&](int t, int k) {
    const u32 slot = ((t >> 1) % 3) * SLOT;
    const int b = t & 1;
    if (k < 2 * NC) fb[b][k >> 1][k & 1] = ldstr(L, aB[k >> 1] + slot + b * 4096 + (k & 1) * 512);
    else if (k < 2 * NC + NT) fa[b][k - 2 * NC] = lds128(L, ((aA ^ (b * 64)) + slot + (k - 2 * NC) * 2048));
    else sv[k - 2 * NC - NT] = lds128(L, aS[k - 2 * NC - NT] + slot);
  };
  auto ldw2 = [&](int s, int lo, int hi) {
    const int ks = ks0 + s;
    const bool isO = ks < 32;
    const char* base = isO ? wo + (size_t)ks * 16 * LDW2 : w2 + (size_t)(ks - 32) * 16 * LDW2;
    const __amdgpu_buffer_rsrc_t rs = __builtin_amdgcn_make_buffer_rsrc((void*)base, 0, ks < NKS ? 16 * LDW2 : 0, 0x00020000);
#pragma unroll
    for (int jj = lo; jj < hi; ++jj)
      W.r[s * 8 + jj] = __builtin_amdgcn_raw_buffer_load_b32(rs, w2voff + (jj & 3) * 1024, (jj >> 2) * 4096, 0);
  };
  const u32x4 ones = {0x3F803F80u, 0x3F803F80u, 0x3F803F80u, 0x3F803F80u};
  STAMP(1);
  wait_bar<10>();
  STAMP(2);
#pragma unroll
  for (int k = 0; k < 2 * NC + NT + NSP; ++k) rd(0, k);
  SB();
  sfor<0, 16>([&](auto tc) {
    constexpr int t = decltype(tc)::v, s = t >> 1, b = t & 1;
    if constexpr (b) { if constexpr (s <= 4) wait_bar<5>(); else if constexpr (s == 5) wait_bar<21>(); else if constexpr (s == 6) wait_bar<32>(); }
    constexpr int nr = t == 15 ? 0 : 2 * NC + NT + (b ? NSP : 0);
    constexpr int g0 = (nr + 1) / 2;
    constexpr int nd = (b && s + 3 < 8) ? ND : 0, nw = t >= 9 ? 8 : 0;
    constexpr int per = (nd + nw + NM - g0 - 1) / (NM - g0);
    constexpr int ndot = b ? 0 : 4 * NSP, dper = (ndot + NM - 1) / NM;
    sfor<0, NM>([&](auto qc) {
      constexpr int q = decltype(qc)::v;
      if constexpr (q < NT * NC) {
        constexpr int c = q / NT, i = q % NT;
        const u32x4 wfr = {fb[b][c][0][0], fb[b][c][0][1], fb[b][c][1][0], fb[b][c][1][1]};
        if constexpr (!FFN && c == 2) acc[i][c] = mfma16(fa[b][i], wfr, acc[i][c]);
        else acc[i][c] = mfma16(wfr, fa[b][i], acc[i][c]);
      } else {
        constexpr int c = q - NT * NC;
        const u32x4 wfr = {fb[b][c][0][0], fb[b][c][0][1], fb[b][c][1][0], fb[b][c][1][1]};
        acc1[c] = mfma16(wfr, ones, acc1[c]);
      }
      if constexpr (2 * q < nr) rd(t + 1, 2 * q);
      if constexpr (2 * q + 1 < nr) rd(t + 1, 2 * q + 1);
      if constexpr (q >= g0) {
        sfor<(q - g0) * per, (q - g0 + 1) * per>([&](auto uc) {
          constexpr int u = decltype(uc)::v;
          if constexpr (u < nd) issue(s + 3, u);
          else if constexpr (u < nd + nw) ldw2(t - 9, u - nd, u - nd + 1);
        });
      }
      sfor<q * dper, (q + 1) * dper>([&](auto uc) {
        constexpr int u = decltype(uc)::v;
        if constexpr (u < ndot) {
          constexpr int i = u >> 2, d = u & 3;
          ssum[i] = dot2(sv[i][d], 0x3F803F80u, ssum[i]);
          ssq[i] = dot2(sv[i][d], sv[i][d], ssq[i]);
        }
      });
      SB();
    });
  });
  if (FFN) asm volatile("" : "+v"(acc1[0]), "+v"(acc1[1]));
#pragma unroll
  for (int i = 0; i < NSP; ++i) asm volatile("" : "+v"(ssum[i]), "+v"(ssq[i]));
  STAMP(3);
#pragma unroll
  for (int i = 0; i < NSP; ++i) {
    const float sm = red8(ssum[i]) * (1.f / 512.f), sq = red8(ssq[i]) * (1.f / 512.f);
    const float var = fmaxf(sq - sm * sm, 0.f);
    const float rs = __builtin_amdgcn_rsqf(var + 1e-5f);
    const int p = i == 0 ? w : (i == 1 ? w + 8 : 16 + (w & 1));
    if (pos == 0) { *(LAS float*)(L + L_MEAN + (p * 8 + r8) * 4) = sm; *(LAS float*)(L + L_RSTD + (p * 8 + r8) * 4) = rs; }
  }
  if (TYPE == 0) {
    const int h = w & 3;
    if (lc == 0) {
      *(LAS f32x4*)(L + L_CS + (h * 16 + lg * 4) * 4) = acc[0][0];
      *(LAS f32x4*)(L + L_CS + (64 + h * 16 + lg * 4) * 4) = acc[0][1];
    }
    if (lg == 0) *(LAS float*)(L + L_CS + (128 + h * 16 + lc) * 4) = acc[0][2][0];
    *(LAS f32x4*)(L + L_KV + h * 2048 + lane * 16) = acc[NT - 1][1];
    *(LAS f32x4*)(L + L_KV + h * 2048 + 1024 + lane * 16) = acc[NT - 1][2];
  }
  if (FFN) {
    if (lc == 0) {
      *(LAS f32x4*)(L + L_CS + ((CB + crot) * 16 + lg * 4) * 4) = acc1[0];
      *(LAS f32x4*)(L + L_CS + ((CB + crot + 1) * 16 + lg * 4) * 4) = acc1[1];
    }
    if (threadIdx.x < 192) *(LAS float*)(L + L_B1 + threadIdx.x * 4) = bf_lo(b1v);
  }
  lds_bar();
  STAMP(4);
  if (!FFN) {
    const int h = w & 3;
    constexpr int NK = 5;
    const int TK = TYPE == 0 ? 0 : 4;
    const f32x4 csq = *(LAS f32x4*)(L + L_CS + (h * 16 + lg * 4) * 4);
    const f32x4 csk = *(LAS f32x4*)(L + L_CS + (64 + h * 16 + lg * 4) * 4);
    const f32x4 csv = splat4(*(LAS float*)(L + L_CS + (128 + h * 16 + lc) * 4));
    u32x2 Qb[4], Kb[NK], Vb[NK];
#pragma unroll
    for (int i = 0; i < NK; ++i) {
      const int T = TK + i;
      f32x4 ak, av;
      if (TYPE == 1 && i == 0) {
        ak = *(LAS f32x4*)(L + L_KV + h * 2048 + lane * 16);
        av = *(LAS f32x4*)(L + L_KV + h * 2048 + 1024 + lane * 16);
      } else {
        ak = acc[TYPE == 0 ? i : (i > 0 ? i - 1 : 0)][1];
        av = acc[TYPE == 0 ? i : (i > 0 ? i - 1 : 0)][2];
      }
      const float rc1 = *(LAS float*)(L + L_RSTD + (T * 16 + lc) * 4);
      const f32x4 mc = splat4(*(LAS float*)(L + L_MEAN + (T * 16 + lc) * 4)), rc = splat4(rc1);
      const f32x4 m4 = *(LAS f32x4*)(L + L_MEAN + (T * 16 + lg * 4) * 4), r4 = *(LAS f32x4*)(L + L_RSTD + (T * 16 + lg * 4) * 4);
      const f32x4 kk = (ak - mc * csk) * rc;
      const f32x4 vv = (av - m4 * csv) * r4;
      Kb[i] = u32x2{pk_bf16(kk[0], kk[1]), pk_bf16(kk[2], kk[3])};
      Vb[i] = u32x2{pk_bf16(vv[0], vv[1]), pk_bf16(vv[2], vv[3])};
      if (i > 0) {
        const f32x4 qq = (acc[TYPE == 0 ? i : i - 1][0] - mc * csq) * splat4(rc1 * (0.25f * 1.4426950408889634f));
        Qb[i - 1] = u32x2{pk_bf16(qq[0], qq[1]), pk_bf16(qq[2], qq[3])};
      }
    }
    SB();
    f32x4 mP, mC;
#pragma unroll
    for (int r = 0; r < 4; ++r) {
      mP[r] = (4 * lg + r >= lc + 2) ? 0.f : -1e30f;
      mC[r] = (4 * lg + r <= lc) ? 0.f : -1e30f;
    }
    const bool odd = lc & 1;
    const u32 sel0 = odd ? 0x01000c0cu : 0x0c0c0100u, sel1 = odd ? 0x03020c0cu : 0x0c0c0302u;
    u32x2 Ob[4];
#pragma unroll
    for (int q = 0; q < 4; ++q) {
      ldw2(7 + 2 * q, 0, 8); ldw2(8 + 2 * q, 0, 8);
      SB();
      f32x4 mp = mP;
      if (TYPE == 0 && q == 0 && g == 0) mp = f32x4{-1e30f, -1e30f, -1e30f, -1e30f};
      const f32x4 sp = mfma16k16(Kb[q], Qb[q], mp), sc = mfma16k16(Kb[q + 1], Qb[q], mC);
      const float a0 = odd ? sp[1] : sp[0], a1 = odd ? sp[3] : sp[2], a2 = odd ? sc[1] : sc[0], a3 = odd ? sc[3] : sc[2];
      const float mx = max_g(fmaxf(fmaxf(a0, a1), fmaxf(a2, a3)));
      const u32 u0 = pk_bf16(__builtin_amdgcn_exp2f(a0 - mx), __builtin_amdgcn_exp2f(a1 - mx));
      const u32 u1 = pk_bf16(__builtin_amdgcn_exp2f(a2 - mx), __builtin_amdgcn_exp2f(a3 - mx));
      const u32x4 pb = {__builtin_amdgcn_perm(0, u0, sel0), __builtin_amdgcn_perm(0, u0, sel1),
                        __builtin_amdgcn_perm(0, u1, sel0), __builtin_amdgcn_perm(0, u1, sel1)};
      const u32x4 vf = {Vb[q][0], Vb[q][1], Vb[q + 1][0], Vb[q + 1][1]};
      const f32x4 z = {0.f, 0.f, 0.f, 0.f};
      const f32x4 o = mfma16(vf, pb, z), l = mfma16(ones, pb, z);
      const f32x4 on = o * splat4(__builtin_amdgcn_rcpf(l[0]));
      Ob[q] = u32x2{pk_bf16(on[0], on[1]), pk_bf16(on[2], on[3])};
      SB();
    }
    const int rt = 4 * g + (TYPE == 0 ? 0 : 2), ks = 4 * j + h;
    __builtin_amdgcn_raw_buffer_store_b128(pair_rows(Ob[0], Ob[1]), rsA2, lane * 16, (rt * NKS + ks) * 1024, AUX_SC1);
    __builtin_amdgcn_raw_buffer_store_b128(pair_rows(Ob[2], Ob[3]), rsA2, lane * 16, ((rt + 1) * NKS + ks) * 1024, AUX_SC1);
    asm volatile("s_waitcnt vmcnt(0)\n\ts_barrier" ::: "memory");
  } else {
    const int m = w & 3;
    f32x4 mc[2], rc[2];
#pragma unroll
    for (int i = 0; i < 2; ++i) {
      mc[i] = splat4(*(LAS float*)(L + L_MEAN + ((2 * m + i) * 16 + lc) * 4));
      rc[i] = splat4(*(LAS float*)(L + L_RSTD + ((2 * m + i) * 16 + lc) * 4));
    }
    const int nct = j == 15 ? 9 : 11;
    const int rt = 4 * g + m, ksb = 32 + 11 * (j - 8);
    const f32x4 C1 = splat4(-2.f * 1.4426950408889634f * 0.7978845608028654f);
    const f32x4 C2 = splat4(-2.f * 1.4426950408889634f * 0.7978845608028654f * 0.044715f);
    const f32x4 ONE = splat4(1.f);
#pragma unroll
    for (int c = 0; c < NC; ++c) {
      if (c < 4) { ldw2(7 + 2 * c, 0, 8); ldw2(8 + 2 * c, 0, 8); }
      SB();
      const int ct = CB + (c + crot >= NC ? c + crot - NC : c + crot);
      const f32x4 cs = *(LAS f32x4*)(L + L_CS + (ct * 16 + lg * 4) * 4);
      const f32x4 bb = *(LAS f32x4*)(L + L_B1 + (ct * 16 + lg * 4) * 4);
      u32x2 hb[2];
#pragma unroll
      for (int i = 0; i < 2; ++i) {
        const f32x4 v = (acc[i][c] - mc[i] * cs) * rc[i] + bb;
        const f32x4 zz = v * (v * v * C2 + C1);
        f32x4 e;
#pragma unroll
        for (int r = 0; r < 4; ++r) e[r] = __builtin_amdgcn_exp2f(zz[r]);
        e += ONE;
#pragma unroll
        for (int r = 0; r < 4; ++r) e[r] = __builtin_amdgcn_rcpf(e[r]);
        const f32x4 y = v * e;
        hb[i] = u32x2{pk_bf16(y[0], y[1]), pk_bf16(y[2], y[3])};
      }
      __builtin_amdgcn_raw_buffer_store_b128(pair_rows(hb[0], hb[1]), rsA2, lane * 16,
                                             ct < nct ? (rt * NKS + ksb + ct) * 1024 : A2_BYTES, AUX_SC1);
      SB();
    }
    asm volatile("s_waitcnt vmcnt(0)\n\ts_barrier" ::: "memory");
  }
}

template <int TYPE>
DEV void body(LAS char* L, const char* __restrict__ x, const char* __restrict__ wq, const char* __restrict__ wo,
              const char* __restrict__ w1, const u16* __restrict__ b1, const char* __restrict__ w2, const char* __restrict__ b2,
              char* __restrict__ out, char* __restrict__ ws, u64 code0, int g, int j, int w, int lane STAMP_ARG) {
  const int mt = 2 * g + ((j >> 2) & 1), nt = (j & 3) + (j >> 3) * 4;
  const __amdgpu_buffer_rsrc_t rsA2 = __builtin_amdgcn_make_buffer_rsrc(ws + WS_A2, 0, A2_BYTES, 0x00020000);
  const int ks0 = w < 6 ? 15 * w : 90 + 14 * (w - 6);
  const u32 w2voff = (lane >> 5) * 8 * LDW2 + nt * 128 + (lane & 31) * 4;
  GAS u64* ctl = (GAS u64*)(ws + g * 4096);
  const u64 tag = __hip_atomic_load(ctl, __ATOMIC_RELAXED, __HIP_MEMORY_SCOPE_AGENT) + 1;
  P2W W;
  phase1<TYPE>(L, x, wq, w1, b1, wo, w2, ws, out, code0, g, j, w, lane, rsA2, w2voff, ks0, W STAMP_PASS);
  if (threadIdx.x == 0) __hip_atomic_store(ctl + 16 * (j + 1), tag, __ATOMIC_RELAXED, __HIP_MEMORY_SCOPE_AGENT);
  STAMP(5);
  const int orow = mt * 64 + (w >> 2) * 32 + (lane & 31), ocol = nt * 64 + (w & 3) * 16 + (lane >> 5) * 8;
  const u32 ooff = (u32)(orow * LDX + ocol * 2);
  const u32x4 xres = *(const GAS u32x4*)(x + ooff);
  const u32x4 bres = *(const GAS u32x4*)(b2 + (u32)(ocol * 2));
  u32x4 wf[15][2];
#pragma unroll
  for (int s = 0; s < 15; ++s)
#pragma unroll
    for (int d = 0; d < 4; ++d) {
      wf[s][0][d] = __builtin_amdgcn_perm(W.r[s * 8 + 2 * d + 1], W.r[s * 8 + 2 * d], 0x05040100u);
      wf[s][1][d] = __builtin_amdgcn_perm(W.r[s * 8 + 2 * d + 1], W.r[s * 8 + 2 * d], 0x07060302u);
    }
#pragma unroll
  for (int s = 0; s < 15; ++s) asm volatile("" : "+v"(wf[s][0]), "+v"(wf[s][1]));
  SB();
  STAMP(6);
  {
    const int ke = min(ks0 + 14, NKS - 1);
    const int plo = ks0 < 32 ? ks0 >> 2 : 8 + (ks0 - 32) / 11, phi = ke < 32 ? ke >> 2 : 8 + (ke - 32) / 11;
    const int pj = min(plo + (lane & 7), phi);
#pragma nounroll
    for (u32 spins = 0; spins < SPIN_CAP; ++spins) {
      const u64 v = __hip_atomic_load(ctl + 16 * (pj + 1), __ATOMIC_RELAXED, __HIP_MEMORY_SCOPE_AGENT);
      if (__all(v == tag)) break;
      __builtin_amdgcn_s_sleep(1);
    }
    __builtin_amdgcn_fence(__ATOMIC_ACQUIRE, "wavefront");
  }
  STAMP(7);
  const int nks = w < 6 ? 15 : 14;
  u32x4 af[2][15];
#pragma unroll
  for (int r = 0; r < 2; ++r)
#pragma unroll
    for (int s = 0; s < 15; ++s) {
      if (r == 1 && s >= 5) continue;
      const int so = (s < nks) ? ((2 * mt + r) * NKS + ks0 + s) * 1024 : A2_BYTES;
      af[r][s] = __builtin_amdgcn_raw_buffer_load_b128(rsA2, lane * 16, so, AUX_SC1);
    }
  SB();
#pragma unroll
  for (int r = 0; r < 2; ++r) {
    f32x16 a0, a1;
#pragma unroll
    for (int i = 0; i < 16; ++i) a0[i] = a1[i] = 0.f;
#pragma unroll
    for (int s = 0; s < 15; ++s) {
      a0 = mfma32(wf[s][0], af[r][s], a0);
      a1 = mfma32(wf[s][1], af[r][s], a1);
      if (r == 0 && s < 10) {
        const int s2 = s + 5;
        const int so = (s2 < nks) ? ((2 * mt + 1) * NKS + ks0 + s2) * 1024 : A2_BYTES;
        af[1][s2] = __builtin_amdgcn_raw_buffer_load_b128(rsA2, lane * 16, so, AUX_SC1);
      }
      SB();
    }
#pragma unroll
    for (int y = 0; y < 4; ++y) {
      *(LAS f32x4*)(L + ((w * 2 + r) * 8 + y * 2) * 1024 + lane * 16) = f32x4{a0[4 * y], a0[4 * y + 1], a0[4 * y + 2], a0[4 * y + 3]};
      *(LAS f32x4*)(L + ((w * 2 + r) * 8 + y * 2 + 1) * 1024 + lane * 16) = f32x4{a1[4 * y], a1[4 * y + 1], a1[4 * y + 2], a1[4 * y + 3]};
    }
    SB();
  }
  STAMP(8);
  lds_bar();
  if (j == 0 && threadIdx.x == 0) __hip_atomic_store(ctl, tag, __ATOMIC_RELAXED, __HIP_MEMORY_SCOPE_AGENT);
  f32x4 s0 = {0.f, 0.f, 0.f, 0.f}, s1 = s0;
#pragma unroll
  for (int sw = 0; sw < 8; ++sw) {
    s0 += *(LAS f32x4*)(L + ((sw * 2 + (w >> 2)) * 8 + (w & 3) * 2) * 1024 + lane * 16);
    s1 += *(LAS f32x4*)(L + ((sw * 2 + (w >> 2)) * 8 + (w & 3) * 2 + 1) * 1024 + lane * 16);
  }
  u32x4 o;
#pragma unroll
  for (int d = 0; d < 4; ++d)
    o[d] = pk_bf16(s0[d] + bf_lo(xres[d]) + bf_lo(bres[d]), s1[d] + bf_hi(xres[d]) + bf_hi(bres[d]));
  *(GAS u32x4*)(out + ooff) = o;
  STAMP(9);
  STAMP_OUT();
}

__global__ __launch_bounds__(512, 2) void fused(const char* __restrict__ x, const char* __restrict__ wq, const char* __restrict__ wo,
                                                const char* __restrict__ w1, const u16* __restrict__ b1, const char* __restrict__ w2,
                                                const char* __restrict__ b2, char* __restrict__ out, char* __restrict__ ws) {
  extern __shared__ __attribute__((aligned(16))) char smem[];
  LAS char* L = (LAS char*)smem;
  STAMP(0);
  const int lane = threadIdx.x & 63, w = __builtin_amdgcn_readfirstlane(threadIdx.x >> 6);
  u64 code0;
  asm volatile("s_getpc_b64 %0" : "=s"(code0));
  code0 &= ~127ull;
  const int b = blockIdx.x, bi = b >> 3, g = (b & 3) * 4 + (bi & 3), j = ((bi >> 2) & 3) + ((b >> 2) & 1) * 4 + (bi >> 4) * 8;
  if (j < 8) {
    if (w < 4) body<0>(L, x, wq, wo, w1, b1, w2, b2, out, ws, code0, g, j, w, lane STAMP_PASS);
    else body<1>(L, x, wq, wo, w1, b1, w2, b2, out, ws, code0, g, j, w, lane STAMP_PASS);
  } else {
    if (w < 4) body<2>(L, x, wq, wo, w1, b1, w2, b2, out, ws, code0, g, j, w, lane STAMP_PASS);
    else body<3>(L, x, wq, wo, w1, b1, w2, b2, out, ws, code0, g, j, w, lane STAMP_PASS);
  }
}
}

template <int BIG> __global__ __launch_bounds__(512, 2) void spin_rt(int ticks) {
  unsigned long long t0 = __builtin_amdgcn_s_memrealtime();
  if (BIG) asm volatile("v_mov_b32 v250, 0" ::: "v250");
  while (__builtin_amdgcn_s_memrealtime() - t0 < (unsigned long long)ticks) __builtin_amdgcn_s_sleep(2);
}
extern "C" void kernel_launch(void* const* d_in, const int* in_sizes, int n_in, void* d_out, int out_size, void* d_ws,
                              size_t ws_size, hipStream_t stream) {
  (void)hipFuncSetAttribute((const void*)fk::fused, hipFuncAttributeMaxDynamicSharedMemorySize, fk::L_TOTAL);
  fk::fused<<<256, 512, fk::L_TOTAL, stream>>>((const char*)d_in[0], (const char*)d_in[1], (const char*)d_in[2], (const char*)d_in[4],
                                               (const u16*)d_in[5], (const char*)d_in[6], (const char*)d_in[7], (char*)d_out,
                                               (char*)d_ws);
  (void)hipFuncSetAttribute((const void*)spin_rt<1>, hipFuncAttributeMaxDynamicSharedMemorySize, 160*1024);
  spin_rt<1><<<256, 512, 142000, stream>>>(2000);
}
```

```cpp
#define STAMPS
#define NSTAMP 12
#define PF_CODE
#define PF_DATA
#define MAP2
#include <hip/hip_runtime.h>
#include <stdint.h>

typedef unsigned u32;
typedef unsigned long long u64;
typedef unsigned short u16;
typedef short s16x4 __attribute__((ext_vector_type(4)));
typedef short s16x8 __attribute__((ext_vector_type(8)));
typedef __bf16 b16x2 __attribute__((ext_vector_type(2)));
typedef float f32x2 __attribute__((ext_vector_type(2)));
typedef float f32x4 __attribute__((ext_vector_type(4)));
typedef float f32x16 __attribute__((ext_vector_type(16)));
typedef u32 u32x2 __attribute__((ext_vector_type(2)));
typedef u32 u32x4 __attribute__((ext_vector_type(4)));
#define LAS __attribute__((address_space(3)))
#define GAS __attribute__((address_space(1)))
#define DEV __device__ __forceinline__

namespace fk {
constexpr int LDX = 1024, LDQKV = 3072, LDW1 = 2752, LDW2 = 1024;
constexpr int NKS = 118;
constexpr int SLOT = 43008, BOFF = 18432;
constexpr int L_MEAN = 3 * SLOT, L_RSTD = L_MEAN + 576, L_CS = L_RSTD + 576, L_B1 = L_CS + 768, L_KV = L_B1 + 768,
              L_JUNK = L_KV + 8192, L_TOTAL = L_JUNK + 2048;
constexpr int WS_A2 = 65536, A2_BYTES = 64 * NKS * 1024;
constexpr int AUX_SC1 = 16;
constexpr u32 SPIN_CAP = 1u << 16;

struct Stamps { u32 t[12]; };
#define STAMP(i) do { u64 t_; asm volatile("s_memrealtime %0\n\ts_waitcnt lgkmcnt(0)" : "=s"(t_) :: "memory"); ST.t[i] = (u32)t_; } while (0)
#define STAMP_ARG , Stamps& ST
#define STAMP_PASS , ST
#define STAMP_OUT() do { if ((threadIdx.x & 63) == 0) { _Pragma("unroll") for (int i_ = 0; i_ < NSTAMP; ++i_) \
  ((u64*)(ws + (64u << 20)))[(blockIdx.x * 8 + (threadIdx.x >> 6)) * 16 + i_] = ST.t[i_]; } } while (0)

#undef STAMP_OUT
#define STAMP_OUT() do { if ((threadIdx.x & 63) == 0) *(volatile u32*)(ws + (100u << 20) + (blockIdx.x * 8 + (threadIdx.x >> 6)) * 4) = ST.t[9]; } while (0)
DEV void dma16(u32 voff, const void* sbase, u32 lds) {
  asm volatile("s_mov_b32 m0, %2\n\ts_nop 0\n\tglobal_load_lds_dwordx4 %0, %1" ::"v"(voff), "s"(sbase), "s"(lds) : "memory");
}
DEV void dma16_masked(u32 voff, const void* sbase, u32 lds, u32 mlo) {
  asm volatile("s_mov_b32 m0, %2\n\ts_mov_b32 exec_lo, %3\n\tglobal_load_lds_dwordx4 %0, %1\n\ts_mov_b32 exec_lo, -1"
               ::"v"(voff), "s"(sbase), "s"(lds), "s"(mlo) : "memory");
}
DEV void dma16_if(u32 voff, const void* sbase, u32 lds, u32 cond) {
  asm volatile("s_cmp_gt_u32 %3, 5\n\ts_cbranch_scc1 1f\n\ts_mov_b32 m0, %2\n\ts_nop 0\n\tglobal_load_lds_dwordx4 %0, %1\n1:"
               ::"v"(voff), "s"(sbase), "s"(lds), "s"(cond) : "memory", "scc");
}
DEV void dma4(u32 voff, const void* sbase, u32 lds) {
  asm volatile("s_mov_b32 m0, %2\n\ts_nop 0\n\tglobal_load_lds_dword %0, %1" ::"v"(voff), "s"(sbase), "s"(lds) : "memory");
}
template <int N> DEV void wait_bar() {
  asm volatile("s_waitcnt vmcnt(%0) lgkmcnt(0)\n\ts_barrier" ::"i"(N) : "memory");
}
DEV void lds_bar() { asm volatile("s_waitcnt lgkmcnt(0)\n\ts_barrier" ::: "memory"); }
#define SB() __builtin_amdgcn_sched_barrier(0)

DEV u32 pk_bf16(float a, float b) {
  f32x2 p = {a, b};
  return __builtin_bit_cast(u32, __builtin_convertvector(p, b16x2));
}
DEV float bf_lo(u32 v) { return __builtin_bit_cast(float, v << 16); }
DEV float bf_hi(u32 v) { return __builtin_bit_cast(float, v & 0xffff0000u); }
DEV float dot2(u32 a, u32 b, float c) {
  return __builtin_amdgcn_fdot2_f32_bf16(__builtin_bit_cast(b16x2, a), __builtin_bit_cast(b16x2, b), c, false);
}
DEV f32x4 mfma16(u32x4 a, u32x4 b, f32x4 c) {
  return __builtin_amdgcn_mfma_f32_16x16x32_bf16(__builtin_bit_cast(s16x8, a), __builtin_bit_cast(s16x8, b), c, 0, 0, 0);
}
DEV f32x4 mfma16k16(u32x2 a, u32x2 b, f32x4 c) {
  return __builtin_amdgcn_mfma_f32_16x16x16bf16_1k(__builtin_bit_cast(s16x4, a), __builtin_bit_cast(s16x4, b), c, 0, 0, 0);
}
DEV f32x16 mfma32(u32x4 a, u32x4 b, f32x16 c) {
  return __builtin_amdgcn_mfma_f32_32x32x16_bf16(__builtin_bit_cast(s16x8, a), __builtin_bit_cast(s16x8, b), c, 0, 0, 0);
}
DEV u32x4 lds128(LAS char* L, u32 off) { return *(LAS u32x4*)(L + off); }
DEV u32x2 ldstr(LAS char* L, u32 off) {
  return __builtin_bit_cast(u32x2, __builtin_amdgcn_ds_read_tr16_b64_v4i16((LAS s16x4*)(L + off)));
}
template <int CTRL> DEV float dpp_add(float v) {
  int t = __builtin_amdgcn_update_dpp(0, __builtin_bit_cast(int, v), CTRL, 0xf, 0xf, true);
  return v + __builtin_bit_cast(float, t);
}
DEV float red8(float v) {
  v = dpp_add<0xB1>(v);
  v = dpp_add<0x4E>(v);
  v = dpp_add<0x141>(v);
  return v;
}
DEV float max_g(float v) {
  u32 u = __builtin_bit_cast(u32, v);
  auto r = __builtin_amdgcn_permlane32_swap(u, u, false, false);
  v = fmaxf(__builtin_bit_cast(float, r[0]), __builtin_bit_cast(float, r[1]));
  u = __builtin_bit_cast(u32, v);
  auto q = __builtin_amdgcn_permlane16_swap(u, u, false, false);
  return fmaxf(__builtin_bit_cast(float, q[0]), __builtin_bit_cast(float, q[1]));
}
DEV u32x4 pair_rows(u32x2 x, u32x2 y) {
  auto a = __builtin_amdgcn_permlane16_swap(x[0], y[0], false, false);
  auto b = __builtin_amdgcn_permlane16_swap(x[1], y[1], false, false);
  return u32x4{a[0], b[0], a[1], b[1]};
}

DEV f32x4 splat4(float v) {
  f32x2 p = {v, v};
  asm volatile("" : "+v"(p));
  return f32x4{p[0], p[1], p[0], p[1]};
}
template <int V> struct IC { static constexpr int v = V; };
template <int LO, int HI, class F> DEV void sfor(F&& f) {
  if constexpr (LO < HI) { f(IC<LO>{}); sfor<LO + 1, HI>(f); }
}
struct P2W { u32 r[120]; };

template <int TYPE>
DEV void phase1(LAS char* L, const char* x, const char* wq, const char* w1, const u16* b1, const char* wo, const char* w2,
                char* ws, const char* outp, u64 code0, int g, int j, int w, int lane, __amdgpu_buffer_rsrc_t rsA2, u32 w2voff, int ks0, P2W& W STAMP_ARG) {
  constexpr bool FFN = TYPE >= 2;
  constexpr int NT = TYPE == 0 ? 5 : (TYPE == 1 ? 4 : 2);
  constexpr int NC = TYPE == 2 ? 6 : (TYPE == 3 ? 5 : 3);
  constexpr int CB = TYPE == 3 ? 6 : 0;
  constexpr int NSP = TYPE == 1 ? 3 : 2;
  constexpr int ND = TYPE == 1 ? 6 : 5;
  constexpr int NM = NT * NC + (FFN ? 2 : 0);
  const int r8 = lane >> 3, pos = lane & 7, lg = lane >> 4, lc = lane & 15;
  const int rot = (j + g) & 7;
  constexpr int ldw = FFN ? LDW1 : LDQKV;
  const u32 voffA = r8 * LDX + ((pos ^ r8) << 4);
  const int swB = (r8 & 2) | ((w & 1) << 2);
  const u32 voffB = r8 * ldw + ((pos ^ swB) << 4);
  const int maxch = (j == 15) ? 1 : 5;
  const u32 voffB2 = FFN ? (u32)(r8 * ldw + (min(pos ^ swB, maxch) << 4)) : voffB;
  const int rowbase = FFN ? 128 * g : 128 * g - 16;
  const int p0row = (!FFN && g == 0 && w < 2) ? 8 * w : rowbase + 8 * w;
  const char* sA0 = x + (size_t)p0row * LDX;
  const char* sA1 = x + (size_t)(rowbase + 8 * (w + 8)) * LDX;
  const char* sA2 = x + (size_t)(rowbase + 8 * (16 + (w & 1))) * LDX;
  const char* sB0 = FFN ? w1 + (size_t)(8 * w) * LDW1 + (j - 8) * 352 : wq + (size_t)(8 * w) * LDQKV + j * 128;
  constexpr int SEGB = FFN ? 128 : 1024;
  const u32 mlo = (w == 0) ? 0xFFFFFF00u : 0xFFFFFFFFu;
  auto issue = [&](int i, int k) {
    const int kc = (i + rot) & 7;
    const u32 slot = (i % 3) * SLOT;
    const u32 va = voffA + kc * 128, vb = voffB + kc * 64 * ldw, vb2 = voffB2 + kc * 64 * ldw;
    if (k == 0) { if (TYPE == 0) dma16_masked(va, sA0, slot + w * 1024, mlo); else dma16(va, sA0, slot + w * 1024); }
    if (k == 1) dma16(vb, sB0, slot + BOFF + w * 1024);
    if (k == 2) dma16(vb, sB0 + SEGB, slot + BOFF + (8 + w) * 1024);
    if (k == 3) dma16(vb2, sB0 + 2 * SEGB, slot + BOFF + (16 + w) * 1024);
    if (k == 4) dma16(va, sA1, slot + (w + 8) * 1024);
    if (k == 5) dma16_if(va, sA2, slot + (16 + (w & 1)) * 1024, w);
  };
#pragma unroll
  for (int i = 0; i < 3; ++i)
#pragma unroll
    for (int k = 0; k < ND; ++k) issue(i, k);
  {
    const int bi = blockIdx.x >> 3;
    if (w == 7) {
      dma4(min(lane * 128 + (bi & 7) * 8192, 61000), (const void*)code0, L_JUNK);
    }
    if (w == 6) {
      const int ln = blockIdx.x * 59 + min(lane, 58);
      if (blockIdx.x * 59 + 58 < 4096) dma4(ln * 128, wo, L_JUNK + 256);
      else dma4((u32)max(ln - 4096, 0) * 128, w2, L_JUNK + 256);
    }
    if (w == 5) {
      dma4((lane & 3) * (2u << 20) + WS_A2 + (blockIdx.x & 63) * 4096, ws, L_JUNK + 512);
      dma4(blockIdx.x * 8192 + lane * 4, outp, L_JUNK + 768);
    }
  }
  u32 b1v = 0;
  if (FFN) b1v = b1[min((j - 8) * 176 + (int)threadIdx.x, 1375)];
  if (TYPE == 0 && w == 0 && lane < 8) {
    const u32x4 one = {0x3F803F80u, 0x3F803F80u, 0x3F803F80u, 0x3F803F80u};
#pragma unroll
    for (int s = 0; s < 3; ++s) *(LAS u32x4*)(L + s * SLOT + lane * 16) = one;
  }
  const int T0 = TYPE == 0 ? 0 : (TYPE == 1 ? 5 : 2 * (w & 3));
  const u32 aA = T0 * 2048 + (lc >> 3) * 1024 + (lc & 7) * 128 + ((lg ^ (lc & 7)) << 4);
  const int tq = lc >> 2, tp = lc & 3;
  const int swR = (tq & 2) | ((lg & 1) << 2);
  const u32 aBl = BOFF + lg * 1024 + tq * 128 + ((tp >> 1) << 4) + (tp & 1) * 8;
  const int crot = min(2 * (w & 3), NC - 2);
  u32 aB[NC];
#pragma unroll
  for (int c = 0; c < NC; ++c) {
    const int cc = c + crot >= NC ? c + crot - NC : c + crot;
    const int ct = FFN ? CB + cc : 4 * c + (w & 3);
    aB[c] = aBl + (ct >> 2) * 8192 + (((2 * (ct & 3)) ^ swR) << 4);
  }
  u32 aS[NSP];
  aS[0] = w * 1024 + lane * 16; aS[1] = (w + 8) * 1024 + lane * 16;
  if (NSP == 3) aS[NSP - 1] = (16 + (w & 1)) * 1024 + lane * 16;

  f32x4 acc[NT][NC], acc1[2];
#pragma unroll
  for (int t = 0; t < NT; ++t)
#pragma unroll
    for (int c = 0; c < NC; ++c) acc[t][c] = f32x4{0.f, 0.f, 0.f, 0.f};
  acc1[0] = acc1[1] = f32x4{0.f, 0.f, 0.f, 0.f};
  float ssum[NSP], ssq[NSP];
#pragma unroll
  for (int i = 0; i < NSP; ++i) ssum[i] = ssq[i] = 0.f;
  u32x4 fa[2][NT], sv[NSP];
  u32x2 fb[2][NC][2];
  auto rd = [&](int t, int k) {
    const u32 slot = ((t >> 1) % 3) * SLOT;
    const int b = t & 1;
    if (k < 2 * NC) fb[b][k >> 1][k & 1] = ldstr(L, aB[k >> 1] + slot + b * 4096 + (k & 1) * 512);
    else if (k < 2 * NC + NT) fa[b][k - 2 * NC] = lds128(L, ((aA ^ (b * 64)) + slot + (k - 2 * NC) * 2048));
    else sv[k - 2 * NC - NT] = lds128(L, aS[k - 2 * NC - NT] + slot);
  };
  auto ldw2 = [&](int s, int lo, int hi) {
    const int ks = ks0 + s;
    const bool isO = ks < 32;
    const char* base = isO ? wo + (size_t)ks * 16 * LDW2 : w2 + (size_t)(ks - 32) * 16 * LDW2;
    const __amdgpu_buffer_rsrc_t rs = __builtin_amdgcn_make_buffer_rsrc((void*)base, 0, ks < NKS ? 16 * LDW2 : 0, 0x00020000);
#pragma unroll
    for (int jj = lo; jj < hi; ++jj)
      W.r[s * 8 + jj] = __builtin_amdgcn_raw_buffer_load_b32(rs, w2voff + (jj & 3) * 1024, (jj >> 2) * 4096, 0);
  };
  const u32x4 ones = {0x3F803F80u, 0x3F803F80u, 0x3F803F80u, 0x3F803F80u};
  STAMP(1);
  wait_bar<10>();
  STAMP(2);
#pragma unroll
  for (int k = 0; k < 2 * NC + NT + NSP; ++k) rd(0, k);
  SB();
  sfor<0, 16>([&](auto tc) {
    constexpr int t = decltype(tc)::v, s = t >> 1, b = t & 1;
    if constexpr (b) { if constexpr (s <= 4) wait_bar<5>(); else if constexpr (s == 5) wait_bar<21>(); else if constexpr (s == 6) wait_bar<32>(); }
    constexpr int nr = t == 15 ? 0 : 2 * NC + NT + (b ? NSP : 0);
    constexpr int g0 = (nr + 1) / 2;
    constexpr int nd = (b && s + 3 < 8) ? ND : 0, nw = t >= 9 ? 8 : 0;
    constexpr int per = (nd + nw + NM - g0 - 1) / (NM - g0);
    constexpr int ndot = b ? 0 : 4 * NSP, dper = (ndot + NM - 1) / NM;
    sfor<0, NM>([&](auto qc) {
      constexpr int q = decltype(qc)::v;
      if constexpr (q < NT * NC) {
        constexpr int c = q / NT, i = q % NT;
        const u32x4 wfr = {fb[b][c][0][0], fb[b][c][0][1], fb[b][c][1][0], fb[b][c][1][1]};
        if constexpr (!FFN && c == 2) acc[i][c] = mfma16(fa[b][i], wfr, acc[i][c]);
        else acc[i][c] = mfma16(wfr, fa[b][i], acc[i][c]);
      } else {
        constexpr int c = q - NT * NC;
        const u32x4 wfr = {fb[b][c][0][0], fb[b][c][0][1], fb[b][c][1][0], fb[b][c][1][1]};
        acc1[c] = mfma16(wfr, ones, acc1[c]);
      }
      if constexpr (2 * q < nr) rd(t + 1, 2 * q);
      if constexpr (2 * q + 1 < nr) rd(t + 1, 2 * q + 1);
      if constexpr (q >= g0) {
        sfor<(q - g0) * per, (q - g0 + 1) * per>([&](auto uc) {
          constexpr int u = decltype(uc)::v;
          if constexpr (u < nd) issue(s + 3, u);
          else if constexpr (u < nd + nw) ldw2(t - 9, u - nd, u - nd + 1);
        });
      }
      sfor<q * dper, (q + 1) * dper>([&](auto uc) {
        constexpr int u = decltype(uc)::v;
        if constexpr (u < ndot) {
          constexpr int i = u >> 2, d = u & 3;
          ssum[i] = dot2(sv[i][d], 0x3F803F80u, ssum[i]);
          ssq[i] = dot2(sv[i][d], sv[i][d], ssq[i]);
        }
      });
      SB();
    });
  });
  if (FFN) asm volatile("" : "+v"(acc1[0]), "+v"(acc1[1]));
#pragma unroll
  for (int i = 0; i < NSP; ++i) asm volatile("" : "+v"(ssum[i]), "+v"(ssq[i]));
  STAMP(3);
#pragma unroll
  for (int i = 0; i < NSP; ++i) {
    const float sm = red8(ssum[i]) * (1.f / 512.f), sq = red8(ssq[i]) * (1.f / 512.f);
    const float var = fmaxf(sq - sm * sm, 0.f);
    const float rs = __builtin_amdgcn_rsqf(var + 1e-5f);
    const int p = i == 0 ? w : (i == 1 ? w + 8 : 16 + (w & 1));
    if (pos == 0) { *(LAS float*)(L + L_MEAN + (p * 8 + r8) * 4) = sm; *(LAS float*)(L + L_RSTD + (p * 8 + r8) * 4) = rs; }
  }
  if (TYPE == 0) {
    const int h = w & 3;
    if (lc == 0) {
      *(LAS f32x4*)(L + L_CS + (h * 16 + lg * 4) * 4) = acc[0][0];
      *(LAS f32x4*)(L + L_CS + (64 + h * 16 + lg * 4) * 4) = acc[0][1];
    }
    if (lg == 0) *(LAS float*)(L + L_CS + (128 + h * 16 + lc) * 4) = acc[0][2][0];
    *(LAS f32x4*)(L + L_KV + h * 2048 + lane * 16) = acc[NT - 1][1];
    *(LAS f32x4*)(L + L_KV + h * 2048 + 1024 + lane * 16) = acc[NT - 1][2];
  }
  if (FFN) {
    if (lc == 0) {
      *(LAS f32x4*)(L + L_CS + ((CB + crot) * 16 + lg * 4) * 4) = acc1[0];
      *(LAS f32x4*)(L + L_CS + ((CB + crot + 1) * 16 + lg * 4) * 4) = acc1[1];
    }
    if (threadIdx.x < 192) *(LAS float*)(L + L_B1 + threadIdx.x * 4) = bf_lo(b1v);
  }
  lds_bar();
  STAMP(4);
  if (!FFN) {
    const int h = w & 3;
    constexpr int NK = 5;
    const int TK = TYPE == 0 ? 0 : 4;
    const f32x4 csq = *(LAS f32x4*)(L + L_CS + (h * 16 + lg * 4) * 4);
    const f32x4 csk = *(LAS f32x4*)(L + L_CS + (64 + h * 16 + lg * 4) * 4);
    const f32x4 csv = splat4(*(LAS float*)(L + L_CS + (128 + h * 16 + lc) * 4));
    u32x2 Qb[4], Kb[NK], Vb[NK];
#pragma unroll
    for (int i = 0; i < NK; ++i) {
      const int T = TK + i;
      f32x4 ak, av;
      if (TYPE == 1 && i == 0) {
        ak = *(LAS f32x4*)(L + L_KV + h * 2048 + lane * 16);
        av = *(LAS f32x4*)(L + L_KV + h * 2048 + 1024 + lane * 16);
      } else {
        ak = acc[TYPE == 0 ? i : (i > 0 ? i - 1 : 0)][1];
        av = acc[TYPE == 0 ? i : (i > 0 ? i - 1 : 0)][2];
      }
      const float rc1 = *(LAS float*)(L + L_RSTD + (T * 16 + lc) * 4);
      const f32x4 mc = splat4(*(LAS float*)(L + L_MEAN + (T * 16 + lc) * 4)), rc = splat4(rc1);
      const f32x4 m4 = *(LAS f32x4*)(L + L_MEAN + (T * 16 + lg * 4) * 4), r4 = *(LAS f32x4*)(L + L_RSTD + (T * 16 + lg * 4) * 4);
      const f32x4 kk = (ak - mc * csk) * rc;
      const f32x4 vv = (av - m4 * csv) * r4;
      Kb[i] = u32x2{pk_bf16(kk[0], kk[1]), pk_bf16(kk[2], kk[3])};
      Vb[i] = u32x2{pk_bf16(vv[0], vv[1]), pk_bf16(vv[2], vv[3])};
      if (i > 0) {
        const f32x4 qq = (acc[TYPE == 0 ? i : i - 1][0] - mc * csq) * splat4(rc1 * (0.25f * 1.4426950408889634f));
        Qb[i - 1] = u32x2{pk_bf16(qq[0], qq[1]), pk_bf16(qq[2], qq[3])};
      }
    }
    SB();
    f32x4 mP, mC;
#pragma unroll
    for (int r = 0; r < 4; ++r) {
      mP[r] = (4 * lg + r >= lc + 2) ? 0.f : -1e30f;
      mC[r] = (4 * lg + r <= lc) ? 0.f : -1e30f;
    }
    const bool odd = lc & 1;
    const u32 sel0 = odd ? 0x01000c0cu : 0x0c0c0100u, sel1 = odd ? 0x03020c0cu : 0x0c0c0302u;
    u32x2 Ob[4];
#pragma unroll
    for (int q = 0; q < 4; ++q) {
      ldw2(7 + 2 * q, 0, 8); ldw2(8 + 2 * q, 0, 8);
      SB();
      f32x4 mp = mP;
      if (TYPE == 0 && q == 0 && g == 0) mp = f32x4{-1e30f, -1e30f, -1e30f, -1e30f};
      const f32x4 sp = mfma16k16(Kb[q], Qb[q], mp), sc = mfma16k16(Kb[q + 1], Qb[q], mC);
      const float a0 = odd ? sp[1] : sp[0], a1 = odd ? sp[3] : sp[2], a2 = odd ? sc[1] : sc[0], a3 = odd ? sc[3] : sc[2];
      const float mx = max_g(fmaxf(fmaxf(a0, a1), fmaxf(a2, a3)));
      const u32 u0 = pk_bf16(__builtin_amdgcn_exp2f(a0 - mx), __builtin_amdgcn_exp2f(a1 - mx));
      const u32 u1 = pk_bf16(__builtin_amdgcn_exp2f(a2 - mx), __builtin_amdgcn_exp2f(a3 - mx));
      const u32x4 pb = {__builtin_amdgcn_perm(0, u0, sel0), __builtin_amdgcn_perm(0, u0, sel1),
                        __builtin_amdgcn_perm(0, u1, sel0), __builtin_amdgcn_perm(0, u1, sel1)};
      const u32x4 vf = {Vb[q][0], Vb[q][1], Vb[q + 1][0], Vb[q + 1][1]};
      const f32x4 z = {0.f, 0.f, 0.f, 0.f};
      const f32x4 o = mfma16(vf, pb, z), l = mfma16(ones, pb, z);
      const f32x4 on = o * splat4(__builtin_amdgcn_rcpf(l[0]));
      Ob[q] = u32x2{pk_bf16(on[0], on[1]), pk_bf16(on[2], on[3])};
      SB();
    }
    const int rt = 4 * g + (TYPE == 0 ? 0 : 2), ks = 4 * j + h;
    __builtin_amdgcn_raw_buffer_store_b128(pair_rows(Ob[0], Ob[1]), rsA2, lane * 16, (rt * NKS + ks) * 1024, AUX_SC1);
    __builtin_amdgcn_raw_buffer_store_b128(pair_rows(Ob[2], Ob[3]), rsA2, lane * 16, ((rt + 1) * NKS + ks) * 1024, AUX_SC1);
    asm volatile("s_waitcnt vmcnt(0)\n\ts_barrier" ::: "memory");
  } else {
    const int m = w & 3;
    f32x4 mc[2], rc[2];
#pragma unroll
    for (int i = 0; i < 2; ++i) {
      mc[i] = splat4(*(LAS float*)(L + L_MEAN + ((2 * m + i) * 16 + lc) * 4));
      rc[i] = splat4(*(LAS float*)(L + L_RSTD + ((2 * m + i) * 16 + lc) * 4));
    }
    const int nct = j == 15 ? 9 : 11;
    const int rt = 4 * g + m, ksb = 32 + 11 * (j - 8);
    const f32x4 C1 = splat4(-2.f * 1.4426950408889634f * 0.7978845608028654f);
    const f32x4 C2 = splat4(-2.f * 1.4426950408889634f * 0.7978845608028654f * 0.044715f);
    const f32x4 ONE = splat4(1.f);
#pragma unroll
    for (int c = 0; c < NC; ++c) {
      if (c < 4) { ldw2(7 + 2 * c, 0, 8); ldw2(8 + 2 * c, 0, 8); }
      SB();
      const int ct = CB + (c + crot >= NC ? c + crot - NC : c + crot);
      const f32x4 cs = *(LAS f32x4*)(L + L_CS + (ct * 16 + lg * 4) * 4);
      const f32x4 bb = *(LAS f32x4*)(L + L_B1 + (ct * 16 + lg * 4) * 4);
      u32x2 hb[2];
#pragma unroll
      for (int i = 0; i < 2; ++i) {
        const f32x4 v = (acc[i][c] - mc[i] * cs) * rc[i] + bb;
        const f32x4 zz = v * (v * v * C2 + C1);
        f32x4 e;
#pragma unroll
        for (int r = 0; r < 4; ++r) e[r] = __builtin_amdgcn_exp2f(zz[r]);
        e += ONE;
#pragma unroll
        for (int r = 0; r < 4; ++r) e[r] = __builtin_amdgcn_rcpf(e[r]);
        const f32x4 y = v * e;
        hb[i] = u32x2{pk_bf16(y[0], y[1]), pk_bf16(y[2], y[3])};
      }
      __builtin_amdgcn_raw_buffer_store_b128(pair_rows(hb[0], hb[1]), rsA2, lane * 16,
                                             ct < nct ? (rt * NKS + ksb + ct) * 1024 : A2_BYTES, AUX_SC1);
      SB();
    }
    asm volatile("s_waitcnt vmcnt(0)\n\ts_barrier" ::: "memory");
  }
}

template <int TYPE>
DEV void body(LAS char* L, const char* __restrict__ x, const char* __restrict__ wq, const char* __restrict__ wo,
              const char* __restrict__ w1, const u16* __restrict__ b1, const char* __restrict__ w2, const char* __restrict__ b2,
              char* __restrict__ out, char* __restrict__ ws, u64 code0, int g, int j, int w, int lane STAMP_ARG) {
  const int mt = 2 * g + ((j >> 2) & 1), nt = (j & 3) + (j >> 3) * 4;
  const __amdgpu_buffer_rsrc_t rsA2 = __builtin_amdgcn_make_buffer_rsrc(ws + WS_A2, 0, A2_BYTES, 0x00020000);
  const int ks0 = w < 6 ? 15 * w : 90 + 14 * (w - 6);
  const u32 w2voff = (lane >> 5) * 8 * LDW2 + nt * 128 + (lane & 31) * 4;
  GAS u64* ctl = (GAS u64*)(ws + g * 4096);
  const u64 tag = __hip_atomic_load(ctl, __ATOMIC_RELAXED, __HIP_MEMORY_SCOPE_AGENT) + 1;
  P2W W;
  phase1<TYPE>(L, x, wq, w1, b1, wo, w2, ws, out, code0, g, j, w, lane, rsA2, w2voff, ks0, W STAMP_PASS);
  if (threadIdx.x == 0) __hip_atomic_store(ctl + 16 * (j + 1), tag, __ATOMIC_RELAXED, __HIP_MEMORY_SCOPE_AGENT);
  STAMP(5);
  const int orow = mt * 64 + (w >> 2) * 32 + (lane & 31), ocol = nt * 64 + (w & 3) * 16 + (lane >> 5) * 8;
  const u32 ooff = (u32)(orow * LDX + ocol * 2);
  const u32x4 xres = *(const GAS u32x4*)(x + ooff);
  const u32x4 bres = *(const GAS u32x4*)(b2 + (u32)(ocol * 2));
  u32x4 wf[15][2];
#pragma unroll
  for (int s = 0; s < 15; ++s)
#pragma unroll
    for (int d = 0; d < 4; ++d) {
      wf[s][0][d] = __builtin_amdgcn_perm(W.r[s * 8 + 2 * d + 1], W.r[s * 8 + 2 * d], 0x05040100u);
      wf[s][1][d] = __builtin_amdgcn_perm(W.r[s * 8 + 2 * d + 1], W.r[s * 8 + 2 * d], 0x07060302u);
    }
#pragma unroll
  for (int s = 0; s < 15; ++s) asm volatile("" : "+v"(wf[s][0]), "+v"(wf[s][1]));
  SB();
  STAMP(6);
  {
    const int ke = min(ks0 + 14, NKS - 1);
    const int plo = ks0 < 32 ? ks0 >> 2 : 8 + (ks0 - 32) / 11, phi = ke < 32 ? ke >> 2 : 8 + (ke - 32) / 11;
    const int pj = min(plo + (lane & 7), phi);
#pragma nounroll
    for (u32 spins = 0; spins < SPIN_CAP; ++spins) {
      const u64 v = __hip_atomic_load(ctl + 16 * (pj + 1), __ATOMIC_RELAXED, __HIP_MEMORY_SCOPE_AGENT);
      if (__all(v == tag)) break;
      __builtin_amdgcn_s_sleep(1);
    }
    __builtin_amdgcn_fence(__ATOMIC_ACQUIRE, "wavefront");
  }
  STAMP(7);
  const int nks = w < 6 ? 15 : 14;
  u32x4 af[2][15];
#pragma unroll
  for (int r = 0; r < 2; ++r)
#pragma unroll
    for (int s = 0; s < 15; ++s) {
      if (r == 1 && s >= 5) continue;
      const int so = (s < nks) ? ((2 * mt + r) * NKS + ks0 + s) * 1024 : A2_BYTES;
      af[r][s] = __builtin_amdgcn_raw_buffer_load_b128(rsA2, lane * 16, so, AUX_SC1);
    }
  SB();
#pragma unroll
  for (int r = 0; r < 2; ++r) {
    f32x16 a0, a1;
#pragma unroll
    for (int i = 0; i < 16; ++i) a0[i] = a1[i] = 0.f;
#pragma unroll
    for (int s = 0; s < 15; ++s) {
      a0 = mfma32(wf[s][0], af[r][s], a0);
      a1 = mfma32(wf[s][1], af[r][s], a1);
      if (r == 0 && s < 10) {
        const int s2 = s + 5;
        const int so = (s2 < nks) ? ((2 * mt + 1) * NKS + ks0 + s2) * 1024 : A2_BYTES;
        af[1][s2] = __builtin_amdgcn_raw_buffer_load_b128(rsA2, lane * 16, so, AUX_SC1);
      }
      SB();
    }
#pragma unroll
    for (int y = 0; y < 4; ++y) {
      *(LAS f32x4*)(L + ((w * 2 + r) * 8 + y * 2) * 1024 + lane * 16) = f32x4{a0[4 * y], a0[4 * y + 1], a0[4 * y + 2], a0[4 * y + 3]};
      *(LAS f32x4*)(L + ((w * 2 + r) * 8 + y * 2 + 1) * 1024 + lane * 16) = f32x4{a1[4 * y], a1[4 * y + 1], a1[4 * y + 2], a1[4 * y + 3]};
    }
    SB();
  }
  STAMP(8);
  lds_bar();
  if (j == 0 && threadIdx.x == 0) __hip_atomic_store(ctl, tag, __ATOMIC_RELAXED, __HIP_MEMORY_SCOPE_AGENT);
  f32x4 s0 = {0.f, 0.f, 0.f, 0.f}, s1 = s0;
#pragma unroll
  for (int sw = 0; sw < 8; ++sw) {
    s0 += *(LAS f32x4*)(L + ((sw * 2 + (w >> 2)) * 8 + (w & 3) * 2) * 1024 + lane * 16);
    s1 += *(LAS f32x4*)(L + ((sw * 2 + (w >> 2)) * 8 + (w & 3) * 2 + 1) * 1024 + lane * 16);
  }
  u32x4 o;
#pragma unroll
  for (int d = 0; d < 4; ++d)
    o[d] = pk_bf16(s0[d] + bf_lo(xres[d]) + bf_lo(bres[d]), s1[d] + bf_hi(xres[d]) + bf_hi(bres[d]));
  *(GAS u32x4*)(out + ooff) = o;
  STAMP(9);
  STAMP_OUT();
}

__global__ __launch_bounds__(512, 2) void fused(const char* __restrict__ x, const char* __restrict__ wq, const char* __restrict__ wo,
                                                const char* __restrict__ w1, const u16* __restrict__ b1, const char* __restrict__ w2,
                                                const char* __restrict__ b2, char* __restrict__ out, char* __restrict__ ws) {
  extern __shared__ __attribute__((aligned(16))) char smem[];
  LAS char* L = (LAS char*)smem;
  Stamps ST;
  STAMP(0);
  const int lane = threadIdx.x & 63, w = __builtin_amdgcn_readfirstlane(threadIdx.x >> 6);
  u64 code0;
  asm volatile("s_getpc_b64 %0" : "=s"(code0));
  code0 &= ~127ull;
  const int b = blockIdx.x, bi = b >> 3, g = (b & 3) * 4 + (bi & 3), j = ((bi >> 2) & 3) + ((b >> 2) & 1) * 4 + (bi >> 4) * 8;
  if (j < 8) {
    if (w < 4) body<0>(L, x, wq, wo, w1, b1, w2, b2, out, ws, code0, g, j, w, lane STAMP_PASS);
    else body<1>(L, x, wq, wo, w1, b1, w2, b2, out, ws, code0, g, j, w, lane STAMP_PASS);
  } else {
    if (w < 4) body<2>(L, x, wq, wo, w1, b1, w2, b2, out, ws, code0, g, j, w, lane STAMP_PASS);
    else body<3>(L, x, wq, wo, w1, b1, w2, b2, out, ws, code0, g, j, w, lane STAMP_PASS);
  }
}
}

__global__ void pre_t(char* ws) {
  if (threadIdx.x == 0 && blockIdx.x < 8) *(volatile unsigned*)(ws + (100u << 20) + blockIdx.x * 256) = (unsigned)__builtin_amdgcn_s_memrealtime();
}
__global__ void post_t(char* ws) {
  unsigned t0 = (unsigned)__builtin_amdgcn_s_memrealtime();
  unsigned m = 0x7fffffff;
  for (int i = threadIdx.x; i < 2048; i += 64) { unsigned v = *(volatile unsigned*)(ws + (100u << 20) + i * 4); m = min(m, t0 - v); }
  for (int o = 32; o; o >>= 1) m = min(m, (unsigned)__shfl_xor((int)m, o));
  const unsigned lim = 2000u + 10u * m;
  while ((unsigned)__builtin_amdgcn_s_memrealtime() - t0 < lim) __builtin_amdgcn_s_sleep(2);
}
extern "C" void kernel_launch(void* const* d_in, const int* in_sizes, int n_in, void* d_out, int out_size, void* d_ws,
                              size_t ws_size, hipStream_t stream) {
  (void)hipFuncSetAttribute((const void*)fk::fused, hipFuncAttributeMaxDynamicSharedMemorySize, fk::L_TOTAL);
  fk::fused<<<256, 512, fk::L_TOTAL, stream>>>((const char*)d_in[0], (const char*)d_in[1], (const char*)d_in[2], (const char*)d_in[4],
                                               (const u16*)d_in[5], (const char*)d_in[6], (const char*)d_in[7], (char*)d_out,
                                               (char*)d_ws);
  post_t<<<8, 64, 0, stream>>>((char*)d_ws);
}
```

```cpp
#define PF_CODE
#define PF_DATA
#define MAP2
#include <hip/hip_runtime.h>
#include <stdint.h>

typedef unsigned u32;
typedef unsigned long long u64;
typedef unsigned short u16;
typedef short s16x4 __attribute__((ext_vector_type(4)));
typedef short s16x8 __attribute__((ext_vector_type(8)));
typedef __bf16 b16x2 __attribute__((ext_vector_type(2)));
typedef float f32x2 __attribute__((ext_vector_type(2)));
typedef float f32x4 __attribute__((ext_vector_type(4)));
typedef float f32x16 __attribute__((ext_vector_type(16)));
typedef u32 u32x2 __attribute__((ext_vector_type(2)));
typedef u32 u32x4 __attribute__((ext_vector_type(4)));
#define LAS __attribute__((address_space(3)))
#define GAS __attribute__((address_space(1)))
#define DEV __device__ __forceinline__

namespace fk {
constexpr int LDX = 1024, LDQKV = 3072, LDW1 = 2752, LDW2 = 1024;
constexpr int NKS = 118;
constexpr int SLOT = 43008, BOFF = 18432;
constexpr int L_MEAN = 3 * SLOT, L_RSTD = L_MEAN + 576, L_CS = L_RSTD + 576, L_B1 = L_CS + 768, L_KV = L_B1 + 768,
              L_JUNK = L_KV + 8192, L_TOTAL = L_JUNK + 2048;
constexpr int WS_A2 = 65536, A2_BYTES = 64 * NKS * 1024;
constexpr int AUX_SC1 = 16;
constexpr u32 SPIN_CAP = 1u << 16;

#define STAMP(i) do {} while (0)
#define STAMP_ARG
#define STAMP_PASS
#define STAMP_OUT() do {} while (0)

DEV void dma16(u32 voff, const void* sbase, u32 lds) {
  asm volatile("s_mov_b32 m0, %2\n\ts_nop 0\n\tglobal_load_lds_dwordx4 %0, %1" ::"v"(voff), "s"(sbase), "s"(lds) : "memory");
}
DEV void dma16_masked(u32 voff, const void* sbase, u32 lds, u32 mlo) {
  asm volatile("s_mov_b32 m0, %2\n\ts_mov_b32 exec_lo, %3\n\tglobal_load_lds_dwordx4 %0, %1\n\ts_mov_b32 exec_lo, -1"
               ::"v"(voff), "s"(sbase), "s"(lds), "s"(mlo) : "memory");
}
DEV void dma16_if(u32 voff, const void* sbase, u32 lds, u32 cond) {
  asm volatile("s_cmp_gt_u32 %3, 5\n\ts_cbranch_scc1 1f\n\ts_mov_b32 m0, %2\n\ts_nop 0\n\tglobal_load_lds_dwordx4 %0, %1\n1:"
               ::"v"(voff), "s"(sbase), "s"(lds), "s"(cond) : "memory", "scc");
}
DEV void dma4(u32 voff, const void* sbase, u32 lds) {
  asm volatile("s_mov_b32 m0, %2\n\ts_nop 0\n\tglobal_load_lds_dword %0, %1" ::"v"(voff), "s"(sbase), "s"(lds) : "memory");
}
template <int N> DEV void wait_bar() {
  asm volatile("s_waitcnt vmcnt(%0) lgkmcnt(0)\n\ts_barrier" ::"i"(N) : "memory");
}
DEV void lds_bar() { asm volatile("s_waitcnt lgkmcnt(0)\n\ts_barrier" ::: "memory"); }
#define SB() __builtin_amdgcn_sched_barrier(0)

DEV u32 pk_bf16(float a, float b) {
  f32x2 p = {a, b};
  return __builtin_bit_cast(u32, __builtin_convertvector(p, b16x2));
}
DEV float bf_lo(u32 v) { return __builtin_bit_cast(float, v << 16); }
DEV float bf_hi(u32 v) { return __builtin_bit_cast(float, v & 0xffff0000u); }
DEV float dot2(u32 a, u32 b, float c) {
  return __builtin_amdgcn_fdot2_f32_bf16(__builtin_bit_cast(b16x2, a), __builtin_bit_cast(b16x2, b), c, false);
}
DEV f32x4 mfma16(u32x4 a, u32x4 b, f32x4 c) {
  return __builtin_amdgcn_mfma_f32_16x16x32_bf16(__builtin_bit_cast(s16x8, a), __builtin_bit_cast(s16x8, b), c, 0, 0, 0);
}
DEV f32x4 mfma16k16(u32x2 a, u32x2 b, f32x4 c) {
  return __builtin_amdgcn_mfma_f32_16x16x16bf16_1k(__builtin_bit_cast(s16x4, a), __builtin_bit_cast(s16x4, b), c, 0, 0, 0);
}
DEV f32x16 mfma32(u32x4 a, u32x4 b, f32x16 c) {
  return __builtin_amdgcn_mfma_f32_32x32x16_bf16(__builtin_bit_cast(s16x8, a), __builtin_bit_cast(s16x8, b), c, 0, 0, 0);
}
DEV u32x4 lds128(LAS char* L, u32 off) { return *(LAS u32x4*)(L + off); }
DEV u32x2 ldstr(LAS char* L, u32 off) {
  return __builtin_bit_cast(u32x2, __builtin_amdgcn_ds_read_tr16_b64_v4i16((LAS s16x4*)(L + off)));
}
template <int CTRL> DEV float dpp_add(float v) {
  int t = __builtin_amdgcn_update_dpp(0, __builtin_bit_cast(int, v), CTRL, 0xf, 0xf, true);
  return v + __builtin_bit_cast(float, t);
}
DEV float red8(float v) {
  v = dpp_add<0xB1>(v);
  v = dpp_add<0x4E>(v);
  v = dpp_add<0x141>(v);
  return v;
}
DEV float max_g(float v) {
  u32 u = __builtin_bit_cast(u32, v);
  auto r = __builtin_amdgcn_permlane32_swap(u, u, false, false);
  v = fmaxf(__builtin_bit_cast(float, r[0]), __builtin_bit_cast(float, r[1]));
  u = __builtin_bit_cast(u32, v);
  auto q = __builtin_amdgcn_permlane16_swap(u, u, false, false);
  return fmaxf(__builtin_bit_cast(float, q[0]), __builtin_bit_cast(float, q[1]));
}
DEV u32x4 pair_rows(u32x2 x, u32x2 y) {
  auto a = __builtin_amdgcn_permlane16_swap(x[0], y[0], false, false);
  auto b = __builtin_amdgcn_permlane16_swap(x[1], y[1], false, false);
  return u32x4{a[0], b[0], a[1], b[1]};
}

DEV f32x4 splat4(float v) {
  f32x2 p = {v, v};
  asm volatile("" : "+v"(p));
  return f32x4{p[0], p[1], p[0], p[1]};
}
template <int V> struct IC { static constexpr int v = V; };
template <int LO, int HI, class F> DEV void sfor(F&& f) {
  if constexpr (LO < HI) { f(IC<LO>{}); sfor<LO + 1, HI>(f); }
}
struct P2W { u32 r[120]; };

template <int TYPE>
DEV void phase1(LAS char* L, const char* x, const char* wq, const char* w1, const u16* b1, const char* wo, const char* w2,
                char* ws, const char* outp, u64 code0, int g, int j, int w, int lane, __amdgpu_buffer_rsrc_t rsA2, u32 w2voff, int ks0, P2W& W STAMP_ARG) {
  constexpr bool FFN = TYPE >= 2;
  constexpr int NT = TYPE == 0 ? 5 : (TYPE == 1 ? 4 : 2);
  constexpr int NC = TYPE == 2 ? 6 : (TYPE == 3 ? 5 : 3);
  constexpr int CB = TYPE == 3 ? 6 : 0;
  constexpr int NSP = TYPE == 1 ? 3 : 2;
  constexpr int ND = TYPE == 1 ? 6 : 5;
  constexpr int NM = NT * NC + (FFN ? 2 : 0);
  const int r8 = lane >> 3, pos = lane & 7, lg = lane >> 4, lc = lane & 15;
  const int rot = (j + g) & 7;
  constexpr int ldw = FFN ? LDW1 : LDQKV;
  const u32 voffA = r8 * LDX + ((pos ^ r8) << 4);
  const int swB = (r8 & 2) | ((w & 1) << 2);
  const u32 voffB = r8 * ldw + ((pos ^ swB) << 4);
  const int maxch = (j == 15) ? 1 : 5;
  const u32 voffB2 = FFN ? (u32)(r8 * ldw + (min(pos ^ swB, maxch) << 4)) : voffB;
  const int rowbase = FFN ? 128 * g : 128 * g - 16;
  const int p0row = (!FFN && g == 0 && w < 2) ? 8 * w : rowbase + 8 * w;
  const char* sA0 = x + (size_t)p0row * LDX;
  const char* sA1 = x + (size_t)(rowbase + 8 * (w + 8)) * LDX;
  const char* sA2 = x + (size_t)(rowbase + 8 * (16 + (w & 1))) * LDX;
  const char* sB0 = FFN ? w1 + (size_t)(8 * w) * LDW1 + (j - 8) * 352 : wq + (size_t)(8 * w) * LDQKV + j * 128;
  constexpr int SEGB = FFN ? 128 : 1024;
  const u32 mlo = (w == 0) ? 0xFFFFFF00u : 0xFFFFFFFFu;
  auto issue = [&](int i, int k) {
    const int kc = (i + rot) & 7;
    const u32 slot = (i % 3) * SLOT;
    const u32 va = voffA + kc * 128, vb = voffB + kc * 64 * ldw, vb2 = voffB2 + kc * 64 * ldw;
    if (k == 0) { if (TYPE == 0) dma16_masked(va, sA0, slot + w * 1024, mlo); else dma16(va, sA0, slot + w * 1024); }
    if (k == 1) dma16(vb, sB0, slot + BOFF + w * 1024);
    if (k == 2) dma16(vb, sB0 + SEGB, slot + BOFF + (8 + w) * 1024);
    if (k == 3) dma16(vb2, sB0 + 2 * SEGB, slot + BOFF + (16 + w) * 1024);
    if (k == 4) dma16(va, sA1, slot + (w + 8) * 1024);
    if (k == 5) dma16_if(va, sA2, slot + (16 + (w & 1)) * 1024, w);
  };
#pragma unroll
  for (int i = 0; i < 3; ++i)
#pragma unroll
    for (int k = 0; k < ND; ++k) issue(i, k);
  {
    const int bi = blockIdx.x >> 3;
    if (w == 7) {
      dma4(min(lane * 128 + (bi & 7) * 8192, 61000), (const void*)code0, L_JUNK);
    }
    if (w == 6) {
      const int ln = blockIdx.x * 59 + min(lane, 58);
      if (blockIdx.x * 59 + 58 < 4096) dma4(ln * 128, wo, L_JUNK + 256);
      else dma4((u32)max(ln - 4096, 0) * 128, w2, L_JUNK + 256);
    }
    if (w == 5) {
      dma4((lane & 3) * (2u << 20) + WS_A2 + (blockIdx.x & 63) * 4096, ws, L_JUNK + 512);
      dma4(blockIdx.x * 8192 + lane * 4, outp, L_JUNK + 768);
    }
  }
  u32 b1v = 0;
  if (FFN) b1v = b1[min((j - 8) * 176 + (int)threadIdx.x, 1375)];
  if (TYPE == 0 && w == 0 && lane < 8) {
    const u32x4 one = {0x3F803F80u, 0x3F803F80u, 0x3F803F80u, 0x3F803F80u};
#pragma unroll
    for (int s = 0; s < 3; ++s) *(LAS u32x4*)(L + s * SLOT + lane * 16) = one;
  }
  const int T0 = TYPE == 0 ? 0 : (TYPE == 1 ? 5 : 2 * (w & 3));
  const u32 aA = T0 * 2048 + (lc >> 3) * 1024 + (lc & 7) * 128 + ((lg ^ (lc & 7)) << 4);
  const int tq = lc >> 2, tp = lc & 3;
  const int swR = (tq & 2) | ((lg & 1) << 2);
  const u32 aBl = BOFF + lg * 1024 + tq * 128 + ((tp >> 1) << 4) + (tp & 1) * 8;
  const int crot = min(2 * (w & 3), NC - 2);
  u32 aB[NC];
#pragma unroll
  for (int c = 0; c < NC; ++c) {
    const int cc = c + crot >= NC ? c + crot - NC : c + crot;
    const int ct = FFN ? CB + cc : 4 * c + (w & 3);
    aB[c] = aBl + (ct >> 2) * 8192 + (((2 * (ct & 3)) ^ swR) << 4);
  }
  u32 aS[NSP];
  aS[0] = w * 1024 + lane * 16; aS[1] = (w + 8) * 1024 + lane * 16;
  if (NSP == 3) aS[NSP - 1] = (16 + (w & 1)) * 1024 + lane * 16;

  f32x4 acc[NT][NC], acc1[2];
#pragma unroll
  for (int t = 0; t < NT; ++t)
#pragma unroll
    for (int c = 0; c < NC; ++c) acc[t][c] = f32x4{0.f, 0.f, 0.f, 0.f};
  acc1[0] = acc1[1] = f32x4{0.f, 0.f, 0.f, 0.f};
  float ssum[NSP], ssq[NSP];
#pragma unroll
  for (int i = 0; i < NSP; ++i) ssum[i] = ssq[i] = 0.f;
  u32x4 fa[2][NT], sv[NSP];
  u32x2 fb[2][NC][2];
  auto rd = [&](int t, int k) {
    const u32 slot = ((t >> 1) % 3) * SLOT;
    const int b = t & 1;
    if (k < 2 * NC) fb[b][k >> 1][k & 1] = ldstr(L, aB[k >> 1] + slot + b * 4096 + (k & 1) * 512);
    else if (k < 2 * NC + NT) fa[b][k - 2 * NC] = lds128(L, ((aA ^ (b * 64)) + slot + (k - 2 * NC) * 2048));
    else sv[k - 2 * NC - NT] = lds128(L, aS[k - 2 * NC - NT] + slot);
  };
  auto ldw2 = [&](int s, int lo, int hi) {
    const int ks = ks0 + s;
    const bool isO = ks < 32;
    const char* base = isO ? wo + (size_t)ks * 16 * LDW2 : w2 + (size_t)(ks - 32) * 16 * LDW2;
    const __amdgpu_buffer_rsrc_t rs = __builtin_amdgcn_make_buffer_rsrc((void*)base, 0, ks < NKS ? 16 * LDW2 : 0, 0x00020000);
#pragma unroll
    for (int jj = lo; jj < hi; ++jj)
      W.r[s * 8 + jj] = __builtin_amdgcn_raw_buffer_load_b32(rs, w2voff + (jj & 3) * 1024, (jj >> 2) * 4096, 0);
  };
  const u32x4 ones = {0x3F803F80u, 0x3F803F80u, 0x3F803F80u, 0x3F803F80u};
  STAMP(1);
  wait_bar<10>();
  STAMP(2);
#pragma unroll
  for (int k = 0; k < 2 * NC + NT + NSP; ++k) rd(0, k);
  SB();
  sfor<0, 16>([&](auto tc) {
    constexpr int t = decltype(tc)::v, s = t >> 1, b = t & 1;
    if constexpr (b) { if constexpr (s <= 4) wait_bar<5>(); else if constexpr (s == 5) wait_bar<21>(); else if constexpr (s == 6) wait_bar<32>(); }
    constexpr int nr = t == 15 ? 0 : 2 * NC + NT + (b ? NSP : 0);
    constexpr int g0 = (nr + 1) / 2;
    constexpr int nd = (b && s + 3 < 8) ? ND : 0, nw = t >= 9 ? 8 : 0;
    constexpr int per = (nd + nw + NM - g0 - 1) / (NM - g0);
    constexpr int ndot = b ? 0 : 4 * NSP, dper = (ndot + NM - 1) / NM;
    sfor<0, NM>([&](auto qc) {
      constexpr int q = decltype(qc)::v;
      if constexpr (q < NT * NC) {
        constexpr int c = q / NT, i = q % NT;
        const u32x4 wfr = {fb[b][c][0][0], fb[b][c][0][1], fb[b][c][1][0], fb[b][c][1][1]};
        if constexpr (!FFN && c == 2) acc[i][c] = mfma16(fa[b][i], wfr, acc[i][c]);
        else acc[i][c] = mfma16(wfr, fa[b][i], acc[i][c]);
      } else {
        constexpr int c = q - NT * NC;
        const u32x4 wfr = {fb[b][c][0][0], fb[b][c][0][1], fb[b][c][1][0], fb[b][c][1][1]};
        acc1[c] = mfma16(wfr, ones, acc1[c]);
      }
      if constexpr (2 * q < nr) rd(t + 1, 2 * q);
      if constexpr (2 * q + 1 < nr) rd(t + 1, 2 * q + 1);
      if constexpr (q >= g0) {
        sfor<(q - g0) * per, (q - g0 + 1) * per>([&](auto uc) {
          constexpr int u = decltype(uc)::v;
          if constexpr (u < nd) issue(s + 3, u);
          else if constexpr (u < nd + nw) ldw2(t - 9, u - nd, u - nd + 1);
        });
      }
      sfor<q * dper, (q + 1) * dper>([&](auto uc) {
        constexpr int u = decltype(uc)::v;
        if constexpr (u < ndot) {
          constexpr int i = u >> 2, d = u & 3;
          ssum[i] = dot2(sv[i][d], 0x3F803F80u, ssum[i]);
          ssq[i] = dot2(sv[i][d], sv[i][d], ssq[i]);
        }
      });
      SB();
    });
  });
  if (FFN) asm volatile("" : "+v"(acc1[0]), "+v"(acc1[1]));
#pragma unroll
  for (int i = 0; i < NSP; ++i) asm volatile("" : "+v"(ssum[i]), "+v"(ssq[i]));
  STAMP(3);
#pragma unroll
  for (int i = 0; i < NSP; ++i) {
    const float sm = red8(ssum[i]) * (1.f / 512.f), sq = red8(ssq[i]) * (1.f / 512.f);
    const float var = fmaxf(sq - sm * sm, 0.f);
    const float rs = __builtin_amdgcn_rsqf(var + 1e-5f);
    const int p = i == 0 ? w : (i == 1 ? w + 8 : 16 + (w & 1));
    if (pos == 0) { *(LAS float*)(L + L_MEAN + (p * 8 + r8) * 4) = sm; *(LAS float*)(L + L_RSTD + (p * 8 + r8) * 4) = rs; }
  }
  if (TYPE == 0) {
    const int h = w & 3;
    if (lc == 0) {
      *(LAS f32x4*)(L + L_CS + (h * 16 + lg * 4) * 4) = acc[0][0];
      *(LAS f32x4*)(L + L_CS + (64 + h * 16 + lg * 4) * 4) = acc[0][1];
    }
    if (lg == 0) *(LAS float*)(L + L_CS + (128 + h * 16 + lc) * 4) = acc[0][2][0];
    *(LAS f32x4*)(L + L_KV + h * 2048 + lane * 16) = acc[NT - 1][1];
    *(LAS f32x4*)(L + L_KV + h * 2048 + 1024 + lane * 16) = acc[NT - 1][2];
  }
  if (FFN) {
    if (lc == 0) {
      *(LAS f32x4*)(L + L_CS + ((CB + crot) * 16 + lg * 4) * 4) = acc1[0];
      *(LAS f32x4*)(L + L_CS + ((CB + crot + 1) * 16 + lg * 4) * 4) = acc1[1];
    }
    if (threadIdx.x < 192) *(LAS float*)(L + L_B1 + threadIdx.x * 4) = bf_lo(b1v);
  }
  lds_bar();
  STAMP(4);
  if (!FFN) {
    const int h = w & 3;
    constexpr int NK = 5;
    const int TK = TYPE == 0 ? 0 : 4;
    const f32x4 csq = *(LAS f32x4*)(L + L_CS + (h * 16 + lg * 4) * 4);
    const f32x4 csk = *(LAS f32x4*)(L + L_CS + (64 + h * 16 + lg * 4) * 4);
    const f32x4 csv = splat4(*(LAS float*)(L + L_CS + (128 + h * 16 + lc) * 4));
    u32x2 Qb[4], Kb[NK], Vb[NK];
#pragma unroll
    for (int i = 0; i < NK; ++i) {
      const int T = TK + i;
      f32x4 ak, av;
      if (TYPE == 1 && i == 0) {
        ak = *(LAS f32x4*)(L + L_KV + h * 2048 + lane * 16);
        av = *(LAS f32x4*)(L + L_KV + h * 2048 + 1024 + lane * 16);
      } else {
        ak = acc[TYPE == 0 ? i : (i > 0 ? i - 1 : 0)][1];
        av = acc[TYPE == 0 ? i : (i > 0 ? i - 1 : 0)][2];
      }
      const float rc1 = *(LAS float*)(L + L_RSTD + (T * 16 + lc) * 4);
      const f32x4 mc = splat4(*(LAS float*)(L + L_MEAN + (T * 16 + lc) * 4)), rc = splat4(rc1);
      const f32x4 m4 = *(LAS f32x4*)(L + L_MEAN + (T * 16 + lg * 4) * 4), r4 = *(LAS f32x4*)(L + L_RSTD + (T * 16 + lg * 4) * 4);
      const f32x4 kk = (ak - mc * csk) * rc;
      const f32x4 vv = (av - m4 * csv) * r4;
      Kb[i] = u32x2{pk_bf16(kk[0], kk[1]), pk_bf16(kk[2], kk[3])};
      Vb[i] = u32x2{pk_bf16(vv[0], vv[1]), pk_bf16(vv[2], vv[3])};
      if (i > 0) {
        const f32x4 qq = (acc[TYPE == 0 ? i : i - 1][0] - mc * csq) * splat4(rc1 * (0.25f * 1.4426950408889634f));
        Qb[i - 1] = u32x2{pk_bf16(qq[0], qq[1]), pk_bf16(qq[2], qq[3])};
      }
    }
    SB();
    f32x4 mP, mC;
#pragma unroll
    for (int r = 0; r < 4; ++r) {
      mP[r] = (4 * lg + r >= lc + 2) ? 0.f : -1e30f;
      mC[r] = (4 * lg + r <= lc) ? 0.f : -1e30f;
    }
    const bool odd = lc & 1;
    const u32 sel0 = odd ? 0x01000c0cu : 0x0c0c0100u, sel1 = odd ? 0x03020c0cu : 0x0c0c0302u;
    u32x2 Ob[4];
#pragma unroll
    for (int q = 0; q < 4; ++q) {
      ldw2(7 + 2 * q, 0, 8); ldw2(8 + 2 * q, 0, 8);
      SB();
      f32x4 mp = mP;
      if (TYPE == 0 && q == 0 && g == 0) mp = f32x4{-1e30f, -1e30f, -1e30f, -1e30f};
      const f32x4 sp = mfma16k16(Kb[q], Qb[q], mp), sc = mfma16k16(Kb[q + 1], Qb[q], mC);
      const float a0 = odd ? sp[1] : sp[0], a1 = odd ? sp[3] : sp[2], a2 = odd ? sc[1] : sc[0], a3 = odd ? sc[3] : sc[2];
      const float mx = max_g(fmaxf(fmaxf(a0, a1), fmaxf(a2, a3)));
      const u32 u0 = pk_bf16(__builtin_amdgcn_exp2f(a0 - mx), __builtin_amdgcn_exp2f(a1 - mx));
      const u32 u1 = pk_bf16(__builtin_amdgcn_exp2f(a2 - mx), __builtin_amdgcn_exp2f(a3 - mx));
      const u32x4 pb = {__builtin_amdgcn_perm(0, u0, sel0), __builtin_amdgcn_perm(0, u0, sel1),
                        __builtin_amdgcn_perm(0, u1, sel0), __builtin_amdgcn_perm(0, u1, sel1)};
      const u32x4 vf = {Vb[q][0], Vb[q][1], Vb[q + 1][0], Vb[q + 1][1]};
      const f32x4 z = {0.f, 0.f, 0.f, 0.f};
      const f32x4 o = mfma16(vf, pb, z), l = mfma16(ones, pb, z);
      const f32x4 on = o * splat4(__builtin_amdgcn_rcpf(l[0]));
      Ob[q] = u32x2{pk_bf16(on[0], on[1]), pk_bf16(on[2], on[3])};
      SB();
    }
    const int rt = 4 * g + (TYPE == 0 ? 0 : 2), ks = 4 * j + h;
    __builtin_amdgcn_raw_buffer_store_b128(pair_rows(Ob[0], Ob[1]), rsA2, lane * 16, (rt * NKS + ks) * 1024, AUX_SC1);
    __builtin_amdgcn_raw_buffer_store_b128(pair_rows(Ob[2], Ob[3]), rsA2, lane * 16, ((rt + 1) * NKS + ks) * 1024, AUX_SC1);
    asm volatile("s_waitcnt vmcnt(0)\n\ts_barrier" ::: "memory");
  } else {
    const int m = w & 3;
    f32x4 mc[2], rc[2];
#pragma unroll
    for (int i = 0; i < 2; ++i) {
      mc[i] = splat4(*(LAS float*)(L + L_MEAN + ((2 * m + i) * 16 + lc) * 4));
      rc[i] = splat4(*(LAS float*)(L + L_RSTD + ((2 * m + i) * 16 + lc) * 4));
    }
    const int nct = j == 15 ? 9 : 11;
    const int rt = 4 * g + m, ksb = 32 + 11 * (j - 8);
    const f32x4 C1 = splat4(-2.f * 1.4426950408889634f * 0.7978845608028654f);
    const f32x4 C2 = splat4(-2.f * 1.4426950408889634f * 0.7978845608028654f * 0.044715f);
    const f32x4 ONE = splat4(1.f);
#pragma unroll
    for (int c = 0; c < NC; ++c) {
      if (c < 4) { ldw2(7 + 2 * c, 0, 8); ldw2(8 + 2 * c, 0, 8); }
      SB();
      const int ct = CB + (c + crot >= NC ? c + crot - NC : c + crot);
      const f32x4 cs = *(LAS f32x4*)(L + L_CS + (ct * 16 + lg * 4) * 4);
      const f32x4 bb = *(LAS f32x4*)(L + L_B1 + (ct * 16 + lg * 4) * 4);
      u32x2 hb[2];
#pragma unroll
      for (int i = 0; i < 2; ++i) {
        const f32x4 v = (acc[i][c] - mc[i] * cs) * rc[i] + bb;
        const f32x4 zz = v * (v * v * C2 + C1);
        f32x4 e;
#pragma unroll
        for (int r = 0; r < 4; ++r) e[r] = __builtin_amdgcn_exp2f(zz[r]);
        e += ONE;
#pragma unroll
        for (int r = 0; r < 4; ++r) e[r] = __builtin_amdgcn_rcpf(e[r]);
        const f32x4 y = v * e;
        hb[i] = u32x2{pk_bf16(y[0], y[1]), pk_bf16(y[2], y[3])};
      }
      __builtin_amdgcn_raw_buffer_store_b128(pair_rows(hb[0], hb[1]), rsA2, lane * 16,
                                             ct < nct ? (rt * NKS + ksb + ct) * 1024 : A2_BYTES, AUX_SC1);
      SB();
    }
    asm volatile("s_waitcnt vmcnt(0)\n\ts_barrier" ::: "memory");
  }
}

template <int TYPE>
DEV void body(LAS char* L, const char* __restrict__ x, const char* __restrict__ wq, const char* __restrict__ wo,
              const char* __restrict__ w1, const u16* __restrict__ b1, const char* __restrict__ w2, const char* __restrict__ b2,
              char* __restrict__ out, char* __restrict__ ws, u64 code0, int g, int j, int w, int lane STAMP_ARG) {
  const int mt = 2 * g + ((j >> 2) & 1), nt = (j & 3) + (j >> 3) * 4;
  const __amdgpu_buffer_rsrc_t rsA2 = __builtin_amdgcn_make_buffer_rsrc(ws + WS_A2, 0, A2_BYTES, 0x00020000);
  const int ks0 = w < 6 ? 15 * w : 90 + 14 * (w - 6);
  const u32 w2voff = (lane >> 5) * 8 * LDW2 + nt * 128 + (lane & 31) * 4;
  GAS u64* ctl = (GAS u64*)(ws + g * 4096);
  const u64 tag = __hip_atomic_load(ctl, __ATOMIC_RELAXED, __HIP_MEMORY_SCOPE_AGENT) + 1;
  P2W W;
  phase1<TYPE>(L, x, wq, w1, b1, wo, w2, ws, out, code0, g, j, w, lane, rsA2, w2voff, ks0, W STAMP_PASS);
  if (threadIdx.x == 0) __hip_atomic_store(ctl + 16 * (j + 1), tag, __ATOMIC_RELAXED, __HIP_MEMORY_SCOPE_AGENT);
  STAMP(5);
  const int orow = mt * 64 + (w >> 2) * 32 + (lane & 31), ocol = nt * 64 + (w & 3) * 16 + (lane >> 5) * 8;
  const u32 ooff = (u32)(orow * LDX + ocol * 2);
  const u32x4 xres = *(const GAS u32x4*)(x + ooff);
  const u32x4 bres = *(const GAS u32x4*)(b2 + (u32)(ocol * 2));
  u32x4 wf[15][2];
#pragma unroll
  for (int s = 0; s < 15; ++s)
#pragma unroll
    for (int d = 0; d < 4; ++d) {
      wf[s][0][d] = __builtin_amdgcn_perm(W.r[s * 8 + 2 * d + 1], W.r[s * 8 + 2 * d], 0x05040100u);
      wf[s][1][d] = __builtin_amdgcn_perm(W.r[s * 8 + 2 * d + 1], W.r[s * 8 + 2 * d], 0x07060302u);
    }
#pragma unroll
  for (int s = 0; s < 15; ++s) asm volatile("" : "+v"(wf[s][0]), "+v"(wf[s][1]));
  SB();
  STAMP(6);
  {
    const int ke = min(ks0 + 14, NKS - 1);
    const int plo = ks0 < 32 ? ks0 >> 2 : 8 + (ks0 - 32) / 11, phi = ke < 32 ? ke >> 2 : 8 + (ke - 32) / 11;
    const int pj = min(plo + (lane & 7), phi);
#pragma nounroll
    for (u32 spins = 0; spins < SPIN_CAP; ++spins) {
      const u64 v = __hip_atomic_load(ctl + 16 * (pj + 1), __ATOMIC_RELAXED, __HIP_MEMORY_SCOPE_AGENT);
      if (__all(v == tag)) break;
      __builtin_amdgcn_s_sleep(1);
    }
    __builtin_amdgcn_fence(__ATOMIC_ACQUIRE, "wavefront");
  }
  STAMP(7);
  const int nks = w < 6 ? 15 : 14;
  u32x4 af[2][15];
#pragma unroll
  for (int r = 0; r < 2; ++r)
#pragma unroll
    for (int s = 0; s < 15; ++s) {
      if (r == 1 && s >= 5) continue;
      const int so = (s < nks) ? ((2 * mt + r) * NKS + ks0 + s) * 1024 : A2_BYTES;
      af[r][s] = __builtin_amdgcn_raw_buffer_load_b128(rsA2, lane * 16, so, AUX_SC1);
    }
  SB();
#pragma unroll
  for (int r = 0; r < 2; ++r) {
    f32x16 a0, a1;
#pragma unroll
    for (int i = 0; i < 16; ++i) a0[i] = a1[i] = 0.f;
#pragma unroll
    for (int s = 0; s < 15; ++s) {
      a0 = mfma32(wf[s][0], af[r][s], a0);
      a1 = mfma32(wf[s][1], af[r][s], a1);
      if (r == 0 && s < 10) {
        const int s2 = s + 5;
        const int so = (s2 < nks) ? ((2 * mt + 1) * NKS + ks0 + s2) * 1024 : A2_BYTES;
        af[1][s2] = __builtin_amdgcn_raw_buffer_load_b128(rsA2, lane * 16, so, AUX_SC1);
      }
      SB();
    }
#pragma unroll
    for (int y = 0; y < 4; ++y) {
      *(LAS f32x4*)(L + ((w * 2 + r) * 8 + y * 2) * 1024 + lane * 16) = f32x4{a0[4 * y], a0[4 * y + 1], a0[4 * y + 2], a0[4 * y + 3]};
      *(LAS f32x4*)(L + ((w * 2 + r) * 8 + y * 2 + 1) * 1024 + lane * 16) = f32x4{a1[4 * y], a1[4 * y + 1], a1[4 * y + 2], a1[4 * y + 3]};
    }
    SB();
  }
  STAMP(8);
  lds_bar();
  if (j == 0 && threadIdx.x == 0) __hip_atomic_store(ctl, tag, __ATOMIC_RELAXED, __HIP_MEMORY_SCOPE_AGENT);
  f32x4 s0 = {0.f, 0.f, 0.f, 0.f}, s1 = s0;
#pragma unroll
  for (int sw = 0; sw < 8; ++sw) {
    s0 += *(LAS f32x4*)(L + ((sw * 2 + (w >> 2)) * 8 + (w & 3) * 2) * 1024 + lane * 16);
    s1 += *(LAS f32x4*)(L + ((sw * 2 + (w >> 2)) * 8 + (w & 3) * 2 + 1) * 1024 + lane * 16);
  }
  u32x4 o;
#pragma unroll
  for (int d = 0; d < 4; ++d)
    o[d] = pk_bf16(s0[d] + bf_lo(xres[d]) + bf_lo(bres[d]), s1[d] + bf_hi(xres[d]) + bf_hi(bres[d]));
  *(GAS u32x4*)(out + ooff) = o;
  STAMP(9);
  STAMP_OUT();
}

__global__ __launch_bounds__(512, 2) void fused(const char* __restrict__ x_, const char* __restrict__ wq_, const char* __restrict__ wo_,
                                                const char* __restrict__ w1_, const u16* __restrict__ b1_, const char* __restrict__ w2_,
                                                const char* __restrict__ b2_, char* __restrict__ out_, char* __restrict__ ws_) {
  extern __shared__ __attribute__((aligned(16))) char smem[];
  LAS char* L = (LAS char*)smem;
  STAMP(0);
  typedef u64 u64x8 __attribute__((ext_vector_type(8)));
  u64x8 ka; u64 kb;
  asm volatile("s_load_dwordx16 %0, %2, 0x0\n\ts_load_dwordx2 %1, %2, 0x40\n\ts_waitcnt lgkmcnt(0)" : "=&s"(ka), "=&s"(kb) : "s"(__builtin_amdgcn_kernarg_segment_ptr()) : "memory");
  const char* x = (const char*)(GAS const char*)ka[0]; const char* wq = (const char*)(GAS const char*)ka[1]; const char* wo = (const char*)(GAS const char*)ka[2];
  const char* w1 = (const char*)(GAS const char*)ka[3]; const u16* b1 = (const u16*)(GAS const u16*)ka[4]; const char* w2 = (const char*)(GAS const char*)ka[5];
  const char* b2 = (const char*)(GAS const char*)ka[6]; char* out = (char*)(GAS char*)ka[7]; char* ws = (char*)(GAS char*)kb;
  STAMP(10);
  const int lane = threadIdx.x & 63, w = __builtin_amdgcn_readfirstlane(threadIdx.x >> 6);
  u64 code0;
  asm volatile("s_getpc_b64 %0" : "=s"(code0));
  code0 &= ~127ull;
  const int b = blockIdx.x, bi = b >> 3, g = (b & 3) * 4 + (bi & 3), j = ((bi >> 2) & 3) + ((b >> 2) & 1) * 4 + (bi >> 4) * 8;
  if (j < 8) {
    if (w < 4) body<0>(L, x, wq, wo, w1, b1, w2, b2, out, ws, code0, g, j, w, lane STAMP_PASS);
    else body<1>(L, x, wq, wo, w1, b1, w2, b2, out, ws, code0, g, j, w, lane STAMP_PASS);
  } else {
    if (w < 4) body<2>(L, x, wq, wo, w1, b1, w2, b2, out, ws, code0, g, j, w, lane STAMP_PASS);
    else body<3>(L, x, wq, wo, w1, b1, w2, b2, out, ws, code0, g, j, w, lane STAMP_PASS);
  }
}
}

extern "C" void kernel_launch(void* const* d_in, const int* in_sizes, int n_in, void* d_out, int out_size, void* d_ws,
                              size_t ws_size, hipStream_t stream) {
  (void)hipFuncSetAttribute((const void*)fk::fused, hipFuncAttributeMaxDynamicSharedMemorySize, fk::L_TOTAL);
  fk::fused<<<256, 512, fk::L_TOTAL, stream>>>((const char*)d_in[0], (const char*)d_in[1], (const char*)d_in[2], (const char*)d_in[4],
                                               (const u16*)d_in[5], (const char*)d_in[6], (const char*)d_in[7], (char*)d_out,
                                               (char*)d_ws);
}
```

```cpp
#include <hip/hip_runtime.h>
#include <stdint.h>

typedef unsigned u32;
typedef unsigned long long u64;
typedef unsigned short u16;
typedef short s16x4 __attribute__((ext_vector_type(4)));
typedef short s16x8 __attribute__((ext_vector_type(8)));
typedef __bf16 b16x2 __attribute__((ext_vector_type(2)));
typedef float f32x2 __attribute__((ext_vector_type(2)));
typedef float f32x4 __attribute__((ext_vector_type(4)));
typedef float f32x16 __attribute__((ext_vector_type(16)));
typedef u32 u32x2 __attribute__((ext_vector_type(2)));
typedef u32 u32x4 __attribute__((ext_vector_type(4)));
#define LAS __attribute__((address_space(3)))
#define GAS __attribute__((address_space(1)))
#define DEV __device__ __forceinline__
#define INL __attribute__((always_inline))

namespace fk {
constexpr int LDX = 1024, LDQKV = 3072, LDW1 = 2752, LDW2 = 1024;
constexpr int NKS = 118;
constexpr int SLOT = 43008, BOFF = 18432;
constexpr int L_MEAN = 3 * SLOT, L_RSTD = L_MEAN + 576, L_CS = L_RSTD + 576, L_B1 = L_CS + 768, L_OWN = L_B1 + 768,
              L_KV = L_MEAN + 3584, L_JUNK = L_KV + 8192, L_SP = L_JUNK + 2048, L_TOTAL = L_SP + 9 * 2048;
static_assert(L_OWN + 576 + 32 <= L_KV && L_TOTAL <= 163840, "LDS map");
constexpr int WS_GRP = 8192, WS_STAT = 4096;
constexpr int WS_A2 = 16 * WS_GRP, A2_BYTES = 64 * NKS * 1024;
constexpr int AUX_SC1 = 16;
constexpr u32 SPIN_CAP = 1u << 16;
constexpr u32 CODE_BYTES = 63000;

#define PRIO 0
#define ILV 1
#define ROTM 7
#define PF1 0
#define S2LATE 0
#define KROT 0
#define RSW 0
#define WIN 8
#define NEPI 12

#define STAMP(i) do {} while (0)
#define STAMP_ARG
#define STAMP_PASS
#define STAMP_OUT() do {} while (0)

template <int IMM> DEV void dma16(u32 voff, const void* sbase, u32 lb) {
  asm volatile("s_add_u32 m0, %2, %3\n\ts_nop 0\n\tglobal_load_lds_dwordx4 %0, %1" ::"v"(voff), "s"(sbase), "s"(lb), "i"(IMM) : "memory", "scc");
}
template <int IMM> DEV void dma16_masked(u32 voff, const void* sbase, u32 lb, u32 mlo) {
  asm volatile("s_add_u32 m0, %2, %4\n\ts_mov_b32 exec_lo, %3\n\tglobal_load_lds_dwordx4 %0, %1\n\ts_mov_b32 exec_lo, -1"
               ::"v"(voff), "s"(sbase), "s"(lb), "s"(mlo), "i"(IMM) : "memory", "scc");
}
template <int IMM> DEV void dma16_if(u32 voff, const void* sbase, u32 lb, u32 cond) {
  asm volatile("s_cmp_gt_u32 %3, 1\n\ts_cbranch_scc1 1f\n\ts_add_u32 m0, %2, %4\n\ts_nop 0\n\tglobal_load_lds_dwordx4 %0, %1\n1:"
               ::"v"(voff), "s"(sbase), "s"(lb), "s"(cond), "i"(IMM) : "memory", "scc");
}
DEV void touch(const void* p) {
  asm volatile("s_mov_b32 m0, %1\n\ts_nop 0\n\tglobal_load_lds_dword %0, off sc1" ::"v"(p), "i"(L_JUNK) : "memory");
}
template <int N> DEV void wait_bar() {
  asm volatile("s_waitcnt vmcnt(%0) lgkmcnt(0)\n\ts_barrier" ::"i"(N) : "memory");
}
template <int N> DEV void wait_vm() { asm volatile("s_waitcnt vmcnt(%0)" ::"i"(N) : "memory"); }
DEV void wait_lds() { asm volatile("s_waitcnt lgkmcnt(0)" ::: "memory"); }
DEV void lds_bar() { asm volatile("s_waitcnt lgkmcnt(0)\n\ts_barrier" ::: "memory"); }
#define SB() __builtin_amdgcn_sched_barrier(0)
#define PIN(x) asm volatile("" : "+v"(x))

DEV u32 f2u(float v) { return __builtin_bit_cast(u32, v); }
DEV float u2f(u32 v) { return __builtin_bit_cast(float, v); }
DEV u32 pk_bf16(float a, float b) {
  f32x2 p = {a, b};
  return __builtin_bit_cast(u32, __builtin_convertvector(p, b16x2));
}
DEV u32x2 pk4(f32x4 v) { return u32x2{pk_bf16(v[0], v[1]), pk_bf16(v[2], v[3])}; }
DEV float bf_lo(u32 v) { return u2f(v << 16); }
DEV float bf_hi(u32 v) { return u2f(v & 0xffff0000u); }
DEV float dot2(u32 a, u32 b, float c) {
  return __builtin_amdgcn_fdot2_f32_bf16(__builtin_bit_cast(b16x2, a), __builtin_bit_cast(b16x2, b), c, false);
}
DEV f32x4 mfma16(u32x4 a, u32x4 b, f32x4 c) {
  return __builtin_amdgcn_mfma_f32_16x16x32_bf16(__builtin_bit_cast(s16x8, a), __builtin_bit_cast(s16x8, b), c, 0, 0, 0);
}
DEV f32x4 mfma16k16(u32x2 a, u32x2 b, f32x4 c) {
  return __builtin_amdgcn_mfma_f32_16x16x16bf16_1k(__builtin_bit_cast(s16x4, a), __builtin_bit_cast(s16x4, b), c, 0, 0, 0);
}
DEV f32x16 mfma32(u32x4 a, u32x4 b, f32x16 c) {
  return __builtin_amdgcn_mfma_f32_32x32x16_bf16(__builtin_bit_cast(s16x8, a), __builtin_bit_cast(s16x8, b), c, 0, 0, 0);
}
DEV u32x4 lds128(LAS char* L, u32 off) { return *(LAS u32x4*)(L + off); }
DEV f32x4 ldsf4(LAS char* L, u32 off) { return *(LAS f32x4*)(L + off); }
DEV float ldsf(LAS char* L, u32 off) { return *(LAS float*)(L + off); }
DEV u32x2 ldstr(LAS char* L, u32 off) {
  return __builtin_bit_cast(u32x2, __builtin_amdgcn_ds_read_tr16_b64_v4i16((LAS s16x4*)(L + off)));
}
template <int CTRL> DEV float dpp_add(float v) {
  int t = __builtin_amdgcn_update_dpp(0, (int)f2u(v), CTRL, 0xf, 0xf, true);
  return v + u2f((u32)t);
}
DEV f32x2 swap16(float a, float b) {
  auto q = __builtin_amdgcn_permlane16_swap(f2u(a), f2u(b), false, false);
  return f32x2{u2f(q[0]), u2f(q[1])};
}
DEV f32x2 swap32(float a, float b) {
  auto q = __builtin_amdgcn_permlane32_swap(f2u(a), f2u(b), false, false);
  return f32x2{u2f(q[0]), u2f(q[1])};
}
DEV float vmax(float a, float b) { float r; asm("v_max_f32 %0, %1, %2" : "=v"(r) : "v"(a), "v"(b)); return r; }
DEV float vmax3(float a, float b, float c) { float r; asm("v_max3_f32 %0, %1, %2, %3" : "=v"(r) : "v"(a), "v"(b), "v"(c)); return r; }
DEV float sel(bool c, float a, float b) { return c ? a : b; }
DEV u32x4 pair_rows(u32x2 x, u32x2 y) {
  auto a = __builtin_amdgcn_permlane16_swap(x[0], y[0], false, false);
  auto b = __builtin_amdgcn_permlane16_swap(x[1], y[1], false, false);
  return u32x4{a[0], b[0], a[1], b[1]};
}
DEV f32x4 splat4(float v) {
  f32x2 p = {v, v};
  asm("" : "+v"(p));
  return f32x4{p[0], p[1], p[0], p[1]};
}
DEV int fresh_tid() {
  int t = threadIdx.x;
  asm volatile("" : "+v"(t));
  return t;
}
DEV void st128(u32x4 v, __amdgpu_buffer_rsrc_t rs, u32 voff, u32 soff) {
  __builtin_amdgcn_raw_buffer_store_b128(v, rs, voff, soff, AUX_SC1);
  asm volatile("s_nop 1" ::"v"(v));
}
template <int V> struct IC { static constexpr int v = V; };
template <int LO, int HI, class F> DEV void sfor(F&& f) {
  if constexpr (LO < HI) { f(IC<LO>{}); sfor<LO + 1, HI>(f); }
}

struct Ptrs { const char *x, *wq, *wo, *w1; const u16* b1; const char *w2, *b2; char *out, *ws; };
struct Ring {
  u32 voffA, voffB, voffB2, stepB, mlo, xcond, wb;
  int rot, ldw, maxch;
  const char *sA0, *sA1, *sB0, *sB1, *sB2;
  DEV void lanes(int lane) {
    const int r8 = lane >> 3, pos = lane & 7;
    const int swB = (r8 & 2) | ((wb >> 8) & 4);
    voffA = r8 * LDX + ((pos ^ r8) << 4);
    voffB = r8 * ldw + ((pos ^ swB) << 4);
    voffB2 = r8 * ldw + (min(pos ^ swB, maxch) << 4);
  }
  template <int I, int K> DEV void issue() const {
    const int kc = (I + rot) & 7;
    constexpr int slot = (I % 3) * SLOT;
    if constexpr (K == 0) dma16_masked<slot>(voffA + kc * 128, sA0, wb, mlo);
    if constexpr (K == 1) dma16<slot + BOFF>(voffB + kc * stepB, sB0, wb);
    if constexpr (K == 2) dma16<slot + BOFF + 8192>(voffB + kc * stepB, sB1, wb);
    if constexpr (K == 3) dma16<slot + BOFF + 16384>(voffB2 + kc * stepB, sB2, wb);
    if constexpr (K == 4) dma16<slot + 8192>(voffA + kc * 128, sA1, wb);
    if constexpr (K == 5) dma16_if<slot + 12288>(voffA + kc * 128 + 32 * LDX, sA1, wb, xcond);
  }
  template <int I, int WANT> DEV void issue5_if(u32 me) const {
    const int kc = (I + rot) & 7;
    constexpr int slot = (I % 3) * SLOT;
    asm volatile("s_cmp_lg_u32 %10, %11\n\ts_cbranch_scc1 1f\n\t"
                 "s_add_u32 m0, %8, %12\n\ts_mov_b32 exec_lo, %9\n\tglobal_load_lds_dwordx4 %0, %3\n\ts_mov_b32 exec_lo, -1\n\t"
                 "s_add_u32 m0, %8, %13\n\ts_nop 0\n\tglobal_load_lds_dwordx4 %1, %4\n\t"
                 "s_add_u32 m0, %8, %14\n\ts_nop 0\n\tglobal_load_lds_dwordx4 %1, %5\n\t"
                 "s_add_u32 m0, %8, %15\n\ts_nop 0\n\tglobal_load_lds_dwordx4 %2, %6\n\t"
                 "s_add_u32 m0, %8, %16\n\ts_nop 0\n\tglobal_load_lds_dwordx4 %0, %7\n1:"
                 ::"v"(voffA + kc * 128), "v"(voffB + kc * stepB), "v"(voffB2 + kc * stepB), "s"(sA0), "s"(sB0), "s"(sB1), "s"(sB2),
                   "s"(sA1), "s"(wb), "s"(mlo), "s"(me), "i"(WANT), "i"(slot), "i"(slot + BOFF), "i"(slot + BOFF + 8192),
                   "i"(slot + BOFF + 16384), "i"(slot + 8192) : "memory", "scc");
  }
};

template <int TYPE>
DEV void body(LAS char* L, const Ptrs P, Ring R, int g, int j, int w STAMP_ARG) {
  constexpr bool FFN = TYPE >= 2;
  constexpr int ODD = TYPE & 1;
  constexpr int NT = TYPE == 0 ? 5 : (TYPE == 1 ? 4 : 2);
  constexpr int NC = TYPE == 2 ? 6 : (TYPE == 3 ? 5 : 3);
  constexpr int CB = TYPE == 3 ? 6 : 0;
  constexpr int ND = TYPE == 1 ? 6 : 5;
  constexpr int NM = NT * NC + (FFN ? 2 : 0);
  constexpr int NRL = FFN ? 2 : 4;
  int tid = fresh_tid();
  int lane = tid & 63;
  R.lanes(lane);
  int lg = lane >> 4, lc = lane & 15;
  GAS u64* ctl = (GAS u64*)(P.ws + g * WS_GRP);
  u32x4 r0, r1, r2;
  u64 epoch;
  if (FFN)
    asm volatile("global_load_dwordx2 %0, %2, %3 sc1\n\tglobal_load_dwordx4 %1, %4, %5"
                 : "=&v"(epoch), "=&v"(r0) : "v"(0u), "s"(ctl), "v"(lane * 16), "s"(P.x + (size_t)(128 * g + 8 * j + w) * LDX) : "memory");
  else
    asm volatile("global_load_dwordx2 %0, %4, %5 sc1\n\tglobal_load_dwordx4 %1, %6, %7\n\t"
                 "global_load_dwordx4 %2, %6, %8\n\tglobal_load_dwordx4 %3, %6, %8 offset:1024"
                 : "=&v"(epoch), "=&v"(r0), "=&v"(r1), "=&v"(r2)
                 : "v"(0u), "s"(ctl), "v"(lane * 16), "s"(P.x + (size_t)(128 * g + 8 * j + w) * LDX),
                   "s"(P.x + (size_t)max(128 * g - 16 + 2 * w, 0) * LDX) : "memory");
  if (PF1 && !ODD) {
    const int n = min((int)blockIdx.x * 155 + w * 39 + min(lane, 38), 39679);
    const u64 p = n < 16384 ? (u64)P.x + (u32)(n * 128) : (n < 28672 ? (u64)P.wq + (u32)((n - 16384) * 128) : (u64)P.w1 + (u32)((n - 28672) * 128));
    touch((const void*)p);
  }
  const int T0 = TYPE == 0 ? 0 : (TYPE == 1 ? 5 : 2 * (w & 3));
  const u32 aA = T0 * 2048 + (lc >> 3) * 1024 + (lc & 7) * 128 + ((lg ^ (lc & 7)) << 4);
  const int tq = lc >> 2, tp = lc & 3;
  const int swR = (tq & 2) | ((lg & 1) << 2);
  const u32 aBl = BOFF + lg * 1024 + tq * 128 + ((tp >> 1) << 4) + (tp & 1) * 8;
  const int crot = min(2 * (w & 3), NC - 2);
  constexpr int NBB = FFN ? NC : 1;
  u32 aB[NBB][2], aAb[2][2];
#pragma unroll
  for (int c = 0; c < NBB; ++c) {
    const int cc = c + crot >= NC ? c + crot - NC : c + crot;
    const int ct = FFN ? CB + cc : (w & 3);
    aB[c][0] = aBl + (ct >> 2) * 8192 + (((2 * (ct & 3)) ^ swR) << 4);
    aB[c][1] = aB[c][0] + 65536;
    asm("" : "+v"(aB[c][0]), "+v"(aB[c][1]));
  }
#pragma unroll
  for (int b = 0; b < 2; ++b) {
    aAb[b][0] = aA ^ (b * 64);
    aAb[b][1] = aAb[b][0] + 65536;
    asm("" : "+v"(aAb[b][0]), "+v"(aAb[b][1]));
  }
  SB();
  if (ODD) sfor<0, ND>([&](auto kc) INL { R.template issue<1, decltype(kc)::v>(); });
  f32x4 acc[NT][NC], acc1[2];
#pragma unroll
  for (int t = 0; t < NT; ++t)
#pragma unroll
    for (int c = 0; c < NC; ++c) acc[t][c] = f32x4{0.f, 0.f, 0.f, 0.f};
  acc1[0] = acc1[1] = f32x4{0.f, 0.f, 0.f, 0.f};
  u32x4 fa[2][NT];
  u32x2 fb[2][NC][2];
  auto rd = [&](int t, int k) INL {
    const int slot = ((t >> 1) % 3) * SLOT, b = t & 1;
    if (k >= 2 && k < 2 + NT) {
      const int o = slot + (k - 2) * 2048, hi = o >= 65536;
      fa[b][k - 2] = lds128(L, aAb[b][hi] + (o - hi * 65536));
    } else {
      const int kk = k < 2 ? k : k - NT, c = kk >> 1;
      const int o = slot + b * 4096 + (kk & 1) * 512 + (FFN ? 0 : c * 8192), hi = o >= 65536;
      fb[b][c][kk & 1] = ldstr(L, aB[FFN ? c : 0][hi] + (o - hi * 65536));
    }
  };
  if (PRIO == 1 && ODD) __builtin_amdgcn_s_setprio(1);
  if (PRIO == 2 && !ODD) __builtin_amdgcn_s_setprio(1);
  STAMP(1);
  wait_bar<5 + NRL + (PF1 && !ODD)>();
  STAMP(2);
#pragma unroll
  for (int k = 0; k < 2 * NC + NT; ++k) rd(0, k);
  SB();
  const int mt = 2 * g + ((j >> 2) & 1), nt = (j & 3) + (j >> 3) * 4;
  const int ks0 = w < 6 ? 15 * w : 90 + 14 * (w - 6), nks = w < 6 ? 15 : 14;
  const int krot = KROT ? (2 * nt + 4 * (g & 3)) % 15 : 0;
  auto kidx = [&](int s) INL -> int { return s + krot >= 15 ? s + krot - 15 : s + krot; };
  const u32 voffW = (lane >> 3) * LDW2 + (((lane & 7) ^ ((lane >> 2) & 4)) << 4) + nt * 128;
  const u64 woa = (u64)P.wo, w2a = (u64)P.w2 - 32 * 16 * LDW2;
  const u32 wx0 = w < 5 ? 2 * SLOT + (16 + w) * 2048 : (11 + w) * 2048;
  const u32 wx1 = w < 5 ? SLOT + (16 + w) * 2048 : (w < 7 ? (14 + w) * 2048 : L_SP + 8 * 2048);
  auto wbase = [&](int s) INL -> u32 { const int k = s % 9; return k == 0 ? 2 * R.wb : (k == 5 ? wx0 : (k == 8 ? wx1 : 4 * R.wb)); };
  auto wdma = [&](auto sc_, auto hc_) INL {
    constexpr int s = decltype(sc_)::v, h = decltype(hc_)::v, k = s % 9;
    constexpr int imm = k == 0 ? L_SP : (k <= 2 ? 2 * SLOT + (k - 1) * 2048 : (k <= 4 ? (k - 3) * 2048 : (k == 6 || k == 7 ? SLOT + (k - 6) * 2048 : 0)));
    const int ks = min(ks0 + kidx(s), NKS - 1);
    const u64 base = (ks < 32 ? woa : w2a) + (u32)(ks * 16 * LDW2);
    dma16<imm + h * 1024>(voffW + h * 8 * LDW2, (const void*)base, wbase(s));
  };
  u32x4 wf[15][2];
  const u32 tb = (lane >> 5) * 1024 + tq * 128 + (((2 * (lg & 1) + (tp >> 1)) ^ ((tq & 2) << 1)) << 4) + (tp & 1) * 8;
  auto wread = [&](int s) INL {
    const int k = s % 9;
    const int imm = k == 0 ? L_SP : (k <= 2 ? 2 * SLOT + (k - 1) * 2048 : (k <= 4 ? (k - 3) * 2048 : (k == 6 || k == 7 ? SLOT + (k - 6) * 2048 : 0)));
#pragma unroll
    for (int f = 0; f < 2; ++f) {
      const u32 a = (tb ^ (f * 64)) + wbase(s) + imm;
      const u32x2 lo = ldstr(L, a), hi = ldstr(L, a + 512);
      wf[s][f] = u32x4{lo[0], lo[1], hi[0], hi[1]};
    }
  };
  const __amdgpu_buffer_rsrc_t rsA2 = __builtin_amdgcn_make_buffer_rsrc(P.ws + WS_A2, 0, A2_BYTES, 0x00020000);
  const __amdgpu_buffer_rsrc_t rsG = __builtin_amdgcn_make_buffer_rsrc(P.ws + g * WS_GRP, 0, WS_GRP, 0x00020000);
  const u32x4 ones = {0x3F803F80u, 0x3F803F80u, 0x3F803F80u, 0x3F803F80u};
  u64 tag = 0;
  u32 b1v = 0;
  u32x4 gst;
  u64 tp2 = 0;
  u32 gsto = 0, gldo = 0, b1o = 0, stw = 0, b1w = 0, csw = 0, csw2 = 0;
  float som = 0.f, sor = 0.f;
  constexpr int NK = 5, TK = TYPE == 0 ? 0 : 4;
  float mc1[NK], rc1[NK];
  f32x4 m4[NK], r4[NK], bb[NC];
  auto rdst = [&](int k) INL {
    if (FFN) {
      if (k < 2) mc1[k] = ldsf(L, L_MEAN + ((2 * (w & 3) + k) * 16 + lc) * 4);
      else if (k < 4) rc1[k - 2] = ldsf(L, L_RSTD + ((2 * (w & 3) + k - 2) * 16 + lc) * 4);
      else if (k < 4 + NC) {
        const int c = k - 4, ct = CB + (c + crot >= NC ? c + crot - NC : c + crot);
        bb[c] = ldsf4(L, L_B1 + (ct * 16 + lg * 4) * 4);
      }
    } else {
      const int i = k >> 2, T = TK + i;
      if (i >= NK) return;
      if ((k & 3) == 0) mc1[i] = ldsf(L, L_MEAN + (T * 16 + lc) * 4);
      if ((k & 3) == 1) rc1[i] = ldsf(L, L_RSTD + (T * 16 + lc) * 4);
      if ((k & 3) == 2) m4[i] = ldsf4(L, L_MEAN + (T * 16 + lg * 4) * 4);
      if ((k & 3) == 3) r4[i] = ldsf4(L, L_RSTD + (T * 16 + lg * 4) * 4);
    }
  };
  sfor<0, 16>([&](auto tc) INL {
    constexpr int t = decltype(tc)::v, s = t >> 1, b = t & 1;
    if constexpr (b) {
      if constexpr (s == 1 || s == 2) wait_bar<6 + ODD>();
      else if constexpr (s == 0) wait_bar<5 + (PF1 && !ODD)>();
      else if constexpr (s <= 4) wait_bar<5>();
      else if constexpr (s == 5) wait_bar<7>();
      else if constexpr (s == 6) wait_bar<4>();
    }
    if constexpr (t == 11) {
      if constexpr (!ODD) asm volatile("" : "+v"(gst), "+v"(b1v));
      if constexpr (!ODD) {
        if (__builtin_expect(!__all(gst[1] == (u32)tag && gst[3] == (u32)tag), 0)) {
#pragma nounroll
          for (u32 spins = 0; spins < SPIN_CAP; ++spins) {
            __builtin_amdgcn_s_sleep(1);
            gst = __builtin_amdgcn_raw_buffer_load_b128(rsG, gldo, 0, AUX_SC1);
            if (__all(gst[1] == (u32)tag && gst[3] == (u32)tag)) break;
          }
        }
      }
      SB();
    }
    constexpr int nr = t == 15 ? 0 : 2 * NC + NT;
    constexpr int st = t == 0 ? 2 : (t + 5) / 2;
    constexpr int k0 = (t == 0 || b) ? 0 : 3, k1 = (st >= 8 || (t == 0 && S2LATE)) ? 0 : ((t == 0 || !b) ? ND : 3);
    constexpr int nd = k1 > k0 ? k1 - k0 : 0, nw = t >= 10 ? 2 : 0, nv = nd + nw;
    sfor<0, NM>([&](auto qc) INL {
      constexpr int q = decltype(qc)::v;
      constexpr int qq = t < 15 ? q : (FFN ? (q + NT * NC) % NM : (TYPE == 1 ? q : (q < 3 ? q * NT : (q < 5 ? (q - 2) * NT + 4 :
                         (q - 5 < 4 ? q - 4 : (q - 5 < 7 ? q - 3 : q - 1))))));
      if constexpr (qq < NT * NC) {
        constexpr int c = qq / NT, i = qq % NT;
        const u32x4 wfr = {fb[b][c][0][0], fb[b][c][0][1], fb[b][c][1][0], fb[b][c][1][1]};
        if constexpr (!FFN && c == 2) acc[i][c] = mfma16(fa[b][i], wfr, acc[i][c]);
        else acc[i][c] = mfma16(wfr, fa[b][i], acc[i][c]);
      } else {
        constexpr int c = qq - NT * NC;
        const u32x4 wfr = {fb[b][c][0][0], fb[b][c][0][1], fb[b][c][1][0], fb[b][c][1][1]};
        acc1[c] = mfma16(wfr, ones, acc1[c]);
      }
      if constexpr (q < nr) rd(t + 1, q);
      if constexpr (t == 15) { rdst(2 * q); rdst(2 * q + 1); }
      if constexpr (t == 9 && q == 0 && !ODD)
        asm volatile("buffer_load_dwordx4 %0, %2, %3, 0 offen sc1\n\tglobal_load_ushort %1, %4, %5"
                     : "=&v"(gst), "=&v"(b1v) : "v"(gldo), "s"(rsG), "v"(b1o), "s"(P.b1) : "memory");
      if constexpr (t == 11 && !ODD) {
        if constexpr (q == NM - 4) *(LAS u32*)(L + stw) = gst[0];
        if constexpr (q == NM - 3) *(LAS u32*)(L + stw + 576) = gst[2];
        if constexpr (q == NM - 2 && FFN) *(LAS float*)(L + b1w) = bf_lo(b1v);
      }
      if constexpr (t == 1 && ODD && q == NM - 1) { som = ldsf(L, L_OWN + (lane & 7) * 4); sor = ldsf(L, L_OWN + 576 + (lane & 7) * 4); }
      sfor<0, nv>([&](auto uc) INL {
        constexpr int u = decltype(uc)::v;
        if constexpr (((2 * u + ODD) * NM) / (2 * nv) == q) {
          if constexpr (u < nd) R.template issue<st, k0 + u>();
          else wdma(IC<t - 10>{}, IC<u - nd>{});
        }
      });
      if constexpr (t == 2 && q == NM - 1) {
        touch((const void*)tp2);
        if (ODD) st128(u32x4{f2u(som), (u32)tag, f2u(sor), (u32)tag}, rsG, gsto, 0);
      }
      if constexpr (t == 15) {
        if constexpr (TYPE == 0) {
          if constexpr (q == 6) *(LAS f32x4*)(L + csw) = acc[0][0];
          if constexpr (q == 7) *(LAS f32x4*)(L + csw + 256) = acc[0][1];
          if constexpr (q == 8) *(LAS float*)(L + csw2) = acc[0][2][0];
          if constexpr (q == 9) *(LAS f32x4*)(L + L_KV + 2 * R.wb + lane * 16) = acc[NT - 1][1];
          if constexpr (q == 10) *(LAS f32x4*)(L + L_KV + 2 * R.wb + 1024 + lane * 16) = acc[NT - 1][2];
        }
        if constexpr (FFN) {
          if constexpr (q == 6) *(LAS f32x4*)(L + csw) = acc1[0];
          if constexpr (q == 7) *(LAS f32x4*)(L + csw + 64) = acc1[1];
        }
      }
      SB();
    });
    if constexpr (t == 0 && S2LATE) sfor<0, ND>([&](auto kc) INL { R.template issue<2, decltype(kc)::v>(); });
    if constexpr (t == 0) {
      if (FFN) asm volatile("s_waitcnt vmcnt(%2)" : "+v"(epoch), "+v"(r0) : "i"(ODD ? 10 : 5 + PF1) : "memory");
      else asm volatile("s_waitcnt vmcnt(%4)" : "+v"(epoch), "+v"(r0), "+v"(r1), "+v"(r2) : "i"(ODD ? 10 : 5 + PF1) : "memory");
      tag = (((u64)(u32)__builtin_amdgcn_readfirstlane((u32)(epoch >> 32)) << 32) | (u32)__builtin_amdgcn_readfirstlane((u32)epoch)) + 1;
      float sS[3] = {0.f, 0.f, 0.f}, sQ[3] = {0.f, 0.f, 0.f};
#pragma unroll
      for (int d = 0; d < 4; ++d) {
        sS[0] = dot2(r0[d], 0x3F803F80u, sS[0]); sQ[0] = dot2(r0[d], r0[d], sQ[0]);
        if (!FFN) {
          sS[1] = dot2(r1[d], 0x3F803F80u, sS[1]); sQ[1] = dot2(r1[d], r1[d], sQ[1]);
          sS[2] = dot2(r2[d], 0x3F803F80u, sS[2]); sQ[2] = dot2(r2[d], r2[d], sQ[2]);
        }
      }
      f32x2 a = swap32(sS[0], sS[FFN ? 0 : 1]), c = swap32(sQ[0], sQ[FFN ? 0 : 1]);
      float S = a[0] + a[1], Q = c[0] + c[1], S2 = S, Q2 = Q;
      if (!FFN) {
        a = swap32(sS[2], sS[2]); c = swap32(sQ[2], sQ[2]);
        S2 = a[0] + a[1]; Q2 = c[0] + c[1];
      }
      a = swap16(S, S2); c = swap16(Q, Q2);
      S = a[0] + a[1]; Q = c[0] + c[1];
      S = dpp_add<0xB1>(S); Q = dpp_add<0xB1>(Q);
      S = dpp_add<0x4E>(S); Q = dpp_add<0x4E>(Q);
      S = dpp_add<0x141>(S); Q = dpp_add<0x141>(Q);
      S = dpp_add<0x140>(S); Q = dpp_add<0x140>(Q);
      const float m = S * (1.f / 512.f), e = Q * (1.f / 512.f);
      u32 adr = L_JUNK + lane * 4;
      adr = lane == 0 ? L_OWN + w * 4 : adr;
      if (!FFN) { adr = lane == 32 ? L_MEAN + w * 8 : adr; adr = lane == 16 ? L_MEAN + w * 8 + 4 : adr; }
      *(LAS float*)(L + adr) = m;
      *(LAS float*)(L + adr + 576) = __builtin_amdgcn_rsqf(fmaxf(e - m * m, 0.f) + 1e-5f);
      {
        const int b_ = blockIdx.x, ln = min(b_ * 59 + w * 8 + (lane & 7), 15103);
        tp2 = (ln < 4096 ? woa : w2a) + (u32)(ln * 128);
        const u64 pa = (u64)P.ws + (u32)(WS_A2 + (lane & 3) * (2u << 20) + (b_ & 63) * 4096), po = (u64)P.out + (u32)(b_ * 8192);
        tp2 = (lane >= 8 && lane < 12) ? pa : tp2;
        tp2 = lane == 12 ? po : tp2;
        gsto = (w == 7 && lane < 8) ? WS_STAT + (j * 8 + lane) * 16 : WS_GRP;
        gldo = WS_STAT + (tid & 127) * 16;
        b1o = 2 * (u32)min(max(j - 8, 0) * 176 + tid, 1375);
        stw = (!ODD && w < 2) ? L_MEAN + (tid + (FFN ? 0 : 16)) * 4 : L_JUNK + lane * 4;
        b1w = tid < 192 ? L_B1 + tid * 4 : L_JUNK + lane * 4;
        csw = lc == 0 ? L_CS + ((FFN ? CB + crot : (w & 3)) * 16 + lg * 4) * 4 : L_JUNK + lane * 16;
        csw2 = lg == 0 ? L_CS + (128 + (w & 3) * 16 + lc) * 4 : L_JUNK + lane * 4;
        asm volatile("" : "+v"(tp2), "+v"(gsto), "+v"(gldo), "+v"(b1o), "+v"(stw), "+v"(b1w), "+v"(csw), "+v"(csw2));
      }
      SB();
    }
  });
  if (PRIO) __builtin_amdgcn_s_setprio(0);
  STAMP(3);
  lds_bar();
  STAMP(4);
  auto slot = [&](auto kc) INL {
    constexpr int k = decltype(kc)::v;
    SB();
    if constexpr (k < NEPI) wdma(IC<6 + k / 2>{}, IC<k % 2>{});
    SB();
  };
  if constexpr (!FFN) {
    const int h = w & 3;
    const f32x4 csq = ldsf4(L, L_CS + (h * 16 + lg * 4) * 4), csk = ldsf4(L, L_CS + (64 + h * 16 + lg * 4) * 4);
    const float csv1 = ldsf(L, L_CS + (128 + h * 16 + lc) * 4);
    f32x4 k4 = acc[0][1], v4 = acc[0][2];
    if (TYPE == 1) { k4 = ldsf4(L, L_KV + h * 2048 + lane * 16); v4 = ldsf4(L, L_KV + h * 2048 + 1024 + lane * 16); }
    slot(IC<0>{});
    wait_vm<7>();
    wread(0); wread(1); wread(2);
    f32x4 mP, mC;
#pragma unroll
    for (int r = 0; r < 4; ++r) {
      mP[r] = (4 * lg + r >= lc + 2) ? 0.f : -1e30f;
      mC[r] = (4 * lg + r <= lc) ? 0.f : -1e30f;
    }
    f32x4 mP0 = mP;
    if (TYPE == 0 && g == 0) mP0 = f32x4{-1e30f, -1e30f, -1e30f, -1e30f};
    const bool odd = lc & 1;
    const u32 sel0 = odd ? 0x01000c0cu : 0x0c0c0100u, sel1 = odd ? 0x03020c0cu : 0x0c0c0302u;
    const f32x4 csv = splat4(csv1);
    u32x2 Qb[4], Kb[NK], Vb[NK];
    sfor<0, NK>([&](auto ic) INL {
      constexpr int i = decltype(ic)::v, ai = TYPE == 0 ? i : (i > 0 ? i - 1 : 0);
      slot(IC<1 + i>{});
      const f32x4 mc = splat4(mc1[i]);
      Kb[i] = pk4(((i == 0 ? k4 : acc[ai][1]) - mc * csk) * splat4(rc1[i]));
      if constexpr (i > 0) Qb[i - 1] = pk4((acc[ai][0] - mc * csq) * splat4(rc1[i] * (0.25f * 1.4426950408889634f)));
      Vb[i] = pk4(((i == 0 ? v4 : acc[ai][2]) - m4[i] * csv) * r4[i]);
    });
    f32x4 sp[4], sc[4];
#pragma unroll
    for (int q = 0; q < 4; ++q) {
      sp[q] = mfma16k16(Kb[q], Qb[q], q == 0 ? mP0 : mP);
      sc[q] = mfma16k16(Kb[q + 1], Qb[q], mC);
    }
    SB();
    wait_lds();
    wait_vm<6>();
    wread(3); wread(4); wread(5);
    slot(IC<6>{});
    f32x4 a[4];
    float mx[4];
#pragma unroll
    for (int q = 0; q < 4; ++q) {
      a[q] = f32x4{sel(odd, sp[q][1], sp[q][0]), sel(odd, sp[q][3], sp[q][2]), sel(odd, sc[q][1], sc[q][0]), sel(odd, sc[q][3], sc[q][2])};
      mx[q] = vmax3(a[q][0], a[q][1], vmax(a[q][2], a[q][3]));
    }
    slot(IC<7>{});
    {
      const f32x2 x = swap32(mx[0], mx[1]), y = swap32(mx[2], mx[3]);
      const f32x2 z = swap16(vmax(x[0], x[1]), vmax(y[0], y[1]));
      const float u = vmax(z[0], z[1]);
      const f32x2 p = swap16(u, u);
      const f32x2 p0 = swap32(p[0], p[0]), p1 = swap32(p[1], p[1]);
      mx[0] = p0[0]; mx[1] = p0[1]; mx[2] = p1[0]; mx[3] = p1[1];
    }
    slot(IC<8>{});
#pragma unroll
    for (int q = 0; q < 4; ++q) a[q] -= splat4(mx[q]);
    slot(IC<9>{});
    sfor<0, 4>([&](auto qc) INL {
      constexpr int q = decltype(qc)::v;
#pragma unroll
      for (int r = 0; r < 4; ++r) a[q][r] = __builtin_amdgcn_exp2f(a[q][r]);
      if constexpr (q & 1) slot(IC<10 + q / 2>{});
    });
    wait_lds();
    const f32x4 z4 = {0.f, 0.f, 0.f, 0.f};
    sfor<0, 4>([&](auto qc) INL {
      constexpr int q = decltype(qc)::v;
      const u32x2 u = pk4(a[q]);
      const u32x4 pb = {__builtin_amdgcn_perm(0, u[0], sel0), __builtin_amdgcn_perm(0, u[0], sel1),
                        __builtin_amdgcn_perm(0, u[1], sel0), __builtin_amdgcn_perm(0, u[1], sel1)};
      const u32x4 vf = {Vb[q][0], Vb[q][1], Vb[q + 1][0], Vb[q + 1][1]};
      sp[q] = mfma16(vf, pb, z4);
      sc[q] = mfma16(ones, pb, z4);
      slot(IC<12 + q>{});
    });
    slot(IC<16>{});
    slot(IC<17>{});
    u32x2 Ob[4];
#pragma unroll
    for (int q = 0; q < 4; ++q) Ob[q] = pk4(sp[q] * splat4(__builtin_amdgcn_rcpf(sc[q][0])));
    const int rt = 4 * g + (TYPE == 0 ? 0 : 2), ks = 4 * j + h;
    st128(pair_rows(Ob[0], Ob[1]), rsA2, lane * 16, (rt * NKS + ks) * 1024);
    st128(pair_rows(Ob[2], Ob[3]), rsA2, lane * 16, ((rt + 1) * NKS + ks) * 1024);
  } else {
    f32x4 cs[NC];
#pragma unroll
    for (int c = 0; c < NC; ++c) {
      const int ct = CB + (c + crot >= NC ? c + crot - NC : c + crot);
      cs[c] = ldsf4(L, L_CS + (ct * 16 + lg * 4) * 4);
    }
    wait_vm<6>();
    wread(0); wread(1); wread(2);
    const f32x4 mc[2] = {splat4(mc1[0]), splat4(mc1[1])}, rc[2] = {splat4(rc1[0]), splat4(rc1[1])};
    const int nct = j == 15 ? 9 : 11;
    const int rt = 4 * g + (w & 3), ksb = 32 + 11 * (j - 8);
    const f32x4 C1 = splat4(-2.f * 1.4426950408889634f * 0.7978845608028654f);
    const f32x4 C2 = splat4(-2.f * 1.4426950408889634f * 0.7978845608028654f * 0.044715f);
    const f32x4 ONE = splat4(1.f);
    sfor<0, 3>([&](auto gc) INL {
      constexpr int G = decltype(gc)::v, c0 = 2 * G, n = c0 + 1 < NC ? 4 : 2;
      if constexpr (G == 1) { wait_lds(); wait_vm<8>(); wread(3); wread(4); wread(5); }
      if constexpr (G == 2) wait_lds();
      f32x4 v[4], e[4];
      slot(IC<6 * G>{});
#pragma unroll
      for (int u = 0; u < n; ++u) v[u] = (acc[u & 1][c0 + (u >> 1)] - mc[u & 1] * cs[c0 + (u >> 1)]) * rc[u & 1] + bb[c0 + (u >> 1)];
      slot(IC<6 * G + 1>{});
#pragma unroll
      for (int u = 0; u < n; ++u) e[u] = v[u] * (v[u] * v[u] * C2 + C1);
      slot(IC<6 * G + 2>{});
#pragma unroll
      for (int u = 0; u < n; ++u) {
#pragma unroll
        for (int r = 0; r < 4; ++r) e[u][r] = __builtin_amdgcn_exp2f(e[u][r]);
        if (u == 1) slot(IC<6 * G + 3>{});
      }
#pragma unroll
      for (int u = 0; u < n; ++u) e[u] += ONE;
      slot(IC<6 * G + 4>{});
#pragma unroll
      for (int u = 0; u < n; ++u) {
#pragma unroll
        for (int r = 0; r < 4; ++r) e[u][r] = __builtin_amdgcn_rcpf(e[u][r]);
        if (u == 1) slot(IC<6 * G + 5>{});
      }
#pragma unroll
      for (int cc = 0; cc < n / 2; ++cc) {
        const int c = c0 + cc, ct = CB + (c + crot >= NC ? c + crot - NC : c + crot);
        st128(pair_rows(pk4(v[2 * cc] * e[2 * cc]), pk4(v[2 * cc + 1] * e[2 * cc + 1])), rsA2, lane * 16,
              ct < nct ? (rt * NKS + ksb + ct) * 1024 : A2_BYTES);
      }
      SB();
    });
  }
  STAMP(5);
  wait_vm<0>();
  STAMP(13);
  asm volatile("s_barrier" ::: "memory");
  if (tid == 0) __hip_atomic_store(ctl + 16 * (j + 1), tag, __ATOMIC_RELAXED, __HIP_MEMORY_SCOPE_AGENT);
  STAMP(6);
  sfor<NEPI, 18>([&](auto kc) INL { wdma(IC<6 + decltype(kc)::v / 2>{}, IC<decltype(kc)::v % 2>{}); });
#pragma unroll
  for (int s = 6; s < 9; ++s) wread(s);
  tid = fresh_tid(); lane = tid & 63; lg = lane >> 4; lc = lane & 15;
  const int rsw = RSW ? nt & 1 : 0;
  const int orow = mt * 64 + (w >> 2) * 16 + lc, ocol = nt * 64 + (w & 3) * 16 + lg * 4;
  const u32 ooff = (u32)(orow * LDX + ocol * 2);
  u32x2 xr0, xr1, bres;
#pragma unroll
  for (int s = 0; s < 9; ++s) asm volatile("" : "+v"(wf[s][0]), "+v"(wf[s][1]));
  STAMP(7);
  {
    const int ke = min(ks0 + 14, NKS - 1);
    const int plo = ks0 < 32 ? ks0 >> 2 : 8 + (ks0 - 32) / 11, phi = ke < 32 ? ke >> 2 : 8 + (ke - 32) / 11;
    const int pj = min(plo + (lane & 7), phi);
#pragma nounroll
    for (u32 spins = 0; spins < SPIN_CAP; ++spins) {
      const u64 v = __hip_atomic_load(ctl + 16 * (pj + 1), __ATOMIC_RELAXED, __HIP_MEMORY_SCOPE_AGENT);
      if (__all(v == tag)) break;
      __builtin_amdgcn_s_sleep(1);
    }
    __builtin_amdgcn_fence(__ATOMIC_ACQUIRE, "wavefront");
  }
  wait_vm<0>();
  STAMP(8);
  u32x4 af[2][15];
  auto lda = [&](int r, int s) INL {
    const int so = (kidx(s) < nks) ? ((2 * mt + (r ^ rsw)) * NKS + ks0 + kidx(s)) * 1024 : A2_BYTES;
    af[r][s] = __builtin_amdgcn_raw_buffer_load_b128(rsA2, lane * 16, so, AUX_SC1);
  };
#pragma unroll
  for (int n = 0; n < WIN; ++n) lda(n / 15, n % 15);
  SB();
  auto poff = [&](int r, int sw, int blk) INL -> u32 {
    return r == 0 ? (blk >> 2) * SLOT + sw * 4096 + (blk & 3) * 1024 : 2 * SLOT + sw * 8192 + blk * 1024;
  };
  const u32 rdl = ((lg & 1) * 32 + (w >> 2) * 16 + lc) * 16;
  const int rblk = (w & 3) * 2 + (lg >> 1);
  auto reduce = [&](int r, u32x2 xr) INL {
    const u32 o = poff(r, 0, rblk) + rdl;
    f32x4 s = ldsf4(L, o);
#pragma unroll
    for (int sw = 1; sw < 8; ++sw) s += ldsf4(L, o + sw * (r == 0 ? 4096 : 8192));
    s += f32x4{bf_lo(xr[0]), bf_hi(xr[0]), bf_lo(xr[1]), bf_hi(xr[1])} + f32x4{bf_lo(bres[0]), bf_hi(bres[0]), bf_lo(bres[1]), bf_hi(bres[1])};
    *(GAS u32x2*)(P.out + ooff + (r ^ rsw) * 32 * LDX) = pk4(s);
  };
  f32x16 pa[2][2];
  auto pwrite = [&](int r, int blk) INL {
    const f32x16& a = pa[r][blk >> 2];
    const int y = blk & 3;
    *(LAS f32x4*)(L + poff(r, w, blk) + lane * 16) = f32x4{a[4 * y], a[4 * y + 1], a[4 * y + 2], a[4 * y + 3]};
  };
  sfor<0, 2>([&](auto rc_) INL {
    constexpr int r = decltype(rc_)::v;
#pragma unroll
    for (int i = 0; i < 16; ++i) pa[r][0][i] = pa[r][1][i] = 0.f;
    sfor<0, 15>([&](auto sc_) INL {
      constexpr int s = decltype(sc_)::v;
      pa[r][0] = mfma32(wf[s][0], af[r][s], pa[r][0]);
      PIN(pa[r][0]);
      if constexpr (ILV == 1 && r == 1 && s >= 1 && s < 5) pwrite(0, 2 * s - 2);
      if constexpr (ILV == 2 && r == 1 && s < 4) pwrite(0, 4 + s);
      pa[r][1] = mfma32(wf[s][1], af[r][s], pa[r][1]);
      if constexpr (r == 0 && s == 0) STAMP(14);
      if constexpr (r == 0 && s == 14) STAMP(15);
      PIN(pa[r][1]);
      if constexpr (ILV == 1 && r == 1 && s >= 1 && s < 5) pwrite(0, 2 * s - 1);
      if constexpr (r == 1 && s == 4) {
        xr0 = *(const GAS u32x2*)(P.x + ooff + rsw * 32 * LDX); xr1 = *(const GAS u32x2*)(P.x + ooff + (rsw ^ 1) * 32 * LDX);
        bres = *(const GAS u32x2*)(P.b2 + (u32)(ocol * 2));
      }
      if constexpr (r * 15 + s + WIN < 30) lda((r * 15 + s + WIN) / 15, (r * 15 + s + WIN) % 15);
      if constexpr (r == 0 && s >= 7 && s < 13) wread(s + 2);
      if constexpr (r == 1 && s == 10) { lds_bar(); reduce(0, xr0); }
      SB();
    });
#pragma unroll
    for (int y = 0; y < ((r == 1 || !ILV) ? 8 : (ILV == 2 ? 4 : 0)); ++y) pwrite(r, y);
    SB();
  });
  STAMP(9);
  lds_bar();
  if (j == 0 && tid == 0) __hip_atomic_store(ctl, tag, __ATOMIC_RELAXED, __HIP_MEMORY_SCOPE_AGENT);
  reduce(1, xr1);
  STAMP(10);
  STAMP_OUT();
  __builtin_amdgcn_endpgm();
}

__global__ __launch_bounds__(512, 2) void fused(const char*, const char*, const char*, const char*, const u16*, const char*, const char*,
                                                char*, char*) {
  extern __shared__ __attribute__((aligned(16))) char smem[];
  LAS char* L = (LAS char*)smem;
  STAMP(0);
  typedef u64 u64x8 __attribute__((ext_vector_type(8)));
  u64x8 ka;
  u64 kb;
  asm volatile("s_load_dwordx16 %0, %2, 0x0\n\ts_load_dwordx2 %1, %2, 0x40"
               : "=&s"(ka), "=&s"(kb) : "s"(__builtin_amdgcn_kernarg_segment_ptr()) : "memory");
  const int lane = threadIdx.x & 63, w = __builtin_amdgcn_readfirstlane(threadIdx.x >> 6);
  const int b = blockIdx.x, bi = b >> 3, g = (b & 3) * 4 + (bi & 3), j = ((bi >> 2) & 3) + ((b >> 2) & 1) * 4 + (bi >> 4) * 8;
  const bool ffn = j >= 8;
  if (w == 7) {
    u64 pc;
    asm volatile("s_getpc_b64 %0" : "=s"(pc));
    touch((const char*)(pc & ~127ull) + min((u32)(lane * 128 + (bi & 7) * 8192), CODE_BYTES));
  }
  const int rowbase = ffn ? 128 * g : 128 * g - 16;
  Ring R;
  R.ldw = ffn ? LDW1 : LDQKV;
  R.maxch = ffn ? (j == 15 ? 1 : 5) : 7;
  R.wb = w * 1024;
  R.rot = (j + g) & ROTM;
  R.lanes(lane);
  R.stepB = 64 * R.ldw;
  R.mlo = (!ffn && w == 0) ? 0xFFFFFF00u : 0xFFFFFFFFu;
  R.xcond = ffn ? 2u : (u32)(w - 4);
  const u32 oA0 = (rowbase + 8 * w < 0 ? 8 * w : rowbase + 8 * w) * LDX, oA1 = (rowbase + 8 * (w + 8)) * LDX;
  const u32 oB = 8 * w * R.ldw + (ffn ? (j - 8) * 352 : j * 128), seg = ffn ? 128 : 1024;
  asm volatile("s_waitcnt lgkmcnt(0)" : "+s"(ka), "+s"(kb)::"memory");
  const Ptrs P = {(const char*)(GAS const char*)ka[0], (const char*)(GAS const char*)ka[1], (const char*)(GAS const char*)ka[2],
                  (const char*)(GAS const char*)ka[3], (const u16*)(GAS const u16*)ka[4], (const char*)(GAS const char*)ka[5],
                  (const char*)(GAS const char*)ka[6], (char*)(GAS char*)ka[7], (char*)(GAS char*)kb};
  STAMP(11);
  R.sA0 = P.x + oA0;
  R.sA1 = P.x + oA1;
  R.sB0 = (const char*)((ffn ? ka[3] : ka[1]) + oB);
  R.sB1 = R.sB0 + seg;
  R.sB2 = R.sB1 + seg;
  sfor<0, 6>([&](auto kc) INL { R.template issue<0, decltype(kc)::v>(); });
  if (w < 4) sfor<0, 5>([&](auto kc) INL { R.template issue<1, decltype(kc)::v>(); });
  if (!ffn && w == 0 && lane < 8) {
    const u32x4 one = {0x3F803F80u, 0x3F803F80u, 0x3F803F80u, 0x3F803F80u};
#pragma unroll
    for (int s = 0; s < 3; ++s) *(LAS u32x4*)(L + s * SLOT + lane * 16) = one;
  }
  STAMP(12);
  if (!ffn) {
    if (w < 4) body<0>(L, P, R, g, j, w STAMP_PASS);
    else body<1>(L, P, R, g, j, w STAMP_PASS);
  } else {
    if (w < 4) body<2>(L, P, R, g, j, w STAMP_PASS);
    else body<3>(L, P, R, g, j, w STAMP_PASS);
  }
}
}

extern "C" void kernel_launch(void* const* d_in, const int* in_sizes, int n_in, void* d_out, int out_size, void* d_ws,
                              size_t ws_size, hipStream_t stream) {
  (void)hipFuncSetAttribute((const void*)fk::fused, hipFuncAttributeMaxDynamicSharedMemorySize, fk::L_TOTAL);
  fk::fused<<<256, 512, fk::L_TOTAL, stream>>>((const char*)d_in[0], (const char*)d_in[1], (const char*)d_in[2], (const char*)d_in[4],
                                               (const u16*)d_in[5], (const char*)d_in[6], (const char*)d_in[7], (char*)d_out,
                                               (char*)d_ws);
}
```

```cpp
#include <hip/hip_runtime.h>
#include <stdint.h>

typedef unsigned u32;
typedef unsigned long long u64;
typedef unsigned short u16;
typedef short s16x4 __attribute__((ext_vector_type(4)));
typedef short s16x8 __attribute__((ext_vector_type(8)));
typedef __bf16 b16x2 __attribute__((ext_vector_type(2)));
typedef float f32x2 __attribute__((ext_vector_type(2)));
typedef float f32x4 __attribute__((ext_vector_type(4)));
typedef float f32x16 __attribute__((ext_vector_type(16)));
typedef u32 u32x2 __attribute__((ext_vector_type(2)));
typedef u32 u32x4 __attribute__((ext_vector_type(4)));
#define LAS __attribute__((address_space(3)))
#define GAS __attribute__((address_space(1)))
#define DEV __device__ __forceinline__
#define INL __attribute__((always_inline))

namespace fk {
constexpr int LDX = 1024, LDQKV = 3072, LDW1 = 2752, LDW2 = 1024;
constexpr int NKS = 118;
constexpr int SLOT = 43008, BOFF = 18432;
constexpr int L_MEAN = 3 * SLOT, L_RSTD = L_MEAN + 576, L_CS = L_RSTD + 576, L_B1 = L_CS + 768, L_OWN = L_B1 + 768,
              L_KV = L_MEAN + 3584, L_JUNK = L_KV + 8192, L_SP = L_JUNK + 2048, L_TOTAL = L_SP + 9 * 2048;
static_assert(L_OWN + 576 + 32 <= L_KV && L_TOTAL <= 163840, "LDS map");
constexpr int WS_GRP = 8192, WS_STAT = 4096;
constexpr int WS_A2 = 16 * WS_GRP, A2_BYTES = 64 * NKS * 1024;
constexpr int AUX_SC1 = 16;
constexpr u32 SPIN_CAP = 1u << 16;
constexpr u32 CODE_BYTES = 63000;

#define PRIO 0
#define ILV 1
#define ROTM 7
#define PF1 0
#define S2LATE 0
#define KROT 0
#define RSW 0
#define ROTX 1
#define RMID 11
#define WIN 6
#define POLLW 7
#define PGAP 2
#define NEPQ 12
#define NEPF 12

#define STAMP(i) do {} while (0)
#define STAMP_ARG
#define STAMP_PASS
#define STAMP_OUT() do {} while (0)

template <int IMM> DEV void dma16(u32 voff, const void* sbase, u32 lb) {
  asm volatile("s_add_u32 m0, %2, %3\n\ts_nop 0\n\tglobal_load_lds_dwordx4 %0, %1" ::"v"(voff), "s"(sbase), "s"(lb), "i"(IMM) : "memory", "scc");
}
template <int IMM> DEV void dma16_masked(u32 voff, const void* sbase, u32 lb, u32 mlo) {
  asm volatile("s_add_u32 m0, %2, %4\n\ts_mov_b32 exec_lo, %3\n\tglobal_load_lds_dwordx4 %0, %1\n\ts_mov_b32 exec_lo, -1"
               ::"v"(voff), "s"(sbase), "s"(lb), "s"(mlo), "i"(IMM) : "memory", "scc");
}
template <int IMM> DEV void dma16_if(u32 voff, const void* sbase, u32 lb, u32 cond) {
  asm volatile("s_cmp_gt_u32 %3, 1\n\ts_cbranch_scc1 1f\n\ts_add_u32 m0, %2, %4\n\ts_nop 0\n\tglobal_load_lds_dwordx4 %0, %1\n1:"
               ::"v"(voff), "s"(sbase), "s"(lb), "s"(cond), "i"(IMM) : "memory", "scc");
}
DEV void touch(const void* p) {
  asm volatile("s_mov_b32 m0, %1\n\ts_nop 0\n\tglobal_load_lds_dword %0, off sc1" ::"v"(p), "i"(L_JUNK) : "memory");
}
template <int N> DEV void wait_bar() {
  asm volatile("s_waitcnt vmcnt(%0) lgkmcnt(0)\n\ts_barrier" ::"i"(N) : "memory");
}
template <int N> DEV void wait_vm() { asm volatile("s_waitcnt vmcnt(%0)" ::"i"(N) : "memory"); }
DEV void wait_lds() { asm volatile("s_waitcnt lgkmcnt(0)" ::: "memory"); }
DEV void lds_bar() { asm volatile("s_waitcnt lgkmcnt(0)\n\ts_barrier" ::: "memory"); }
#define SB() __builtin_amdgcn_sched_barrier(0)
#define PIN(x) asm volatile("" : "+v"(x))

DEV u32 f2u(float v) { return __builtin_bit_cast(u32, v); }
DEV float u2f(u32 v) { return __builtin_bit_cast(float, v); }
DEV u32 pk_bf16(float a, float b) {
  f32x2 p = {a, b};
  return __builtin_bit_cast(u32, __builtin_convertvector(p, b16x2));
}
DEV u32x2 pk4(f32x4 v) { return u32x2{pk_bf16(v[0], v[1]), pk_bf16(v[2], v[3])}; }
DEV float bf_lo(u32 v) { return u2f(v << 16); }
DEV float bf_hi(u32 v) { return u2f(v & 0xffff0000u); }
DEV float dot2(u32 a, u32 b, float c) {
  return __builtin_amdgcn_fdot2_f32_bf16(__builtin_bit_cast(b16x2, a), __builtin_bit_cast(b16x2, b), c, false);
}
DEV f32x4 mfma16(u32x4 a, u32x4 b, f32x4 c) {
  return __builtin_amdgcn_mfma_f32_16x16x32_bf16(__builtin_bit_cast(s16x8, a), __builtin_bit_cast(s16x8, b), c, 0, 0, 0);
}
DEV f32x4 mfma16k16(u32x2 a, u32x2 b, f32x4 c) {
  return __builtin_amdgcn_mfma_f32_16x16x16bf16_1k(__builtin_bit_cast(s16x4, a), __builtin_bit_cast(s16x4, b), c, 0, 0, 0);
}
DEV f32x16 mfma32(u32x4 a, u32x4 b, f32x16 c) {
  return __builtin_amdgcn_mfma_f32_32x32x16_bf16(__builtin_bit_cast(s16x8, a), __builtin_bit_cast(s16x8, b), c, 0, 0, 0);
}
DEV u32x4 lds128(LAS char* L, u32 off) { return *(LAS u32x4*)(L + off); }
DEV f32x4 ldsf4(LAS char* L, u32 off) { return *(LAS f32x4*)(L + off); }
DEV float ldsf(LAS char* L, u32 off) { return *(LAS float*)(L + off); }
DEV u32x2 ldstr(LAS char* L, u32 off) {
  return __builtin_bit_cast(u32x2, __builtin_amdgcn_ds_read_tr16_b64_v4i16((LAS s16x4*)(L + off)));
}
template <int CTRL> DEV float dpp_add(float v) {
  int t = __builtin_amdgcn_update_dpp(0, (int)f2u(v), CTRL, 0xf, 0xf, true);
  return v + u2f((u32)t);
}
DEV f32x2 swap16(float a, float b) {
  auto q = __builtin_amdgcn_permlane16_swap(f2u(a), f2u(b), false, false);
  return f32x2{u2f(q[0]), u2f(q[1])};
}
DEV f32x2 swap32(float a, float b) {
  auto q = __builtin_amdgcn_permlane32_swap(f2u(a), f2u(b), false, false);
  return f32x2{u2f(q[0]), u2f(q[1])};
}
DEV float vmax(float a, float b) { return __builtin_amdgcn_fmed3f(a, b, __builtin_inff()); }
DEV float sel(bool c, float a, float b) { return c ? a : b; }
DEV u32x4 pair_rows(u32x2 x, u32x2 y) {
  auto a = __builtin_amdgcn_permlane16_swap(x[0], y[0], false, false);
  auto b = __builtin_amdgcn_permlane16_swap(x[1], y[1], false, false);
  return u32x4{a[0], b[0], a[1], b[1]};
}
DEV f32x4 splat4(float v) { return f32x4{v, v, v, v}; }
DEV int fresh_tid() {
  int t = threadIdx.x;
  asm volatile("" : "+v"(t));
  return t;
}
DEV void st128(u32x4 v, __amdgpu_buffer_rsrc_t rs, u32 voff, u32 soff) {
  __builtin_amdgcn_raw_buffer_store_b128(v, rs, voff, soff, AUX_SC1);
  asm volatile("s_nop 1" ::"v"(v));
}
template <int V> struct IC { static constexpr int v = V; };
template <int LO, int HI, class F> DEV void sfor(F&& f) {
  if constexpr (LO < HI) { f(IC<LO>{}); sfor<LO + 1, HI>(f); }
}

struct Ptrs { const char *x, *wq, *wo, *w1; const u16* b1; const char *w2, *b2; char *out, *ws; };
struct Ring {
  u32 voffA, voffB, voffB2, stepB, mlo, xcond, wb;
  int rot, ldw, maxch;
  const char *sA0, *sA1, *sB0, *sB1, *sB2;
  DEV void lanes(int lane) {
    const int r8 = lane >> 3, pos = lane & 7;
    const int swB = (r8 & 2) | ((wb >> 8) & 4);
    voffA = r8 * LDX + ((pos ^ r8) << 4);
    voffB = r8 * ldw + ((pos ^ swB) << 4);
    voffB2 = r8 * ldw + (min(pos ^ swB, maxch) << 4);
  }
  template <int I, int K> DEV void issue() const {
    const int kc = (I + rot) & 7;
    constexpr int slot = (I % 3) * SLOT;
    if constexpr (K == 0) dma16_masked<slot>(voffA + kc * 128, sA0, wb, mlo);
    if constexpr (K == 1) dma16<slot + BOFF>(voffB + kc * stepB, sB0, wb);
    if constexpr (K == 2) dma16<slot + BOFF + 8192>(voffB + kc * stepB, sB1, wb);
    if constexpr (K == 3) dma16<slot + BOFF + 16384>(voffB2 + kc * stepB, sB2, wb);
    if constexpr (K == 4) dma16<slot + 8192>(voffA + kc * 128, sA1, wb);
    if constexpr (K == 5) dma16_if<slot + 12288>(voffA + kc * 128 + 32 * LDX, sA1, wb, xcond);
  }
  template <int I, int WANT> DEV void issue5_if(u32 me) const {
    const int kc = (I + rot) & 7;
    constexpr int slot = (I % 3) * SLOT;
    asm volatile("s_cmp_lg_u32 %10, %11\n\ts_cbranch_scc1 1f\n\t"
                 "s_add_u32 m0, %8, %12\n\ts_mov_b32 exec_lo, %9\n\tglobal_load_lds_dwordx4 %0, %3\n\ts_mov_b32 exec_lo, -1\n\t"
                 "s_add_u32 m0, %8, %13\n\ts_nop 0\n\tglobal_load_lds_dwordx4 %1, %4\n\t"
                 "s_add_u32 m0, %8, %14\n\ts_nop 0\n\tglobal_load_lds_dwordx4 %1, %5\n\t"
                 "s_add_u32 m0, %8, %15\n\ts_nop 0\n\tglobal_load_lds_dwordx4 %2, %6\n\t"
                 "s_add_u32 m0, %8, %16\n\ts_nop 0\n\tglobal_load_lds_dwordx4 %0, %7\n1:"
                 ::"v"(voffA + kc * 128), "v"(voffB + kc * stepB), "v"(voffB2 + kc * stepB), "s"(sA0), "s"(sB0), "s"(sB1), "s"(sB2),
                   "s"(sA1), "s"(wb), "s"(mlo), "s"(me), "i"(WANT), "i"(slot), "i"(slot + BOFF), "i"(slot + BOFF + 8192),
                   "i"(slot + BOFF + 16384), "i"(slot + 8192) : "memory", "scc");
  }
};

template <int TYPE>
DEV void body(LAS char* L, const Ptrs P, Ring R, int g, int j, int w STAMP_ARG) {
  constexpr bool FFN = TYPE >= 2;
  constexpr int ODD = TYPE & 1;
  constexpr int NT = TYPE == 0 ? 5 : (TYPE == 1 ? 4 : 2);
  constexpr int NC = TYPE == 2 ? 6 : (TYPE == 3 ? 5 : 3);
  constexpr int CB = TYPE == 3 ? 6 : 0;
  constexpr int ND = TYPE == 1 ? 6 : 5;
  constexpr int NM = NT * NC + (FFN ? 2 : 0);
  constexpr int NEPI = FFN ? NEPF : NEPQ;
  constexpr int NRL = FFN ? 2 : 4;
  int tid = fresh_tid();
  int lane = tid & 63;
  R.lanes(lane);
  int lg = lane >> 4, lc = lane & 15;
  GAS u64* ctl = (GAS u64*)(P.ws + g * WS_GRP);
  u32x4 r0, r1, r2;
  u64 epoch;
  if (FFN)
    asm volatile("global_load_dwordx2 %0, %2, %3 sc1\n\tglobal_load_dwordx4 %1, %4, %5"
                 : "=&v"(epoch), "=&v"(r0) : "v"(0u), "s"(ctl), "v"(lane * 16), "s"(P.x + (size_t)(128 * g + 8 * j + w) * LDX) : "memory");
  else
    asm volatile("global_load_dwordx2 %0, %4, %5 sc1\n\tglobal_load_dwordx4 %1, %6, %7\n\t"
                 "global_load_dwordx4 %2, %6, %8\n\tglobal_load_dwordx4 %3, %6, %8 offset:1024"
                 : "=&v"(epoch), "=&v"(r0), "=&v"(r1), "=&v"(r2)
                 : "v"(0u), "s"(ctl), "v"(lane * 16), "s"(P.x + (size_t)(128 * g + 8 * j + w) * LDX),
                   "s"(P.x + (size_t)max(128 * g - 16 + 2 * w, 0) * LDX) : "memory");
  if (PF1 && !ODD) {
    const int n = min((int)blockIdx.x * 155 + w * 39 + min(lane, 38), 39679);
    const u64 p = n < 16384 ? (u64)P.x + (u32)(n * 128) : (n < 28672 ? (u64)P.wq + (u32)((n - 16384) * 128) : (u64)P.w1 + (u32)((n - 28672) * 128));
    touch((const void*)p);
  }
  const int T0 = TYPE == 0 ? 0 : (TYPE == 1 ? 5 : 2 * (w & 3));
  const u32 aA = T0 * 2048 + (lc >> 3) * 1024 + (lc & 7) * 128 + ((lg ^ (lc & 7)) << 4);
  const int tq = lc >> 2, tp = lc & 3;
  const int swR = (tq & 2) | ((lg & 1) << 2);
  const u32 aBl = BOFF + lg * 1024 + tq * 128 + ((tp >> 1) << 4) + (tp & 1) * 8;
  const int crot = min(2 * (w & 3), NC - 2);
  constexpr int NBB = FFN ? NC : 1;
  u32 aB[NBB][2], aAb[2][2];
#pragma unroll
  for (int c = 0; c < NBB; ++c) {
    const int cc = c + crot >= NC ? c + crot - NC : c + crot;
    const int ct = FFN ? CB + cc : (w & 3);
    aB[c][0] = aBl + (ct >> 2) * 8192 + (((2 * (ct & 3)) ^ swR) << 4);
    aB[c][1] = aB[c][0] + 65536;
    asm("" : "+v"(aB[c][0]), "+v"(aB[c][1]));
  }
#pragma unroll
  for (int b = 0; b < 2; ++b) {
    aAb[b][0] = aA ^ (b * 64);
    aAb[b][1] = aAb[b][0] + 65536;
    asm("" : "+v"(aAb[b][0]), "+v"(aAb[b][1]));
  }
  SB();
  if (ODD) sfor<0, ND>([&](auto kc) INL { R.template issue<1, decltype(kc)::v>(); });
  f32x4 acc[NT][NC], acc1[2];
#pragma unroll
  for (int t = 0; t < NT; ++t)
#pragma unroll
    for (int c = 0; c < NC; ++c) acc[t][c] = f32x4{0.f, 0.f, 0.f, 0.f};
  acc1[0] = acc1[1] = f32x4{0.f, 0.f, 0.f, 0.f};
  u32x4 fa[2][NT];
  u32x2 fb[2][NC][2];
  auto rd = [&](int t, int k) INL {
    const int slot = ((t >> 1) % 3) * SLOT, b = t & 1;
    if (k >= 2 && k < 2 + NT) {
      const int o = slot + (k - 2) * 2048, hi = o >= 65536;
      fa[b][k - 2] = lds128(L, aAb[b][hi] + (o - hi * 65536));
    } else {
      const int kk = k < 2 ? k : k - NT, c = kk >> 1;
      const int o = slot + b * 4096 + (kk & 1) * 512 + (FFN ? 0 : c * 8192), hi = o >= 65536;
      fb[b][c][kk & 1] = ldstr(L, aB[FFN ? c : 0][hi] + (o - hi * 65536));
    }
  };
  const int mt = 2 * g + ((j >> 2) & 1), nt = (j & 3) + (j >> 3) * 4;
  const int ks0 = w < 6 ? 15 * w : 90 + 14 * (w - 6), nks = w < 6 ? 15 : 14;
  const int krot = KROT ? (2 * nt + 4 * (g & 3)) % 15 : 0;
  auto kidx = [&](int s) INL -> int { return s + krot >= 15 ? s + krot - 15 : s + krot; };
  const u32 voffW = (lane >> 3) * LDW2 + (((lane & 7) ^ ((lane >> 2) & 4)) << 4) + nt * 128;
  const u64 woa = (u64)P.wo, w2a = (u64)P.w2 - 32 * 16 * LDW2;
  const u32 wx0 = w < 5 ? 2 * SLOT + (16 + w) * 2048 : (11 + w) * 2048;
  const u32 wx1 = w < 5 ? SLOT + (16 + w) * 2048 : (w < 7 ? (14 + w) * 2048 : L_SP + 8 * 2048);
  auto wbase = [&](int s) INL -> u32 { const int k = s % 9; return k == 0 ? 2 * R.wb : (k == 5 ? wx0 : (k == 8 ? wx1 : 4 * R.wb)); };
  auto wdma = [&](auto sc_, auto hc_) INL {
    constexpr int s = decltype(sc_)::v, h = decltype(hc_)::v, k = s % 9;
    constexpr int imm = k == 0 ? L_SP : (k <= 2 ? 2 * SLOT + (k - 1) * 2048 : (k <= 4 ? (k - 3) * 2048 : (k == 6 || k == 7 ? SLOT + (k - 6) * 2048 : 0)));
    const int ks = min(ks0 + kidx(s), NKS - 1);
    const u64 base = (ks < 32 ? woa : w2a) + (u32)(ks * 16 * LDW2);
    dma16<imm + h * 1024>(voffW + h * 8 * LDW2, (const void*)base, wbase(s));
  };
  u32x4 wf[15][2];
  const u32 tb = (lane >> 5) * 1024 + tq * 128 + (((2 * (lg & 1) + (tp >> 1)) ^ ((tq & 2) << 1)) << 4) + (tp & 1) * 8;
  const __amdgpu_buffer_rsrc_t rsA2 = __builtin_amdgcn_make_buffer_rsrc(P.ws + WS_A2, 0, A2_BYTES, 0x00020000);
  const __amdgpu_buffer_rsrc_t rsG = __builtin_amdgcn_make_buffer_rsrc(P.ws + g * WS_GRP, 0, WS_GRP, 0x00020000);
  const u32x4 ones = {0x3F803F80u, 0x3F803F80u, 0x3F803F80u, 0x3F803F80u};
  u64 tag = 0;
  u32 b1v = 0;
  u32x4 gst;
  u64 tp2 = 0;
  u32 gsto = 0, gldo = 0, b1o = 0, stw = 0, b1w = 0, csw = 0, csw2 = 0;
  float som = 0.f, sor = 0.f;
  f32x4 mP, mP0, mC;
  u32 sel0 = 0, sel1 = 0, wra[4][2];
  auto wread = [&](int s) INL {
    const int k = s % 9;
#pragma unroll
    for (int f = 0; f < 2; ++f) {
      const u32 a = k == 0 ? wra[0][f] : (k <= 2 ? wra[2][f] + (k - 1) * 2048 : (k <= 4 ? wra[1][f] + (k - 3) * 2048 :
                    (k == 6 || k == 7 ? wra[1][f] + SLOT + (k - 6) * 2048 : wra[1][f] - 4 * R.wb + wbase(s))));
      const u32x2 lo = ldstr(L, a), hi = ldstr(L, a + 512);
      wf[s][f] = u32x4{lo[0], lo[1], hi[0], hi[1]};
    }
  };
  constexpr int NK = 5, TK = TYPE == 0 ? 0 : 4;
  float mc1[NK], rc1[NK];
  f32x4 m4[NK], r4[NK], bb[NC];
  auto rdst = [&](int k) INL {
    if (FFN) {
      if (k < 2) mc1[k] = ldsf(L, L_MEAN + ((2 * (w & 3) + k) * 16 + lc) * 4);
      else if (k < 4) rc1[k - 2] = ldsf(L, L_RSTD + ((2 * (w & 3) + k - 2) * 16 + lc) * 4);
      else if (k < 4 + NC) {
        const int c = k - 4, ct = CB + (c + crot >= NC ? c + crot - NC : c + crot);
        bb[c] = ldsf4(L, L_B1 + (ct * 16 + lg * 4) * 4);
      }
    } else {
      const int i = k >> 2, T = TK + i;
      if (i >= NK) return;
      if ((k & 3) == 0) mc1[i] = ldsf(L, L_MEAN + (T * 16 + lc) * 4);
      if ((k & 3) == 1) rc1[i] = ldsf(L, L_RSTD + (T * 16 + lc) * 4);
      if ((k & 3) == 2) m4[i] = ldsf4(L, L_MEAN + (T * 16 + lg * 4) * 4);
      if ((k & 3) == 3) r4[i] = ldsf4(L, L_RSTD + (T * 16 + lg * 4) * 4);
    }
  };
      {
        const int b_ = blockIdx.x, ln = min(b_ * 59 + w * 8 + (lane & 7), 15103);
        tp2 = (ln < 4096 ? woa : w2a) + (u32)(ln * 128);
        const u64 pa = (u64)P.ws + (u32)(WS_A2 + (lane & 3) * (2u << 20) + (b_ & 63) * 4096), po = (u64)P.out + (u32)(b_ * 8192);
        tp2 = (lane >= 8 && lane < 12) ? pa : tp2;
        tp2 = lane == 12 ? po : tp2;
        gsto = (w == 7 && lane < 8) ? WS_STAT + (j * 8 + lane) * 16 : WS_GRP;
        gldo = WS_STAT + (tid & 127) * 16;
        b1o = 2 * (u32)min(max(j - 8, 0) * 176 + tid, 1375);
        stw = (!ODD && w < 2) ? L_MEAN + (tid + (FFN ? 0 : 16)) * 4 : L_JUNK + lane * 4;
        b1w = tid < 192 ? L_B1 + tid * 4 : L_JUNK + lane * 4;
        csw = lc == 0 ? L_CS + ((FFN ? CB + crot : (w & 3)) * 16 + lg * 4) * 4 : L_JUNK + lane * 16;
        csw2 = lg == 0 ? L_CS + (128 + (w & 3) * 16 + lc) * 4 : L_JUNK + lane * 4;
        if (!FFN) {
#pragma unroll
          for (int r = 0; r < 4; ++r) {
            mP[r] = (4 * lg + r >= lc + 2) ? 0.f : -1e30f;
            mP0[r] = (TYPE == 0 && g == 0) ? -1e30f : mP[r];
            mC[r] = (4 * lg + r <= lc) ? 0.f : -1e30f;
          }
          sel0 = (lc & 1) ? 0x01000c0cu : 0x0c0c0100u; sel1 = (lc & 1) ? 0x03020c0cu : 0x0c0c0302u;
          asm volatile("" : "+v"(mP), "+v"(mC), "+v"(sel0), "+v"(sel1));
          if (TYPE == 0) asm volatile("" : "+v"(mP0));
        }
#pragma unroll
        for (int f = 0; f < 2; ++f) {
          wra[0][f] = (tb ^ (f * 64)) + 2 * R.wb + L_SP;
          wra[1][f] = (tb ^ (f * 64)) + 4 * R.wb;
          wra[2][f] = (tb ^ (f * 64)) + 4 * R.wb + 2 * SLOT;
          asm volatile("" : "+v"(wra[0][f]), "+v"(wra[1][f]), "+v"(wra[2][f]));
        }
        asm volatile("" : "+v"(tp2), "+v"(gsto), "+v"(gldo), "+v"(b1o), "+v"(stw), "+v"(b1w), "+v"(csw), "+v"(csw2));
      }
  SB();
  if (PRIO == 1 && ODD) __builtin_amdgcn_s_setprio(1);
  if (PRIO == 2 && !ODD) __builtin_amdgcn_s_setprio(1);
  STAMP(1);
  wait_bar<5 + NRL + (PF1 && !ODD)>();
  STAMP(2);
#pragma unroll
  for (int k = 0; k < 2 * NC + NT; ++k) rd(0, k);
  SB();
  sfor<0, 16>([&](auto tc) INL {
    constexpr int t = decltype(tc)::v, s = t >> 1, b = t & 1;
    if constexpr (b) {
      if constexpr (s == 1 || s == 2) wait_bar<6 + ODD>();
      else if constexpr (s == 0) wait_bar<5 + (PF1 && !ODD)>();
      else if constexpr (s <= 4) wait_bar<5>();
      else if constexpr (s == 5) wait_bar<7>();
      else if constexpr (s == 6) wait_bar<4>();
    }
    if constexpr (t == 11) {
      if constexpr (!ODD) asm volatile("" : "+v"(gst), "+v"(b1v));
      if constexpr (!ODD) {
        if (__builtin_expect(!__all(gst[1] == (u32)tag && gst[3] == (u32)tag), 0)) {
#pragma nounroll
          for (u32 spins = 0; spins < SPIN_CAP; ++spins) {
            __builtin_amdgcn_s_sleep(1);
            gst = __builtin_amdgcn_raw_buffer_load_b128(rsG, gldo, 0, AUX_SC1);
            if (__all(gst[1] == (u32)tag && gst[3] == (u32)tag)) break;
          }
        }
      }
      SB();
    }
    constexpr int nr = t == 15 ? 0 : 2 * NC + NT;
    constexpr int st = t == 0 ? 2 : (t + 5) / 2;
    constexpr int k0 = (t == 0 || b) ? 0 : 3, k1 = (st >= 8 || (t == 0 && S2LATE)) ? 0 : ((t == 0 || !b) ? ND : 3);
    constexpr int nd = k1 > k0 ? k1 - k0 : 0, nw = t >= 10 ? 2 : 0, nv = nd + nw;
    sfor<0, NM>([&](auto qc) INL {
      constexpr int q = decltype(qc)::v;
      constexpr int qq = t < 15 ? q : (FFN ? (q + NT * NC) % NM : (TYPE == 1 ? q : (q < 3 ? q * NT : (q < 5 ? (q - 2) * NT + 4 :
                         (q - 5 < 4 ? q - 4 : (q - 5 < 7 ? q - 3 : q - 1))))));
      if constexpr (qq < NT * NC) {
        constexpr int c = qq / NT, i = qq % NT;
        const u32x4 wfr = {fb[b][c][0][0], fb[b][c][0][1], fb[b][c][1][0], fb[b][c][1][1]};
        if constexpr (!FFN && c == 2) acc[i][c] = mfma16(fa[b][i], wfr, acc[i][c]);
        else acc[i][c] = mfma16(wfr, fa[b][i], acc[i][c]);
      } else {
        constexpr int c = qq - NT * NC;
        const u32x4 wfr = {fb[b][c][0][0], fb[b][c][0][1], fb[b][c][1][0], fb[b][c][1][1]};
        acc1[c] = mfma16(wfr, ones, acc1[c]);
      }
      if constexpr (q < nr) rd(t + 1, q);
      if constexpr (t == 15) { rdst(2 * q); rdst(2 * q + 1); }
      if constexpr (t == 9 && q == 0 && !ODD)
        asm volatile("buffer_load_dwordx4 %0, %2, %3, 0 offen sc1\n\tglobal_load_ushort %1, %4, %5"
                     : "=&v"(gst), "=&v"(b1v) : "v"(gldo), "s"(rsG), "v"(b1o), "s"(P.b1) : "memory");
      if constexpr (t == 11 && !ODD) {
        if constexpr (q == NM - 4) *(LAS u32*)(L + stw) = gst[0];
        if constexpr (q == NM - 3) *(LAS u32*)(L + stw + 576) = gst[2];
        if constexpr (q == NM - 2 && FFN) *(LAS float*)(L + b1w) = bf_lo(b1v);
      }
      if constexpr (t == 1 && ODD && q == NM - 1) { som = ldsf(L, L_OWN + (lane & 7) * 4); sor = ldsf(L, L_OWN + 576 + (lane & 7) * 4); }
      sfor<0, nv>([&](auto uc) INL {
        constexpr int u = decltype(uc)::v;
        if constexpr (((2 * u + ODD) * NM) / (2 * nv) == q) {
          if constexpr (u < nd) R.template issue<st, k0 + u>();
          else wdma(IC<t - 10>{}, IC<u - nd>{});
        }
      });
      if constexpr (t == 2 && q == NM - 1) {
        touch((const void*)tp2);
        if (ODD) st128(u32x4{f2u(som), (u32)tag, f2u(sor), (u32)tag}, rsG, gsto, 0);
      }
      if constexpr (t == 15) {
        if constexpr (TYPE == 0) {
          if constexpr (q == 6) *(LAS f32x4*)(L + csw) = acc[0][0];
          if constexpr (q == 7) *(LAS f32x4*)(L + csw + 256) = acc[0][1];
          if constexpr (q == 8) *(LAS float*)(L + csw2) = acc[0][2][0];
          if constexpr (q == 9) *(LAS f32x4*)(L + L_KV + 2 * R.wb + lane * 16) = acc[NT - 1][1];
          if constexpr (q == 10) *(LAS f32x4*)(L + L_KV + 2 * R.wb + 1024 + lane * 16) = acc[NT - 1][2];
        }
        if constexpr (FFN) {
          if constexpr (q == 6) *(LAS f32x4*)(L + csw) = acc1[0];
          if constexpr (q == 7) *(LAS f32x4*)(L + csw + 64) = acc1[1];
        }
      }
      SB();
    });
    if constexpr (t == 0 && S2LATE) sfor<0, ND>([&](auto kc) INL { R.template issue<2, decltype(kc)::v>(); });
    if constexpr (t == 0) {
      if (FFN) asm volatile("s_waitcnt vmcnt(%2)" : "+v"(epoch), "+v"(r0) : "i"(ODD ? 10 : 5 + PF1) : "memory");
      else asm volatile("s_waitcnt vmcnt(%4)" : "+v"(epoch), "+v"(r0), "+v"(r1), "+v"(r2) : "i"(ODD ? 10 : 5 + PF1) : "memory");
      tag = (((u64)(u32)__builtin_amdgcn_readfirstlane((u32)(epoch >> 32)) << 32) | (u32)__builtin_amdgcn_readfirstlane((u32)epoch)) + 1;
      float sS[3] = {0.f, 0.f, 0.f}, sQ[3] = {0.f, 0.f, 0.f};
#pragma unroll
      for (int d = 0; d < 4; ++d) {
        sS[0] = dot2(r0[d], 0x3F803F80u, sS[0]); sQ[0] = dot2(r0[d], r0[d], sQ[0]);
        if (!FFN) {
          sS[1] = dot2(r1[d], 0x3F803F80u, sS[1]); sQ[1] = dot2(r1[d], r1[d], sQ[1]);
          sS[2] = dot2(r2[d], 0x3F803F80u, sS[2]); sQ[2] = dot2(r2[d], r2[d], sQ[2]);
        }
      }
      f32x2 a = swap32(sS[0], sS[FFN ? 0 : 1]), c = swap32(sQ[0], sQ[FFN ? 0 : 1]);
      float S = a[0] + a[1], Q = c[0] + c[1], S2 = S, Q2 = Q;
      if (!FFN) {
        a = swap32(sS[2], sS[2]); c = swap32(sQ[2], sQ[2]);
        S2 = a[0] + a[1]; Q2 = c[0] + c[1];
      }
      a = swap16(S, S2); c = swap16(Q, Q2);
      S = a[0] + a[1]; Q = c[0] + c[1];
      S = dpp_add<0xB1>(S); Q = dpp_add<0xB1>(Q);
      S = dpp_add<0x4E>(S); Q = dpp_add<0x4E>(Q);
      S = dpp_add<0x141>(S); Q = dpp_add<0x141>(Q);
      S = dpp_add<0x140>(S); Q = dpp_add<0x140>(Q);
      const float m = S * (1.f / 512.f), e = Q * (1.f / 512.f);
      u32 adr = L_JUNK + lane * 4;
      adr = lane == 0 ? L_OWN + w * 4 : adr;
      if (!FFN) { adr = lane == 32 ? L_MEAN + w * 8 : adr; adr = lane == 16 ? L_MEAN + w * 8 + 4 : adr; }
      *(LAS float*)(L + adr) = -m;
      *(LAS float*)(L + adr + 576) = __builtin_amdgcn_rsqf(fmaxf(e - m * m, 0.f) + 1e-5f);
      SB();
    }
  });
  if (PRIO) __builtin_amdgcn_s_setprio(0);
  STAMP(3);
  lds_bar();
  STAMP(4);
  auto slot = [&](auto kc) INL {
    constexpr int k = decltype(kc)::v;
    SB();
    if constexpr (k < NEPI) wdma(IC<6 + k / 2>{}, IC<k % 2>{});
    SB();
  };
  if constexpr (!FFN) {
    const int h = w & 3;
    const f32x4 csq = ldsf4(L, L_CS + (h * 16 + lg * 4) * 4), csk = ldsf4(L, L_CS + (64 + h * 16 + lg * 4) * 4);
    const float csv1 = ldsf(L, L_CS + (128 + h * 16 + lc) * 4);
    f32x4 k4 = acc[0][1], v4 = acc[0][2];
    if (TYPE == 1) { k4 = ldsf4(L, L_KV + h * 2048 + lane * 16); v4 = ldsf4(L, L_KV + h * 2048 + 1024 + lane * 16); }
    slot(IC<0>{});
    wait_vm<7>();
    wread(0); wread(1); wread(2);
    const bool odd = lc & 1;
    const f32x4 csv = splat4(csv1);
    u32x2 Qb[4], Kb[NK], Vb[NK];
    sfor<0, NK>([&](auto ic) INL {
      constexpr int i = decltype(ic)::v, ai = TYPE == 0 ? i : (i > 0 ? i - 1 : 0);
      slot(IC<1 + i>{});
      const f32x4 mc = splat4(mc1[i]);
      Kb[i] = pk4(((i == 0 ? k4 : acc[ai][1]) + mc * csk) * splat4(rc1[i]));
      if constexpr (i > 0) Qb[i - 1] = pk4((acc[ai][0] + mc * csq) * splat4(rc1[i] * (0.25f * 1.4426950408889634f)));
      Vb[i] = pk4(((i == 0 ? v4 : acc[ai][2]) + m4[i] * csv) * r4[i]);
    });
    f32x4 sp[4], sc[4];
#pragma unroll
    for (int q = 0; q < 4; ++q) {
      sp[q] = mfma16k16(Kb[q], Qb[q], q == 0 ? mP0 : mP);
      sc[q] = mfma16k16(Kb[q + 1], Qb[q], mC);
    }
    SB();
    wait_lds();
    wait_vm<6>();
    wread(3); wread(4); wread(5);
    slot(IC<6>{});
    f32x4 a[4];
    float mx[4];
#pragma unroll
    for (int q = 0; q < 4; ++q) {
      a[q] = f32x4{sel(odd, sp[q][1], sp[q][0]), sel(odd, sp[q][3], sp[q][2]), sel(odd, sc[q][1], sc[q][0]), sel(odd, sc[q][3], sc[q][2])};
      mx[q] = vmax(vmax(a[q][0], a[q][1]), vmax(a[q][2], a[q][3]));
    }
    slot(IC<7>{});
    {
      const f32x2 x = swap32(mx[0], mx[1]), y = swap32(mx[2], mx[3]);
      const f32x2 z = swap16(vmax(x[0], x[1]), vmax(y[0], y[1]));
      const float u = vmax(z[0], z[1]);
      const f32x2 p = swap16(u, u);
      const f32x2 p0 = swap32(p[0], p[0]), p1 = swap32(p[1], p[1]);
      mx[0] = p0[0]; mx[1] = p0[1]; mx[2] = p1[0]; mx[3] = p1[1];
    }
    slot(IC<8>{});
#pragma unroll
    for (int q = 0; q < 4; ++q) a[q] -= splat4(mx[q]);
    slot(IC<9>{});
    sfor<0, 4>([&](auto qc) INL {
      constexpr int q = decltype(qc)::v;
#pragma unroll
      for (int r = 0; r < 4; ++r) a[q][r] = __builtin_amdgcn_exp2f(a[q][r]);
      if constexpr (q & 1) slot(IC<10 + q / 2>{});
    });
    wait_lds();
    const f32x4 z4 = {0.f, 0.f, 0.f, 0.f};
    u32x4 pb[4];
    sfor<0, 4>([&](auto qc) INL {
      constexpr int q = decltype(qc)::v;
      const u32x2 u = pk4(a[q]);
      pb[q] = u32x4{__builtin_amdgcn_perm(0, u[0], sel0), __builtin_amdgcn_perm(0, u[0], sel1),
                    __builtin_amdgcn_perm(0, u[1], sel0), __builtin_amdgcn_perm(0, u[1], sel1)};
      if constexpr (q & 1) slot(IC<12 + q / 2>{});
    });
#pragma unroll
    for (int q = 0; q < 4; ++q) {
      sc[q] = mfma16(ones, pb[q], z4);
      sp[q] = mfma16k16(Vb[q], u32x2{pb[q][0], pb[q][1]}, z4);
    }
#pragma unroll
    for (int q = 0; q < 4; ++q) sp[q] = mfma16k16(Vb[q + 1], u32x2{pb[q][2], pb[q][3]}, sp[q]);
    slot(IC<14>{}); slot(IC<15>{}); slot(IC<16>{}); slot(IC<17>{});
    u32x2 Ob[4];
#pragma unroll
    for (int q = 0; q < 4; ++q) Ob[q] = pk4(sp[q] * splat4(__builtin_amdgcn_rcpf(sc[q][0])));
    const int rt = 4 * g + (TYPE == 0 ? 0 : 2), ks = 4 * j + h;
    st128(pair_rows(Ob[0], Ob[1]), rsA2, lane * 16, (rt * NKS + ks) * 1024);
    st128(pair_rows(Ob[2], Ob[3]), rsA2, lane * 16, ((rt + 1) * NKS + ks) * 1024);
  } else {
    f32x4 cs[NC];
#pragma unroll
    for (int c = 0; c < NC; ++c) {
      const int ct = CB + (c + crot >= NC ? c + crot - NC : c + crot);
      cs[c] = ldsf4(L, L_CS + (ct * 16 + lg * 4) * 4);
    }
    wait_vm<6>();
    wread(0); wread(1); wread(2);
    const f32x4 mc[2] = {splat4(mc1[0]), splat4(mc1[1])}, rc[2] = {splat4(rc1[0]), splat4(rc1[1])};
    const int nct = j == 15 ? 9 : 11;
    const int rt = 4 * g + (w & 3), ksb = 32 + 11 * (j - 8);
    const f32x4 C1 = splat4(-2.f * 1.4426950408889634f * 0.7978845608028654f);
    const f32x4 C2 = splat4(-2.f * 1.4426950408889634f * 0.7978845608028654f * 0.044715f);
    const f32x4 ONE = splat4(1.f);
    sfor<0, 3>([&](auto gc) INL {
      constexpr int G = decltype(gc)::v, c0 = 2 * G, n = c0 + 1 < NC ? 4 : 2;
      if constexpr (G == 1) { wait_lds(); wait_vm<8>(); wread(3); wread(4); wread(5); }
      if constexpr (G == 2) wait_lds();
      f32x4 v[4], e[4];
      slot(IC<6 * G>{});
#pragma unroll
      for (int u = 0; u < n; ++u) v[u] = (acc[u & 1][c0 + (u >> 1)] + mc[u & 1] * cs[c0 + (u >> 1)]) * rc[u & 1] + bb[c0 + (u >> 1)];
      slot(IC<6 * G + 1>{});
#pragma unroll
      for (int u = 0; u < n; ++u) e[u] = v[u] * (v[u] * v[u] * C2 + C1);
      slot(IC<6 * G + 2>{});
#pragma unroll
      for (int u = 0; u < n; ++u) {
#pragma unroll
        for (int r = 0; r < 4; ++r) e[u][r] = __builtin_amdgcn_exp2f(e[u][r]);
        if (u == 1) slot(IC<6 * G + 3>{});
      }
#pragma unroll
      for (int u = 0; u < n; ++u) e[u] += ONE;
      slot(IC<6 * G + 4>{});
#pragma unroll
      for (int u = 0; u < n; ++u) {
#pragma unroll
        for (int r = 0; r < 4; ++r) e[u][r] = __builtin_amdgcn_rcpf(e[u][r]);
        if (u == 1) slot(IC<6 * G + 5>{});
      }
#pragma unroll
      for (int cc = 0; cc < n / 2; ++cc) {
        const int c = c0 + cc, ct = CB + (c + crot >= NC ? c + crot - NC : c + crot);
        st128(pair_rows(pk4(v[2 * cc] * e[2 * cc]), pk4(v[2 * cc + 1] * e[2 * cc + 1])), rsA2, lane * 16,
              ct < nct ? (rt * NKS + ksb + ct) * 1024 : A2_BYTES);
      }
      SB();
    });
  }
  STAMP(5);
  wait_vm<0>();
  STAMP(13);
  asm volatile("s_barrier" ::: "memory");
  if (tid == 0) __hip_atomic_store(ctl + 16 * (j + 1), tag, __ATOMIC_RELAXED, __HIP_MEMORY_SCOPE_AGENT);
  STAMP(6);
  sfor<NEPI, 18>([&](auto kc) INL { wdma(IC<6 + decltype(kc)::v / 2>{}, IC<decltype(kc)::v % 2>{}); });
#pragma unroll
  for (int s = 6; s < 9; ++s) wread(s);
  tid = fresh_tid(); lane = tid & 63; lg = lane >> 4; lc = lane & 15;
  const u32 ooff = (u32)((mt * 64 + 4 * w + ((lane >> 3) & 3)) * LDX + (nt * 64 + (lane & 7) * 8) * 2);
  f32x4 xb0, xb1;
#pragma unroll
  for (int s = 0; s < 9; ++s) asm volatile("" : "+v"(wf[s][0]), "+v"(wf[s][1]));
  STAMP(7);
  u64 v0, v1, v2, v3;
  wait_vm<0>();
  {
    const u32 fo = 128 * (lc + 1);
    u32 spins = SPIN_CAP;
    asm volatile(
        "s_cmp_lg_u32 %9, %10\n\ts_cbranch_scc1 2f\n\t"
        "global_load_dwordx2 %0, %5, %6 sc1\n\ts_sleep %8\n\tglobal_load_dwordx2 %1, %5, %6 sc1\n\ts_sleep %8\n\t"
        "global_load_dwordx2 %2, %5, %6 sc1\n\ts_sleep %8\n"
        "1:\n\t"
        "global_load_dwordx2 %3, %5, %6 sc1\n\ts_waitcnt vmcnt(3)\n\tv_cmp_ne_u64 vcc, %7, %0\n\ts_cbranch_vccz 2f\n\ts_sleep %8\n\t"
        "global_load_dwordx2 %0, %5, %6 sc1\n\ts_waitcnt vmcnt(3)\n\tv_cmp_ne_u64 vcc, %7, %1\n\ts_cbranch_vccz 2f\n\ts_sleep %8\n\t"
        "global_load_dwordx2 %1, %5, %6 sc1\n\ts_waitcnt vmcnt(3)\n\tv_cmp_ne_u64 vcc, %7, %2\n\ts_cbranch_vccz 2f\n\ts_sleep %8\n\t"
        "global_load_dwordx2 %2, %5, %6 sc1\n\ts_waitcnt vmcnt(3)\n\tv_cmp_ne_u64 vcc, %7, %3\n\ts_cbranch_vccz 2f\n\ts_sleep %8\n\t"
        "s_sub_u32 %4, %4, 1\n\ts_cmp_lg_u32 %4, 0\n\ts_cbranch_scc1 1b\n"
        "2:\n\ts_barrier"
        : "=&v"(v0), "=&v"(v1), "=&v"(v2), "=&v"(v3), "+s"(spins)
        : "v"(fo), "s"(ctl), "s"(tag), "i"(PGAP), "s"(w), "i"(POLLW) : "memory", "vcc", "scc");
  }
  STAMP(8);
  u32x4 af[2][15];
  auto lda = [&](int r, int s) INL {
    const int so = (kidx(s) < nks) ? ((2 * mt + r) * NKS + ks0 + kidx(s)) * 1024 : A2_BYTES;
    af[r][s] = __builtin_amdgcn_raw_buffer_load_b128(rsA2, lane * 16, so, AUX_SC1);
  };
#pragma unroll
  for (int n = 0; n < WIN; ++n) lda(n / 15, n % 15);
  SB();
  u32 pw[4];
#pragma unroll
  for (int y = 0; y < 4; ++y) pw[y] = ((lane + 4 * y) & 63) * 16 + y * 1024 + 4 * R.wb;
  const int blk = lane & 7;
  const u32 prd = (((lane & 32) + 4 * w + ((lane >> 3) & 3) + 4 * (blk & 3)) & 63) * 16 + (blk & 3) * 1024;
  const u32 prd0 = prd + (blk >> 2) * SLOT, prd1 = prd + (blk >> 2) * 4096 + 2 * SLOT;
  const __amdgpu_buffer_rsrc_t rsO = __builtin_amdgcn_make_buffer_rsrc(P.out, 0, 2048 * LDX, 0x00020000);
  auto reduce = [&](int r, f32x4 xb) INL {
    f32x4 p[8];
#pragma unroll
    for (int sw = 0; sw < 8; ++sw) p[sw] = ldsf4(L, r == 0 ? prd0 + sw * 4096 : prd1 + sw * 8192);
    SB();
    const u32x2 o = pk4(((p[0] + p[1]) + (p[2] + p[3])) + ((p[4] + p[5]) + (p[6] + p[7])) + xb);
    auto e0 = __builtin_amdgcn_permlane32_swap(o[0], o[0], false, false), e1 = __builtin_amdgcn_permlane32_swap(o[1], o[1], false, false);
    __builtin_amdgcn_raw_buffer_store_b128(u32x4{e0[0], e1[0], e0[1], e1[1]}, rsO, lane < 32 ? ooff + r * 32 * LDX : 2048 * LDX, 0, 0);
  };
  f32x16 pa[2][2];
  auto pwrite = [&](int r, int b8) INL {
    const f32x16& a = pa[r][b8 >> 2];
    const int y = b8 & 3;
    *(LAS f32x4*)(L + pw[y] + (r == 0 ? (b8 >> 2) * SLOT : 2 * SLOT + 4 * R.wb + (b8 >> 2) * 4096)) =
        f32x4{a[4 * y], a[4 * y + 1], a[4 * y + 2], a[4 * y + 3]};
  };
  u32x2 xr0, xr1, bres;
  sfor<0, 2>([&](auto rc_) INL {
    constexpr int r = decltype(rc_)::v;
#pragma unroll
    for (int i = 0; i < 16; ++i) pa[r][0][i] = pa[r][1][i] = 0.f;
    sfor<0, 15>([&](auto sc_) INL {
      constexpr int s = decltype(sc_)::v;
      pa[r][0] = mfma32(wf[s][0], af[r][s], pa[r][0]);
      PIN(pa[r][0]);
      if constexpr (r == 1 && s >= 1 && s < 5) pwrite(0, 2 * s - 2);
      pa[r][1] = mfma32(wf[s][1], af[r][s], pa[r][1]);
      PIN(pa[r][1]);
      if constexpr (r == 1 && s >= 1 && s < 5) pwrite(0, 2 * s - 1);
      if constexpr (r == 0 && s == 0) asm volatile("" ::"v"(v0), "v"(v1), "v"(v2), "v"(v3));
      if constexpr (r == 0 && s == 0) STAMP(14);
      if constexpr (r == 0 && s == 14) STAMP(15);
      if constexpr (r == 0 && s == 1) {
        const u32 o4 = ooff + (lane >> 5) * 8;
        xr0 = *(const GAS u32x2*)(P.x + o4); xr1 = *(const GAS u32x2*)(P.x + (o4 + 32 * LDX));
        bres = *(const GAS u32x2*)(P.b2 + (o4 & 1023));
      }
      if constexpr (r * 15 + s + WIN < 30) lda((r * 15 + s + WIN) / 15, (r * 15 + s + WIN) % 15);
      if constexpr (r == 0 && s >= 7 && s < 13) wread(s + 2);
      if constexpr (r == 0 && s == 14) {
        const f32x4 bv = {bf_lo(bres[0]), bf_hi(bres[0]), bf_lo(bres[1]), bf_hi(bres[1])};
        xb0 = f32x4{bf_lo(xr0[0]), bf_hi(xr0[0]), bf_lo(xr0[1]), bf_hi(xr0[1])} + bv;
        xb1 = f32x4{bf_lo(xr1[0]), bf_hi(xr1[0]), bf_lo(xr1[1]), bf_hi(xr1[1])} + bv;
        PIN(xb0); PIN(xb1);
      }
      if constexpr (r == 1 && s == RMID) { lds_bar(); reduce(0, xb0); }
      SB();
    });
  });
#pragma unroll
  for (int y = 0; y < 8; ++y) pwrite(1, y);
  STAMP(9);
  lds_bar();
  reduce(1, xb1);
  if (j == 0 && tid == 0) __hip_atomic_store(ctl, tag, __ATOMIC_RELAXED, __HIP_MEMORY_SCOPE_AGENT);
  STAMP(10);
  STAMP_OUT();
  __builtin_amdgcn_endpgm();
}

__global__ __launch_bounds__(512, 2) void fused(const char*, const char*, const char*, const char*, const u16*, const char*, const char*,
                                                char*, char*) {
  extern __shared__ __attribute__((aligned(16))) char smem[];
  LAS char* L = (LAS char*)smem;
  STAMP(0);
  typedef u64 u64x8 __attribute__((ext_vector_type(8)));
  u64x8 ka;
  u64 kb;
  asm volatile("s_load_dwordx16 %0, %2, 0x0\n\ts_load_dwordx2 %1, %2, 0x40"
               : "=&s"(ka), "=&s"(kb) : "s"(__builtin_amdgcn_kernarg_segment_ptr()) : "memory");
  const int lane = threadIdx.x & 63, w = __builtin_amdgcn_readfirstlane(threadIdx.x >> 6);
  const int b = blockIdx.x, bi = b >> 3, g = (b & 3) * 4 + (bi & 3), j = ((bi >> 2) & 3) + ((b >> 2) & 1) * 4 + (bi >> 4) * 8;
  const bool ffn = j >= 8;
  if (w == 7) {
    u64 pc;
    asm volatile("s_getpc_b64 %0" : "=s"(pc));
    touch((const char*)(pc & ~127ull) + min((u32)(lane * 128 + (bi & 7) * 8192), CODE_BYTES));
  }
  const int rowbase = ffn ? 128 * g : 128 * g - 16;
  Ring R;
  R.ldw = ffn ? LDW1 : LDQKV;
  R.maxch = ffn ? (j == 15 ? 1 : 5) : 7;
  R.wb = w * 1024;
  R.rot = ROTX == 0 ? 0 : (ROTX == 1 ? (j + g) & 7 : (ROTX == 2 ? (2 * (b & 3) + (b & 4)) & 7 : (ROTX == 3 ? (2 * (b & 3) + (b & 4) + (bi & 1)) & 7 : (b & 1) * 4)));
  R.lanes(lane);
  R.stepB = 64 * R.ldw;
  R.mlo = (!ffn && w == 0) ? 0xFFFFFF00u : 0xFFFFFFFFu;
  R.xcond = ffn ? 2u : (u32)(w - 4);
  const u32 oA0 = (rowbase + 8 * w < 0 ? 8 * w : rowbase + 8 * w) * LDX, oA1 = (rowbase + 8 * (w + 8)) * LDX;
  const u32 oB = 8 * w * R.ldw + (ffn ? (j - 8) * 352 : j * 128), seg = ffn ? 128 : 1024;
  asm volatile("s_waitcnt lgkmcnt(0)" : "+s"(ka), "+s"(kb)::"memory");
  const Ptrs P = {(const char*)(GAS const char*)ka[0], (const char*)(GAS const char*)ka[1], (const char*)(GAS const char*)ka[2],
                  (const char*)(GAS const char*)ka[3], (const u16*)(GAS const u16*)ka[4], (const char*)(GAS const char*)ka[5],
                  (const char*)(GAS const char*)ka[6], (char*)(GAS char*)ka[7], (char*)(GAS char*)kb};
  STAMP(11);
  R.sA0 = P.x + oA0;
  R.sA1 = P.x + oA1;
  R.sB0 = (const char*)((ffn ? ka[3] : ka[1]) + oB);
  R.sB1 = R.sB0 + seg;
  R.sB2 = R.sB1 + seg;
  sfor<0, 6>([&](auto kc) INL { R.template issue<0, decltype(kc)::v>(); });
  if (w < 4) sfor<0, 5>([&](auto kc) INL { R.template issue<1, decltype(kc)::v>(); });
  if (!ffn && w == 0 && lane < 8) {
    const u32x4 one = {0x3F803F80u, 0x3F803F80u, 0x3F803F80u, 0x3F803F80u};
#pragma unroll
    for (int s = 0; s < 3; ++s) *(LAS u32x4*)(L + s * SLOT + lane * 16) = one;
  }
  STAMP(12);
  if (!ffn) {
    if (w < 4) body<0>(L, P, R, g, j, w STAMP_PASS);
    else body<1>(L, P, R, g, j, w STAMP_PASS);
  } else {
    if (w < 4) body<2>(L, P, R, g, j, w STAMP_PASS);
    else body<3>(L, P, R, g, j, w STAMP_PASS);
  }
}
}

extern "C" void kernel_launch(void* const* d_in, const int* in_sizes, int n_in, void* d_out, int out_size, void* d_ws,
                              size_t ws_size, hipStream_t stream) {
  (void)hipFuncSetAttribute((const void*)fk::fused, hipFuncAttributeMaxDynamicSharedMemorySize, fk::L_TOTAL);
  fk::fused<<<256, 512, fk::L_TOTAL, stream>>>((const char*)d_in[0], (const char*)d_in[1], (const char*)d_in[2], (const char*)d_in[4],
                                               (const u16*)d_in[5], (const char*)d_in[6], (const char*)d_in[7], (char*)d_out,
                                               (char*)d_ws);
}
```

```cpp
#define STAMPS
#define NSTAMP 16
#define AMP 0
#include <hip/hip_runtime.h>
#include <stdint.h>

typedef unsigned u32;
typedef unsigned long long u64;
typedef unsigned short u16;
typedef short s16x4 __attribute__((ext_vector_type(4)));
typedef short s16x8 __attribute__((ext_vector_type(8)));
typedef __bf16 b16x2 __attribute__((ext_vector_type(2)));
typedef float f32x2 __attribute__((ext_vector_type(2)));
typedef float f32x4 __attribute__((ext_vector_type(4)));
typedef float f32x16 __attribute__((ext_vector_type(16)));
typedef u32 u32x2 __attribute__((ext_vector_type(2)));
typedef u32 u32x4 __attribute__((ext_vector_type(4)));
#define LAS __attribute__((address_space(3)))
#define GAS __attribute__((address_space(1)))
#define DEV __device__ __forceinline__
#define INL __attribute__((always_inline))

namespace fk {
constexpr int LDX = 1024, LDQKV = 3072, LDW1 = 2752, LDW2 = 1024;
constexpr int NKS = 118;
constexpr int SLOT = 43008, BOFF = 18432;
constexpr int L_MEAN = 3 * SLOT, L_RSTD = L_MEAN + 576, L_CS = L_RSTD + 576, L_B1 = L_CS + 768, L_OWN = L_B1 + 768,
              L_KV = L_MEAN + 3584, L_JUNK = L_KV + 8192, L_SP = L_JUNK + 2048, L_TOTAL = L_SP + 9 * 2048;
static_assert(L_OWN + 576 + 32 <= L_KV && L_TOTAL <= 163840, "LDS map");
constexpr int WS_GRP = 8192, WS_STAT = 4096;
constexpr int WS_A2 = 16 * WS_GRP, A2_BYTES = 64 * NKS * 1024;
constexpr int AUX_SC1 = 16;
constexpr u32 SPIN_CAP = 1u << 16;

#define PRIO 0
#define ILV 1
#define ROTM 7
#define PF1 0
#define S2LATE 0
#define KROT 0
#define RSW 0
#define ROTX 1
#define RMID 11
#define WIN 6
#define POLLW 7
#define PGAP 2
#define NEPQ 12
#define NEPF 12

struct Stamps { u32 t[16]; };
#define STAMP(i) do { u64 t_; asm volatile("s_memrealtime %0\n\ts_waitcnt lgkmcnt(0)" : "=s"(t_) :: "memory"); ST.t[i] = (u32)t_; } while (0)
#define STAMP_ARG , Stamps& ST
#define STAMP_PASS , ST
#define STAMP_OUT() do { if ((threadIdx.x & 63) == 0) { _Pragma("unroll") for (int i_ = 0; i_ < NSTAMP; ++i_) \
  ((u64*)(P.ws + (64u << 20)))[(blockIdx.x * 8 + (threadIdx.x >> 6)) * 16 + i_] = ST.t[i_]; } } while (0)

#undef STAMP_OUT
#define STAMP_OUT() do { const u32 lim_ = 4000u + 10u * (ST.t[AMP] - ST.t[0]); \
  while ((u32)__builtin_amdgcn_s_memrealtime() - ST.t[0] < lim_) __builtin_amdgcn_s_sleep(2); } while (0)
template <int IMM> DEV void dma16(u32 voff, const void* sbase, u32 lb) {
  asm volatile("s_add_u32 m0, %2, %3\n\ts_nop 0\n\tglobal_load_lds_dwordx4 %0, %1" ::"v"(voff), "s"(sbase), "s"(lb), "i"(IMM) : "memory", "scc");
}
template <int IMM> DEV void dma16_masked(u32 voff, const void* sbase, u32 lb, u32 mlo) {
  asm volatile("s_add_u32 m0, %2, %4\n\ts_mov_b32 exec_lo, %3\n\tglobal_load_lds_dwordx4 %0, %1\n\ts_mov_b32 exec_lo, -1"
               ::"v"(voff), "s"(sbase), "s"(lb), "s"(mlo), "i"(IMM) : "memory", "scc");
}
template <int IMM> DEV void dma16_if(u32 voff, const void* sbase, u32 lb, u32 cond) {
  asm volatile("s_cmp_gt_u32 %3, 1\n\ts_cbranch_scc1 1f\n\ts_add_u32 m0, %2, %4\n\ts_nop 0\n\tglobal_load_lds_dwordx4 %0, %1\n1:"
               ::"v"(voff), "s"(sbase), "s"(lb), "s"(cond), "i"(IMM) : "memory", "scc");
}
DEV void touch(const void* p) {
  asm volatile("s_mov_b32 m0, %1\n\ts_nop 0\n\tglobal_load_lds_dword %0, off sc1" ::"v"(p), "i"(L_JUNK) : "memory");
}
template <int N> DEV void wait_bar() {
  asm volatile("s_waitcnt vmcnt(%0) lgkmcnt(0)\n\ts_barrier" ::"i"(N) : "memory");
}
template <int N> DEV void wait_vm() { asm volatile("s_waitcnt vmcnt(%0)" ::"i"(N) : "memory"); }
DEV void wait_lds() { asm volatile("s_waitcnt lgkmcnt(0)" ::: "memory"); }
DEV void lds_bar() { asm volatile("s_waitcnt lgkmcnt(0)\n\ts_barrier" ::: "memory"); }
#define SB() __builtin_amdgcn_sched_barrier(0)
#define PIN(x) asm volatile("" : "+v"(x))

DEV u32 f2u(float v) { return __builtin_bit_cast(u32, v); }
DEV float u2f(u32 v) { return __builtin_bit_cast(float, v); }
DEV u32 pk_bf16(float a, float b) {
  f32x2 p = {a, b};
  return __builtin_bit_cast(u32, __builtin_convertvector(p, b16x2));
}
DEV u32x2 pk4(f32x4 v) { return u32x2{pk_bf16(v[0], v[1]), pk_bf16(v[2], v[3])}; }
DEV float bf_lo(u32 v) { return u2f(v << 16); }
DEV float bf_hi(u32 v) { return u2f(v & 0xffff0000u); }
DEV float dot2(u32 a, u32 b, float c) {
  return __builtin_amdgcn_fdot2_f32_bf16(__builtin_bit_cast(b16x2, a), __builtin_bit_cast(b16x2, b), c, false);
}
DEV f32x4 mfma16(u32x4 a, u32x4 b, f32x4 c) {
  return __builtin_amdgcn_mfma_f32_16x16x32_bf16(__builtin_bit_cast(s16x8, a), __builtin_bit_cast(s16x8, b), c, 0, 0, 0);
}
DEV f32x4 mfma16k16(u32x2 a, u32x2 b, f32x4 c) {
  return __builtin_amdgcn_mfma_f32_16x16x16bf16_1k(__builtin_bit_cast(s16x4, a), __builtin_bit_cast(s16x4, b), c, 0, 0, 0);
}
DEV f32x16 mfma32(u32x4 a, u32x4 b, f32x16 c) {
  return __builtin_amdgcn_mfma_f32_32x32x16_bf16(__builtin_bit_cast(s16x8, a), __builtin_bit_cast(s16x8, b), c, 0, 0, 0);
}
DEV u32x4 lds128(LAS char* L, u32 off) { return *(LAS u32x4*)(L + off); }
DEV f32x4 ldsf4(LAS char* L, u32 off) { return *(LAS f32x4*)(L + off); }
DEV float ldsf(LAS char* L, u32 off) { return *(LAS float*)(L + off); }
DEV u32x2 ldstr(LAS char* L, u32 off) {
  return __builtin_bit_cast(u32x2, __builtin_amdgcn_ds_read_tr16_b64_v4i16((LAS s16x4*)(L + off)));
}
template <int CTRL> DEV float dpp_add(float v) {
  int t = __builtin_amdgcn_update_dpp(0, (int)f2u(v), CTRL, 0xf, 0xf, true);
  return v + u2f((u32)t);
}
DEV f32x2 swap16(float a, float b) {
  auto q = __builtin_amdgcn_permlane16_swap(f2u(a), f2u(b), false, false);
  return f32x2{u2f(q[0]), u2f(q[1])};
}
DEV f32x2 swap32(float a, float b) {
  auto q = __builtin_amdgcn_permlane32_swap(f2u(a), f2u(b), false, false);
  return f32x2{u2f(q[0]), u2f(q[1])};
}
DEV float vmax(float a, float b) { return __builtin_amdgcn_fmed3f(a, b, __builtin_inff()); }
DEV float sel(bool c, float a, float b) { return c ? a : b; }
DEV u32x4 pair_rows(u32x2 x, u32x2 y) {
  auto a = __builtin_amdgcn_permlane16_swap(x[0], y[0], false, false);
  auto b = __builtin_amdgcn_permlane16_swap(x[1], y[1], false, false);
  return u32x4{a[0], b[0], a[1], b[1]};
}
DEV f32x4 splat4(float v) { return f32x4{v, v, v, v}; }
DEV int fresh_tid() {
  int t = threadIdx.x;
  asm volatile("" : "+v"(t));
  return t;
}
DEV void st128(u32x4 v, __amdgpu_buffer_rsrc_t rs, u32 voff, u32 soff) {
  __builtin_amdgcn_raw_buffer_store_b128(v, rs, voff, soff, AUX_SC1);
  asm volatile("s_nop 1" ::"v"(v));
}
template <int V> struct IC { static constexpr int v = V; };
template <int LO, int HI, class F> DEV void sfor(F&& f) {
  if constexpr (LO < HI) { f(IC<LO>{}); sfor<LO + 1, HI>(f); }
}

struct Ptrs { const char *x, *wq, *wo, *w1; const u16* b1; const char *w2, *b2; char *out, *ws; };
struct Ring {
  u32 voffA, voffB, voffB2, stepB, mlo, xcond, wb;
  int rot, ldw, maxch;
  const char *sA0, *sA1, *sB0, *sB1, *sB2;
  DEV void lanes(int lane) {
    const int r8 = lane >> 3, pos = lane & 7;
    const int swB = (r8 & 2) | ((wb >> 8) & 4);
    voffA = r8 * LDX + ((pos ^ r8) << 4);
    voffB = r8 * ldw + ((pos ^ swB) << 4);
    voffB2 = r8 * ldw + (min(pos ^ swB, maxch) << 4);
  }
  template <int I, int K> DEV void issue() const {
    const int kc = (I + rot) & 7;
    constexpr int slot = (I % 3) * SLOT;
    if constexpr (K == 0) dma16_masked<slot>(voffA + kc * 128, sA0, wb, mlo);
    if constexpr (K == 1) dma16<slot + BOFF>(voffB + kc * stepB, sB0, wb);
    if constexpr (K == 2) dma16<slot + BOFF + 8192>(voffB + kc * stepB, sB1, wb);
    if constexpr (K == 3) dma16<slot + BOFF + 16384>(voffB2 + kc * stepB, sB2, wb);
    if constexpr (K == 4) dma16<slot + 8192>(voffA + kc * 128, sA1, wb);
    if constexpr (K == 5) dma16_if<slot + 12288>(voffA + kc * 128 + 32 * LDX, sA1, wb, xcond);
  }
  template <int I, int WANT> DEV void issue5_if(u32 me) const {
    const int kc = (I + rot) & 7;
    constexpr int slot = (I % 3) * SLOT;
    asm volatile("s_cmp_lg_u32 %10, %11\n\ts_cbranch_scc1 1f\n\t"
                 "s_add_u32 m0, %8, %12\n\ts_mov_b32 exec_lo, %9\n\tglobal_load_lds_dwordx4 %0, %3\n\ts_mov_b32 exec_lo, -1\n\t"
                 "s_add_u32 m0, %8, %13\n\ts_nop 0\n\tglobal_load_lds_dwordx4 %1, %4\n\t"
                 "s_add_u32 m0, %8, %14\n\ts_nop 0\n\tglobal_load_lds_dwordx4 %1, %5\n\t"
                 "s_add_u32 m0, %8, %15\n\ts_nop 0\n\tglobal_load_lds_dwordx4 %2, %6\n\t"
                 "s_add_u32 m0, %8, %16\n\ts_nop 0\n\tglobal_load_lds_dwordx4 %0, %7\n1:"
                 ::"v"(voffA + kc * 128), "v"(voffB + kc * stepB), "v"(voffB2 + kc * stepB), "s"(sA0), "s"(sB0), "s"(sB1), "s"(sB2),
                   "s"(sA1), "s"(wb), "s"(mlo), "s"(me), "i"(WANT), "i"(slot), "i"(slot + BOFF), "i"(slot + BOFF + 8192),
                   "i"(slot + BOFF + 16384), "i"(slot + 8192) : "memory", "scc");
  }
};

template <int TYPE>
DEV void body(LAS char* L, const Ptrs P, Ring R, int g, int j, int w STAMP_ARG) {
  constexpr bool FFN = TYPE >= 2;
  constexpr int ODD = TYPE & 1;
  constexpr int NT = TYPE == 0 ? 5 : (TYPE == 1 ? 4 : 2);
  constexpr int NC = TYPE == 2 ? 6 : (TYPE == 3 ? 5 : 3);
  constexpr int CB = TYPE == 3 ? 6 : 0;
  constexpr int ND = TYPE == 1 ? 6 : 5;
  constexpr int NM = NT * NC + (FFN ? 2 : 0);
  constexpr int NEPI = FFN ? NEPF : NEPQ;
  constexpr int NRL = FFN ? 2 : 4;
  int tid = fresh_tid();
  int lane = tid & 63;
  R.lanes(lane);
  int lg = lane >> 4, lc = lane & 15;
  GAS u64* ctl = (GAS u64*)(P.ws + g * WS_GRP);
  u32x4 r0, r1, r2;
  u64 epoch;
  if (FFN)
    asm volatile("global_load_dwordx2 %0, %2, %3 sc1\n\tglobal_load_dwordx4 %1, %4, %5"
                 : "=&v"(epoch), "=&v"(r0) : "v"(0u), "s"(ctl), "v"(lane * 16), "s"(P.x + (size_t)(128 * g + 8 * j + w) * LDX) : "memory");
  else
    asm volatile("global_load_dwordx2 %0, %4, %5 sc1\n\tglobal_load_dwordx4 %1, %6, %7\n\t"
                 "global_load_dwordx4 %2, %6, %8\n\tglobal_load_dwordx4 %3, %6, %8 offset:1024"
                 : "=&v"(epoch), "=&v"(r0), "=&v"(r1), "=&v"(r2)
                 : "v"(0u), "s"(ctl), "v"(lane * 16), "s"(P.x + (size_t)(128 * g + 8 * j + w) * LDX),
                   "s"(P.x + (size_t)max(128 * g - 16 + 2 * w, 0) * LDX) : "memory");
  if (PF1 && !ODD) {
    const int n = min((int)blockIdx.x * 155 + w * 39 + min(lane, 38), 39679);
    const u64 p = n < 16384 ? (u64)P.x + (u32)(n * 128) : (n < 28672 ? (u64)P.wq + (u32)((n - 16384) * 128) : (u64)P.w1 + (u32)((n - 28672) * 128));
    touch((const void*)p);
  }
  const int T0 = TYPE == 0 ? 0 : (TYPE == 1 ? 5 : 2 * (w & 3));
  const u32 aA = T0 * 2048 + (lc >> 3) * 1024 + (lc & 7) * 128 + ((lg ^ (lc & 7)) << 4);
  const int tq = lc >> 2, tp = lc & 3;
  const int swR = (tq & 2) | ((lg & 1) << 2);
  const u32 aBl = BOFF + lg * 1024 + tq * 128 + ((tp >> 1) << 4) + (tp & 1) * 8;
  const int crot = min(2 * (w & 3), NC - 2);
  constexpr int NBB = FFN ? NC : 1;
  u32 aB[NBB][2], aAb[2][2];
#pragma unroll
  for (int c = 0; c < NBB; ++c) {
    const int cc = c + crot >= NC ? c + crot - NC : c + crot;
    const int ct = FFN ? CB + cc : (w & 3);
    aB[c][0] = aBl + (ct >> 2) * 8192 + (((2 * (ct & 3)) ^ swR) << 4);
    aB[c][1] = aB[c][0] + 65536;
    asm("" : "+v"(aB[c][0]), "+v"(aB[c][1]));
  }
#pragma unroll
  for (int b = 0; b < 2; ++b) {
    aAb[b][0] = aA ^ (b * 64);
    aAb[b][1] = aAb[b][0] + 65536;
    asm("" : "+v"(aAb[b][0]), "+v"(aAb[b][1]));
  }
  SB();
  if (ODD) sfor<0, ND>([&](auto kc) INL { R.template issue<1, decltype(kc)::v>(); });
  f32x4 acc[NT][NC], acc1[2];
#pragma unroll
  for (int t = 0; t < NT; ++t)
#pragma unroll
    for (int c = 0; c < NC; ++c) acc[t][c] = f32x4{0.f, 0.f, 0.f, 0.f};
  acc1[0] = acc1[1] = f32x4{0.f, 0.f, 0.f, 0.f};
  u32x4 fa[2][NT];
  u32x2 fb[2][NC][2];
  auto rd = [&](int t, int k) INL {
    const int slot = ((t >> 1) % 3) * SLOT, b = t & 1;
    if (k >= 2 && k < 2 + NT) {
      const int o = slot + (k - 2) * 2048, hi = o >= 65536;
      fa[b][k - 2] = lds128(L, aAb[b][hi] + (o - hi * 65536));
    } else {
      const int kk = k < 2 ? k : k - NT, c = kk >> 1;
      const int o = slot + b * 4096 + (kk & 1) * 512 + (FFN ? 0 : c * 8192), hi = o >= 65536;
      fb[b][c][kk & 1] = ldstr(L, aB[FFN ? c : 0][hi] + (o - hi * 65536));
    }
  };
  const int mt = 2 * g + ((j >> 2) & 1), nt = (j & 3) + (j >> 3) * 4;
  const int ks0 = w < 6 ? 15 * w : 90 + 14 * (w - 6), nks = w < 6 ? 15 : 14;
  const int krot = KROT ? (2 * nt + 4 * (g & 3)) % 15 : 0;
  auto kidx = [&](int s) INL -> int { return s + krot >= 15 ? s + krot - 15 : s + krot; };
  const u32 voffW = (lane >> 3) * LDW2 + (((lane & 7) ^ ((lane >> 2) & 4)) << 4) + nt * 128;
  const u64 woa = (u64)P.wo, w2a = (u64)P.w2 - 32 * 16 * LDW2;
  const u32 wx0 = w < 5 ? 2 * SLOT + (16 + w) * 2048 : (11 + w) * 2048;
  const u32 wx1 = w < 5 ? SLOT + (16 + w) * 2048 : (w < 7 ? (14 + w) * 2048 : L_SP + 8 * 2048);
  auto wbase = [&](int s) INL -> u32 { const int k = s % 9; return k == 0 ? 2 * R.wb : (k == 5 ? wx0 : (k == 8 ? wx1 : 4 * R.wb)); };
  auto wdma = [&](auto sc_, auto hc_) INL {
    constexpr int s = decltype(sc_)::v, h = decltype(hc_)::v, k = s % 9;
    constexpr int imm = k == 0 ? L_SP : (k <= 2 ? 2 * SLOT + (k - 1) * 2048 : (k <= 4 ? (k - 3) * 2048 : (k == 6 || k == 7 ? SLOT + (k - 6) * 2048 : 0)));
    const int ks = min(ks0 + kidx(s), NKS - 1);
    const u64 base = (ks < 32 ? woa : w2a) + (u32)(ks * 16 * LDW2);
    dma16<imm + h * 1024>(voffW + h * 8 * LDW2, (const void*)base, wbase(s));
  };
  u32x4 wf[15][2];
  const u32 tb = (lane >> 5) * 1024 + tq * 128 + (((2 * (lg & 1) + (tp >> 1)) ^ ((tq & 2) << 1)) << 4) + (tp & 1) * 8;
  const __amdgpu_buffer_rsrc_t rsA2 = __builtin_amdgcn_make_buffer_rsrc(P.ws + WS_A2, 0, A2_BYTES, 0x00020000);
  const __amdgpu_buffer_rsrc_t rsG = __builtin_amdgcn_make_buffer_rsrc(P.ws + g * WS_GRP, 0, WS_GRP, 0x00020000);
  const u32x4 ones = {0x3F803F80u, 0x3F803F80u, 0x3F803F80u, 0x3F803F80u};
  u64 tag = 0;
  u32 b1v = 0;
  u32x4 gst;
  u64 tp2 = 0;
  u32 gsto = 0, gldo = 0, b1o = 0, stw = 0, b1w = 0, csw = 0, csw2 = 0;
  float som = 0.f, sor = 0.f;
  f32x4 mP, mP0, mC;
  u32 sel0 = 0, sel1 = 0, wra[4][2];
  auto wread = [&](int s) INL {
    const int k = s % 9;
#pragma unroll
    for (int f = 0; f < 2; ++f) {
      const u32 a = k == 0 ? wra[0][f] : (k <= 2 ? wra[2][f] + (k - 1) * 2048 : (k <= 4 ? wra[1][f] + (k - 3) * 2048 :
                    (k == 6 || k == 7 ? wra[1][f] + SLOT + (k - 6) * 2048 : wra[1][f] - 4 * R.wb + wbase(s))));
      const u32x2 lo = ldstr(L, a), hi = ldstr(L, a + 512);
      wf[s][f] = u32x4{lo[0], lo[1], hi[0], hi[1]};
    }
  };
  constexpr int NK = 5, TK = TYPE == 0 ? 0 : 4;
  float mc1[NK], rc1[NK];
  f32x4 m4[NK], r4[NK], bb[NC];
  auto rdst = [&](int k) INL {
    if (FFN) {
      if (k < 2) mc1[k] = ldsf(L, L_MEAN + ((2 * (w & 3) + k) * 16 + lc) * 4);
      else if (k < 4) rc1[k - 2] = ldsf(L, L_RSTD + ((2 * (w & 3) + k - 2) * 16 + lc) * 4);
      else if (k < 4 + NC) {
        const int c = k - 4, ct = CB + (c + crot >= NC ? c + crot - NC : c + crot);
        bb[c] = ldsf4(L, L_B1 + (ct * 16 + lg * 4) * 4);
      }
    } else {
      const int i = k >> 2, T = TK + i;
      if (i >= NK) return;
      if ((k & 3) == 0) mc1[i] = ldsf(L, L_MEAN + (T * 16 + lc) * 4);
      if ((k & 3) == 1) rc1[i] = ldsf(L, L_RSTD + (T * 16 + lc) * 4);
      if ((k & 3) == 2) m4[i] = ldsf4(L, L_MEAN + (T * 16 + lg * 4) * 4);
      if ((k & 3) == 3) r4[i] = ldsf4(L, L_RSTD + (T * 16 + lg * 4) * 4);
    }
  };
      {
        const int b_ = blockIdx.x, ln = min(b_ * 59 + w * 8 + (lane & 7), 15103);
        tp2 = (ln < 4096 ? woa : w2a) + (u32)(ln * 128);
        const u64 pa = (u64)P.ws + (u32)(WS_A2 + (lane & 3) * (2u << 20) + (b_ & 63) * 4096), po = (u64)P.out + (u32)(b_ * 8192);
        tp2 = (lane >= 8 && lane < 12) ? pa : tp2;
        tp2 = lane == 12 ? po : tp2;
        gsto = (w == 7 && lane < 8) ? WS_STAT + (j * 8 + lane) * 16 : WS_GRP;
        gldo = WS_STAT + (tid & 127) * 16;
        b1o = 2 * (u32)min(max(j - 8, 0) * 176 + tid, 1375);
        stw = (!ODD && w < 2) ? L_MEAN + (tid + (FFN ? 0 : 16)) * 4 : L_JUNK + lane * 4;
        b1w = tid < 192 ? L_B1 + tid * 4 : L_JUNK + lane * 4;
        csw = lc == 0 ? L_CS + ((FFN ? CB + crot : (w & 3)) * 16 + lg * 4) * 4 : L_JUNK + lane * 16;
        csw2 = lg == 0 ? L_CS + (128 + (w & 3) * 16 + lc) * 4 : L_JUNK + lane * 4;
        if (!FFN) {
#pragma unroll
          for (int r = 0; r < 4; ++r) {
            mP[r] = (4 * lg + r >= lc + 2) ? 0.f : -1e30f;
            mP0[r] = (TYPE == 0 && g == 0) ? -1e30f : mP[r];
            mC[r] = (4 * lg + r <= lc) ? 0.f : -1e30f;
          }
          sel0 = (lc & 1) ? 0x01000c0cu : 0x0c0c0100u; sel1 = (lc & 1) ? 0x03020c0cu : 0x0c0c0302u;
          asm volatile("" : "+v"(mP), "+v"(mC), "+v"(sel0), "+v"(sel1));
          if (TYPE == 0) asm volatile("" : "+v"(mP0));
        }
#pragma unroll
        for (int f = 0; f < 2; ++f) {
          wra[0][f] = (tb ^ (f * 64)) + 2 * R.wb + L_SP;
          wra[1][f] = (tb ^ (f * 64)) + 4 * R.wb;
          wra[2][f] = (tb ^ (f * 64)) + 4 * R.wb + 2 * SLOT;
          asm volatile("" : "+v"(wra[0][f]), "+v"(wra[1][f]), "+v"(wra[2][f]));
        }
        asm volatile("" : "+v"(tp2), "+v"(gsto), "+v"(gldo), "+v"(b1o), "+v"(stw), "+v"(b1w), "+v"(csw), "+v"(csw2));
      }
  SB();
  if (PRIO == 1 && ODD) __builtin_amdgcn_s_setprio(1);
  if (PRIO == 2 && !ODD) __builtin_amdgcn_s_setprio(1);
  STAMP(1);
  wait_bar<5 + NRL + (PF1 && !ODD)>();
  STAMP(2);
#pragma unroll
  for (int k = 0; k < 2 * NC + NT; ++k) rd(0, k);
  SB();
  sfor<0, 16>([&](auto tc) INL {
    constexpr int t = decltype(tc)::v, s = t >> 1, b = t & 1;
    if constexpr (b) {
      if constexpr (s == 1 || s == 2) wait_bar<6 + ODD>();
      else if constexpr (s == 0) wait_bar<5 + (PF1 && !ODD)>();
      else if constexpr (s <= 4) wait_bar<5>();
      else if constexpr (s == 5) wait_bar<7>();
      else if constexpr (s == 6) wait_bar<4>();
    }
    if constexpr (t == 11) {
      if constexpr (!ODD) asm volatile("" : "+v"(gst), "+v"(b1v));
      if constexpr (!ODD) {
        if (__builtin_expect(!__all(gst[1] == (u32)tag && gst[3] == (u32)tag), 0)) {
#pragma nounroll
          for (u32 spins = 0; spins < SPIN_CAP; ++spins) {
            __builtin_amdgcn_s_sleep(1);
            gst = __builtin_amdgcn_raw_buffer_load_b128(rsG, gldo, 0, AUX_SC1);
            if (__all(gst[1] == (u32)tag && gst[3] == (u32)tag)) break;
          }
        }
      }
      SB();
    }
    constexpr int nr = t == 15 ? 0 : 2 * NC + NT;
    constexpr int st = t == 0 ? 2 : (t + 5) / 2;
    constexpr int k0 = (t == 0 || b) ? 0 : 3, k1 = (st >= 8 || (t == 0 && S2LATE)) ? 0 : ((t == 0 || !b) ? ND : 3);
    constexpr int nd = k1 > k0 ? k1 - k0 : 0, nw = t >= 10 ? 2 : 0, nv = nd + nw;
    sfor<0, NM>([&](auto qc) INL {
      constexpr int q = decltype(qc)::v;
      constexpr int qq = t < 15 ? q : (FFN ? (q + NT * NC) % NM : (TYPE == 1 ? q : (q < 3 ? q * NT : (q < 5 ? (q - 2) * NT + 4 :
                         (q - 5 < 4 ? q - 4 : (q - 5 < 7 ? q - 3 : q - 1))))));
      if constexpr (qq < NT * NC) {
        constexpr int c = qq / NT, i = qq % NT;
        const u32x4 wfr = {fb[b][c][0][0], fb[b][c][0][1], fb[b][c][1][0], fb[b][c][1][1]};
        if constexpr (!FFN && c == 2) acc[i][c] = mfma16(fa[b][i], wfr, acc[i][c]);
        else acc[i][c] = mfma16(wfr, fa[b][i], acc[i][c]);
      } else {
        constexpr int c = qq - NT * NC;
        const u32x4 wfr = {fb[b][c][0][0], fb[b][c][0][1], fb[b][c][1][0], fb[b][c][1][1]};
        acc1[c] = mfma16(wfr, ones, acc1[c]);
      }
      if constexpr (q < nr) rd(t + 1, q);
      if constexpr (t == 15) { rdst(2 * q); rdst(2 * q + 1); }
      if constexpr (t == 9 && q == 0 && !ODD)
        asm volatile("buffer_load_dwordx4 %0, %2, %3, 0 offen sc1\n\tglobal_load_ushort %1, %4, %5"
                     : "=&v"(gst), "=&v"(b1v) : "v"(gldo), "s"(rsG), "v"(b1o), "s"(P.b1) : "memory");
      if constexpr (t == 11 && !ODD) {
        if constexpr (q == NM - 4) *(LAS u32*)(L + stw) = gst[0];
        if constexpr (q == NM - 3) *(LAS u32*)(L + stw + 576) = gst[2];
        if constexpr (q == NM - 2 && FFN) *(LAS float*)(L + b1w) = bf_lo(b1v);
      }
      if constexpr (t == 1 && ODD && q == NM - 1) { som = ldsf(L, L_OWN + (lane & 7) * 4); sor = ldsf(L, L_OWN + 576 + (lane & 7) * 4); }
      sfor<0, nv>([&](auto uc) INL {
        constexpr int u = decltype(uc)::v;
        if constexpr (((2 * u + ODD) * NM) / (2 * nv) == q) {
          if constexpr (u < nd) R.template issue<st, k0 + u>();
          else wdma(IC<t - 10>{}, IC<u - nd>{});
        }
      });
      if constexpr (t == 2 && q == NM - 1) {
        touch((const void*)tp2);
        if (ODD) st128(u32x4{f2u(som), (u32)tag, f2u(sor), (u32)tag}, rsG, gsto, 0);
      }
      if constexpr (t == 15) {
        if constexpr (TYPE == 0) {
          if constexpr (q == 6) *(LAS f32x4*)(L + csw) = acc[0][0];
          if constexpr (q == 7) *(LAS f32x4*)(L + csw + 256) = acc[0][1];
          if constexpr (q == 8) *(LAS float*)(L + csw2) = acc[0][2][0];
          if constexpr (q == 9) *(LAS f32x4*)(L + L_KV + 2 * R.wb + lane * 16) = acc[NT - 1][1];
          if constexpr (q == 10) *(LAS f32x4*)(L + L_KV + 2 * R.wb + 1024 + lane * 16) = acc[NT - 1][2];
        }
        if constexpr (FFN) {
          if constexpr (q == 6) *(LAS f32x4*)(L + csw) = acc1[0];
          if constexpr (q == 7) *(LAS f32x4*)(L + csw + 64) = acc1[1];
        }
      }
      SB();
    });
    if constexpr (t == 0 && S2LATE) sfor<0, ND>([&](auto kc) INL { R.template issue<2, decltype(kc)::v>(); });
    if constexpr (t == 0) {
      if (FFN) asm volatile("s_waitcnt vmcnt(%2)" : "+v"(epoch), "+v"(r0) : "i"(ODD ? 10 : 5 + PF1) : "memory");
      else asm volatile("s_waitcnt vmcnt(%4)" : "+v"(epoch), "+v"(r0), "+v"(r1), "+v"(r2) : "i"(ODD ? 10 : 5 + PF1) : "memory");
      tag = (((u64)(u32)__builtin_amdgcn_readfirstlane((u32)(epoch >> 32)) << 32) | (u32)__builtin_amdgcn_readfirstlane((u32)epoch)) + 1;
      float sS[3] = {0.f, 0.f, 0.f}, sQ[3] = {0.f, 0.f, 0.f};
#pragma unroll
      for (int d = 0; d < 4; ++d) {
        sS[0] = dot2(r0[d], 0x3F803F80u, sS[0]); sQ[0] = dot2(r0[d], r0[d], sQ[0]);
        if (!FFN) {
          sS[1] = dot2(r1[d], 0x3F803F80u, sS[1]); sQ[1] = dot2(r1[d], r1[d], sQ[1]);
          sS[2] = dot2(r2[d], 0x3F803F80u, sS[2]); sQ[2] = dot2(r2[d], r2[d], sQ[2]);
        }
      }
      f32x2 a = swap32(sS[0], sS[FFN ? 0 : 1]), c = swap32(sQ[0], sQ[FFN ? 0 : 1]);
      float S = a[0] + a[1], Q = c[0] + c[1], S2 = S, Q2 = Q;
      if (!FFN) {
        a = swap32(sS[2], sS[2]); c = swap32(sQ[2], sQ[2]);
        S2 = a[0] + a[1]; Q2 = c[0] + c[1];
      }
      a = swap16(S, S2); c = swap16(Q, Q2);
      S = a[0] + a[1]; Q = c[0] + c[1];
      S = dpp_add<0xB1>(S); Q = dpp_add<0xB1>(Q);
      S = dpp_add<0x4E>(S); Q = dpp_add<0x4E>(Q);
      S = dpp_add<0x141>(S); Q = dpp_add<0x141>(Q);
      S = dpp_add<0x140>(S); Q = dpp_add<0x140>(Q);
      const float m = S * (1.f / 512.f), e = Q * (1.f / 512.f);
      u32 adr = L_JUNK + lane * 4;
      adr = lane == 0 ? L_OWN + w * 4 : adr;
      if (!FFN) { adr = lane == 32 ? L_MEAN + w * 8 : adr; adr = lane == 16 ? L_MEAN + w * 8 + 4 : adr; }
      *(LAS float*)(L + adr) = -m;
      *(LAS float*)(L + adr + 576) = __builtin_amdgcn_rsqf(fmaxf(e - m * m, 0.f) + 1e-5f);
      SB();
    }
  });
  if (PRIO) __builtin_amdgcn_s_setprio(0);
  STAMP(3);
  lds_bar();
  STAMP(4);
  auto slot = [&](auto kc) INL {
    constexpr int k = decltype(kc)::v;
    SB();
    if constexpr (k < NEPI) wdma(IC<6 + k / 2>{}, IC<k % 2>{});
    SB();
  };
  if constexpr (!FFN) {
    const int h = w & 3;
    const f32x4 csq = ldsf4(L, L_CS + (h * 16 + lg * 4) * 4), csk = ldsf4(L, L_CS + (64 + h * 16 + lg * 4) * 4);
    const float csv1 = ldsf(L, L_CS + (128 + h * 16 + lc) * 4);
    f32x4 k4 = acc[0][1], v4 = acc[0][2];
    if (TYPE == 1) { k4 = ldsf4(L, L_KV + h * 2048 + lane * 16); v4 = ldsf4(L, L_KV + h * 2048 + 1024 + lane * 16); }
    slot(IC<0>{});
    wait_vm<7>();
    wread(0); wread(1); wread(2);
    const bool odd = lc & 1;
    const f32x4 csv = splat4(csv1);
    u32x2 Qb[4], Kb[NK], Vb[NK];
    sfor<0, NK>([&](auto ic) INL {
      constexpr int i = decltype(ic)::v, ai = TYPE == 0 ? i : (i > 0 ? i - 1 : 0);
      slot(IC<1 + i>{});
      const f32x4 mc = splat4(mc1[i]);
      Kb[i] = pk4(((i == 0 ? k4 : acc[ai][1]) + mc * csk) * splat4(rc1[i]));
      if constexpr (i > 0) Qb[i - 1] = pk4((acc[ai][0] + mc * csq) * splat4(rc1[i] * (0.25f * 1.4426950408889634f)));
      Vb[i] = pk4(((i == 0 ? v4 : acc[ai][2]) + m4[i] * csv) * r4[i]);
    });
    f32x4 sp[4], sc[4];
#pragma unroll
    for (int q = 0; q < 4; ++q) {
      sp[q] = mfma16k16(Kb[q], Qb[q], q == 0 ? mP0 : mP);
      sc[q] = mfma16k16(Kb[q + 1], Qb[q], mC);
    }
    SB();
    wait_lds();
    wait_vm<6>();
    wread(3); wread(4); wread(5);
    slot(IC<6>{});
    f32x4 a[4];
    float mx[4];
#pragma unroll
    for (int q = 0; q < 4; ++q) {
      a[q] = f32x4{sel(odd, sp[q][1], sp[q][0]), sel(odd, sp[q][3], sp[q][2]), sel(odd, sc[q][1], sc[q][0]), sel(odd, sc[q][3], sc[q][2])};
      mx[q] = vmax(vmax(a[q][0], a[q][1]), vmax(a[q][2], a[q][3]));
    }
    slot(IC<7>{});
    {
      const f32x2 x = swap32(mx[0], mx[1]), y = swap32(mx[2], mx[3]);
      const f32x2 z = swap16(vmax(x[0], x[1]), vmax(y[0], y[1]));
      const float u = vmax(z[0], z[1]);
      const f32x2 p = swap16(u, u);
      const f32x2 p0 = swap32(p[0], p[0]), p1 = swap32(p[1], p[1]);
      mx[0] = p0[0]; mx[1] = p0[1]; mx[2] = p1[0]; mx[3] = p1[1];
    }
    slot(IC<8>{});
#pragma unroll
    for (int q = 0; q < 4; ++q) a[q] -= splat4(mx[q]);
    slot(IC<9>{});
    sfor<0, 4>([&](auto qc) INL {
      constexpr int q = decltype(qc)::v;
#pragma unroll
      for (int r = 0; r < 4; ++r) a[q][r] = __builtin_amdgcn_exp2f(a[q][r]);
      if constexpr (q & 1) slot(IC<10 + q / 2>{});
    });
    wait_lds();
    const f32x4 z4 = {0.f, 0.f, 0.f, 0.f};
    u32x4 pb[4];
    sfor<0, 4>([&](auto qc) INL {
      constexpr int q = decltype(qc)::v;
      const u32x2 u = pk4(a[q]);
      pb[q] = u32x4{__builtin_amdgcn_perm(0, u[0], sel0), __builtin_amdgcn_perm(0, u[0], sel1),
                    __builtin_amdgcn_perm(0, u[1], sel0), __builtin_amdgcn_perm(0, u[1], sel1)};
      if constexpr (q & 1) slot(IC<12 + q / 2>{});
    });
#pragma unroll
    for (int q = 0; q < 4; ++q) {
      sc[q] = mfma16(ones, pb[q], z4);
      sp[q] = mfma16k16(Vb[q], u32x2{pb[q][0], pb[q][1]}, z4);
    }
#pragma unroll
    for (int q = 0; q < 4; ++q) sp[q] = mfma16k16(Vb[q + 1], u32x2{pb[q][2], pb[q][3]}, sp[q]);
    slot(IC<14>{}); slot(IC<15>{}); slot(IC<16>{}); slot(IC<17>{});
    u32x2 Ob[4];
#pragma unroll
    for (int q = 0; q < 4; ++q) Ob[q] = pk4(sp[q] * splat4(__builtin_amdgcn_rcpf(sc[q][0])));
    const int rt = 4 * g + (TYPE == 0 ? 0 : 2), ks = 4 * j + h;
    st128(pair_rows(Ob[0], Ob[1]), rsA2, lane * 16, (rt * NKS + ks) * 1024);
    st128(pair_rows(Ob[2], Ob[3]), rsA2, lane * 16, ((rt + 1) * NKS + ks) * 1024);
  } else {
    f32x4 cs[NC];
#pragma unroll
    for (int c = 0; c < NC; ++c) {
      const int ct = CB + (c + crot >= NC ? c + crot - NC : c + crot);
      cs[c] = ldsf4(L, L_CS + (ct * 16 + lg * 4) * 4);
    }
    wait_vm<6>();
    wread(0); wread(1); wread(2);
    const f32x4 mc[2] = {splat4(mc1[0]), splat4(mc1[1])}, rc[2] = {splat4(rc1[0]), splat4(rc1[1])};
    const int nct = j == 15 ? 9 : 11;
    const int rt = 4 * g + (w & 3), ksb = 32 + 11 * (j - 8);
    const f32x4 C1 = splat4(-2.f * 1.4426950408889634f * 0.7978845608028654f);
    const f32x4 C2 = splat4(-2.f * 1.4426950408889634f * 0.7978845608028654f * 0.044715f);
    const f32x4 ONE = splat4(1.f);
    sfor<0, 3>([&](auto gc) INL {
      constexpr int G = decltype(gc)::v, c0 = 2 * G, n = c0 + 1 < NC ? 4 : 2;
      if constexpr (G == 1) { wait_lds(); wait_vm<8>(); wread(3); wread(4); wread(5); }
      if constexpr (G == 2) wait_lds();
      f32x4 v[4], e[4];
      slot(IC<6 * G>{});
#pragma unroll
      for (int u = 0; u < n; ++u) v[u] = (acc[u & 1][c0 + (u >> 1)] + mc[u & 1] * cs[c0 + (u >> 1)]) * rc[u & 1] + bb[c0 + (u >> 1)];
      slot(IC<6 * G + 1>{});
#pragma unroll
      for (int u = 0; u < n; ++u) e[u] = v[u] * (v[u] * v[u] * C2 + C1);
      slot(IC<6 * G + 2>{});
#pragma unroll
      for (int u = 0; u < n; ++u) {
#pragma unroll
        for (int r = 0; r < 4; ++r) e[u][r] = __builtin_amdgcn_exp2f(e[u][r]);
        if (u == 1) slot(IC<6 * G + 3>{});
      }
#pragma unroll
      for (int u = 0; u < n; ++u) e[u] += ONE;
      slot(IC<6 * G + 4>{});
#pragma unroll
      for (int u = 0; u < n; ++u) {
#pragma unroll
        for (int r = 0; r < 4; ++r) e[u][r] = __builtin_amdgcn_rcpf(e[u][r]);
        if (u == 1) slot(IC<6 * G + 5>{});
      }
#pragma unroll
      for (int cc = 0; cc < n / 2; ++cc) {
        const int c = c0 + cc, ct = CB + (c + crot >= NC ? c + crot - NC : c + crot);
        st128(pair_rows(pk4(v[2 * cc] * e[2 * cc]), pk4(v[2 * cc + 1] * e[2 * cc + 1])), rsA2, lane * 16,
              ct < nct ? (rt * NKS + ksb + ct) * 1024 : A2_BYTES);
      }
      SB();
    });
  }
  STAMP(5);
  wait_vm<0>();
  STAMP(13);
  asm volatile("s_barrier" ::: "memory");
  if (tid == 0) __hip_atomic_store(ctl + 16 * (j + 1), tag, __ATOMIC_RELAXED, __HIP_MEMORY_SCOPE_AGENT);
  STAMP(6);
  sfor<NEPI, 18>([&](auto kc) INL { wdma(IC<6 + decltype(kc)::v / 2>{}, IC<decltype(kc)::v % 2>{}); });
#pragma unroll
  for (int s = 6; s < 9; ++s) wread(s);
  tid = fresh_tid(); lane = tid & 63; lg = lane >> 4; lc = lane & 15;
  const u32 ooff = (u32)((mt * 64 + 4 * w + ((lane >> 3) & 3)) * LDX + (nt * 64 + (lane & 7) * 8) * 2);
  f32x4 xb0, xb1;
#pragma unroll
  for (int s = 0; s < 9; ++s) asm volatile("" : "+v"(wf[s][0]), "+v"(wf[s][1]));
  STAMP(7);
  u64 v0, v1, v2, v3;
  wait_vm<0>();
  {
    const u32 fo = 128 * (lc + 1);
    u32 spins = SPIN_CAP;
    asm volatile(
        "s_cmp_lg_u32 %9, %10\n\ts_cbranch_scc1 2f\n\t"
        "global_load_dwordx2 %0, %5, %6 sc1\n\ts_sleep %8\n\tglobal_load_dwordx2 %1, %5, %6 sc1\n\ts_sleep %8\n\t"
        "global_load_dwordx2 %2, %5, %6 sc1\n\ts_sleep %8\n"
        "1:\n\t"
        "global_load_dwordx2 %3, %5, %6 sc1\n\ts_waitcnt vmcnt(3)\n\tv_cmp_ne_u64 vcc, %7, %0\n\ts_cbranch_vccz 2f\n\ts_sleep %8\n\t"
        "global_load_dwordx2 %0, %5, %6 sc1\n\ts_waitcnt vmcnt(3)\n\tv_cmp_ne_u64 vcc, %7, %1\n\ts_cbranch_vccz 2f\n\ts_sleep %8\n\t"
        "global_load_dwordx2 %1, %5, %6 sc1\n\ts_waitcnt vmcnt(3)\n\tv_cmp_ne_u64 vcc, %7, %2\n\ts_cbranch_vccz 2f\n\ts_sleep %8\n\t"
        "global_load_dwordx2 %2, %5, %6 sc1\n\ts_waitcnt vmcnt(3)\n\tv_cmp_ne_u64 vcc, %7, %3\n\ts_cbranch_vccz 2f\n\ts_sleep %8\n\t"
        "s_sub_u32 %4, %4, 1\n\ts_cmp_lg_u32 %4, 0\n\ts_cbranch_scc1 1b\n"
        "2:\n\ts_barrier"
        : "=&v"(v0), "=&v"(v1), "=&v"(v2), "=&v"(v3), "+s"(spins)
        : "v"(fo), "s"(ctl), "s"(tag), "i"(PGAP), "s"(w), "i"(POLLW) : "memory", "vcc", "scc");
  }
  STAMP(8);
  u32x4 af[2][15];
  auto lda = [&](int r, int s) INL {
    const int so = (kidx(s) < nks) ? ((2 * mt + r) * NKS + ks0 + kidx(s)) * 1024 : A2_BYTES;
    af[r][s] = __builtin_amdgcn_raw_buffer_load_b128(rsA2, lane * 16, so, AUX_SC1);
  };
#pragma unroll
  for (int n = 0; n < WIN; ++n) lda(n / 15, n % 15);
  SB();
  u32 pw[4];
#pragma unroll
  for (int y = 0; y < 4; ++y) pw[y] = ((lane + 4 * y) & 63) * 16 + y * 1024 + 4 * R.wb;
  const int blk = lane & 7;
  const u32 prd = (((lane & 32) + 4 * w + ((lane >> 3) & 3) + 4 * (blk & 3)) & 63) * 16 + (blk & 3) * 1024;
  const u32 prd0 = prd + (blk >> 2) * SLOT, prd1 = prd + (blk >> 2) * 4096 + 2 * SLOT;
  const __amdgpu_buffer_rsrc_t rsO = __builtin_amdgcn_make_buffer_rsrc(P.out, 0, 2048 * LDX, 0x00020000);
  auto reduce = [&](int r, f32x4 xb) INL {
    f32x4 p[8];
#pragma unroll
    for (int sw = 0; sw < 8; ++sw) p[sw] = ldsf4(L, r == 0 ? prd0 + sw * 4096 : prd1 + sw * 8192);
    SB();
    const u32x2 o = pk4(((p[0] + p[1]) + (p[2] + p[3])) + ((p[4] + p[5]) + (p[6] + p[7])) + xb);
    auto e0 = __builtin_amdgcn_permlane32_swap(o[0], o[0], false, false), e1 = __builtin_amdgcn_permlane32_swap(o[1], o[1], false, false);
    __builtin_amdgcn_raw_buffer_store_b128(u32x4{e0[0], e1[0], e0[1], e1[1]}, rsO, lane < 32 ? ooff + r * 32 * LDX : 2048 * LDX, 0, 0);
  };
  f32x16 pa[2][2];
  auto pwrite = [&](int r, int b8) INL {
    const f32x16& a = pa[r][b8 >> 2];
    const int y = b8 & 3;
    *(LAS f32x4*)(L + pw[y] + (r == 0 ? (b8 >> 2) * SLOT : 2 * SLOT + 4 * R.wb + (b8 >> 2) * 4096)) =
        f32x4{a[4 * y], a[4 * y + 1], a[4 * y + 2], a[4 * y + 3]};
  };
  u32x2 xr0, xr1, bres;
  sfor<0, 2>([&](auto rc_) INL {
    constexpr int r = decltype(rc_)::v;
#pragma unroll
    for (int i = 0; i < 16; ++i) pa[r][0][i] = pa[r][1][i] = 0.f;
    sfor<0, 15>([&](auto sc_) INL {
      constexpr int s = decltype(sc_)::v;
      pa[r][0] = mfma32(wf[s][0], af[r][s], pa[r][0]);
      PIN(pa[r][0]);
      if constexpr (r == 1 && s >= 1 && s < 5) pwrite(0, 2 * s - 2);
      pa[r][1] = mfma32(wf[s][1], af[r][s], pa[r][1]);
      PIN(pa[r][1]);
      if constexpr (r == 1 && s >= 1 && s < 5) pwrite(0, 2 * s - 1);
      if constexpr (r == 0 && s == 0) asm volatile("" ::"v"(v0), "v"(v1), "v"(v2), "v"(v3));
      if constexpr (r == 0 && s == 0) STAMP(14);
      if constexpr (r == 0 && s == 14) STAMP(15);
      if constexpr (r == 0 && s == 1) {
        const u32 o4 = ooff + (lane >> 5) * 8;
        xr0 = *(const GAS u32x2*)(P.x + o4); xr1 = *(const GAS u32x2*)(P.x + (o4 + 32 * LDX));
        bres = *(const GAS u32x2*)(P.b2 + (o4 & 1023));
      }
      if constexpr (r * 15 + s + WIN < 30) lda((r * 15 + s + WIN) / 15, (r * 15 + s + WIN) % 15);
      if constexpr (r == 0 && s >= 7 && s < 13) wread(s + 2);
      if constexpr (r == 0 && s == 14) {
        const f32x4 bv = {bf_lo(bres[0]), bf_hi(bres[0]), bf_lo(bres[1]), bf_hi(bres[1])};
        xb0 = f32x4{bf_lo(xr0[0]), bf_hi(xr0[0]), bf_lo(xr0[1]), bf_hi(xr0[1])} + bv;
        xb1 = f32x4{bf_lo(xr1[0]), bf_hi(xr1[0]), bf_lo(xr1[1]), bf_hi(xr1[1])} + bv;
        PIN(xb0); PIN(xb1);
      }
      if constexpr (r == 1 && s == RMID) { lds_bar(); reduce(0, xb0); }
      SB();
    });
  });
#pragma unroll
  for (int y = 0; y < 8; ++y) pwrite(1, y);
  STAMP(9);
  lds_bar();
  reduce(1, xb1);
  if (j == 0 && tid == 0) __hip_atomic_store(ctl, tag, __ATOMIC_RELAXED, __HIP_MEMORY_SCOPE_AGENT);
  STAMP(10);
  STAMP_OUT();
  asm volatile(".Lfk_end%c0:" ::"i"(TYPE));
  __builtin_amdgcn_endpgm();
}

__global__ __launch_bounds__(512, 2) void fused(const char*, const char*, const char*, const char*, const u16*, const char*, const char*,
                                                char*, char*) {
  extern __shared__ __attribute__((aligned(16))) char smem[];
  LAS char* L = (LAS char*)smem;
  Stamps ST;
  STAMP(0);
  typedef u64 u64x8 __attribute__((ext_vector_type(8)));
  u64x8 ka;
  u64 kb;
  asm volatile("s_load_dwordx16 %0, %2, 0x0\n\ts_load_dwordx2 %1, %2, 0x40"
               : "=&s"(ka), "=&s"(kb) : "s"(__builtin_amdgcn_kernarg_segment_ptr()) : "memory");
  const int lane = threadIdx.x & 63, w = __builtin_amdgcn_readfirstlane(threadIdx.x >> 6);
  const int b = blockIdx.x, bi = b >> 3, g = (b & 3) * 4 + (bi & 3), j = ((bi >> 2) & 3) + ((b >> 2) & 1) * 4 + (bi >> 4) * 8;
  const bool ffn = j >= 8;
  if (w == 7) {
    u64 pc;
    u32 len;
    asm volatile("s_getpc_b64 %0\n.Lfk_here:\n\ts_mov_b32 %1, .Lfk_end0-.Lfk_here\n\ts_max_u32 %1, %1, .Lfk_end1-.Lfk_here\n\t"
                 "s_max_u32 %1, %1, .Lfk_end2-.Lfk_here\n\ts_max_u32 %1, %1, .Lfk_end3-.Lfk_here" : "=s"(pc), "=s"(len)::"scc");
    touch((const char*)pc + min((u32)(lane * 128 + (bi & 7) * 8192), len));
  }
  const int rowbase = ffn ? 128 * g : 128 * g - 16;
  Ring R;
  R.ldw = ffn ? LDW1 : LDQKV;
  R.maxch = ffn ? (j == 15 ? 1 : 5) : 7;
  R.wb = w * 1024;
  R.rot = ROTX == 0 ? 0 : (ROTX == 1 ? (j + g) & 7 : (ROTX == 2 ? (2 * (b & 3) + (b & 4)) & 7 : (ROTX == 3 ? (2 * (b & 3) + (b & 4) + (bi & 1)) & 7 : (b & 1) * 4)));
  R.lanes(lane);
  R.stepB = 64 * R.ldw;
  R.mlo = (!ffn && w == 0) ? 0xFFFFFF00u : 0xFFFFFFFFu;
  R.xcond = ffn ? 2u : (u32)(w - 4);
  const u32 oA0 = (rowbase + 8 * w < 0 ? 8 * w : rowbase + 8 * w) * LDX, oA1 = (rowbase + 8 * (w + 8)) * LDX;
  const u32 oB = 8 * w * R.ldw + (ffn ? (j - 8) * 352 : j * 128), seg = ffn ? 128 : 1024;
  asm volatile("s_waitcnt lgkmcnt(0)" : "+s"(ka), "+s"(kb)::"memory");
  const Ptrs P = {(const char*)(GAS const char*)ka[0], (const char*)(GAS const char*)ka[1], (const char*)(GAS const char*)ka[2],
                  (const char*)(GAS const char*)ka[3], (const u16*)(GAS const u16*)ka[4], (const char*)(GAS const char*)ka[5],
                  (const char*)(GAS const char*)ka[6], (char*)(GAS char*)ka[7], (char*)(GAS char*)kb};
  STAMP(11);
  R.sA0 = P.x + oA0;
  R.sA1 = P.x + oA1;
  R.sB0 = (const char*)((ffn ? ka[3] : ka[1]) + oB);
  R.sB1 = R.sB0 + seg;
  R.sB2 = R.sB1 + seg;
  sfor<0, 6>([&](auto kc) INL { R.template issue<0, decltype(kc)::v>(); });
  if (w < 4) sfor<0, 5>([&](auto kc) INL { R.template issue<1, decltype(kc)::v>(); });
  if (!ffn && w == 0 && lane < 8) {
    const u32x4 one = {0x3F803F80u, 0x3F803F80u, 0x3F803F80u, 0x3F803F80u};
#pragma unroll
    for (int s = 0; s < 3; ++s) *(LAS u32x4*)(L + s * SLOT + lane * 16) = one;
  }
  STAMP(12);
  if (!ffn) {
    if (w < 4) body<0>(L, P, R, g, j, w STAMP_PASS);
    else body<1>(L, P, R, g, j, w STAMP_PASS);
  } else {
    if (w < 4) body<2>(L, P, R, g, j, w STAMP_PASS);
    else body<3>(L, P, R, g, j, w STAMP_PASS);
  }
}
}

extern "C" void kernel_launch(void* const* d_in, const int* in_sizes, int n_in, void* d_out, int out_size, void* d_ws,
                              size_t ws_size, hipStream_t stream) {
  (void)hipFuncSetAttribute((const void*)fk::fused, hipFuncAttributeMaxDynamicSharedMemorySize, fk::L_TOTAL);
  fk::fused<<<256, 512, fk::L_TOTAL, stream>>>((const char*)d_in[0], (const char*)d_in[1], (const char*)d_in[2], (const char*)d_in[4],
                                               (const u16*)d_in[5], (const char*)d_in[6], (const char*)d_in[7], (char*)d_out,
                                               (char*)d_ws);
}
```

```cpp
#define PRO 3
#include <hip/hip_runtime.h>
#include <stdint.h>

typedef unsigned u32;
typedef unsigned long long u64;
typedef unsigned short u16;
typedef short s16x4 __attribute__((ext_vector_type(4)));
typedef short s16x8 __attribute__((ext_vector_type(8)));
typedef __bf16 b16x2 __attribute__((ext_vector_type(2)));
typedef float f32x2 __attribute__((ext_vector_type(2)));
typedef float f32x4 __attribute__((ext_vector_type(4)));
typedef float f32x16 __attribute__((ext_vector_type(16)));
typedef u32 u32x2 __attribute__((ext_vector_type(2)));
typedef u32 u32x4 __attribute__((ext_vector_type(4)));
#define LAS __attribute__((address_space(3)))
#define GAS __attribute__((address_space(1)))
#define DEV __device__ __forceinline__
#define INL __attribute__((always_inline))

namespace fk {
constexpr int LDX = 1024, LDQKV = 3072, LDW1 = 2752, LDW2 = 1024;
constexpr int NKS = 118;
constexpr int SLOT = 43008, BOFF = 18432;
constexpr int L_MEAN = 3 * SLOT, L_RSTD = L_MEAN + 576, L_CS = L_RSTD + 576, L_B1 = L_CS + 768, L_OWN = L_B1 + 768,
              L_KV = L_MEAN + 3584, L_JUNK = L_KV + 8192, L_SP = L_JUNK + 2048, L_TOTAL = L_SP + 9 * 2048;
static_assert(L_OWN + 576 + 32 <= L_KV && L_TOTAL <= 163840, "LDS map");
constexpr int WS_GRP = 8192, WS_STAT = 4096;
constexpr int WS_A2 = 16 * WS_GRP, A2_BYTES = 64 * NKS * 1024;
constexpr int AUX_SC1 = 16;
constexpr u32 SPIN_CAP = 1u << 16;

#define PRIO 0
#define ILV 1
#define ROTM 7
#define PF1 0
#define S2LATE 0
#define KROT 0
#define RSW 0
#define ROTX 1
#define NPRE 2
#define MAP 2
#define RMID 11
#define WIN 6
#define POLLW 7
#define PGAP 2
#define NEPQ 12
#define NEPF 12

#define STAMP(i) do {} while (0)
#define STAMP_ARG
#define STAMP_PASS
#define STAMP_OUT() do {} while (0)

template <int IMM> DEV void dma16(u32 voff, const void* sbase, u32 lb) {
  asm volatile("s_add_u32 m0, %2, %3\n\ts_nop 0\n\tglobal_load_lds_dwordx4 %0, %1" ::"v"(voff), "s"(sbase), "s"(lb), "i"(IMM) : "memory", "scc");
}
template <int IMM> DEV void dma16_masked(u32 voff, const void* sbase, u32 lb, u32 mlo) {
  asm volatile("s_add_u32 m0, %2, %4\n\ts_mov_b32 exec_lo, %3\n\tglobal_load_lds_dwordx4 %0, %1\n\ts_mov_b32 exec_lo, -1"
               ::"v"(voff), "s"(sbase), "s"(lb), "s"(mlo), "i"(IMM) : "memory", "scc");
}
template <int IMM> DEV void dma16_if(u32 voff, const void* sbase, u32 lb, u32 cond) {
  asm volatile("s_cmp_gt_u32 %3, 1\n\ts_cbranch_scc1 1f\n\ts_add_u32 m0, %2, %4\n\ts_nop 0\n\tglobal_load_lds_dwordx4 %0, %1\n1:"
               ::"v"(voff), "s"(sbase), "s"(lb), "s"(cond), "i"(IMM) : "memory", "scc");
}
DEV void touch(const void* p) {
  asm volatile("s_mov_b32 m0, %1\n\ts_nop 0\n\tglobal_load_lds_dword %0, off sc1" ::"v"(p), "i"(L_JUNK) : "memory");
}
template <int N> DEV void wait_bar() {
  asm volatile("s_waitcnt vmcnt(%0) lgkmcnt(0)\n\ts_barrier" ::"i"(N) : "memory");
}
template <int N> DEV void wait_vm() { asm volatile("s_waitcnt vmcnt(%0)" ::"i"(N) : "memory"); }
DEV void wait_lds() { asm volatile("s_waitcnt lgkmcnt(0)" ::: "memory"); }
DEV void lds_bar() { asm volatile("s_waitcnt lgkmcnt(0)\n\ts_barrier" ::: "memory"); }
#define SB() __builtin_amdgcn_sched_barrier(0)
#define PIN(x) asm volatile("" : "+v"(x))

DEV u32 f2u(float v) { return __builtin_bit_cast(u32, v); }
DEV float u2f(u32 v) { return __builtin_bit_cast(float, v); }
DEV u32 pk_bf16(float a, float b) {
  f32x2 p = {a, b};
  return __builtin_bit_cast(u32, __builtin_convertvector(p, b16x2));
}
DEV u32x2 pk4(f32x4 v) { return u32x2{pk_bf16(v[0], v[1]), pk_bf16(v[2], v[3])}; }
DEV float bf_lo(u32 v) { return u2f(v << 16); }
DEV float bf_hi(u32 v) { return u2f(v & 0xffff0000u); }
DEV float dot2(u32 a, u32 b, float c) {
  return __builtin_amdgcn_fdot2_f32_bf16(__builtin_bit_cast(b16x2, a), __builtin_bit_cast(b16x2, b), c, false);
}
DEV f32x4 mfma16(u32x4 a, u32x4 b, f32x4 c) {
  return __builtin_amdgcn_mfma_f32_16x16x32_bf16(__builtin_bit_cast(s16x8, a), __builtin_bit_cast(s16x8, b), c, 0, 0, 0);
}
DEV f32x4 mfma16k16(u32x2 a, u32x2 b, f32x4 c) {
  return __builtin_amdgcn_mfma_f32_16x16x16bf16_1k(__builtin_bit_cast(s16x4, a), __builtin_bit_cast(s16x4, b), c, 0, 0, 0);
}
DEV f32x16 mfma32(u32x4 a, u32x4 b, f32x16 c) {
  return __builtin_amdgcn_mfma_f32_32x32x16_bf16(__builtin_bit_cast(s16x8, a), __builtin_bit_cast(s16x8, b), c, 0, 0, 0);
}
DEV u32x4 lds128(LAS char* L, u32 off) { return *(LAS u32x4*)(L + off); }
DEV f32x4 ldsf4(LAS char* L, u32 off) { return *(LAS f32x4*)(L + off); }
DEV float ldsf(LAS char* L, u32 off) { return *(LAS float*)(L + off); }
DEV u32x2 ldstr(LAS char* L, u32 off) {
  return __builtin_bit_cast(u32x2, __builtin_amdgcn_ds_read_tr16_b64_v4i16((LAS s16x4*)(L + off)));
}
template <int CTRL> DEV float dpp_add(float v) {
  int t = __builtin_amdgcn_update_dpp(0, (int)f2u(v), CTRL, 0xf, 0xf, true);
  return v + u2f((u32)t);
}
DEV f32x2 swap16(float a, float b) {
  auto q = __builtin_amdgcn_permlane16_swap(f2u(a), f2u(b), false, false);
  return f32x2{u2f(q[0]), u2f(q[1])};
}
DEV f32x2 swap32(float a, float b) {
  auto q = __builtin_amdgcn_permlane32_swap(f2u(a), f2u(b), false, false);
  return f32x2{u2f(q[0]), u2f(q[1])};
}
DEV float vmax(float a, float b) { return __builtin_amdgcn_fmed3f(a, b, __builtin_inff()); }
DEV float sel(bool c, float a, float b) { return c ? a : b; }
DEV u32x4 pair_rows(u32x2 x, u32x2 y) {
  auto a = __builtin_amdgcn_permlane16_swap(x[0], y[0], false, false);
  auto b = __builtin_amdgcn_permlane16_swap(x[1], y[1], false, false);
  return u32x4{a[0], b[0], a[1], b[1]};
}
DEV f32x4 splat4(float v) { return f32x4{v, v, v, v}; }
DEV int fresh_tid() {
  int t = threadIdx.x;
  asm volatile("" : "+v"(t));
  return t;
}
DEV void st128(u32x4 v, __amdgpu_buffer_rsrc_t rs, u32 voff, u32 soff) {
  __builtin_amdgcn_raw_buffer_store_b128(v, rs, voff, soff, AUX_SC1);
  asm volatile("s_nop 1" ::"v"(v));
}
template <int V> struct IC { static constexpr int v = V; };
template <int LO, int HI, class F> DEV void sfor(F&& f) {
  if constexpr (LO < HI) { f(IC<LO>{}); sfor<LO + 1, HI>(f); }
}

struct Ptrs { const char *x, *wq, *wo, *w1; const u16* b1; const char *w2, *b2; char *out, *ws; };
struct Ring {
  u32 voffA, voffB, voffB2, stepB, mlo, xcond, wb;
  int rot, ldw, maxch;
  const char *sA0, *sA1, *sB0, *sB1, *sB2;
  DEV void lanes(int lane) {
    const int r8 = lane >> 3, pos = lane & 7;
    const int swB = (r8 & 2) | ((wb >> 8) & 4);
    voffA = r8 * LDX + ((pos ^ r8) << 4);
    voffB = r8 * ldw + ((pos ^ swB) << 4);
    voffB2 = r8 * ldw + (min(pos ^ swB, maxch) << 4);
  }
  template <int I, int K> DEV void issue() const {
    const int kc = (I + rot) & 7;
    constexpr int slot = (I % 3) * SLOT;
    if constexpr (K == 0) dma16_masked<slot>(voffA + kc * 128, sA0, wb, mlo);
    if constexpr (K == 1) dma16<slot + BOFF>(voffB + kc * stepB, sB0, wb);
    if constexpr (K == 2) dma16<slot + BOFF + 8192>(voffB + kc * stepB, sB1, wb);
    if constexpr (K == 3) dma16<slot + BOFF + 16384>(voffB2 + kc * stepB, sB2, wb);
    if constexpr (K == 4) dma16<slot + 8192>(voffA + kc * 128, sA1, wb);
    if constexpr (K == 5) dma16_if<slot + 12288>(voffA + kc * 128 + 32 * LDX, sA1, wb, xcond);
  }
  template <int I, int WANT> DEV void issue5_if(u32 me) const {
    const int kc = (I + rot) & 7;
    constexpr int slot = (I % 3) * SLOT;
    asm volatile("s_cmp_lg_u32 %10, %11\n\ts_cbranch_scc1 1f\n\t"
                 "s_add_u32 m0, %8, %12\n\ts_mov_b32 exec_lo, %9\n\tglobal_load_lds_dwordx4 %0, %3\n\ts_mov_b32 exec_lo, -1\n\t"
                 "s_add_u32 m0, %8, %13\n\ts_nop 0\n\tglobal_load_lds_dwordx4 %1, %4\n\t"
                 "s_add_u32 m0, %8, %14\n\ts_nop 0\n\tglobal_load_lds_dwordx4 %1, %5\n\t"
                 "s_add_u32 m0, %8, %15\n\ts_nop 0\n\tglobal_load_lds_dwordx4 %2, %6\n\t"
                 "s_add_u32 m0, %8, %16\n\ts_nop 0\n\tglobal_load_lds_dwordx4 %0, %7\n1:"
                 ::"v"(voffA + kc * 128), "v"(voffB + kc * stepB), "v"(voffB2 + kc * stepB), "s"(sA0), "s"(sB0), "s"(sB1), "s"(sB2),
                   "s"(sA1), "s"(wb), "s"(mlo), "s"(me), "i"(WANT), "i"(slot), "i"(slot + BOFF), "i"(slot + BOFF + 8192),
                   "i"(slot + BOFF + 16384), "i"(slot + 8192) : "memory", "scc");
  }
};

template <int TYPE>
DEV void body(LAS char* L, const Ptrs P, Ring R, int g, int j, int w STAMP_ARG) {
  constexpr bool FFN = TYPE >= 2;
  constexpr int ODD = TYPE & 1;
  constexpr int NT = TYPE == 0 ? 5 : (TYPE == 1 ? 4 : 2);
  constexpr int NC = TYPE == 2 ? 6 : (TYPE == 3 ? 5 : 3);
  constexpr int CB = TYPE == 3 ? 6 : 0;
  constexpr int ND = TYPE == 1 ? 6 : 5;
  constexpr int NM = NT * NC + (FFN ? 2 : 0);
  constexpr int NEPI = FFN ? NEPF : NEPQ;
  constexpr int RW = (PRO == 3 || PRO == 5) ? 5 : (PRO == 4 ? 10 - NPRE : ((ODD || PRO == 2) ? 10 : 5));
  constexpr int NRL = FFN ? 2 : 4;
  int tid = fresh_tid();
  int lane = tid & 63;
  R.lanes(lane);
  int lg = lane >> 4, lc = lane & 15;
  GAS u64* ctl = (GAS u64*)(P.ws + g * WS_GRP);
  u32x4 r0, r1, r2;
  u64 epoch;
  if (PRO < 2) {
  if (FFN)
    asm volatile("global_load_dwordx2 %0, %2, %3 sc1\n\tglobal_load_dwordx4 %1, %4, %5"
                 : "=&v"(epoch), "=&v"(r0) : "v"(0u), "s"(ctl), "v"(lane * 16), "s"(P.x + (size_t)(128 * g + 8 * j + w) * LDX) : "memory");
  else
    asm volatile("global_load_dwordx2 %0, %4, %5 sc1\n\tglobal_load_dwordx4 %1, %6, %7\n\t"
                 "global_load_dwordx4 %2, %6, %8\n\tglobal_load_dwordx4 %3, %6, %8 offset:1024"
                 : "=&v"(epoch), "=&v"(r0), "=&v"(r1), "=&v"(r2)
                 : "v"(0u), "s"(ctl), "v"(lane * 16), "s"(P.x + (size_t)(128 * g + 8 * j + w) * LDX),
                   "s"(P.x + (size_t)max(128 * g - 16 + 2 * w, 0) * LDX) : "memory");
  }
  if (PF1 && !ODD) {
    const int n = min((int)blockIdx.x * 155 + w * 39 + min(lane, 38), 39679);
    const u64 p = n < 16384 ? (u64)P.x + (u32)(n * 128) : (n < 28672 ? (u64)P.wq + (u32)((n - 16384) * 128) : (u64)P.w1 + (u32)((n - 28672) * 128));
    touch((const void*)p);
  }
  const int T0 = TYPE == 0 ? 0 : (TYPE == 1 ? 5 : 2 * (w & 3));
  const u32 aA = T0 * 2048 + (lc >> 3) * 1024 + (lc & 7) * 128 + ((lg ^ (lc & 7)) << 4);
  const int tq = lc >> 2, tp = lc & 3;
  const int swR = (tq & 2) | ((lg & 1) << 2);
  const u32 aBl = BOFF + lg * 1024 + tq * 128 + ((tp >> 1) << 4) + (tp & 1) * 8;
  const int crot = min(2 * (w & 3), NC - 2);
  constexpr int NBB = FFN ? NC : 1;
  u32 aB[NBB][2], aAb[2][2];
#pragma unroll
  for (int c = 0; c < NBB; ++c) {
    const int cc = c + crot >= NC ? c + crot - NC : c + crot;
    const int ct = FFN ? CB + cc : (w & 3);
    aB[c][0] = aBl + (ct >> 2) * 8192 + (((2 * (ct & 3)) ^ swR) << 4);
    aB[c][1] = aB[c][0] + 65536;
    asm("" : "+v"(aB[c][0]), "+v"(aB[c][1]));
  }
#pragma unroll
  for (int b = 0; b < 2; ++b) {
    aAb[b][0] = aA ^ (b * 64);
    aAb[b][1] = aAb[b][0] + 65536;
    asm("" : "+v"(aAb[b][0]), "+v"(aAb[b][1]));
  }
  SB();
  if (ODD && PRO == 0) sfor<0, ND>([&](auto kc) INL { R.template issue<1, decltype(kc)::v>(); });
  f32x4 acc[NT][NC], acc1[2];
#pragma unroll
  for (int t = 0; t < NT; ++t)
#pragma unroll
    for (int c = 0; c < NC; ++c) acc[t][c] = f32x4{0.f, 0.f, 0.f, 0.f};
  acc1[0] = acc1[1] = f32x4{0.f, 0.f, 0.f, 0.f};
  u32x4 fa[2][NT];
  u32x2 fb[2][NC][2];
  auto rd = [&](int t, int k) INL {
    const int slot = ((t >> 1) % 3) * SLOT, b = t & 1;
    if (k >= 2 && k < 2 + NT) {
      const int o = slot + (k - 2) * 2048, hi = o >= 65536;
      fa[b][k - 2] = lds128(L, aAb[b][hi] + (o - hi * 65536));
    } else {
      const int kk = k < 2 ? k : k - NT, c = kk >> 1;
      const int o = slot + b * 4096 + (kk & 1) * 512 + (FFN ? 0 : c * 8192), hi = o >= 65536;
      fb[b][c][kk & 1] = ldstr(L, aB[FFN ? c : 0][hi] + (o - hi * 65536));
    }
  };
  const int mt = 2 * g + ((j >> 2) & 1), nt = (j & 3) + (j >> 3) * 4;
  const int ks0 = w < 6 ? 15 * w : 90 + 14 * (w - 6), nks = w < 6 ? 15 : 14;
  const int krot = KROT ? (2 * nt + 4 * (g & 3)) % 15 : 0;
  auto kidx = [&](int s) INL -> int { return s + krot >= 15 ? s + krot - 15 : s + krot; };
  const u32 voffW = (lane >> 3) * LDW2 + (((lane & 7) ^ ((lane >> 2) & 4)) << 4) + nt * 128;
  const u64 woa = (u64)P.wo, w2a = (u64)P.w2 - 32 * 16 * LDW2;
  const u32 wx0 = w < 5 ? 2 * SLOT + (16 + w) * 2048 : (11 + w) * 2048;
  const u32 wx1 = w < 5 ? SLOT + (16 + w) * 2048 : (w < 7 ? (14 + w) * 2048 : L_SP + 8 * 2048);
  auto wbase = [&](int s) INL -> u32 { const int k = s % 9; return k == 0 ? 2 * R.wb : (k == 5 ? wx0 : (k == 8 ? wx1 : 4 * R.wb)); };
  auto wdma = [&](auto sc_, auto hc_) INL {
    constexpr int s = decltype(sc_)::v, h = decltype(hc_)::v, k = s % 9;
    constexpr int imm = k == 0 ? L_SP : (k <= 2 ? 2 * SLOT + (k - 1) * 2048 : (k <= 4 ? (k - 3) * 2048 : (k == 6 || k == 7 ? SLOT + (k - 6) * 2048 : 0)));
    const int ks = min(ks0 + kidx(s), NKS - 1);
    const u64 base = (ks < 32 ? woa : w2a) + (u32)(ks * 16 * LDW2);
    dma16<imm + h * 1024>(voffW + h * 8 * LDW2, (const void*)base, wbase(s));
  };
  u32x4 wf[15][2];
  const u32 tb = (lane >> 5) * 1024 + tq * 128 + (((2 * (lg & 1) + (tp >> 1)) ^ ((tq & 2) << 1)) << 4) + (tp & 1) * 8;
  const __amdgpu_buffer_rsrc_t rsA2 = __builtin_amdgcn_make_buffer_rsrc(P.ws + WS_A2, 0, A2_BYTES, 0x00020000);
  const __amdgpu_buffer_rsrc_t rsG = __builtin_amdgcn_make_buffer_rsrc(P.ws + g * WS_GRP, 0, WS_GRP, 0x00020000);
  const u32x4 ones = {0x3F803F80u, 0x3F803F80u, 0x3F803F80u, 0x3F803F80u};
  u64 tag = 0;
  u32 b1v = 0;
  u32x4 gst;
  u64 tp2 = 0;
  u32 gsto = 0, gldo = 0, b1o = 0, stw = 0, b1w = 0, csw = 0, csw2 = 0;
  float som = 0.f, sor = 0.f;
  f32x4 mP, mP0, mC;
  u32 sel0 = 0, sel1 = 0, wra[4][2];
  auto wread = [&](int s) INL {
    const int k = s % 9;
#pragma unroll
    for (int f = 0; f < 2; ++f) {
      const u32 a = k == 0 ? wra[0][f] : (k <= 2 ? wra[2][f] + (k - 1) * 2048 : (k <= 4 ? wra[1][f] + (k - 3) * 2048 :
                    (k == 6 || k == 7 ? wra[1][f] + SLOT + (k - 6) * 2048 : wra[1][f] - 4 * R.wb + wbase(s))));
      const u32x2 lo = ldstr(L, a), hi = ldstr(L, a + 512);
      wf[s][f] = u32x4{lo[0], lo[1], hi[0], hi[1]};
    }
  };
  constexpr int NK = 5, TK = TYPE == 0 ? 0 : 4;
  float mc1[NK], rc1[NK];
  f32x4 m4[NK], r4[NK], bb[NC];
  auto rdst = [&](int k) INL {
    if (FFN) {
      if (k < 2) mc1[k] = ldsf(L, L_MEAN + ((2 * (w & 3) + k) * 16 + lc) * 4);
      else if (k < 4) rc1[k - 2] = ldsf(L, L_RSTD + ((2 * (w & 3) + k - 2) * 16 + lc) * 4);
      else if (k < 4 + NC) {
        const int c = k - 4, ct = CB + (c + crot >= NC ? c + crot - NC : c + crot);
        bb[c] = ldsf4(L, L_B1 + (ct * 16 + lg * 4) * 4);
      }
    } else {
      const int i = k >> 2, T = TK + i;
      if (i >= NK) return;
      if ((k & 3) == 0) mc1[i] = ldsf(L, L_MEAN + (T * 16 + lc) * 4);
      if ((k & 3) == 1) rc1[i] = ldsf(L, L_RSTD + (T * 16 + lc) * 4);
      if ((k & 3) == 2) m4[i] = ldsf4(L, L_MEAN + (T * 16 + lg * 4) * 4);
      if ((k & 3) == 3) r4[i] = ldsf4(L, L_RSTD + (T * 16 + lg * 4) * 4);
    }
  };
      {
        const int b_ = blockIdx.x, ln = min(b_ * 59 + w * 8 + (lane & 7), 15103);
        tp2 = (ln < 4096 ? woa : w2a) + (u32)(ln * 128);
        const u64 pa = (u64)P.ws + (u32)(WS_A2 + (lane & 3) * (2u << 20) + (b_ & 63) * 4096), po = (u64)P.out + (u32)(b_ * 8192);
        tp2 = (lane >= 8 && lane < 12) ? pa : tp2;
        tp2 = lane == 12 ? po : tp2;
        gsto = (w == 7 && lane < 8) ? WS_STAT + (j * 8 + lane) * 16 : WS_GRP;
        gldo = WS_STAT + (tid & 127) * 16;
        b1o = 2 * (u32)min(max(j - 8, 0) * 176 + tid, 1375);
        stw = (!ODD && w < 2) ? L_MEAN + (tid + (FFN ? 0 : 16)) * 4 : L_JUNK + lane * 4;
        b1w = tid < 192 ? L_B1 + tid * 4 : L_JUNK + lane * 4;
        csw = lc == 0 ? L_CS + ((FFN ? CB + crot : (w & 3)) * 16 + lg * 4) * 4 : L_JUNK + lane * 16;
        csw2 = lg == 0 ? L_CS + (128 + (w & 3) * 16 + lc) * 4 : L_JUNK + lane * 4;
        if (!FFN) {
#pragma unroll
          for (int r = 0; r < 4; ++r) {
            mP[r] = (4 * lg + r >= lc + 2) ? 0.f : -1e30f;
            mP0[r] = (TYPE == 0 && g == 0) ? -1e30f : mP[r];
            mC[r] = (4 * lg + r <= lc) ? 0.f : -1e30f;
          }
          sel0 = (lc & 1) ? 0x01000c0cu : 0x0c0c0100u; sel1 = (lc & 1) ? 0x03020c0cu : 0x0c0c0302u;
          asm volatile("" : "+v"(mP), "+v"(mC), "+v"(sel0), "+v"(sel1));
          if (TYPE == 0) asm volatile("" : "+v"(mP0));
        }
#pragma unroll
        for (int f = 0; f < 2; ++f) {
          wra[0][f] = (tb ^ (f * 64)) + 2 * R.wb + L_SP;
          wra[1][f] = (tb ^ (f * 64)) + 4 * R.wb;
          wra[2][f] = (tb ^ (f * 64)) + 4 * R.wb + 2 * SLOT;
          asm volatile("" : "+v"(wra[0][f]), "+v"(wra[1][f]), "+v"(wra[2][f]));
        }
        asm volatile("" : "+v"(tp2), "+v"(gsto), "+v"(gldo), "+v"(b1o), "+v"(stw), "+v"(b1w), "+v"(csw), "+v"(csw2));
      }
  SB();
  if (PRO == 2 || PRO == 4) {
  if (FFN)
    asm volatile("global_load_dwordx2 %0, %2, %3 sc1\n\tglobal_load_dwordx4 %1, %4, %5"
                 : "=&v"(epoch), "=&v"(r0) : "v"(0u), "s"(ctl), "v"(lane * 16), "s"(P.x + (size_t)(128 * g + 8 * j + w) * LDX) : "memory");
  else
    asm volatile("global_load_dwordx2 %0, %4, %5 sc1\n\tglobal_load_dwordx4 %1, %6, %7\n\t"
                 "global_load_dwordx4 %2, %6, %8\n\tglobal_load_dwordx4 %3, %6, %8 offset:1024"
                 : "=&v"(epoch), "=&v"(r0), "=&v"(r1), "=&v"(r2)
                 : "v"(0u), "s"(ctl), "v"(lane * 16), "s"(P.x + (size_t)(128 * g + 8 * j + w) * LDX),
                   "s"(P.x + (size_t)max(128 * g - 16 + 2 * w, 0) * LDX) : "memory");
  }
  if ((ODD && PRO == 1) || PRO >= 2) sfor<(PRO >= 4 ? NPRE : 0), ND>([&](auto kc) INL { R.template issue<1, decltype(kc)::v>(); });
  if (PRO == 3 || PRO == 5) {
  if (FFN)
    asm volatile("global_load_dwordx2 %0, %2, %3 sc1\n\tglobal_load_dwordx4 %1, %4, %5"
                 : "=&v"(epoch), "=&v"(r0) : "v"(0u), "s"(ctl), "v"(lane * 16), "s"(P.x + (size_t)(128 * g + 8 * j + w) * LDX) : "memory");
  else
    asm volatile("global_load_dwordx2 %0, %4, %5 sc1\n\tglobal_load_dwordx4 %1, %6, %7\n\t"
                 "global_load_dwordx4 %2, %6, %8\n\tglobal_load_dwordx4 %3, %6, %8 offset:1024"
                 : "=&v"(epoch), "=&v"(r0), "=&v"(r1), "=&v"(r2)
                 : "v"(0u), "s"(ctl), "v"(lane * 16), "s"(P.x + (size_t)(128 * g + 8 * j + w) * LDX),
                   "s"(P.x + (size_t)max(128 * g - 16 + 2 * w, 0) * LDX) : "memory");
  }
  if (PRIO == 1 && ODD) __builtin_amdgcn_s_setprio(1);
  if (PRIO == 2 && !ODD) __builtin_amdgcn_s_setprio(1);
  STAMP(1);
  wait_bar<5 + NRL + (PF1 && !ODD)>();
  STAMP(2);
#pragma unroll
  for (int k = 0; k < 2 * NC + NT; ++k) rd(0, k);
  SB();
  sfor<0, 16>([&](auto tc) INL {
    constexpr int t = decltype(tc)::v, s = t >> 1, b = t & 1;
    if constexpr (b) {
      if constexpr (s == 1 || s == 2) wait_bar<6 + ODD>();
      else if constexpr (s == 0) wait_bar<5 + (PF1 && !ODD)>();
      else if constexpr (s <= 4) wait_bar<5>();
      else if constexpr (s == 5) wait_bar<7>();
      else if constexpr (s == 6) wait_bar<4>();
    }
    if constexpr (t == 11) {
      if constexpr (!ODD) asm volatile("" : "+v"(gst), "+v"(b1v));
      if constexpr (!ODD) {
        if (__builtin_expect(!__all(gst[1] == (u32)tag && gst[3] == (u32)tag), 0)) {
#pragma nounroll
          for (u32 spins = 0; spins < SPIN_CAP; ++spins) {
            __builtin_amdgcn_s_sleep(1);
            gst = __builtin_amdgcn_raw_buffer_load_b128(rsG, gldo, 0, AUX_SC1);
            if (__all(gst[1] == (u32)tag && gst[3] == (u32)tag)) break;
          }
        }
      }
      SB();
    }
    constexpr int nr = t == 15 ? 0 : 2 * NC + NT;
    constexpr int st = t == 0 ? 2 : (t + 5) / 2;
    constexpr int k0 = (t == 0 || b) ? 0 : 3, k1 = (st >= 8 || (t == 0 && S2LATE)) ? 0 : ((t == 0 || !b) ? ND : 3);
    constexpr int nd = k1 > k0 ? k1 - k0 : 0, nw = t >= 10 ? 2 : 0, nv = nd + nw;
    sfor<0, NM>([&](auto qc) INL {
      constexpr int q = decltype(qc)::v;
      constexpr int qq = t < 15 ? q : (FFN ? (q + NT * NC) % NM : (TYPE == 1 ? q : (q < 3 ? q * NT : (q < 5 ? (q - 2) * NT + 4 :
                         (q - 5 < 4 ? q - 4 : (q - 5 < 7 ? q - 3 : q - 1))))));
      if constexpr (qq < NT * NC) {
        constexpr int c = qq / NT, i = qq % NT;
        const u32x4 wfr = {fb[b][c][0][0], fb[b][c][0][1], fb[b][c][1][0], fb[b][c][1][1]};
        if constexpr (!FFN && c == 2) acc[i][c] = mfma16(fa[b][i], wfr, acc[i][c]);
        else acc[i][c] = mfma16(wfr, fa[b][i], acc[i][c]);
      } else {
        constexpr int c = qq - NT * NC;
        const u32x4 wfr = {fb[b][c][0][0], fb[b][c][0][1], fb[b][c][1][0], fb[b][c][1][1]};
        acc1[c] = mfma16(wfr, ones, acc1[c]);
      }
      if constexpr (q < nr) rd(t + 1, q);
      if constexpr (t == 15) { rdst(2 * q); rdst(2 * q + 1); }
      if constexpr (t == 9 && q == 0 && !ODD)
        asm volatile("buffer_load_dwordx4 %0, %2, %3, 0 offen sc1\n\tglobal_load_ushort %1, %4, %5"
                     : "=&v"(gst), "=&v"(b1v) : "v"(gldo), "s"(rsG), "v"(b1o), "s"(P.b1) : "memory");
      if constexpr (t == 11 && !ODD) {
        if constexpr (q == NM - 4) *(LAS u32*)(L + stw) = gst[0];
        if constexpr (q == NM - 3) *(LAS u32*)(L + stw + 576) = gst[2];
        if constexpr (q == NM - 2 && FFN) *(LAS float*)(L + b1w) = bf_lo(b1v);
      }
      if constexpr (t == 1 && ODD && q == NM - 1) { som = ldsf(L, L_OWN + (lane & 7) * 4); sor = ldsf(L, L_OWN + 576 + (lane & 7) * 4); }
      sfor<0, nv>([&](auto uc) INL {
        constexpr int u = decltype(uc)::v;
        if constexpr (((2 * u + ODD) * NM) / (2 * nv) == q) {
          if constexpr (u < nd) R.template issue<st, k0 + u>();
          else wdma(IC<t - 10>{}, IC<u - nd>{});
        }
      });
      if constexpr (t == 2 && q == NM - 1) {
        touch((const void*)tp2);
        if (ODD) st128(u32x4{f2u(som), (u32)tag, f2u(sor), (u32)tag}, rsG, gsto, 0);
      }
      if constexpr (t == 15) {
        if constexpr (TYPE == 0) {
          if constexpr (q == 6) *(LAS f32x4*)(L + csw) = acc[0][0];
          if constexpr (q == 7) *(LAS f32x4*)(L + csw + 256) = acc[0][1];
          if constexpr (q == 8) *(LAS float*)(L + csw2) = acc[0][2][0];
          if constexpr (q == 9) *(LAS f32x4*)(L + L_KV + 2 * R.wb + lane * 16) = acc[NT - 1][1];
          if constexpr (q == 10) *(LAS f32x4*)(L + L_KV + 2 * R.wb + 1024 + lane * 16) = acc[NT - 1][2];
        }
        if constexpr (FFN) {
          if constexpr (q == 6) *(LAS f32x4*)(L + csw) = acc1[0];
          if constexpr (q == 7) *(LAS f32x4*)(L + csw + 64) = acc1[1];
        }
      }
      SB();
    });
    if constexpr (t == 0 && S2LATE) sfor<0, ND>([&](auto kc) INL { R.template issue<2, decltype(kc)::v>(); });
    if constexpr (t == 0) {
      if (FFN) asm volatile("s_waitcnt vmcnt(%2)" : "+v"(epoch), "+v"(r0) : "i"(RW) : "memory");
      else asm volatile("s_waitcnt vmcnt(%4)" : "+v"(epoch), "+v"(r0), "+v"(r1), "+v"(r2) : "i"(RW) : "memory");
      tag = (((u64)(u32)__builtin_amdgcn_readfirstlane((u32)(epoch >> 32)) << 32) | (u32)__builtin_amdgcn_readfirstlane((u32)epoch)) + 1;
      float sS[3] = {0.f, 0.f, 0.f}, sQ[3] = {0.f, 0.f, 0.f};
#pragma unroll
      for (int d = 0; d < 4; ++d) {
        sS[0] = dot2(r0[d], 0x3F803F80u, sS[0]); sQ[0] = dot2(r0[d], r0[d], sQ[0]);
        if (!FFN) {
          sS[1] = dot2(r1[d], 0x3F803F80u, sS[1]); sQ[1] = dot2(r1[d], r1[d], sQ[1]);
          sS[2] = dot2(r2[d], 0x3F803F80u, sS[2]); sQ[2] = dot2(r2[d], r2[d], sQ[2]);
        }
      }
      f32x2 a = swap32(sS[0], sS[FFN ? 0 : 1]), c = swap32(sQ[0], sQ[FFN ? 0 : 1]);
      float S = a[0] + a[1], Q = c[0] + c[1], S2 = S, Q2 = Q;
      if (!FFN) {
        a = swap32(sS[2], sS[2]); c = swap32(sQ[2], sQ[2]);
        S2 = a[0] + a[1]; Q2 = c[0] + c[1];
      }
      a = swap16(S, S2); c = swap16(Q, Q2);
      S = a[0] + a[1]; Q = c[0] + c[1];
      S = dpp_add<0xB1>(S); Q = dpp_add<0xB1>(Q);
      S = dpp_add<0x4E>(S); Q = dpp_add<0x4E>(Q);
      S = dpp_add<0x141>(S); Q = dpp_add<0x141>(Q);
      S = dpp_add<0x140>(S); Q = dpp_add<0x140>(Q);
      const float m = S * (1.f / 512.f), e = Q * (1.f / 512.f);
      u32 adr = L_JUNK + lane * 4;
      adr = lane == 0 ? L_OWN + w * 4 : adr;
      if (!FFN) { adr = lane == 32 ? L_MEAN + w * 8 : adr; adr = lane == 16 ? L_MEAN + w * 8 + 4 : adr; }
      *(LAS float*)(L + adr) = -m;
      *(LAS float*)(L + adr + 576) = __builtin_amdgcn_rsqf(fmaxf(e - m * m, 0.f) + 1e-5f);
      SB();
    }
  });
  if (PRIO) __builtin_amdgcn_s_setprio(0);
  STAMP(3);
  lds_bar();
  STAMP(4);
  auto slot = [&](auto kc) INL {
    constexpr int k = decltype(kc)::v;
    SB();
    if constexpr (k < NEPI) wdma(IC<6 + k / 2>{}, IC<k % 2>{});
    SB();
  };
  if constexpr (!FFN) {
    const int h = w & 3;
    const f32x4 csq = ldsf4(L, L_CS + (h * 16 + lg * 4) * 4), csk = ldsf4(L, L_CS + (64 + h * 16 + lg * 4) * 4);
    const float csv1 = ldsf(L, L_CS + (128 + h * 16 + lc) * 4);
    f32x4 k4 = acc[0][1], v4 = acc[0][2];
    if (TYPE == 1) { k4 = ldsf4(L, L_KV + h * 2048 + lane * 16); v4 = ldsf4(L, L_KV + h * 2048 + 1024 + lane * 16); }
    slot(IC<0>{});
    wait_vm<7>();
    wread(0); wread(1); wread(2);
    const bool odd = lc & 1;
    const f32x4 csv = splat4(csv1);
    u32x2 Qb[4], Kb[NK], Vb[NK];
    sfor<0, NK>([&](auto ic) INL {
      constexpr int i = decltype(ic)::v, ai = TYPE == 0 ? i : (i > 0 ? i - 1 : 0);
      slot(IC<1 + i>{});
      const f32x4 mc = splat4(mc1[i]);
      Kb[i] = pk4(((i == 0 ? k4 : acc[ai][1]) + mc * csk) * splat4(rc1[i]));
      if constexpr (i > 0) Qb[i - 1] = pk4((acc[ai][0] + mc * csq) * splat4(rc1[i] * (0.25f * 1.4426950408889634f)));
      Vb[i] = pk4(((i == 0 ? v4 : acc[ai][2]) + m4[i] * csv) * r4[i]);
    });
    f32x4 sp[4], sc[4];
#pragma unroll
    for (int q = 0; q < 4; ++q) {
      sp[q] = mfma16k16(Kb[q], Qb[q], q == 0 ? mP0 : mP);
      sc[q] = mfma16k16(Kb[q + 1], Qb[q], mC);
    }
    SB();
    wait_lds();
    wait_vm<6>();
    wread(3); wread(4); wread(5);
    slot(IC<6>{});
    f32x4 a[4];
    float mx[4];
#pragma unroll
    for (int q = 0; q < 4; ++q) {
      a[q] = f32x4{sel(odd, sp[q][1], sp[q][0]), sel(odd, sp[q][3], sp[q][2]), sel(odd, sc[q][1], sc[q][0]), sel(odd, sc[q][3], sc[q][2])};
      mx[q] = vmax(vmax(a[q][0], a[q][1]), vmax(a[q][2], a[q][3]));
    }
    slot(IC<7>{});
    {
      const f32x2 x = swap32(mx[0], mx[1]), y = swap32(mx[2], mx[3]);
      const f32x2 z = swap16(vmax(x[0], x[1]), vmax(y[0], y[1]));
      const float u = vmax(z[0], z[1]);
      const f32x2 p = swap16(u, u);
      const f32x2 p0 = swap32(p[0], p[0]), p1 = swap32(p[1], p[1]);
      mx[0] = p0[0]; mx[1] = p0[1]; mx[2] = p1[0]; mx[3] = p1[1];
    }
    slot(IC<8>{});
#pragma unroll
    for (int q = 0; q < 4; ++q) a[q] -= splat4(mx[q]);
    slot(IC<9>{});
    sfor<0, 4>([&](auto qc) INL {
      constexpr int q = decltype(qc)::v;
#pragma unroll
      for (int r = 0; r < 4; ++r) a[q][r] = __builtin_amdgcn_exp2f(a[q][r]);
      if constexpr (q & 1) slot(IC<10 + q / 2>{});
    });
    wait_lds();
    const f32x4 z4 = {0.f, 0.f, 0.f, 0.f};
    u32x4 pb[4];
    sfor<0, 4>([&](auto qc) INL {
      constexpr int q = decltype(qc)::v;
      const u32x2 u = pk4(a[q]);
      pb[q] = u32x4{__builtin_amdgcn_perm(0, u[0], sel0), __builtin_amdgcn_perm(0, u[0], sel1),
                    __builtin_amdgcn_perm(0, u[1], sel0), __builtin_amdgcn_perm(0, u[1], sel1)};
      if constexpr (q & 1) slot(IC<12 + q / 2>{});
    });
#pragma unroll
    for (int q = 0; q < 4; ++q) {
      sc[q] = mfma16(ones, pb[q], z4);
      sp[q] = mfma16k16(Vb[q], u32x2{pb[q][0], pb[q][1]}, z4);
    }
#pragma unroll
    for (int q = 0; q < 4; ++q) sp[q] = mfma16k16(Vb[q + 1], u32x2{pb[q][2], pb[q][3]}, sp[q]);
    slot(IC<14>{}); slot(IC<15>{}); slot(IC<16>{}); slot(IC<17>{});
    u32x2 Ob[4];
#pragma unroll
    for (int q = 0; q < 4; ++q) Ob[q] = pk4(sp[q] * splat4(__builtin_amdgcn_rcpf(sc[q][0])));
    const int rt = 4 * g + (TYPE == 0 ? 0 : 2), ks = 4 * j + h;
    st128(pair_rows(Ob[0], Ob[1]), rsA2, lane * 16, (rt * NKS + ks) * 1024);
    st128(pair_rows(Ob[2], Ob[3]), rsA2, lane * 16, ((rt + 1) * NKS + ks) * 1024);
  } else {
    f32x4 cs[NC];
#pragma unroll
    for (int c = 0; c < NC; ++c) {
      const int ct = CB + (c + crot >= NC ? c + crot - NC : c + crot);
      cs[c] = ldsf4(L, L_CS + (ct * 16 + lg * 4) * 4);
    }
    wait_vm<6>();
    wread(0); wread(1); wread(2);
    const f32x4 mc[2] = {splat4(mc1[0]), splat4(mc1[1])}, rc[2] = {splat4(rc1[0]), splat4(rc1[1])};
    const int nct = j == 15 ? 9 : 11;
    const int rt = 4 * g + (w & 3), ksb = 32 + 11 * (j - 8);
    const f32x4 C1 = splat4(-2.f * 1.4426950408889634f * 0.7978845608028654f);
    const f32x4 C2 = splat4(-2.f * 1.4426950408889634f * 0.7978845608028654f * 0.044715f);
    const f32x4 ONE = splat4(1.f);
    sfor<0, 3>([&](auto gc) INL {
      constexpr int G = decltype(gc)::v, c0 = 2 * G, n = c0 + 1 < NC ? 4 : 2;
      if constexpr (G == 1) { wait_lds(); wait_vm<8>(); wread(3); wread(4); wread(5); }
      if constexpr (G == 2) wait_lds();
      f32x4 v[4], e[4];
      slot(IC<6 * G>{});
#pragma unroll
      for (int u = 0; u < n; ++u) v[u] = (acc[u & 1][c0 + (u >> 1)] + mc[u & 1] * cs[c0 + (u >> 1)]) * rc[u & 1] + bb[c0 + (u >> 1)];
      slot(IC<6 * G + 1>{});
#pragma unroll
      for (int u = 0; u < n; ++u) e[u] = v[u] * (v[u] * v[u] * C2 + C1);
      slot(IC<6 * G + 2>{});
#pragma unroll
      for (int u = 0; u < n; ++u) {
#pragma unroll
        for (int r = 0; r < 4; ++r) e[u][r] = __builtin_amdgcn_exp2f(e[u][r]);
        if (u == 1) slot(IC<6 * G + 3>{});
      }
#pragma unroll
      for (int u = 0; u < n; ++u) e[u] += ONE;
      slot(IC<6 * G + 4>{});
#pragma unroll
      for (int u = 0; u < n; ++u) {
#pragma unroll
        for (int r = 0; r < 4; ++r) e[u][r] = __builtin_amdgcn_rcpf(e[u][r]);
        if (u == 1) slot(IC<6 * G + 5>{});
      }
#pragma unroll
      for (int cc = 0; cc < n / 2; ++cc) {
        const int c = c0 + cc, ct = CB + (c + crot >= NC ? c + crot - NC : c + crot);
        st128(pair_rows(pk4(v[2 * cc] * e[2 * cc]), pk4(v[2 * cc + 1] * e[2 * cc + 1])), rsA2, lane * 16,
              ct < nct ? (rt * NKS + ksb + ct) * 1024 : A2_BYTES);
      }
      SB();
    });
  }
  STAMP(5);
  wait_vm<0>();
  STAMP(13);
  asm volatile("s_barrier" ::: "memory");
  if (tid == 0) __hip_atomic_store(ctl + 16 * (j + 1), tag, __ATOMIC_RELAXED, __HIP_MEMORY_SCOPE_AGENT);
  STAMP(6);
  sfor<NEPI, 18>([&](auto kc) INL { wdma(IC<6 + decltype(kc)::v / 2>{}, IC<decltype(kc)::v % 2>{}); });
#pragma unroll
  for (int s = 6; s < 9; ++s) wread(s);
  tid = fresh_tid(); lane = tid & 63; lg = lane >> 4; lc = lane & 15;
  const u32 ooff = (u32)((mt * 64 + 4 * w + ((lane >> 3) & 3)) * LDX + (nt * 64 + (lane & 7) * 8) * 2);
  f32x4 xb0, xb1;
#pragma unroll
  for (int s = 0; s < 9; ++s) asm volatile("" : "+v"(wf[s][0]), "+v"(wf[s][1]));
  STAMP(7);
  u64 v0, v1, v2, v3;
  wait_vm<0>();
  {
    const u32 fo = 128 * (lc + 1);
    u32 spins = SPIN_CAP;
    asm volatile(
        "s_cmp_lg_u32 %9, %10\n\ts_cbranch_scc1 2f\n\t"
        "global_load_dwordx2 %0, %5, %6 sc1\n\ts_sleep %8\n\tglobal_load_dwordx2 %1, %5, %6 sc1\n\ts_sleep %8\n\t"
        "global_load_dwordx2 %2, %5, %6 sc1\n\ts_sleep %8\n"
        "1:\n\t"
        "global_load_dwordx2 %3, %5, %6 sc1\n\ts_waitcnt vmcnt(3)\n\tv_cmp_ne_u64 vcc, %7, %0\n\ts_cbranch_vccz 2f\n\ts_sleep %8\n\t"
        "global_load_dwordx2 %0, %5, %6 sc1\n\ts_waitcnt vmcnt(3)\n\tv_cmp_ne_u64 vcc, %7, %1\n\ts_cbranch_vccz 2f\n\ts_sleep %8\n\t"
        "global_load_dwordx2 %1, %5, %6 sc1\n\ts_waitcnt vmcnt(3)\n\tv_cmp_ne_u64 vcc, %7, %2\n\ts_cbranch_vccz 2f\n\ts_sleep %8\n\t"
        "global_load_dwordx2 %2, %5, %6 sc1\n\ts_waitcnt vmcnt(3)\n\tv_cmp_ne_u64 vcc, %7, %3\n\ts_cbranch_vccz 2f\n\ts_sleep %8\n\t"
        "s_sub_u32 %4, %4, 1\n\ts_cmp_lg_u32 %4, 0\n\ts_cbranch_scc1 1b\n"
        "2:\n\ts_barrier"
        : "=&v"(v0), "=&v"(v1), "=&v"(v2), "=&v"(v3), "+s"(spins)
        : "v"(fo), "s"(ctl), "s"(tag), "i"(PGAP), "s"(w), "i"(POLLW) : "memory", "vcc", "scc");
  }
  STAMP(8);
  u32x4 af[2][15];
  auto lda = [&](int r, int s) INL {
    const int so = (kidx(s) < nks) ? ((2 * mt + r) * NKS + ks0 + kidx(s)) * 1024 : A2_BYTES;
    af[r][s] = __builtin_amdgcn_raw_buffer_load_b128(rsA2, lane * 16, so, AUX_SC1);
  };
#pragma unroll
  for (int n = 0; n < WIN; ++n) lda(n / 15, n % 15);
  SB();
  u32 pw[4];
#pragma unroll
  for (int y = 0; y < 4; ++y) pw[y] = ((lane + 4 * y) & 63) * 16 + y * 1024 + 4 * R.wb;
  const int blk = lane & 7;
  const u32 prd = (((lane & 32) + 4 * w + ((lane >> 3) & 3) + 4 * (blk & 3)) & 63) * 16 + (blk & 3) * 1024;
  const u32 prd0 = prd + (blk >> 2) * SLOT, prd1 = prd + (blk >> 2) * 4096 + 2 * SLOT;
  const __amdgpu_buffer_rsrc_t rsO = __builtin_amdgcn_make_buffer_rsrc(P.out, 0, 2048 * LDX, 0x00020000);
  auto reduce = [&](int r, f32x4 xb) INL {
    f32x4 p[8];
#pragma unroll
    for (int sw = 0; sw < 8; ++sw) p[sw] = ldsf4(L, r == 0 ? prd0 + sw * 4096 : prd1 + sw * 8192);
    SB();
    const u32x2 o = pk4(((p[0] + p[1]) + (p[2] + p[3])) + ((p[4] + p[5]) + (p[6] + p[7])) + xb);
    auto e0 = __builtin_amdgcn_permlane32_swap(o[0], o[0], false, false), e1 = __builtin_amdgcn_permlane32_swap(o[1], o[1], false, false);
    __builtin_amdgcn_raw_buffer_store_b128(u32x4{e0[0], e1[0], e0[1], e1[1]}, rsO, lane < 32 ? ooff + r * 32 * LDX : 2048 * LDX, 0, 0);
  };
  f32x16 pa[2][2];
  auto pwrite = [&](int r, int b8) INL {
    const f32x16& a = pa[r][b8 >> 2];
    const int y = b8 & 3;
    *(LAS f32x4*)(L + pw[y] + (r == 0 ? (b8 >> 2) * SLOT : 2 * SLOT + 4 * R.wb + (b8 >> 2) * 4096)) =
        f32x4{a[4 * y], a[4 * y + 1], a[4 * y + 2], a[4 * y + 3]};
  };
  u32x2 xr0, xr1, bres;
  sfor<0, 2>([&](auto rc_) INL {
    constexpr int r = decltype(rc_)::v;
#pragma unroll
    for (int i = 0; i < 16; ++i) pa[r][0][i] = pa[r][1][i] = 0.f;
    sfor<0, 15>([&](auto sc_) INL {
      constexpr int s = decltype(sc_)::v;
      pa[r][0] = mfma32(wf[s][0], af[r][s], pa[r][0]);
      PIN(pa[r][0]);
      if constexpr (r == 1 && s >= 1 && s < 5) pwrite(0, 2 * s - 2);
      pa[r][1] = mfma32(wf[s][1], af[r][s], pa[r][1]);
      PIN(pa[r][1]);
      if constexpr (r == 1 && s >= 1 && s < 5) pwrite(0, 2 * s - 1);
      if constexpr (r == 0 && s == 0) asm volatile("" ::"v"(v0), "v"(v1), "v"(v2), "v"(v3));
      if constexpr (r == 0 && s == 0) STAMP(14);
      if constexpr (r == 0 && s == 14) STAMP(15);
      if constexpr (r == 0 && s == 1) {
        const u32 o4 = ooff + (lane >> 5) * 8;
        xr0 = *(const GAS u32x2*)(P.x + o4); xr1 = *(const GAS u32x2*)(P.x + (o4 + 32 * LDX));
        bres = *(const GAS u32x2*)(P.b2 + (o4 & 1023));
      }
      if constexpr (r * 15 + s + WIN < 30) lda((r * 15 + s + WIN) / 15, (r * 15 + s + WIN) % 15);
      if constexpr (r == 0 && s >= 7 && s < 13) wread(s + 2);
      if constexpr (r == 0 && s == 14) {
        const f32x4 bv = {bf_lo(bres[0]), bf_hi(bres[0]), bf_lo(bres[1]), bf_hi(bres[1])};
        xb0 = f32x4{bf_lo(xr0[0]), bf_hi(xr0[0]), bf_lo(xr0[1]), bf_hi(xr0[1])} + bv;
        xb1 = f32x4{bf_lo(xr1[0]), bf_hi(xr1[0]), bf_lo(xr1[1]), bf_hi(xr1[1])} + bv;
        PIN(xb0); PIN(xb1);
      }
      if constexpr (r == 1 && s == RMID) { lds_bar(); reduce(0, xb0); }
      SB();
    });
  });
#pragma unroll
  for (int y = 0; y < 8; ++y) pwrite(1, y);
  STAMP(9);
  lds_bar();
  reduce(1, xb1);
  if (j == 0 && tid == 0) __hip_atomic_store(ctl, tag, __ATOMIC_RELAXED, __HIP_MEMORY_SCOPE_AGENT);
  STAMP(10);
  STAMP_OUT();
  asm volatile(".Lfk_end%c0:" ::"i"(TYPE));
  __builtin_amdgcn_endpgm();
}

__global__ __launch_bounds__(512, 2) void fused(const char*, const char*, const char*, const char*, const u16*, const char*, const char*,
                                                char*, char*) {
  extern __shared__ __attribute__((aligned(16))) char smem[];
  LAS char* L = (LAS char*)smem;
  STAMP(0);
  typedef u64 u64x8 __attribute__((ext_vector_type(8)));
  u64x8 ka;
  u64 kb;
  asm volatile("s_load_dwordx16 %0, %2, 0x0\n\ts_load_dwordx2 %1, %2, 0x40"
               : "=&s"(ka), "=&s"(kb) : "s"(__builtin_amdgcn_kernarg_segment_ptr()) : "memory");
  const int lane = threadIdx.x & 63, w = __builtin_amdgcn_readfirstlane(threadIdx.x >> 6);
  const int b = blockIdx.x, bi = b >> 3;
  const int g = MAP == 2 ? (b & 3) * 4 + (bi & 3) : (b & 7) * 2 + (bi & 1), j = MAP == 2 ? ((bi >> 2) & 3) + ((b >> 2) & 1) * 4 + (bi >> 4) * 8 : bi >> 1;
  const bool ffn = j >= 8;
  if (w == 7) {
    u64 pc;
    u32 len;
    asm volatile("s_getpc_b64 %0\n.Lfk_here:\n\ts_mov_b32 %1, .Lfk_end0-.Lfk_here\n\ts_max_u32 %1, %1, .Lfk_end1-.Lfk_here\n\t"
                 "s_max_u32 %1, %1, .Lfk_end2-.Lfk_here\n\ts_max_u32 %1, %1, .Lfk_end3-.Lfk_here" : "=s"(pc), "=s"(len)::"scc");
    touch((const char*)pc + min((u32)(lane * 128 + (bi & 7) * 8192), len));
  }
  const int rowbase = ffn ? 128 * g : 128 * g - 16;
  Ring R;
  R.ldw = ffn ? LDW1 : LDQKV;
  R.maxch = ffn ? (j == 15 ? 1 : 5) : 7;
  R.wb = w * 1024;
  R.rot = ROTX == 0 ? 0 : (ROTX == 1 ? (j + g) & 7 : (ROTX == 2 ? (2 * (b & 3) + (b & 4)) & 7 : (ROTX == 3 ? (2 * (b & 3) + (b & 4) + (bi & 1)) & 7 : (b & 1) * 4)));
  R.lanes(lane);
  R.stepB = 64 * R.ldw;
  R.mlo = (!ffn && w == 0) ? 0xFFFFFF00u : 0xFFFFFFFFu;
  R.xcond = ffn ? 2u : (u32)(w - 4);
  const u32 oA0 = (rowbase + 8 * w < 0 ? 8 * w : rowbase + 8 * w) * LDX, oA1 = (rowbase + 8 * (w + 8)) * LDX;
  const u32 oB = 8 * w * R.ldw + (ffn ? (j - 8) * 352 : j * 128), seg = ffn ? 128 : 1024;
  asm volatile("s_waitcnt lgkmcnt(0)" : "+s"(ka), "+s"(kb)::"memory");
  const Ptrs P = {(const char*)(GAS const char*)ka[0], (const char*)(GAS const char*)ka[1], (const char*)(GAS const char*)ka[2],
                  (const char*)(GAS const char*)ka[3], (const u16*)(GAS const u16*)ka[4], (const char*)(GAS const char*)ka[5],
                  (const char*)(GAS const char*)ka[6], (char*)(GAS char*)ka[7], (char*)(GAS char*)kb};
  STAMP(11);
  R.sA0 = P.x + oA0;
  R.sA1 = P.x + oA1;
  R.sB0 = (const char*)((ffn ? ka[3] : ka[1]) + oB);
  R.sB1 = R.sB0 + seg;
  R.sB2 = R.sB1 + seg;
  sfor<0, 6>([&](auto kc) INL { R.template issue<0, decltype(kc)::v>(); });
  if (PRO >= 4) sfor<0, NPRE>([&](auto kc) INL { R.template issue<1, decltype(kc)::v>(); });
  if (w < 4 && PRO < 2) sfor<0, 5>([&](auto kc) INL { R.template issue<1, decltype(kc)::v>(); });
  if (!ffn && w == 0 && lane < 8) {
    const u32x4 one = {0x3F803F80u, 0x3F803F80u, 0x3F803F80u, 0x3F803F80u};
#pragma unroll
    for (int s = 0; s < 3; ++s) *(LAS u32x4*)(L + s * SLOT + lane * 16) = one;
  }
  STAMP(12);
  if (!ffn) {
    if (w < 4) body<0>(L, P, R, g, j, w STAMP_PASS);
    else body<1>(L, P, R, g, j, w STAMP_PASS);
  } else {
    if (w < 4) body<2>(L, P, R, g, j, w STAMP_PASS);
    else body<3>(L, P, R, g, j, w STAMP_PASS);
  }
}
}

extern "C" void kernel_launch(void* const* d_in, const int* in_sizes, int n_in, void* d_out, int out_size, void* d_ws,
                              size_t ws_size, hipStream_t stream) {
  (void)hipFuncSetAttribute((const void*)fk::fused, hipFuncAttributeMaxDynamicSharedMemorySize, fk::L_TOTAL);
  fk::fused<<<256, 512, fk::L_TOTAL, stream>>>((const char*)d_in[0], (const char*)d_in[1], (const char*)d_in[2], (const char*)d_in[4],
                                               (const u16*)d_in[5], (const char*)d_in[6], (const char*)d_in[7], (char*)d_out,
                                               (char*)d_ws);
}
```

```cpp
#include <hip/hip_runtime.h>
#include <stdint.h>

typedef unsigned u32;
typedef unsigned long long u64;
typedef unsigned short u16;
typedef short s16x4 __attribute__((ext_vector_type(4)));
typedef short s16x8 __attribute__((ext_vector_type(8)));
typedef __bf16 b16x2 __attribute__((ext_vector_type(2)));
typedef float f32x2 __attribute__((ext_vector_type(2)));
typedef float f32x4 __attribute__((ext_vector_type(4)));
typedef float f32x16 __attribute__((ext_vector_type(16)));
typedef u32 u32x2 __attribute__((ext_vector_type(2)));
typedef u32 u32x4 __attribute__((ext_vector_type(4)));
#define LAS __attribute__((address_space(3)))
#define GAS __attribute__((address_space(1)))
#define DEV __device__ __forceinline__
#define INL __attribute__((always_inline))

namespace fk {
constexpr int LDX = 1024, LDQKV = 3072, LDW1 = 2752, LDW2 = 1024;
constexpr int NKS = 118;
constexpr int SLOT = 43008, BOFF = 18432;
constexpr int L_MEAN = 3 * SLOT, L_RSTD = L_MEAN + 576, L_CS = L_RSTD + 576, L_B1 = L_CS + 768, L_OWN = L_B1 + 768,
              L_KV = L_MEAN + 3584, L_JUNK = L_KV + 8192, L_SP = L_JUNK + 2048, L_TOTAL = L_SP + 9 * 2048;
static_assert(L_OWN + 576 + 32 <= L_KV && L_TOTAL <= 163840, "LDS map");
constexpr int WS_GRP = 8192, WS_STAT = 4096;
constexpr int WS_A2 = 16 * WS_GRP, A2_BYTES = 64 * NKS * 1024;
constexpr int AUX_SC1 = 16;
constexpr u32 SPIN_CAP = 1u << 16;

constexpr int NEPI = 12;
constexpr int WIN = 6;
constexpr int RMID = 11;
constexpr int PGAP = 2;

#define STAMP(i) do {} while (0)
#define STAMP_ARG
#define STAMP_PASS
#define STAMP_OUT() do {} while (0)

template <int IMM> DEV void dma16(u32 voff, const void* sbase, u32 lb) {
  asm volatile("s_add_u32 m0, %2, %3\n\ts_nop 0\n\tglobal_load_lds_dwordx4 %0, %1" ::"v"(voff), "s"(sbase), "s"(lb), "i"(IMM) : "memory", "scc");
}
template <int IMM> DEV void dma16_masked(u32 voff, const void* sbase, u32 lb, u32 mlo) {
  asm volatile("s_add_u32 m0, %2, %4\n\ts_mov_b32 exec_lo, %3\n\tglobal_load_lds_dwordx4 %0, %1\n\ts_mov_b32 exec_lo, -1"
               ::"v"(voff), "s"(sbase), "s"(lb), "s"(mlo), "i"(IMM) : "memory", "scc");
}
template <int IMM> DEV void dma16_if(u32 voff, const void* sbase, u32 lb, u32 cond) {
  asm volatile("s_cmp_gt_u32 %3, 1\n\ts_cbranch_scc1 1f\n\ts_add_u32 m0, %2, %4\n\ts_nop 0\n\tglobal_load_lds_dwordx4 %0, %1\n1:"
               ::"v"(voff), "s"(sbase), "s"(lb), "s"(cond), "i"(IMM) : "memory", "scc");
}
DEV void touch(const void* p) {
  asm volatile("s_mov_b32 m0, %1\n\ts_nop 0\n\tglobal_load_lds_dword %0, off sc1" ::"v"(p), "i"(L_JUNK) : "memory");
}
template <int N> DEV void wait_bar() {
  asm volatile("s_waitcnt vmcnt(%0) lgkmcnt(0)\n\ts_barrier" ::"i"(N) : "memory");
}
template <int N> DEV void wait_vm() { asm volatile("s_waitcnt vmcnt(%0)" ::"i"(N) : "memory"); }
DEV void wait_lds() { asm volatile("s_waitcnt lgkmcnt(0)" ::: "memory"); }
DEV void lds_bar() { asm volatile("s_waitcnt lgkmcnt(0)\n\ts_barrier" ::: "memory"); }
#define SB() __builtin_amdgcn_sched_barrier(0)
#define PIN(x) asm volatile("" : "+v"(x))

DEV u32 f2u(float v) { return __builtin_bit_cast(u32, v); }
DEV float u2f(u32 v) { return __builtin_bit_cast(float, v); }
DEV u32 pk_bf16(float a, float b) {
  f32x2 p = {a, b};
  return __builtin_bit_cast(u32, __builtin_convertvector(p, b16x2));
}
DEV u32x2 pk4(f32x4 v) { return u32x2{pk_bf16(v[0], v[1]), pk_bf16(v[2], v[3])}; }
DEV float bf_lo(u32 v) { return u2f(v << 16); }
DEV float bf_hi(u32 v) { return u2f(v & 0xffff0000u); }
DEV float dot2(u32 a, u32 b, float c) {
  return __builtin_amdgcn_fdot2_f32_bf16(__builtin_bit_cast(b16x2, a), __builtin_bit_cast(b16x2, b), c, false);
}
DEV f32x4 mfma16(u32x4 a, u32x4 b, f32x4 c) {
  return __builtin_amdgcn_mfma_f32_16x16x32_bf16(__builtin_bit_cast(s16x8, a), __builtin_bit_cast(s16x8, b), c, 0, 0, 0);
}
DEV f32x4 mfma16k16(u32x2 a, u32x2 b, f32x4 c) {
  return __builtin_amdgcn_mfma_f32_16x16x16bf16_1k(__builtin_bit_cast(s16x4, a), __builtin_bit_cast(s16x4, b), c, 0, 0, 0);
}
DEV f32x16 mfma32(u32x4 a, u32x4 b, f32x16 c) {
  return __builtin_amdgcn_mfma_f32_32x32x16_bf16(__builtin_bit_cast(s16x8, a), __builtin_bit_cast(s16x8, b), c, 0, 0, 0);
}
DEV u32x4 lds128(LAS char* L, u32 off) { return *(LAS u32x4*)(L + off); }
DEV f32x4 ldsf4(LAS char* L, u32 off) { return *(LAS f32x4*)(L + off); }
DEV float ldsf(LAS char* L, u32 off) { return *(LAS float*)(L + off); }
DEV u32x2 ldstr(LAS char* L, u32 off) {
  return __builtin_bit_cast(u32x2, __builtin_amdgcn_ds_read_tr16_b64_v4i16((LAS s16x4*)(L + off)));
}
template <int CTRL> DEV float dpp_add(float v) {
  int t = __builtin_amdgcn_update_dpp(0, (int)f2u(v), CTRL, 0xf, 0xf, true);
  return v + u2f((u32)t);
}
DEV f32x2 swap16(float a, float b) {
  auto q = __builtin_amdgcn_permlane16_swap(f2u(a), f2u(b), false, false);
  return f32x2{u2f(q[0]), u2f(q[1])};
}
DEV f32x2 swap32(float a, float b) {
  auto q = __builtin_amdgcn_permlane32_swap(f2u(a), f2u(b), false, false);
  return f32x2{u2f(q[0]), u2f(q[1])};
}
DEV float vmax(float a, float b) { return __builtin_amdgcn_fmed3f(a, b, __builtin_inff()); }
DEV float sel(bool c, float a, float b) { return c ? a : b; }
DEV u32x4 pair_rows(u32x2 x, u32x2 y) {
  auto a = __builtin_amdgcn_permlane16_swap(x[0], y[0], false, false);
  auto b = __builtin_amdgcn_permlane16_swap(x[1], y[1], false, false);
  return u32x4{a[0], b[0], a[1], b[1]};
}
DEV f32x4 splat4(float v) { return f32x4{v, v, v, v}; }
DEV int fresh_tid() {
  int t = threadIdx.x;
  asm volatile("" : "+v"(t));
  return t;
}
DEV void st128(u32x4 v, __amdgpu_buffer_rsrc_t rs, u32 voff, u32 soff) {
  __builtin_amdgcn_raw_buffer_store_b128(v, rs, voff, soff, AUX_SC1);
  asm volatile("s_nop 1" ::"v"(v));
}
template <int V> struct IC { static constexpr int v = V; };
template <int LO, int HI, class F> DEV void sfor(F&& f) {
  if constexpr (LO < HI) { f(IC<LO>{}); sfor<LO + 1, HI>(f); }
}

struct Ptrs { const char *x, *wq, *wo, *w1; const u16* b1; const char *w2, *b2; char *out, *ws; };
struct Ring {
  u32 voffA, voffB, voffB2, stepB, mlo, xcond, wb;
  int rot, ldw, maxch;
  const char *sA0, *sA1, *sB0, *sB1, *sB2;
  DEV void lanes(int lane) {
    const int r8 = lane >> 3, pos = lane & 7;
    const int swB = (r8 & 2) | ((wb >> 8) & 4);
    voffA = r8 * LDX + ((pos ^ r8) << 4);
    voffB = r8 * ldw + ((pos ^ swB) << 4);
    voffB2 = r8 * ldw + (min(pos ^ swB, maxch) << 4);
  }
  template <int I, int K> DEV void issue() const {
    const int kc = (I + rot) & 7;
    constexpr int slot = (I % 3) * SLOT;
    if constexpr (K == 0) dma16_masked<slot>(voffA + kc * 128, sA0, wb, mlo);
    if constexpr (K == 1) dma16<slot + BOFF>(voffB + kc * stepB, sB0, wb);
    if constexpr (K == 2) dma16<slot + BOFF + 8192>(voffB + kc * stepB, sB1, wb);
    if constexpr (K == 3) dma16<slot + BOFF + 16384>(voffB2 + kc * stepB, sB2, wb);
    if constexpr (K == 4) dma16<slot + 8192>(voffA + kc * 128, sA1, wb);
    if constexpr (K == 5) dma16_if<slot + 12288>(voffA + kc * 128 + 32 * LDX, sA1, wb, xcond);
  }
};

template <int TYPE>
DEV void body(LAS char* L, const Ptrs P, Ring R, int g, int j, int w STAMP_ARG) {
  constexpr bool FFN = TYPE >= 2;
  constexpr int ODD = TYPE & 1;
  constexpr int NT = TYPE == 0 ? 5 : (TYPE == 1 ? 4 : 2);
  constexpr int NC = TYPE == 2 ? 6 : (TYPE == 3 ? 5 : 3);
  constexpr int CB = TYPE == 3 ? 6 : 0;
  constexpr int ND = TYPE == 1 ? 6 : 5;
  constexpr int NM = NT * NC + (FFN ? 2 : 0);
  constexpr int NRL = FFN ? 2 : 4;
  int tid = fresh_tid();
  int lane = tid & 63;
  R.lanes(lane);
  int lg = lane >> 4, lc = lane & 15;
  GAS u64* ctl = (GAS u64*)(P.ws + g * WS_GRP);
  u32x4 r0, r1, r2;
  u64 epoch;
  const int T0 = TYPE == 0 ? 0 : (TYPE == 1 ? 5 : 2 * (w & 3));
  const u32 aA = T0 * 2048 + (lc >> 3) * 1024 + (lc & 7) * 128 + ((lg ^ (lc & 7)) << 4);
  const int tq = lc >> 2, tp = lc & 3;
  const int swR = (tq & 2) | ((lg & 1) << 2);
  const u32 aBl = BOFF + lg * 1024 + tq * 128 + ((tp >> 1) << 4) + (tp & 1) * 8;
  const int crot = min(2 * (w & 3), NC - 2);
  constexpr int NBB = FFN ? NC : 1;
  u32 aB[NBB][2], aAb[2][2];
#pragma unroll
  for (int c = 0; c < NBB; ++c) {
    const int cc = c + crot >= NC ? c + crot - NC : c + crot;
    const int ct = FFN ? CB + cc : (w & 3);
    aB[c][0] = aBl + (ct >> 2) * 8192 + (((2 * (ct & 3)) ^ swR) << 4);
    aB[c][1] = aB[c][0] + 65536;
    asm("" : "+v"(aB[c][0]), "+v"(aB[c][1]));
  }
#pragma unroll
  for (int b = 0; b < 2; ++b) {
    aAb[b][0] = aA ^ (b * 64);
    aAb[b][1] = aAb[b][0] + 65536;
    asm("" : "+v"(aAb[b][0]), "+v"(aAb[b][1]));
  }
  f32x4 acc[NT][NC], acc1[2];
#pragma unroll
  for (int t = 0; t < NT; ++t)
#pragma unroll
    for (int c = 0; c < NC; ++c) acc[t][c] = f32x4{0.f, 0.f, 0.f, 0.f};
  acc1[0] = acc1[1] = f32x4{0.f, 0.f, 0.f, 0.f};
  u32x4 fa[2][NT];
  u32x2 fb[2][NC][2];
  auto rd = [&](int t, int k) INL {
    const int slot = ((t >> 1) % 3) * SLOT, b = t & 1;
    if (k >= 2 && k < 2 + NT) {
      const int o = slot + (k - 2) * 2048, hi = o >= 65536;
      fa[b][k - 2] = lds128(L, aAb[b][hi] + (o - hi * 65536));
    } else {
      const int kk = k < 2 ? k : k - NT, c = kk >> 1;
      const int o = slot + b * 4096 + (kk & 1) * 512 + (FFN ? 0 : c * 8192), hi = o >= 65536;
      fb[b][c][kk & 1] = ldstr(L, aB[FFN ? c : 0][hi] + (o - hi * 65536));
    }
  };
  const int mt = 2 * g + ((j >> 2) & 1), nt = (j & 3) + (j >> 3) * 4;
  const int ks0 = w < 6 ? 15 * w : 90 + 14 * (w - 6), nks = w < 6 ? 15 : 14;
  const u32 voffW = (lane >> 3) * LDW2 + (((lane & 7) ^ ((lane >> 2) & 4)) << 4) + nt * 128;
  const u64 woa = (u64)P.wo, w2a = (u64)P.w2 - 32 * 16 * LDW2;
  const u32 wx0 = w < 5 ? 2 * SLOT + (16 + w) * 2048 : (11 + w) * 2048;
  const u32 wx1 = w < 5 ? SLOT + (16 + w) * 2048 : (w < 7 ? (14 + w) * 2048 : L_SP + 8 * 2048);
  auto wbase = [&](int s) INL -> u32 { const int k = s % 9; return k == 0 ? 2 * R.wb : (k == 5 ? wx0 : (k == 8 ? wx1 : 4 * R.wb)); };
  auto wdma = [&](auto sc_, auto hc_) INL {
    constexpr int s = decltype(sc_)::v, h = decltype(hc_)::v, k = s % 9;
    constexpr int imm = k == 0 ? L_SP : (k <= 2 ? 2 * SLOT + (k - 1) * 2048 : (k <= 4 ? (k - 3) * 2048 : (k == 6 || k == 7 ? SLOT + (k - 6) * 2048 : 0)));
    const int ks = min(ks0 + s, NKS - 1);
    const u64 base = (ks < 32 ? woa : w2a) + (u32)(ks * 16 * LDW2);
    dma16<imm + h * 1024>(voffW + h * 8 * LDW2, (const void*)base, wbase(s));
  };
  u32x4 wf[15][2];
  const u32 tb = (lane >> 5) * 1024 + tq * 128 + (((2 * (lg & 1) + (tp >> 1)) ^ ((tq & 2) << 1)) << 4) + (tp & 1) * 8;
  const __amdgpu_buffer_rsrc_t rsA2 = __builtin_amdgcn_make_buffer_rsrc(P.ws + WS_A2, 0, A2_BYTES, 0x00020000);
  const __amdgpu_buffer_rsrc_t rsG = __builtin_amdgcn_make_buffer_rsrc(P.ws + g * WS_GRP, 0, WS_GRP, 0x00020000);
  const u32x4 ones = {0x3F803F80u, 0x3F803F80u, 0x3F803F80u, 0x3F803F80u};
  u64 tag = 0;
  u32 b1v = 0;
  u32x4 gst;
  u64 tp2 = 0;
  u32 gsto = 0, gldo = 0, b1o = 0, stw = 0, b1w = 0, csw = 0, csw2 = 0;
  float som = 0.f, sor = 0.f;
  f32x4 mP, mP0, mC;
  u32 sel0 = 0, sel1 = 0, wra[4][2];
  auto wread = [&](int s) INL {
    const int k = s % 9;
#pragma unroll
    for (int f = 0; f < 2; ++f) {
      const u32 a = k == 0 ? wra[0][f] : (k <= 2 ? wra[2][f] + (k - 1) * 2048 : (k <= 4 ? wra[1][f] + (k - 3) * 2048 :
                    (k == 6 || k == 7 ? wra[1][f] + SLOT + (k - 6) * 2048 : wra[1][f] - 4 * R.wb + wbase(s))));
      const u32x2 lo = ldstr(L, a), hi = ldstr(L, a + 512);
      wf[s][f] = u32x4{lo[0], lo[1], hi[0], hi[1]};
    }
  };
  constexpr int NK = 5, TK = TYPE == 0 ? 0 : 4;
  float mc1[NK], rc1[NK];
  f32x4 m4[NK], r4[NK], bb[NC];
  auto rdst = [&](int k) INL {
    if (FFN) {
      if (k < 2) mc1[k] = ldsf(L, L_MEAN + ((2 * (w & 3) + k) * 16 + lc) * 4);
      else if (k < 4) rc1[k - 2] = ldsf(L, L_RSTD + ((2 * (w & 3) + k - 2) * 16 + lc) * 4);
      else if (k < 4 + NC) {
        const int c = k - 4, ct = CB + (c + crot >= NC ? c + crot - NC : c + crot);
        bb[c] = ldsf4(L, L_B1 + (ct * 16 + lg * 4) * 4);
      }
    } else {
      const int i = k >> 2, T = TK + i;
      if (i >= NK) return;
      if ((k & 3) == 0) mc1[i] = ldsf(L, L_MEAN + (T * 16 + lc) * 4);
      if ((k & 3) == 1) rc1[i] = ldsf(L, L_RSTD + (T * 16 + lc) * 4);
      if ((k & 3) == 2) m4[i] = ldsf4(L, L_MEAN + (T * 16 + lg * 4) * 4);
      if ((k & 3) == 3) r4[i] = ldsf4(L, L_RSTD + (T * 16 + lg * 4) * 4);
    }
  };
      {
        const int b_ = blockIdx.x, ln = min(b_ * 59 + w * 8 + (lane & 7), 15103);
        tp2 = (ln < 4096 ? woa : w2a) + (u32)(ln * 128);
        const u64 pa = (u64)P.ws + (u32)(WS_A2 + (lane & 3) * (2u << 20) + (b_ & 63) * 4096), po = (u64)P.out + (u32)(b_ * 8192);
        tp2 = (lane >= 8 && lane < 12) ? pa : tp2;
        tp2 = lane == 12 ? po : tp2;
        gsto = (w == 7 && lane < 8) ? WS_STAT + (j * 8 + lane) * 16 : WS_GRP;
        gldo = WS_STAT + (tid & 127) * 16;
        b1o = 2 * (u32)min(max(j - 8, 0) * 176 + tid, 1375);
        stw = (!ODD && w < 2) ? L_MEAN + (tid + (FFN ? 0 : 16)) * 4 : L_JUNK + lane * 4;
        b1w = tid < 192 ? L_B1 + tid * 4 : L_JUNK + lane * 4;
        csw = lc == 0 ? L_CS + ((FFN ? CB + crot : (w & 3)) * 16 + lg * 4) * 4 : L_JUNK + lane * 16;
        csw2 = lg == 0 ? L_CS + (128 + (w & 3) * 16 + lc) * 4 : L_JUNK + lane * 4;
        if (!FFN) {
#pragma unroll
          for (int r = 0; r < 4; ++r) {
            mP[r] = (4 * lg + r >= lc + 2) ? 0.f : -1e30f;
            mP0[r] = (TYPE == 0 && g == 0) ? -1e30f : mP[r];
            mC[r] = (4 * lg + r <= lc) ? 0.f : -1e30f;
          }
          sel0 = (lc & 1) ? 0x01000c0cu : 0x0c0c0100u; sel1 = (lc & 1) ? 0x03020c0cu : 0x0c0c0302u;
          asm volatile("" : "+v"(mP), "+v"(mC), "+v"(sel0), "+v"(sel1));
          if (TYPE == 0) asm volatile("" : "+v"(mP0));
        }
#pragma unroll
        for (int f = 0; f < 2; ++f) {
          wra[0][f] = (tb ^ (f * 64)) + 2 * R.wb + L_SP;
          wra[1][f] = (tb ^ (f * 64)) + 4 * R.wb;
          wra[2][f] = (tb ^ (f * 64)) + 4 * R.wb + 2 * SLOT;
          asm volatile("" : "+v"(wra[0][f]), "+v"(wra[1][f]), "+v"(wra[2][f]));
        }
        asm volatile("" : "+v"(tp2), "+v"(gsto), "+v"(gldo), "+v"(b1o), "+v"(stw), "+v"(b1w), "+v"(csw), "+v"(csw2));
      }
  SB();
  sfor<0, ND>([&](auto kc) INL { R.template issue<1, decltype(kc)::v>(); });
  if (FFN)
    asm volatile("global_load_dwordx2 %0, %2, %3 sc1\n\tglobal_load_dwordx4 %1, %4, %5"
                 : "=&v"(epoch), "=&v"(r0) : "v"(0u), "s"(ctl), "v"(lane * 16), "s"(P.x + (size_t)(128 * g + 8 * j + w) * LDX) : "memory");
  else
    asm volatile("global_load_dwordx2 %0, %4, %5 sc1\n\tglobal_load_dwordx4 %1, %6, %7\n\t"
                 "global_load_dwordx4 %2, %6, %8\n\tglobal_load_dwordx4 %3, %6, %8 offset:1024"
                 : "=&v"(epoch), "=&v"(r0), "=&v"(r1), "=&v"(r2)
                 : "v"(0u), "s"(ctl), "v"(lane * 16), "s"(P.x + (size_t)(128 * g + 8 * j + w) * LDX),
                   "s"(P.x + (size_t)max(128 * g - 16 + 2 * w, 0) * LDX) : "memory");
  STAMP(1);
  wait_bar<5 + NRL>();
  STAMP(2);
#pragma unroll
  for (int k = 0; k < 2 * NC + NT; ++k) rd(0, k);
  SB();
  sfor<0, 16>([&](auto tc) INL {
    constexpr int t = decltype(tc)::v, s = t >> 1, b = t & 1;
    if constexpr (b) {
      if constexpr (s == 1 || s == 2) wait_bar<6 + ODD>();
      else if constexpr (s <= 4) wait_bar<5>();
      else if constexpr (s == 5) wait_bar<7>();
      else if constexpr (s == 6) wait_bar<4>();
    }
    if constexpr (t == 11) {
      if constexpr (!ODD) asm volatile("" : "+v"(gst), "+v"(b1v));
      if constexpr (!ODD) {
        if (__builtin_expect(!__all(gst[1] == (u32)tag && gst[3] == (u32)tag), 0)) {
#pragma nounroll
          for (u32 spins = 0; spins < SPIN_CAP; ++spins) {
            __builtin_amdgcn_s_sleep(1);
            gst = __builtin_amdgcn_raw_buffer_load_b128(rsG, gldo, 0, AUX_SC1);
            if (__all(gst[1] == (u32)tag && gst[3] == (u32)tag)) break;
          }
        }
      }
      SB();
    }
    constexpr int nr = t == 15 ? 0 : 2 * NC + NT;
    constexpr int st = t == 0 ? 2 : (t + 5) / 2;
    constexpr int k0 = (t == 0 || b) ? 0 : 3, k1 = st >= 8 ? 0 : ((t == 0 || !b) ? ND : 3);
    constexpr int nd = k1 > k0 ? k1 - k0 : 0, nw = t >= 10 ? 2 : 0, nv = nd + nw;
    sfor<0, NM>([&](auto qc) INL {
      constexpr int q = decltype(qc)::v;
      constexpr int qq = t < 15 ? q : (FFN ? (q + NT * NC) % NM : (TYPE == 1 ? q : (q < 3 ? q * NT : (q < 5 ? (q - 2) * NT + 4 :
                         (q - 5 < 4 ? q - 4 : (q - 5 < 7 ? q - 3 : q - 1))))));
      if constexpr (qq < NT * NC) {
        constexpr int c = qq / NT, i = qq % NT;
        const u32x4 wfr = {fb[b][c][0][0], fb[b][c][0][1], fb[b][c][1][0], fb[b][c][1][1]};
        if constexpr (!FFN && c == 2) acc[i][c] = mfma16(fa[b][i], wfr, acc[i][c]);
        else acc[i][c] = mfma16(wfr, fa[b][i], acc[i][c]);
        PIN(acc[i][c]);
      } else {
        constexpr int c = qq - NT * NC;
        const u32x4 wfr = {fb[b][c][0][0], fb[b][c][0][1], fb[b][c][1][0], fb[b][c][1][1]};
        acc1[c] = mfma16(wfr, ones, acc1[c]);
        PIN(acc1[c]);
      }
      if constexpr (q < nr) rd(t + 1, q);
      if constexpr (t == 15) { rdst(2 * q); rdst(2 * q + 1); }
      if constexpr (t == 9 && q == 0 && !ODD)
        asm volatile("buffer_load_dwordx4 %0, %2, %3, 0 offen sc1\n\tglobal_load_ushort %1, %4, %5"
                     : "=&v"(gst), "=&v"(b1v) : "v"(gldo), "s"(rsG), "v"(b1o), "s"(P.b1) : "memory");
      if constexpr (t == 11 && !ODD) {
        if constexpr (q == NM - 4) *(LAS u32*)(L + stw) = gst[0];
        if constexpr (q == NM - 3) *(LAS u32*)(L + stw + 576) = gst[2];
        if constexpr (q == NM - 2 && FFN) *(LAS float*)(L + b1w) = bf_lo(b1v);
      }
      if constexpr (t == 1 && ODD && q == NM - 1) { som = ldsf(L, L_OWN + (lane & 7) * 4); sor = ldsf(L, L_OWN + 576 + (lane & 7) * 4); }
      sfor<0, nv>([&](auto uc) INL {
        constexpr int u = decltype(uc)::v;
        if constexpr (((2 * u + ODD) * NM) / (2 * nv) == q) {
          if constexpr (u < nd) R.template issue<st, k0 + u>();
          else wdma(IC<t - 10>{}, IC<u - nd>{});
        }
      });
      if constexpr (t == 2 && q == NM - 1) {
        touch((const void*)tp2);
        if (ODD) st128(u32x4{f2u(som), (u32)tag, f2u(sor), (u32)tag}, rsG, gsto, 0);
      }
      if constexpr (t == 15) {
        if constexpr (TYPE == 0) {
          if constexpr (q == 6) *(LAS f32x4*)(L + csw) = acc[0][0];
          if constexpr (q == 7) *(LAS f32x4*)(L + csw + 256) = acc[0][1];
          if constexpr (q == 8) *(LAS float*)(L + csw2) = acc[0][2][0];
          if constexpr (q == 9) *(LAS f32x4*)(L + L_KV + 2 * R.wb + lane * 16) = acc[NT - 1][1];
          if constexpr (q == 10) *(LAS f32x4*)(L + L_KV + 2 * R.wb + 1024 + lane * 16) = acc[NT - 1][2];
        }
        if constexpr (FFN) {
          if constexpr (q == 6) *(LAS f32x4*)(L + csw) = acc1[0];
          if constexpr (q == 7) *(LAS f32x4*)(L + csw + 64) = acc1[1];
        }
      }
      SB();
    });
    if constexpr (t == 0) {
      if (FFN) asm volatile("s_waitcnt vmcnt(%2)" : "+v"(epoch), "+v"(r0) : "i"(5) : "memory");
      else asm volatile("s_waitcnt vmcnt(%4)" : "+v"(epoch), "+v"(r0), "+v"(r1), "+v"(r2) : "i"(5) : "memory");
      tag = (((u64)(u32)__builtin_amdgcn_readfirstlane((u32)(epoch >> 32)) << 32) | (u32)__builtin_amdgcn_readfirstlane((u32)epoch)) + 1;
      float sS[3] = {0.f, 0.f, 0.f}, sQ[3] = {0.f, 0.f, 0.f};
#pragma unroll
      for (int d = 0; d < 4; ++d) {
        sS[0] = dot2(r0[d], 0x3F803F80u, sS[0]); sQ[0] = dot2(r0[d], r0[d], sQ[0]);
        if (!FFN) {
          sS[1] = dot2(r1[d], 0x3F803F80u, sS[1]); sQ[1] = dot2(r1[d], r1[d], sQ[1]);
          sS[2] = dot2(r2[d], 0x3F803F80u, sS[2]); sQ[2] = dot2(r2[d], r2[d], sQ[2]);
        }
      }
      f32x2 a = swap32(sS[0], sS[FFN ? 0 : 1]), c = swap32(sQ[0], sQ[FFN ? 0 : 1]);
      float S = a[0] + a[1], Q = c[0] + c[1], S2 = S, Q2 = Q;
      if (!FFN) {
        a = swap32(sS[2], sS[2]); c = swap32(sQ[2], sQ[2]);
        S2 = a[0] + a[1]; Q2 = c[0] + c[1];
      }
      a = swap16(S, S2); c = swap16(Q, Q2);
      S = a[0] + a[1]; Q = c[0] + c[1];
      S = dpp_add<0xB1>(S); Q = dpp_add<0xB1>(Q);
      S = dpp_add<0x4E>(S); Q = dpp_add<0x4E>(Q);
      S = dpp_add<0x141>(S); Q = dpp_add<0x141>(Q);
      S = dpp_add<0x140>(S); Q = dpp_add<0x140>(Q);
      const float m = S * (1.f / 512.f), e = Q * (1.f / 512.f);
      u32 adr = L_JUNK + lane * 4;
      adr = lane == 0 ? L_OWN + w * 4 : adr;
      if (!FFN) { adr = lane == 32 ? L_MEAN + w * 8 : adr; adr = lane == 16 ? L_MEAN + w * 8 + 4 : adr; }
      *(LAS float*)(L + adr) = -m;
      *(LAS float*)(L + adr + 576) = __builtin_amdgcn_rsqf(fmaxf(e - m * m, 0.f) + 1e-5f);
      SB();
    }
  });
  STAMP(3);
  lds_bar();
  STAMP(4);
  auto slot = [&](auto kc) INL {
    constexpr int k = decltype(kc)::v;
    SB();
    if constexpr (k < NEPI) wdma(IC<6 + k / 2>{}, IC<k % 2>{});
    SB();
  };
  if constexpr (!FFN) {
    const int h = w & 3;
    const f32x4 csq = ldsf4(L, L_CS + (h * 16 + lg * 4) * 4), csk = ldsf4(L, L_CS + (64 + h * 16 + lg * 4) * 4);
    const float csv1 = ldsf(L, L_CS + (128 + h * 16 + lc) * 4);
    f32x4 k4 = acc[0][1], v4 = acc[0][2];
    if (TYPE == 1) { k4 = ldsf4(L, L_KV + h * 2048 + lane * 16); v4 = ldsf4(L, L_KV + h * 2048 + 1024 + lane * 16); }
    slot(IC<0>{});
    wait_vm<7>();
    wread(0); wread(1); wread(2);
    const bool odd = lc & 1;
    const f32x4 csv = splat4(csv1);
    u32x2 Qb[4], Kb[NK], Vb[NK];
    sfor<0, NK>([&](auto ic) INL {
      constexpr int i = decltype(ic)::v, ai = TYPE == 0 ? i : (i > 0 ? i - 1 : 0);
      slot(IC<1 + i>{});
      const f32x4 mc = splat4(mc1[i]);
      Kb[i] = pk4(((i == 0 ? k4 : acc[ai][1]) + mc * csk) * splat4(rc1[i]));
      if constexpr (i > 0) Qb[i - 1] = pk4((acc[ai][0] + mc * csq) * splat4(rc1[i] * (0.25f * 1.4426950408889634f)));
      Vb[i] = pk4(((i == 0 ? v4 : acc[ai][2]) + m4[i] * csv) * r4[i]);
    });
    f32x4 sp[4], sc[4];
#pragma unroll
    for (int q = 0; q < 4; ++q) {
      sp[q] = mfma16k16(Kb[q], Qb[q], q == 0 ? mP0 : mP);
      sc[q] = mfma16k16(Kb[q + 1], Qb[q], mC);
    }
    SB();
    wait_lds();
    wait_vm<6>();
    wread(3); wread(4); wread(5);
    slot(IC<6>{});
    f32x4 a[4];
    float mx[4];
#pragma unroll
    for (int q = 0; q < 4; ++q) {
      a[q] = f32x4{sel(odd, sp[q][1], sp[q][0]), sel(odd, sp[q][3], sp[q][2]), sel(odd, sc[q][1], sc[q][0]), sel(odd, sc[q][3], sc[q][2])};
      mx[q] = vmax(vmax(a[q][0], a[q][1]), vmax(a[q][2], a[q][3]));
    }
    slot(IC<7>{});
    {
      const f32x2 x = swap32(mx[0], mx[1]), y = swap32(mx[2], mx[3]);
      const f32x2 z = swap16(vmax(x[0], x[1]), vmax(y[0], y[1]));
      const float u = vmax(z[0], z[1]);
      const f32x2 p = swap16(u, u);
      const f32x2 p0 = swap32(p[0], p[0]), p1 = swap32(p[1], p[1]);
      mx[0] = p0[0]; mx[1] = p0[1]; mx[2] = p1[0]; mx[3] = p1[1];
    }
    slot(IC<8>{});
#pragma unroll
    for (int q = 0; q < 4; ++q) a[q] -= splat4(mx[q]);
    slot(IC<9>{});
    sfor<0, 4>([&](auto qc) INL {
      constexpr int q = decltype(qc)::v;
#pragma unroll
      for (int r = 0; r < 4; ++r) a[q][r] = __builtin_amdgcn_exp2f(a[q][r]);
      if constexpr (q & 1) slot(IC<10 + q / 2>{});
    });
    wait_lds();
    const f32x4 z4 = {0.f, 0.f, 0.f, 0.f};
    u32x4 pb[4];
    sfor<0, 4>([&](auto qc) INL {
      constexpr int q = decltype(qc)::v;
      const u32x2 u = pk4(a[q]);
      pb[q] = u32x4{__builtin_amdgcn_perm(0, u[0], sel0), __builtin_amdgcn_perm(0, u[0], sel1),
                    __builtin_amdgcn_perm(0, u[1], sel0), __builtin_amdgcn_perm(0, u[1], sel1)};
      if constexpr (q & 1) slot(IC<12 + q / 2>{});
    });
#pragma unroll
    for (int q = 0; q < 4; ++q) {
      sc[q] = mfma16(ones, pb[q], z4);
      sp[q] = mfma16k16(Vb[q], u32x2{pb[q][0], pb[q][1]}, z4);
    }
#pragma unroll
    for (int q = 0; q < 4; ++q) sp[q] = mfma16k16(Vb[q + 1], u32x2{pb[q][2], pb[q][3]}, sp[q]);
    slot(IC<14>{}); slot(IC<15>{}); slot(IC<16>{}); slot(IC<17>{});
    u32x2 Ob[4];
#pragma unroll
    for (int q = 0; q < 4; ++q) Ob[q] = pk4(sp[q] * splat4(__builtin_amdgcn_rcpf(sc[q][0])));
    const int rt = 4 * g + (TYPE == 0 ? 0 : 2), ks = 4 * j + h;
    st128(pair_rows(Ob[0], Ob[1]), rsA2, lane * 16, (rt * NKS + ks) * 1024);
    st128(pair_rows(Ob[2], Ob[3]), rsA2, lane * 16, ((rt + 1) * NKS + ks) * 1024);
  } else {
    f32x4 cs[NC];
#pragma unroll
    for (int c = 0; c < NC; ++c) {
      const int ct = CB + (c + crot >= NC ? c + crot - NC : c + crot);
      cs[c] = ldsf4(L, L_CS + (ct * 16 + lg * 4) * 4);
    }
    wait_vm<6>();
    wread(0); wread(1); wread(2);
    const f32x4 mc[2] = {splat4(mc1[0]), splat4(mc1[1])}, rc[2] = {splat4(rc1[0]), splat4(rc1[1])};
    const int nct = j == 15 ? 9 : 11;
    const int rt = 4 * g + (w & 3), ksb = 32 + 11 * (j - 8);
    const f32x4 C1 = splat4(-2.f * 1.4426950408889634f * 0.7978845608028654f);
    const f32x4 C2 = splat4(-2.f * 1.4426950408889634f * 0.7978845608028654f * 0.044715f);
    const f32x4 ONE = splat4(1.f);
    sfor<0, 3>([&](auto gc) INL {
      constexpr int G = decltype(gc)::v, c0 = 2 * G, n = c0 + 1 < NC ? 4 : 2;
      if constexpr (G == 1) { wait_lds(); wait_vm<8>(); wread(3); wread(4); wread(5); }
      if constexpr (G == 2) wait_lds();
      f32x4 v[4], e[4];
      slot(IC<6 * G>{});
#pragma unroll
      for (int u = 0; u < n; ++u) v[u] = (acc[u & 1][c0 + (u >> 1)] + mc[u & 1] * cs[c0 + (u >> 1)]) * rc[u & 1] + bb[c0 + (u >> 1)];
      slot(IC<6 * G + 1>{});
#pragma unroll
      for (int u = 0; u < n; ++u) e[u] = v[u] * (v[u] * v[u] * C2 + C1);
      slot(IC<6 * G + 2>{});
#pragma unroll
      for (int u = 0; u < n; ++u) {
#pragma unroll
        for (int r = 0; r < 4; ++r) e[u][r] = __builtin_amdgcn_exp2f(e[u][r]);
        if (u == 1) slot(IC<6 * G + 3>{});
      }
#pragma unroll
      for (int u = 0; u < n; ++u) e[u] += ONE;
      slot(IC<6 * G + 4>{});
#pragma unroll
      for (int u = 0; u < n; ++u) {
#pragma unroll
        for (int r = 0; r < 4; ++r) e[u][r] = __builtin_amdgcn_rcpf(e[u][r]);
        if (u == 1) slot(IC<6 * G + 5>{});
      }
#pragma unroll
      for (int cc = 0; cc < n / 2; ++cc) {
        const int c = c0 + cc, ct = CB + (c + crot >= NC ? c + crot - NC : c + crot);
        st128(pair_rows(pk4(v[2 * cc] * e[2 * cc]), pk4(v[2 * cc + 1] * e[2 * cc + 1])), rsA2, lane * 16,
              ct < nct ? (rt * NKS + ksb + ct) * 1024 : A2_BYTES);
      }
      SB();
    });
  }
  STAMP(5);
  wait_vm<0>();
  STAMP(13);
  asm volatile("s_barrier" ::: "memory");
  if (tid == 0) __hip_atomic_store(ctl + 16 * (j + 1), tag, __ATOMIC_RELAXED, __HIP_MEMORY_SCOPE_AGENT);
  STAMP(6);
  sfor<NEPI, 18>([&](auto kc) INL { wdma(IC<6 + decltype(kc)::v / 2>{}, IC<decltype(kc)::v % 2>{}); });
#pragma unroll
  for (int s = 6; s < 9; ++s) wread(s);
  tid = fresh_tid(); lane = tid & 63; lg = lane >> 4; lc = lane & 15;
  const u32 ooff = (u32)((mt * 64 + 4 * w + ((lane >> 3) & 3)) * LDX + (nt * 64 + (lane & 7) * 8) * 2);
  f32x4 xb0, xb1;
#pragma unroll
  for (int s = 0; s < 9; ++s) asm volatile("" : "+v"(wf[s][0]), "+v"(wf[s][1]));
  STAMP(7);
  u64 v0, v1, v2, v3;
  wait_vm<0>();
  {
    const u32 fo = 128 * (lc + 1);
    u32 spins = SPIN_CAP;
    asm volatile(
        "s_cmp_lg_u32 %9, %10\n\ts_cbranch_scc1 2f\n\t"
        "global_load_dwordx2 %0, %5, %6 sc1\n\ts_sleep %8\n\tglobal_load_dwordx2 %1, %5, %6 sc1\n\ts_sleep %8\n\t"
        "global_load_dwordx2 %2, %5, %6 sc1\n\ts_sleep %8\n"
        "1:\n\t"
        "global_load_dwordx2 %3, %5, %6 sc1\n\ts_waitcnt vmcnt(3)\n\tv_cmp_ne_u64 vcc, %7, %0\n\ts_cbranch_vccz 2f\n\ts_sleep %8\n\t"
        "global_load_dwordx2 %0, %5, %6 sc1\n\ts_waitcnt vmcnt(3)\n\tv_cmp_ne_u64 vcc, %7, %1\n\ts_cbranch_vccz 2f\n\ts_sleep %8\n\t"
        "global_load_dwordx2 %1, %5, %6 sc1\n\ts_waitcnt vmcnt(3)\n\tv_cmp_ne_u64 vcc, %7, %2\n\ts_cbranch_vccz 2f\n\ts_sleep %8\n\t"
        "global_load_dwordx2 %2, %5, %6 sc1\n\ts_waitcnt vmcnt(3)\n\tv_cmp_ne_u64 vcc, %7, %3\n\ts_cbranch_vccz 2f\n\ts_sleep %8\n\t"
        "s_sub_u32 %4, %4, 1\n\ts_cmp_lg_u32 %4, 0\n\ts_cbranch_scc1 1b\n"
        "2:\n\ts_barrier"
        : "=&v"(v0), "=&v"(v1), "=&v"(v2), "=&v"(v3), "+s"(spins)
        : "v"(fo), "s"(ctl), "s"(tag), "i"(PGAP), "s"(w), "i"(7) : "memory", "vcc", "scc");
  }
  STAMP(8);
  u32x4 af[2][15];
  auto lda = [&](int r, int s) INL {
    const int so = (s < nks) ? ((2 * mt + r) * NKS + ks0 + s) * 1024 : A2_BYTES;
    af[r][s] = __builtin_amdgcn_raw_buffer_load_b128(rsA2, lane * 16, so, AUX_SC1);
  };
#pragma unroll
  for (int n = 0; n < WIN; ++n) lda(n / 15, n % 15);
  SB();
  u32 pw[4];
#pragma unroll
  for (int y = 0; y < 4; ++y) pw[y] = ((lane + 4 * y) & 63) * 16 + y * 1024 + 4 * R.wb;
  const int blk = lane & 7;
  const u32 prd = (((lane & 32) + 4 * w + ((lane >> 3) & 3) + 4 * (blk & 3)) & 63) * 16 + (blk & 3) * 1024;
  const u32 prd0 = prd + (blk >> 2) * SLOT, prd1 = prd + (blk >> 2) * 4096 + 2 * SLOT;
  const __amdgpu_buffer_rsrc_t rsO = __builtin_amdgcn_make_buffer_rsrc(P.out, 0, 2048 * LDX, 0x00020000);
  auto reduce = [&](int r, f32x4 xb) INL {
    f32x4 p[8];
#pragma unroll
    for (int sw = 0; sw < 8; ++sw) p[sw] = ldsf4(L, r == 0 ? prd0 + sw * 4096 : prd1 + sw * 8192);
    SB();
    const u32x2 o = pk4(((p[0] + p[1]) + (p[2] + p[3])) + ((p[4] + p[5]) + (p[6] + p[7])) + xb);
    auto e0 = __builtin_amdgcn_permlane32_swap(o[0], o[0], false, false), e1 = __builtin_amdgcn_permlane32_swap(o[1], o[1], false, false);
    __builtin_amdgcn_raw_buffer_store_b128(u32x4{e0[0], e1[0], e0[1], e1[1]}, rsO, lane < 32 ? ooff + r * 32 * LDX : 2048 * LDX, 0, 17);
  };
  f32x16 pa[2][2];
  auto pwrite = [&](int r, int b8) INL {
    const f32x16& a = pa[r][b8 >> 2];
    const int y = b8 & 3;
    *(LAS f32x4*)(L + pw[y] + (r == 0 ? (b8 >> 2) * SLOT : 2 * SLOT + 4 * R.wb + (b8 >> 2) * 4096)) =
        f32x4{a[4 * y], a[4 * y + 1], a[4 * y + 2], a[4 * y + 3]};
  };
  u32x2 xr0, xr1, bres;
  sfor<0, 2>([&](auto rc_) INL {
    constexpr int r = decltype(rc_)::v;
#pragma unroll
    for (int i = 0; i < 16; ++i) pa[r][0][i] = pa[r][1][i] = 0.f;
    sfor<0, 15>([&](auto sc_) INL {
      constexpr int s = decltype(sc_)::v;
      pa[r][0] = mfma32(wf[s][0], af[r][s], pa[r][0]);
      PIN(pa[r][0]);
      if constexpr (r == 1 && s >= 1 && s < 5) pwrite(0, 2 * s - 2);
      pa[r][1] = mfma32(wf[s][1], af[r][s], pa[r][1]);
      PIN(pa[r][1]);
      if constexpr (r == 1 && s >= 1 && s < 5) pwrite(0, 2 * s - 1);
      if constexpr (r == 0 && s == 2) asm volatile("" ::"v"(v0), "v"(v1), "v"(v2), "v"(v3));
      if constexpr (r == 0 && s == 0) STAMP(14);
      if constexpr (r == 0 && s == 14) STAMP(15);
      if constexpr (r == 0 && s == 1) {
        const u32 o4 = ooff + (lane >> 5) * 8;
        xr0 = *(const GAS u32x2*)(P.x + o4); xr1 = *(const GAS u32x2*)(P.x + (o4 + 32 * LDX));
        bres = *(const GAS u32x2*)(P.b2 + (o4 & 1023));
      }
      if constexpr (r * 15 + s + WIN < 30) lda((r * 15 + s + WIN) / 15, (r * 15 + s + WIN) % 15);
      if constexpr (r == 0 && s >= 7 && s < 13) wread(s + 2);
      if constexpr (r == 0 && s == 14) {
        const f32x4 bv = {bf_lo(bres[0]), bf_hi(bres[0]), bf_lo(bres[1]), bf_hi(bres[1])};
        xb0 = f32x4{bf_lo(xr0[0]), bf_hi(xr0[0]), bf_lo(xr0[1]), bf_hi(xr0[1])} + bv;
        xb1 = f32x4{bf_lo(xr1[0]), bf_hi(xr1[0]), bf_lo(xr1[1]), bf_hi(xr1[1])} + bv;
        PIN(xb0); PIN(xb1);
      }
      if constexpr (r == 1 && s == RMID) { lds_bar(); reduce(0, xb0); }
      SB();
    });
  });
#pragma unroll
  for (int y = 0; y < 8; ++y) pwrite(1, y);
  STAMP(9);
  lds_bar();
  reduce(1, xb1);
  if (j == 0 && tid == 0) __hip_atomic_store(ctl, tag, __ATOMIC_RELAXED, __HIP_MEMORY_SCOPE_AGENT);
  STAMP(10);
  STAMP_OUT();
  asm volatile(".Lfk_end%c0:" ::"i"(TYPE));
  __builtin_amdgcn_endpgm();
}

__global__ __launch_bounds__(512, 2) void fused(const char*, const char*, const char*, const char*, const u16*, const char*, const char*,
                                                char*, char*) {
  extern __shared__ __attribute__((aligned(16))) char smem[];
  LAS char* L = (LAS char*)smem;
  STAMP(0);
  typedef u64 u64x8 __attribute__((ext_vector_type(8)));
  u64x8 ka;
  u64 kb;
  asm volatile("s_load_dwordx16 %0, %2, 0x0\n\ts_load_dwordx2 %1, %2, 0x40"
               : "=&s"(ka), "=&s"(kb) : "s"(__builtin_amdgcn_kernarg_segment_ptr()) : "memory");
  const int lane = threadIdx.x & 63, w = __builtin_amdgcn_readfirstlane(threadIdx.x >> 6);
  const int b = blockIdx.x, bi = b >> 3;
  const int g = (b & 3) * 4 + (bi & 3), j = ((bi >> 2) & 3) + ((b >> 2) & 1) * 4 + (bi >> 4) * 8;
  const bool ffn = j >= 8;
  if (w == 7) {
    u64 pc;
    u32 len;
    asm volatile("s_getpc_b64 %0\n.Lfk_here:\n\ts_mov_b32 %1, .Lfk_end0-.Lfk_here\n\ts_max_u32 %1, %1, .Lfk_end1-.Lfk_here\n\t"
                 "s_max_u32 %1, %1, .Lfk_end2-.Lfk_here\n\ts_max_u32 %1, %1, .Lfk_end3-.Lfk_here" : "=s"(pc), "=s"(len)::"scc");
    touch((const char*)pc + min((u32)(lane * 128 + (bi & 7) * 8192), len));
  }
  const int rowbase = ffn ? 128 * g : 128 * g - 16;
  Ring R;
  R.ldw = ffn ? LDW1 : LDQKV;
  R.maxch = ffn ? (j == 15 ? 1 : 5) : 7;
  R.wb = w * 1024;
  R.rot = (j + g) & 7;
  R.lanes(lane);
  R.stepB = 64 * R.ldw;
  R.mlo = (!ffn && w == 0) ? 0xFFFFFF00u : 0xFFFFFFFFu;
  R.xcond = ffn ? 2u : (u32)(w - 4);
  const u32 oA0 = (rowbase + 8 * w < 0 ? 8 * w : rowbase + 8 * w) * LDX, oA1 = (rowbase + 8 * (w + 8)) * LDX;
  const u32 oB = 8 * w * R.ldw + (ffn ? (j - 8) * 352 : j * 128), seg = ffn ? 128 : 1024;
  asm volatile("s_waitcnt lgkmcnt(0)" : "+s"(ka), "+s"(kb)::"memory");
  const Ptrs P = {(const char*)(GAS const char*)ka[0], (const char*)(GAS const char*)ka[1], (const char*)(GAS const char*)ka[2],
                  (const char*)(GAS const char*)ka[3], (const u16*)(GAS const u16*)ka[4], (const char*)(GAS const char*)ka[5],
                  (const char*)(GAS const char*)ka[6], (char*)(GAS char*)ka[7], (char*)(GAS char*)kb};
  STAMP(11);
  R.sA0 = P.x + oA0;
  R.sA1 = P.x + oA1;
  R.sB0 = (const char*)((ffn ? ka[3] : ka[1]) + oB);
  R.sB1 = R.sB0 + seg;
  R.sB2 = R.sB1 + seg;
  sfor<0, 6>([&](auto kc) INL { R.template issue<0, decltype(kc)::v>(); });
  if (!ffn && w == 0 && lane < 8) {
    const u32x4 one = {0x3F803F80u, 0x3F803F80u, 0x3F803F80u, 0x3F803F80u};
#pragma unroll
    for (int s = 0; s < 3; ++s) *(LAS u32x4*)(L + s * SLOT + lane * 16) = one;
  }
  STAMP(12);
  if (!ffn) {
    if (w < 4) body<0>(L, P, R, g, j, w STAMP_PASS);
    else body<1>(L, P, R, g, j, w STAMP_PASS);
  } else {
    if (w < 4) body<2>(L, P, R, g, j, w STAMP_PASS);
    else body<3>(L, P, R, g, j, w STAMP_PASS);
  }
}
}

extern "C" void kernel_launch(void* const* d_in, const int* in_sizes, int n_in, void* d_out, int out_size, void* d_ws,
                              size_t ws_size, hipStream_t stream) {
  (void)hipFuncSetAttribute((const void*)fk::fused, hipFuncAttributeMaxDynamicSharedMemorySize, fk::L_TOTAL);
  fk::fused<<<256, 512, fk::L_TOTAL, stream>>>((const char*)d_in[0], (const char*)d_in[1], (const char*)d_in[2], (const char*)d_in[4],
                                               (const u16*)d_in[5], (const char*)d_in[6], (const char*)d_in[7], (char*)d_out,
                                               (char*)d_ws);
}
```

```cpp
#define TB 0
#define TF 0
#define TE 0
#include <hip/hip_runtime.h>
#include <stdint.h>

typedef unsigned u32;
typedef unsigned long long u64;
typedef unsigned short u16;
typedef short s16x4 __attribute__((ext_vector_type(4)));
typedef short s16x8 __attribute__((ext_vector_type(8)));
typedef __bf16 b16x2 __attribute__((ext_vector_type(2)));
typedef float f32x2 __attribute__((ext_vector_type(2)));
typedef float f32x4 __attribute__((ext_vector_type(4)));
typedef float f32x16 __attribute__((ext_vector_type(16)));
typedef u32 u32x2 __attribute__((ext_vector_type(2)));
typedef u32 u32x4 __attribute__((ext_vector_type(4)));
#define LAS __attribute__((address_space(3)))
#define GAS __attribute__((address_space(1)))
#define DEV __device__ __forceinline__
#define INL __attribute__((always_inline))

namespace fk {
constexpr int LDX = 1024, LDQKV = 3072, LDW1 = 2752, LDW2 = 1024;
constexpr int NKS = 118;
constexpr int SLOT = 43008, BOFF = 18432;
constexpr int L_MEAN = 3 * SLOT, L_RSTD = L_MEAN + 576, L_CS = L_RSTD + 576, L_B1 = L_CS + 768, L_OWN = L_B1 + 768,
              L_KV = L_MEAN + 3584, L_JUNK = L_KV + 8192, L_SP = L_JUNK + 2048, L_TOTAL = L_SP + 9 * 2048;
static_assert(L_OWN + 576 + 32 <= L_KV && L_TOTAL <= 163840, "LDS map");
constexpr int WS_GRP = 8192, WS_STAT = 4096;
constexpr int WS_A2 = 16 * WS_GRP, A2_BYTES = 64 * NKS * 1024;
constexpr int AUX_SC0 = 1, AUX_SC1 = 16;
constexpr u32 SPIN_CAP = 1u << 16;

constexpr int NEPI = 12;
constexpr int WIN = 6;
constexpr int RMID = 11;
constexpr int PGAP = 2;

#define STAMP(i) do {} while (0)
#define STAMP_ARG
#define STAMP_PASS
#define STAMP_OUT() do {} while (0)

template <int IMM> DEV void dma16(u32 voff, const void* sbase, u32 lb) {
  asm volatile("s_add_u32 m0, %2, %3\n\ts_nop 0\n\tglobal_load_lds_dwordx4 %0, %1" ::"v"(voff), "s"(sbase), "s"(lb), "i"(IMM) : "memory", "scc");
}
template <int IMM> DEV void dma16_masked(u32 voff, const void* sbase, u32 lb, u32 mlo) {
  asm volatile("s_add_u32 m0, %2, %4\n\ts_mov_b32 exec_lo, %3\n\tglobal_load_lds_dwordx4 %0, %1\n\ts_mov_b32 exec_lo, -1"
               ::"v"(voff), "s"(sbase), "s"(lb), "s"(mlo), "i"(IMM) : "memory", "scc");
}
template <int IMM> DEV void dma16_if(u32 voff, const void* sbase, u32 lb, u32 cond) {
  asm volatile("s_cmp_gt_u32 %3, 1\n\ts_cbranch_scc1 1f\n\ts_add_u32 m0, %2, %4\n\ts_nop 0\n\tglobal_load_lds_dwordx4 %0, %1\n1:"
               ::"v"(voff), "s"(sbase), "s"(lb), "s"(cond), "i"(IMM) : "memory", "scc");
}
DEV void touch(const void* p) {
  asm volatile("s_mov_b32 m0, %1\n\ts_nop 0\n\tglobal_load_lds_dword %0, off sc1" ::"v"(p), "i"(L_JUNK) : "memory");
}
template <int N> DEV void wait_bar() {
  asm volatile("s_waitcnt vmcnt(%0) lgkmcnt(0)\n\ts_barrier" ::"i"(N) : "memory");
}
template <int N> DEV void wait_vm() { asm volatile("s_waitcnt vmcnt(%0)" ::"i"(N) : "memory"); }
DEV void wait_lds() { asm volatile("s_waitcnt lgkmcnt(0)" ::: "memory"); }
DEV void lds_bar() { asm volatile("s_waitcnt lgkmcnt(0)\n\ts_barrier" ::: "memory"); }
#define SB() __builtin_amdgcn_sched_barrier(0)
#define PIN(x) asm volatile("" : "+v"(x))

DEV u32 f2u(float v) { return __builtin_bit_cast(u32, v); }
DEV float u2f(u32 v) { return __builtin_bit_cast(float, v); }
DEV u32 pk_bf16(float a, float b) {
  f32x2 p = {a, b};
  return __builtin_bit_cast(u32, __builtin_convertvector(p, b16x2));
}
DEV u32x2 pk4(f32x4 v) { return u32x2{pk_bf16(v[0], v[1]), pk_bf16(v[2], v[3])}; }
DEV float bf_lo(u32 v) { return u2f(v << 16); }
DEV float bf_hi(u32 v) { return u2f(v & 0xffff0000u); }
DEV float dot2(u32 a, u32 b, float c) {
  return __builtin_amdgcn_fdot2_f32_bf16(__builtin_bit_cast(b16x2, a), __builtin_bit_cast(b16x2, b), c, false);
}
DEV f32x4 mfma16(u32x4 a, u32x4 b, f32x4 c) {
  return __builtin_amdgcn_mfma_f32_16x16x32_bf16(__builtin_bit_cast(s16x8, a), __builtin_bit_cast(s16x8, b), c, 0, 0, 0);
}
DEV f32x4 mfma16k16(u32x2 a, u32x2 b, f32x4 c) {
  return __builtin_amdgcn_mfma_f32_16x16x16bf16_1k(__builtin_bit_cast(s16x4, a), __builtin_bit_cast(s16x4, b), c, 0, 0, 0);
}
DEV f32x16 mfma32(u32x4 a, u32x4 b, f32x16 c) {
  return __builtin_amdgcn_mfma_f32_32x32x16_bf16(__builtin_bit_cast(s16x8, a), __builtin_bit_cast(s16x8, b), c, 0, 0, 0);
}
DEV u32x4 lds128(LAS char* L, u32 off) { return *(LAS u32x4*)(L + off); }
DEV f32x4 ldsf4(LAS char* L, u32 off) { return *(LAS f32x4*)(L + off); }
DEV float ldsf(LAS char* L, u32 off) { return *(LAS float*)(L + off); }
DEV u32x2 ldstr(LAS char* L, u32 off) {
  return __builtin_bit_cast(u32x2, __builtin_amdgcn_ds_read_tr16_b64_v4i16((LAS s16x4*)(L + off)));
}
template <int CTRL> DEV float dpp_add(float v) {
  int t = __builtin_amdgcn_update_dpp(0, (int)f2u(v), CTRL, 0xf, 0xf, true);
  return v + u2f((u32)t);
}
DEV f32x2 swap16(float a, float b) {
  auto q = __builtin_amdgcn_permlane16_swap(f2u(a), f2u(b), false, false);
  return f32x2{u2f(q[0]), u2f(q[1])};
}
DEV f32x2 swap32(float a, float b) {
  auto q = __builtin_amdgcn_permlane32_swap(f2u(a), f2u(b), false, false);
  return f32x2{u2f(q[0]), u2f(q[1])};
}
DEV float vmax(float a, float b) { return __builtin_amdgcn_fmed3f(a, b, __builtin_inff()); }
DEV float sel(bool c, float a, float b) { return c ? a : b; }
DEV u32x4 pair_rows(u32x2 x, u32x2 y) {
  auto a = __builtin_amdgcn_permlane16_swap(x[0], y[0], false, false);
  auto b = __builtin_amdgcn_permlane16_swap(x[1], y[1], false, false);
  return u32x4{a[0], b[0], a[1], b[1]};
}
DEV f32x4 splat4(float v) { return f32x4{v, v, v, v}; }
DEV int fresh_tid() {
  int t = threadIdx.x;
  asm volatile("" : "+v"(t));
  return t;
}
DEV void st128(u32x4 v, __amdgpu_buffer_rsrc_t rs, u32 voff, u32 soff) {
  __builtin_amdgcn_raw_buffer_store_b128(v, rs, voff, soff, AUX_SC1);
  asm volatile("s_nop 1" ::"v"(v));
}
template <int V> struct IC { static constexpr int v = V; };
template <int LO, int HI, class F> DEV void sfor(F&& f) {
  if constexpr (LO < HI) { f(IC<LO>{}); sfor<LO + 1, HI>(f); }
}

struct Ptrs { const char *x, *wq, *wo, *w1; const u16* b1; const char *w2, *b2; char *out, *ws; };
struct Ring {
  u32 voffA, voffB, voffB2, stepB, mlo, xcond, wb;
  int rot, ldw, maxch;
  const char *sA0, *sA1, *sB0, *sB1, *sB2;
  DEV void lanes(int lane) {
    const int r8 = lane >> 3, pos = lane & 7;
    const int swB = (r8 & 2) | ((wb >> 8) & 4);
    voffA = r8 * LDX + ((pos ^ r8) << 4);
    voffB = r8 * ldw + ((pos ^ swB) << 4);
    voffB2 = r8 * ldw + (min(pos ^ swB, maxch) << 4);
  }
  template <int I, int K> DEV void issue() const {
    const int kc = (I + rot) & 7;
    constexpr int slot = (I % 3) * SLOT;
    if constexpr (K == 0) dma16_masked<slot>(voffA + kc * 128, sA0, wb, mlo);
    if constexpr (K == 1) dma16<slot + BOFF>(voffB + kc * stepB, sB0, wb);
    if constexpr (K == 2) dma16<slot + BOFF + 8192>(voffB + kc * stepB, sB1, wb);
    if constexpr (K == 3) dma16<slot + BOFF + 16384>(voffB2 + kc * stepB, sB2, wb);
    if constexpr (K == 4) dma16<slot + 8192>(voffA + kc * 128, sA1, wb);
    if constexpr (K == 5) dma16_if<slot + 12288>(voffA + kc * 128 + 32 * LDX, sA1, wb, xcond);
  }
};

template <int TYPE>
DEV void body(LAS char* L, const Ptrs P, Ring R, int g, int j, int w STAMP_ARG) {
  constexpr bool FFN = TYPE >= 2;
  constexpr int ODD = TYPE & 1;
  constexpr int NT = TYPE == 0 ? 5 : (TYPE == 1 ? 4 : 2);
  constexpr int NC = TYPE == 2 ? 6 : (TYPE == 3 ? 5 : 3);
  constexpr int CB = TYPE == 3 ? 6 : 0;
  constexpr int ND = TYPE == 1 ? 6 : 5;
  constexpr int NM = NT * NC + (FFN ? 2 : 0);
  constexpr int NRL = FFN ? 2 : 4;
  int tid = fresh_tid();
  int lane = tid & 63;
  R.lanes(lane);
  int lg = lane >> 4, lc = lane & 15;
  GAS u64* ctl = (GAS u64*)(P.ws + g * WS_GRP);
  u32x4 r0, r1, r2;
  u64 epoch;
  const int T0 = TYPE == 0 ? 0 : (TYPE == 1 ? 5 : 2 * (w & 3));
  const u32 aA = T0 * 2048 + (lc >> 3) * 1024 + (lc & 7) * 128 + ((lg ^ (lc & 7)) << 4);
  const int tq = lc >> 2, tp = lc & 3;
  const int swR = (tq & 2) | ((lg & 1) << 2);
  const u32 aBl = BOFF + lg * 1024 + tq * 128 + ((tp >> 1) << 4) + (tp & 1) * 8;
  const int crot = min(2 * (w & 3), NC - 2);
  constexpr int NBB = FFN ? NC : 1;
  u32 aB[NBB][2], aAb[2][2];
#pragma unroll
  for (int c = 0; c < NBB; ++c) {
    const int cc = c + crot >= NC ? c + crot - NC : c + crot;
    const int ct = FFN ? CB + cc : (w & 3);
    aB[c][0] = aBl + (ct >> 2) * 8192 + (((2 * (ct & 3)) ^ swR) << 4);
    aB[c][1] = aB[c][0] + 65536;
    asm("" : "+v"(aB[c][0]), "+v"(aB[c][1]));
  }
#pragma unroll
  for (int b = 0; b < 2; ++b) {
    aAb[b][0] = aA ^ (b * 64);
    aAb[b][1] = aAb[b][0] + 65536;
    asm("" : "+v"(aAb[b][0]), "+v"(aAb[b][1]));
  }
  f32x4 acc[NT][NC], acc1[2];
#pragma unroll
  for (int t = 0; t < NT; ++t)
#pragma unroll
    for (int c = 0; c < NC; ++c) acc[t][c] = f32x4{0.f, 0.f, 0.f, 0.f};
  acc1[0] = acc1[1] = f32x4{0.f, 0.f, 0.f, 0.f};
  u32x4 fa[2][NT];
  u32x2 fb[2][NC][2];
  auto rd = [&](int t, int k) INL {
    const int slot = ((t >> 1) % 3) * SLOT, b = t & 1;
    if (k >= 2 && k < 2 + NT) {
      const int o = slot + (k - 2) * 2048, hi = o >= 65536;
      fa[b][k - 2] = lds128(L, aAb[b][hi] + (o - hi * 65536));
    } else {
      const int kk = k < 2 ? k : k - NT, c = kk >> 1;
      const int o = slot + b * 4096 + (kk & 1) * 512 + (FFN ? 0 : c * 8192), hi = o >= 65536;
      fb[b][c][kk & 1] = ldstr(L, aB[FFN ? c : 0][hi] + (o - hi * 65536));
    }
  };
  const int mt = 2 * g + ((j >> 2) & 1), nt = (j & 3) + (j >> 3) * 4;
  const int ks0 = w < 6 ? 15 * w : 90 + 14 * (w - 6), nks = w < 6 ? 15 : 14;
  const u32 voffW = (lane >> 3) * LDW2 + (((lane & 7) ^ ((lane >> 2) & 4)) << 4) + nt * 128;
  const u64 woa = (u64)P.wo, w2a = (u64)P.w2 - 32 * 16 * LDW2;
  const u32 wx0 = w < 5 ? 2 * SLOT + (16 + w) * 2048 : (11 + w) * 2048;
  const u32 wx1 = w < 5 ? SLOT + (16 + w) * 2048 : (w < 7 ? (14 + w) * 2048 : L_SP + 8 * 2048);
  auto wbase = [&](int s) INL -> u32 { const int k = s % 9; return k == 0 ? 2 * R.wb : (k == 5 ? wx0 : (k == 8 ? wx1 : 4 * R.wb)); };
  auto wdma = [&](auto sc_, auto hc_) INL {
    constexpr int s = decltype(sc_)::v, h = decltype(hc_)::v, k = s % 9;
    constexpr int imm = k == 0 ? L_SP : (k <= 2 ? 2 * SLOT + (k - 1) * 2048 : (k <= 4 ? (k - 3) * 2048 : (k == 6 || k == 7 ? SLOT + (k - 6) * 2048 : 0)));
    const int ks = min(ks0 + s, NKS - 1);
    const u64 base = (ks < 32 ? woa : w2a) + (u32)(ks * 16 * LDW2);
    dma16<imm + h * 1024>(voffW + h * 8 * LDW2, (const void*)base, wbase(s));
  };
  u32x4 wf[15][2];
  const u32 tb = (lane >> 5) * 1024 + tq * 128 + (((2 * (lg & 1) + (tp >> 1)) ^ ((tq & 2) << 1)) << 4) + (tp & 1) * 8;
  const __amdgpu_buffer_rsrc_t rsA2 = __builtin_amdgcn_make_buffer_rsrc(P.ws + WS_A2, 0, A2_BYTES, 0x00020000);
  const __amdgpu_buffer_rsrc_t rsG = __builtin_amdgcn_make_buffer_rsrc(P.ws + g * WS_GRP, 0, WS_GRP, 0x00020000);
  const u32x4 ones = {0x3F803F80u, 0x3F803F80u, 0x3F803F80u, 0x3F803F80u};
  u64 tag = 0;
  u32 b1v = 0;
  u32x4 gst;
  u64 tp2 = 0;
  u32 gsto = 0, gldo = 0, b1o = 0, stw = 0, b1w = 0, csw = 0, csw2 = 0;
  float som = 0.f, sor = 0.f;
  f32x4 mP, mP0, mC;
  u32 sel0 = 0, sel1 = 0, wra[4][2];
  auto wread = [&](int s) INL {
    const int k = s % 9;
#pragma unroll
    for (int f = 0; f < 2; ++f) {
      const u32 a = k == 0 ? wra[0][f] : (k <= 2 ? wra[2][f] + (k - 1) * 2048 : (k <= 4 ? wra[1][f] + (k - 3) * 2048 :
                    (k == 6 || k == 7 ? wra[1][f] + SLOT + (k - 6) * 2048 : wra[1][f] - 4 * R.wb + wbase(s))));
      const u32x2 lo = ldstr(L, a), hi = ldstr(L, a + 512);
      wf[s][f] = u32x4{lo[0], lo[1], hi[0], hi[1]};
    }
  };
  constexpr int NK = 5, TK = TYPE == 0 ? 0 : 4;
  float mc1[NK], rc1[NK];
  f32x4 m4[NK], r4[NK], bb[NC];
  auto rdst = [&](int k) INL {
    if (FFN) {
      if (k < 2) mc1[k] = ldsf(L, L_MEAN + ((2 * (w & 3) + k) * 16 + lc) * 4);
      else if (k < 4) rc1[k - 2] = ldsf(L, L_RSTD + ((2 * (w & 3) + k - 2) * 16 + lc) * 4);
      else if (k < 4 + NC) {
        const int c = k - 4, ct = CB + (c + crot >= NC ? c + crot - NC : c + crot);
        bb[c] = ldsf4(L, L_B1 + (ct * 16 + lg * 4) * 4);
      }
    } else {
      const int i = k >> 2, T = TK + i;
      if (i >= NK) return;
      if ((k & 3) == 0) mc1[i] = ldsf(L, L_MEAN + (T * 16 + lc) * 4);
      if ((k & 3) == 1) rc1[i] = ldsf(L, L_RSTD + (T * 16 + lc) * 4);
      if ((k & 3) == 2) m4[i] = ldsf4(L, L_MEAN + (T * 16 + lg * 4) * 4);
      if ((k & 3) == 3) r4[i] = ldsf4(L, L_RSTD + (T * 16 + lg * 4) * 4);
    }
  };
      {
        const int b_ = blockIdx.x, ln = min(b_ * 59 + w * 8 + (lane & 7), 15103);
        tp2 = (ln < 4096 ? woa : w2a) + (u32)(ln * 128);
        const u64 pb = ((lane & 8) ? (u64)P.b1 + (u32)min(max(j - 8, 0) * 352 + (lane & 3) * 128, 2748) : (u64)P.b2 + (u32)(nt * 128));
        const u64 pf = (u64)ctl + (u32)(128 * (lc + 1));
        tp2 = (TB && lg == 1) ? pb : tp2;
        tp2 = (TF && lg == 2 && w == 0) ? pf : tp2;
        gsto = (w == 7 && lane < 8) ? WS_STAT + (j * 8 + lane) * 16 : WS_GRP;
        gldo = WS_STAT + (tid & 127) * 16;
        b1o = 2 * (u32)min(max(j - 8, 0) * 176 + tid, 1375);
        stw = (!ODD && w < 2) ? L_MEAN + (tid + (FFN ? 0 : 16)) * 4 : L_JUNK + lane * 4;
        b1w = tid < 192 ? L_B1 + tid * 4 : L_JUNK + lane * 4;
        csw = lc == 0 ? L_CS + ((FFN ? CB + crot : (w & 3)) * 16 + lg * 4) * 4 : L_JUNK + lane * 16;
        csw2 = lg == 0 ? L_CS + (128 + (w & 3) * 16 + lc) * 4 : L_JUNK + lane * 4;
        if (!FFN) {
#pragma unroll
          for (int r = 0; r < 4; ++r) {
            mP[r] = (4 * lg + r >= lc + 2) ? 0.f : -1e30f;
            mP0[r] = (TYPE == 0 && g == 0) ? -1e30f : mP[r];
            mC[r] = (4 * lg + r <= lc) ? 0.f : -1e30f;
          }
          sel0 = (lc & 1) ? 0x01000c0cu : 0x0c0c0100u; sel1 = (lc & 1) ? 0x03020c0cu : 0x0c0c0302u;
          asm volatile("" : "+v"(mP), "+v"(mC), "+v"(sel0), "+v"(sel1));
          if (TYPE == 0) asm volatile("" : "+v"(mP0));
        }
#pragma unroll
        for (int f = 0; f < 2; ++f) {
          wra[0][f] = (tb ^ (f * 64)) + 2 * R.wb + L_SP;
          wra[1][f] = (tb ^ (f * 64)) + 4 * R.wb;
          wra[2][f] = (tb ^ (f * 64)) + 4 * R.wb + 2 * SLOT;
          asm volatile("" : "+v"(wra[0][f]), "+v"(wra[1][f]), "+v"(wra[2][f]));
        }
        asm volatile("" : "+v"(tp2), "+v"(gsto), "+v"(gldo), "+v"(b1o), "+v"(stw), "+v"(b1w), "+v"(csw), "+v"(csw2));
      }
  SB();
  sfor<0, ND>([&](auto kc) INL { R.template issue<1, decltype(kc)::v>(); });
  if (FFN)
    asm volatile("global_load_dwordx2 %0, %2, %3 sc1\n\tglobal_load_dwordx4 %1, %4, %5"
                 : "=&v"(epoch), "=&v"(r0) : "v"(0u), "s"(ctl), "v"(lane * 16), "s"(P.x + (size_t)(128 * g + 8 * j + w) * LDX) : "memory");
  else
    asm volatile("global_load_dwordx2 %0, %4, %5 sc1\n\tglobal_load_dwordx4 %1, %6, %7\n\t"
                 "global_load_dwordx4 %2, %6, %8\n\tglobal_load_dwordx4 %3, %6, %8 offset:1024"
                 : "=&v"(epoch), "=&v"(r0), "=&v"(r1), "=&v"(r2)
                 : "v"(0u), "s"(ctl), "v"(lane * 16), "s"(P.x + (size_t)(128 * g + 8 * j + w) * LDX),
                   "s"(P.x + (size_t)max(128 * g - 16 + 2 * w, 0) * LDX) : "memory");
  STAMP(1);
  wait_bar<5 + NRL>();
  STAMP(2);
#pragma unroll
  for (int k = 0; k < 2 * NC + NT; ++k) rd(0, k);
  SB();
  sfor<0, 16>([&](auto tc) INL {
    constexpr int t = decltype(tc)::v, s = t >> 1, b = t & 1;
    if constexpr (b) {
      if constexpr (s == 1 || s == 2) wait_bar<6 + ODD>();
      else if constexpr (s <= 4) wait_bar<5>();
      else if constexpr (s == 5) wait_bar<7>();
      else if constexpr (s == 6) wait_bar<4>();
    }
    if constexpr (t == 11) {
      if constexpr (!ODD) asm volatile("" : "+v"(gst), "+v"(b1v));
      if constexpr (!ODD) {
        if (__builtin_expect(!__all(gst[1] == (u32)tag && gst[3] == (u32)tag), 0)) {
#pragma nounroll
          for (u32 spins = 0; spins < SPIN_CAP; ++spins) {
            __builtin_amdgcn_s_sleep(1);
            gst = __builtin_amdgcn_raw_buffer_load_b128(rsG, gldo, 0, AUX_SC1);
            if (__all(gst[1] == (u32)tag && gst[3] == (u32)tag)) break;
          }
        }
      }
      SB();
    }
    constexpr int nr = t == 15 ? 0 : 2 * NC + NT;
    constexpr int st = t == 0 ? 2 : (t + 5) / 2;
    constexpr int k0 = (t == 0 || b) ? 0 : 3, k1 = st >= 8 ? 0 : ((t == 0 || !b) ? ND : 3);
    constexpr int nd = k1 > k0 ? k1 - k0 : 0, nw = t >= 10 ? 2 : 0, nv = nd + nw;
    sfor<0, NM>([&](auto qc) INL {
      constexpr int q = decltype(qc)::v;
      constexpr int qq = t < 15 ? q : (FFN ? (q + NT * NC) % NM : (TYPE == 1 ? q : (q < 3 ? q * NT : (q < 5 ? (q - 2) * NT + 4 :
                         (q - 5 < 4 ? q - 4 : (q - 5 < 7 ? q - 3 : q - 1))))));
      if constexpr (qq < NT * NC) {
        constexpr int c = qq / NT, i = qq % NT;
        const u32x4 wfr = {fb[b][c][0][0], fb[b][c][0][1], fb[b][c][1][0], fb[b][c][1][1]};
        if constexpr (!FFN && c == 2) acc[i][c] = mfma16(fa[b][i], wfr, acc[i][c]);
        else acc[i][c] = mfma16(wfr, fa[b][i], acc[i][c]);
        PIN(acc[i][c]);
      } else {
        constexpr int c = qq - NT * NC;
        const u32x4 wfr = {fb[b][c][0][0], fb[b][c][0][1], fb[b][c][1][0], fb[b][c][1][1]};
        acc1[c] = mfma16(wfr, ones, acc1[c]);
        PIN(acc1[c]);
      }
      if constexpr (q < nr) rd(t + 1, q);
      if constexpr (t == 15) { rdst(2 * q); rdst(2 * q + 1); }
      if constexpr (t == 9 && q == 0 && !ODD)
        asm volatile("buffer_load_dwordx4 %0, %2, %3, 0 offen sc1\n\tglobal_load_ushort %1, %4, %5"
                     : "=&v"(gst), "=&v"(b1v) : "v"(gldo), "s"(rsG), "v"(b1o), "s"(P.b1) : "memory");
      if constexpr (t == 11 && !ODD) {
        if constexpr (q == NM - 4) *(LAS u32*)(L + stw) = gst[0];
        if constexpr (q == NM - 3) *(LAS u32*)(L + stw + 576) = gst[2];
        if constexpr (q == NM - 2 && FFN) *(LAS float*)(L + b1w) = bf_lo(b1v);
      }
      if constexpr (t == 1 && ODD && q == NM - 1) { som = ldsf(L, L_OWN + (lane & 7) * 4); sor = ldsf(L, L_OWN + 576 + (lane & 7) * 4); }
      sfor<0, nv>([&](auto uc) INL {
        constexpr int u = decltype(uc)::v;
        if constexpr (((2 * u + ODD) * NM) / (2 * nv) == q) {
          if constexpr (u < nd) R.template issue<st, k0 + u>();
          else wdma(IC<t - 10>{}, IC<u - nd>{});
        }
      });
      if constexpr (t == 2 && q == NM - 1) {
        touch((const void*)tp2);
        if (ODD) st128(u32x4{f2u(som), (u32)tag, f2u(sor), (u32)tag}, rsG, gsto, 0);
      }
      if constexpr (t == 15) {
        if constexpr (TYPE == 0) {
          if constexpr (q == 6) *(LAS f32x4*)(L + csw) = acc[0][0];
          if constexpr (q == 7) *(LAS f32x4*)(L + csw + 256) = acc[0][1];
          if constexpr (q == 8) *(LAS float*)(L + csw2) = acc[0][2][0];
          if constexpr (q == 9) *(LAS f32x4*)(L + L_KV + 2 * R.wb + lane * 16) = acc[NT - 1][1];
          if constexpr (q == 10) *(LAS f32x4*)(L + L_KV + 2 * R.wb + 1024 + lane * 16) = acc[NT - 1][2];
        }
        if constexpr (FFN) {
          if constexpr (q == 6) *(LAS f32x4*)(L + csw) = acc1[0];
          if constexpr (q == 7) *(LAS f32x4*)(L + csw + 64) = acc1[1];
        }
      }
      SB();
    });
    if constexpr (t == 0) {
      if (FFN) asm volatile("s_waitcnt vmcnt(%2)" : "+v"(epoch), "+v"(r0) : "i"(5) : "memory");
      else asm volatile("s_waitcnt vmcnt(%4)" : "+v"(epoch), "+v"(r0), "+v"(r1), "+v"(r2) : "i"(5) : "memory");
      tag = (((u64)(u32)__builtin_amdgcn_readfirstlane((u32)(epoch >> 32)) << 32) | (u32)__builtin_amdgcn_readfirstlane((u32)epoch)) + 1;
      float sS[3] = {0.f, 0.f, 0.f}, sQ[3] = {0.f, 0.f, 0.f};
#pragma unroll
      for (int d = 0; d < 4; ++d) {
        sS[0] = dot2(r0[d], 0x3F803F80u, sS[0]); sQ[0] = dot2(r0[d], r0[d], sQ[0]);
        if (!FFN) {
          sS[1] = dot2(r1[d], 0x3F803F80u, sS[1]); sQ[1] = dot2(r1[d], r1[d], sQ[1]);
          sS[2] = dot2(r2[d], 0x3F803F80u, sS[2]); sQ[2] = dot2(r2[d], r2[d], sQ[2]);
        }
      }
      f32x2 a = swap32(sS[0], sS[FFN ? 0 : 1]), c = swap32(sQ[0], sQ[FFN ? 0 : 1]);
      float S = a[0] + a[1], Q = c[0] + c[1], S2 = S, Q2 = Q;
      if (!FFN) {
        a = swap32(sS[2], sS[2]); c = swap32(sQ[2], sQ[2]);
        S2 = a[0] + a[1]; Q2 = c[0] + c[1];
      }
      a = swap16(S, S2); c = swap16(Q, Q2);
      S = a[0] + a[1]; Q = c[0] + c[1];
      S = dpp_add<0xB1>(S); Q = dpp_add<0xB1>(Q);
      S = dpp_add<0x4E>(S); Q = dpp_add<0x4E>(Q);
      S = dpp_add<0x141>(S); Q = dpp_add<0x141>(Q);
      S = dpp_add<0x140>(S); Q = dpp_add<0x140>(Q);
      const float m = S * (1.f / 512.f), e = Q * (1.f / 512.f);
      u32 adr = L_JUNK + lane * 4;
      adr = lane == 0 ? L_OWN + w * 4 : adr;
      if (!FFN) { adr = lane == 32 ? L_MEAN + w * 8 : adr; adr = lane == 16 ? L_MEAN + w * 8 + 4 : adr; }
      *(LAS float*)(L + adr) = -m;
      *(LAS float*)(L + adr + 576) = __builtin_amdgcn_rsqf(fmaxf(e - m * m, 0.f) + 1e-5f);
      SB();
    }
  });
  STAMP(3);
  lds_bar();
  STAMP(4);
  auto slot = [&](auto kc) INL {
    constexpr int k = decltype(kc)::v;
    SB();
    if constexpr (k < NEPI) wdma(IC<6 + k / 2>{}, IC<k % 2>{});
    SB();
  };
  if constexpr (!FFN) {
    const int h = w & 3;
    const f32x4 csq = ldsf4(L, L_CS + (h * 16 + lg * 4) * 4), csk = ldsf4(L, L_CS + (64 + h * 16 + lg * 4) * 4);
    const float csv1 = ldsf(L, L_CS + (128 + h * 16 + lc) * 4);
    f32x4 k4 = acc[0][1], v4 = acc[0][2];
    if (TYPE == 1) { k4 = ldsf4(L, L_KV + h * 2048 + lane * 16); v4 = ldsf4(L, L_KV + h * 2048 + 1024 + lane * 16); }
    slot(IC<0>{});
    wait_vm<7>();
    wread(0); wread(1); wread(2);
    const bool odd = lc & 1;
    const f32x4 csv = splat4(csv1);
    u32x2 Qb[4], Kb[NK], Vb[NK];
    sfor<0, NK>([&](auto ic) INL {
      constexpr int i = decltype(ic)::v, ai = TYPE == 0 ? i : (i > 0 ? i - 1 : 0);
      slot(IC<1 + i>{});
      const f32x4 mc = splat4(mc1[i]);
      Kb[i] = pk4(((i == 0 ? k4 : acc[ai][1]) + mc * csk) * splat4(rc1[i]));
      if constexpr (i > 0) Qb[i - 1] = pk4((acc[ai][0] + mc * csq) * splat4(rc1[i] * (0.25f * 1.4426950408889634f)));
      Vb[i] = pk4(((i == 0 ? v4 : acc[ai][2]) + m4[i] * csv) * r4[i]);
    });
    f32x4 sp[4], sc[4];
#pragma unroll
    for (int q = 0; q < 4; ++q) {
      sp[q] = mfma16k16(Kb[q], Qb[q], q == 0 ? mP0 : mP);
      sc[q] = mfma16k16(Kb[q + 1], Qb[q], mC);
    }
    SB();
    wait_lds();
    wait_vm<6>();
    wread(3); wread(4); wread(5);
    slot(IC<6>{});
    f32x4 a[4];
    float mx[4];
#pragma unroll
    for (int q = 0; q < 4; ++q) {
      a[q] = f32x4{sel(odd, sp[q][1], sp[q][0]), sel(odd, sp[q][3], sp[q][2]), sel(odd, sc[q][1], sc[q][0]), sel(odd, sc[q][3], sc[q][2])};
      mx[q] = vmax(vmax(a[q][0], a[q][1]), vmax(a[q][2], a[q][3]));
    }
    slot(IC<7>{});
    {
      const f32x2 x = swap32(mx[0], mx[1]), y = swap32(mx[2], mx[3]);
      const f32x2 z = swap16(vmax(x[0], x[1]), vmax(y[0], y[1]));
      const float u = vmax(z[0], z[1]);
      const f32x2 p = swap16(u, u);
      const f32x2 p0 = swap32(p[0], p[0]), p1 = swap32(p[1], p[1]);
      mx[0] = p0[0]; mx[1] = p0[1]; mx[2] = p1[0]; mx[3] = p1[1];
    }
    slot(IC<8>{});
#pragma unroll
    for (int q = 0; q < 4; ++q) a[q] -= splat4(mx[q]);
    slot(IC<9>{});
    sfor<0, 4>([&](auto qc) INL {
      constexpr int q = decltype(qc)::v;
#pragma unroll
      for (int r = 0; r < 4; ++r) a[q][r] = __builtin_amdgcn_exp2f(a[q][r]);
      if constexpr (q & 1) slot(IC<10 + q / 2>{});
    });
    wait_lds();
    const f32x4 z4 = {0.f, 0.f, 0.f, 0.f};
    u32x4 pb[4];
    sfor<0, 4>([&](auto qc) INL {
      constexpr int q = decltype(qc)::v;
      const u32x2 u = pk4(a[q]);
      pb[q] = u32x4{__builtin_amdgcn_perm(0, u[0], sel0), __builtin_amdgcn_perm(0, u[0], sel1),
                    __builtin_amdgcn_perm(0, u[1], sel0), __builtin_amdgcn_perm(0, u[1], sel1)};
      if constexpr (q & 1) slot(IC<12 + q / 2>{});
    });
#pragma unroll
    for (int q = 0; q < 4; ++q) {
      sc[q] = mfma16(ones, pb[q], z4);
      sp[q] = mfma16k16(Vb[q], u32x2{pb[q][0], pb[q][1]}, z4);
    }
#pragma unroll
    for (int q = 0; q < 4; ++q) sp[q] = mfma16k16(Vb[q + 1], u32x2{pb[q][2], pb[q][3]}, sp[q]);
    slot(IC<14>{}); slot(IC<15>{}); slot(IC<16>{}); slot(IC<17>{});
    u32x2 Ob[4];
#pragma unroll
    for (int q = 0; q < 4; ++q) Ob[q] = pk4(sp[q] * splat4(__builtin_amdgcn_rcpf(sc[q][0])));
    const int rt = 4 * g + (TYPE == 0 ? 0 : 2), ks = 4 * j + h;
    st128(pair_rows(Ob[0], Ob[1]), rsA2, lane * 16, (rt * NKS + ks) * 1024);
    st128(pair_rows(Ob[2], Ob[3]), rsA2, lane * 16, ((rt + 1) * NKS + ks) * 1024);
  } else {
    f32x4 cs[NC];
#pragma unroll
    for (int c = 0; c < NC; ++c) {
      const int ct = CB + (c + crot >= NC ? c + crot - NC : c + crot);
      cs[c] = ldsf4(L, L_CS + (ct * 16 + lg * 4) * 4);
    }
    wait_vm<6>();
    wread(0); wread(1); wread(2);
    const f32x4 mc[2] = {splat4(mc1[0]), splat4(mc1[1])}, rc[2] = {splat4(rc1[0]), splat4(rc1[1])};
    const int nct = j == 15 ? 9 : 11;
    const int rt = 4 * g + (w & 3), ksb = 32 + 11 * (j - 8);
    const f32x4 C1 = splat4(-2.f * 1.4426950408889634f * 0.7978845608028654f);
    const f32x4 C2 = splat4(-2.f * 1.4426950408889634f * 0.7978845608028654f * 0.044715f);
    const f32x4 ONE = splat4(1.f);
    sfor<0, 3>([&](auto gc) INL {
      constexpr int G = decltype(gc)::v, c0 = 2 * G, n = c0 + 1 < NC ? 4 : 2;
      if constexpr (G == 1) { wait_lds(); wait_vm<8>(); wread(3); wread(4); wread(5); }
      if constexpr (G == 2) wait_lds();
      f32x4 v[4], e[4];
      slot(IC<6 * G>{});
#pragma unroll
      for (int u = 0; u < n; ++u) v[u] = (acc[u & 1][c0 + (u >> 1)] + mc[u & 1] * cs[c0 + (u >> 1)]) * rc[u & 1] + bb[c0 + (u >> 1)];
      slot(IC<6 * G + 1>{});
#pragma unroll
      for (int u = 0; u < n; ++u) e[u] = v[u] * (v[u] * v[u] * C2 + C1);
      slot(IC<6 * G + 2>{});
#pragma unroll
      for (int u = 0; u < n; ++u) {
#pragma unroll
        for (int r = 0; r < 4; ++r) e[u][r] = __builtin_amdgcn_exp2f(e[u][r]);
        if (u == 1) slot(IC<6 * G + 3>{});
      }
#pragma unroll
      for (int u = 0; u < n; ++u) e[u] += ONE;
      slot(IC<6 * G + 4>{});
#pragma unroll
      for (int u = 0; u < n; ++u) {
#pragma unroll
        for (int r = 0; r < 4; ++r) e[u][r] = __builtin_amdgcn_rcpf(e[u][r]);
        if (u == 1) slot(IC<6 * G + 5>{});
      }
#pragma unroll
      for (int cc = 0; cc < n / 2; ++cc) {
        const int c = c0 + cc, ct = CB + (c + crot >= NC ? c + crot - NC : c + crot);
        st128(pair_rows(pk4(v[2 * cc] * e[2 * cc]), pk4(v[2 * cc + 1] * e[2 * cc + 1])), rsA2, lane * 16,
              ct < nct ? (rt * NKS + ksb + ct) * 1024 : A2_BYTES);
      }
      SB();
    });
  }
  STAMP(5);
  wait_vm<0>();
  STAMP(13);
  asm volatile("s_barrier" ::: "memory");
  if (tid == 0) __hip_atomic_store(ctl + 16 * (j + 1), tag, __ATOMIC_RELAXED, __HIP_MEMORY_SCOPE_AGENT);
  STAMP(6);
  sfor<NEPI, 18>([&](auto kc) INL { wdma(IC<6 + decltype(kc)::v / 2>{}, IC<decltype(kc)::v % 2>{}); });
#pragma unroll
  for (int s = 6; s < 9; ++s) wread(s);
  tid = fresh_tid(); lane = tid & 63; lg = lane >> 4; lc = lane & 15;
  const u32 ooff = (u32)((mt * 64 + 4 * w + ((lane >> 3) & 3)) * LDX + (nt * 64 + (lane & 7) * 8) * 2);
  f32x4 xb0, xb1;
#pragma unroll
  for (int s = 0; s < 9; ++s) asm volatile("" : "+v"(wf[s][0]), "+v"(wf[s][1]));
  STAMP(7);
  u64 v0, v1, v2, v3;
  wait_vm<0>();
  {
    const u32 fo = 128 * (lc + 1);
    u32 spins = SPIN_CAP;
    asm volatile(
        "s_cmp_lg_u32 %9, %10\n\ts_cbranch_scc1 2f\n\t"
        "global_load_dwordx2 %0, %5, %6 sc1\n\ts_sleep %8\n\tglobal_load_dwordx2 %1, %5, %6 sc1\n\ts_sleep %8\n\t"
        "global_load_dwordx2 %2, %5, %6 sc1\n\ts_sleep %8\n"
        "1:\n\t"
        "global_load_dwordx2 %3, %5, %6 sc1\n\ts_waitcnt vmcnt(3)\n\tv_cmp_ne_u64 vcc, %7, %0\n\ts_cbranch_vccz 2f\n\ts_sleep %8\n\t"
        "global_load_dwordx2 %0, %5, %6 sc1\n\ts_waitcnt vmcnt(3)\n\tv_cmp_ne_u64 vcc, %7, %1\n\ts_cbranch_vccz 2f\n\ts_sleep %8\n\t"
        "global_load_dwordx2 %1, %5, %6 sc1\n\ts_waitcnt vmcnt(3)\n\tv_cmp_ne_u64 vcc, %7, %2\n\ts_cbranch_vccz 2f\n\ts_sleep %8\n\t"
        "global_load_dwordx2 %2, %5, %6 sc1\n\ts_waitcnt vmcnt(3)\n\tv_cmp_ne_u64 vcc, %7, %3\n\ts_cbranch_vccz 2f\n\ts_sleep %8\n\t"
        "s_sub_u32 %4, %4, 1\n\ts_cmp_lg_u32 %4, 0\n\ts_cbranch_scc1 1b\n"
        "2:\n\ts_barrier"
        : "=&v"(v0), "=&v"(v1), "=&v"(v2), "=&v"(v3), "+s"(spins)
        : "v"(fo), "s"(ctl), "s"(tag), "i"(PGAP), "s"(w), "i"(7) : "memory", "vcc", "scc");
  }
  STAMP(8);
  u32x4 af[2][15];
  auto lda = [&](int r, int s) INL {
    const int so = (s < nks) ? ((2 * mt + r) * NKS + ks0 + s) * 1024 : A2_BYTES;
    af[r][s] = __builtin_amdgcn_raw_buffer_load_b128(rsA2, lane * 16, so, AUX_SC1);
  };
#pragma unroll
  for (int n = 0; n < WIN; ++n) lda(n / 15, n % 15);
  SB();
  u32 pw[4];
#pragma unroll
  for (int y = 0; y < 4; ++y) pw[y] = ((lane + 4 * y) & 63) * 16 + y * 1024 + 4 * R.wb;
  const int blk = lane & 7;
  const u32 prd = (((lane & 32) + 4 * w + ((lane >> 3) & 3) + 4 * (blk & 3)) & 63) * 16 + (blk & 3) * 1024;
  const u32 prd0 = prd + (blk >> 2) * SLOT, prd1 = prd + (blk >> 2) * 4096 + 2 * SLOT;
  const __amdgpu_buffer_rsrc_t rsO = __builtin_amdgcn_make_buffer_rsrc(P.out, 0, 2048 * LDX, 0x00020000);
  auto reduce = [&](int r, f32x4 xb) INL {
    f32x4 p[8];
#pragma unroll
    for (int sw = 0; sw < 8; ++sw) p[sw] = ldsf4(L, r == 0 ? prd0 + sw * 4096 : prd1 + sw * 8192);
    SB();
    const u32x2 o = pk4(((p[0] + p[1]) + (p[2] + p[3])) + ((p[4] + p[5]) + (p[6] + p[7])) + xb);
    auto e0 = __builtin_amdgcn_permlane32_swap(o[0], o[0], false, false), e1 = __builtin_amdgcn_permlane32_swap(o[1], o[1], false, false);
    __builtin_amdgcn_raw_buffer_store_b128(u32x4{e0[0], e1[0], e0[1], e1[1]}, rsO, lane < 32 ? ooff + r * 32 * LDX : 2048 * LDX, 0, AUX_SC0 | AUX_SC1);
  };
  f32x16 pa[2][2];
  auto pwrite = [&](int r, int b8) INL {
    const f32x16& a = pa[r][b8 >> 2];
    const int y = b8 & 3;
    *(LAS f32x4*)(L + pw[y] + (r == 0 ? (b8 >> 2) * SLOT : 2 * SLOT + 4 * R.wb + (b8 >> 2) * 4096)) =
        f32x4{a[4 * y], a[4 * y + 1], a[4 * y + 2], a[4 * y + 3]};
  };
  u32x2 xr0, xr1, bres;
  sfor<0, 2>([&](auto rc_) INL {
    constexpr int r = decltype(rc_)::v;
#pragma unroll
    for (int i = 0; i < 16; ++i) pa[r][0][i] = pa[r][1][i] = 0.f;
    sfor<0, 15>([&](auto sc_) INL {
      constexpr int s = decltype(sc_)::v;
      pa[r][0] = mfma32(wf[s][0], af[r][s], pa[r][0]);
      PIN(pa[r][0]);
      if constexpr (r == 1 && s >= 1 && s < 5) pwrite(0, 2 * s - 2);
      pa[r][1] = mfma32(wf[s][1], af[r][s], pa[r][1]);
      PIN(pa[r][1]);
      if constexpr (r == 1 && s >= 1 && s < 5) pwrite(0, 2 * s - 1);
      if constexpr (r == 0 && s == 2) asm volatile("" ::"v"(v0), "v"(v1), "v"(v2), "v"(v3));
      if constexpr (r == 0 && s == 0) STAMP(14);
      if constexpr (r == 0 && s == 14) STAMP(15);
      if constexpr (r == 0 && s == 1) {
        const u32 o4 = ooff + (lane >> 5) * 8;
        xr0 = *(const GAS u32x2*)(P.x + o4); xr1 = *(const GAS u32x2*)(P.x + (o4 + 32 * LDX));
        bres = *(const GAS u32x2*)(P.b2 + (o4 & 1023));
      }
      if constexpr (r * 15 + s + WIN < 30) lda((r * 15 + s + WIN) / 15, (r * 15 + s + WIN) % 15);
      if constexpr (r == 0 && s >= 7 && s < 13) wread(s + 2);
      if constexpr (r == 0 && s == 14) {
        const f32x4 bv = {bf_lo(bres[0]), bf_hi(bres[0]), bf_lo(bres[1]), bf_hi(bres[1])};
        xb0 = f32x4{bf_lo(xr0[0]), bf_hi(xr0[0]), bf_lo(xr0[1]), bf_hi(xr0[1])} + bv;
        xb1 = f32x4{bf_lo(xr1[0]), bf_hi(xr1[0]), bf_lo(xr1[1]), bf_hi(xr1[1])} + bv;
        PIN(xb0); PIN(xb1);
      }
      if constexpr (r == 1 && s == RMID) { lds_bar(); reduce(0, xb0); }
      SB();
    });
  });
#pragma unroll
  for (int y = 0; y < 8; ++y) pwrite(1, y);
  STAMP(9);
  lds_bar();
  reduce(1, xb1);
  if (j == 0 && tid == 0) __hip_atomic_store(ctl, tag, __ATOMIC_RELAXED, __HIP_MEMORY_SCOPE_AGENT);
  STAMP(10);
  STAMP_OUT();
  asm volatile(".Lfk_end%c0:" ::"i"(TYPE));
  __builtin_amdgcn_endpgm();
}

__global__ __launch_bounds__(512, 2) void fused(const char*, const char*, const char*, const char*, const u16*, const char*, const char*,
                                                char*, char*) {
  extern __shared__ __attribute__((aligned(16))) char smem[];
  LAS char* L = (LAS char*)smem;
  STAMP(0);
  typedef u64 u64x8 __attribute__((ext_vector_type(8)));
  u64x8 ka;
  u64 kb;
  asm volatile("s_load_dwordx16 %0, %2, 0x0\n\ts_load_dwordx2 %1, %2, 0x40"
               : "=&s"(ka), "=&s"(kb) : "s"(__builtin_amdgcn_kernarg_segment_ptr()) : "memory");
  const int lane = threadIdx.x & 63, w = __builtin_amdgcn_readfirstlane(threadIdx.x >> 6);
  const int b = blockIdx.x, bi = b >> 3;
  const int g = (b & 3) * 4 + (bi & 3), j = ((bi >> 2) & 3) + ((b >> 2) & 1) * 4 + (bi >> 4) * 8;
  const bool ffn = j >= 8;
  if (w == 7) {
    u64 pc;
    u32 len;
    asm volatile("s_getpc_b64 %0\n.Lfk_here:\n\ts_mov_b32 %1, .Lfk_end0-.Lfk_here\n\ts_max_u32 %1, %1, .Lfk_end1-.Lfk_here\n\t"
                 "s_max_u32 %1, %1, .Lfk_end2-.Lfk_here\n\ts_max_u32 %1, %1, .Lfk_end3-.Lfk_here" : "=s"(pc), "=s"(len)::"scc");
    touch((const char*)pc + min((u32)(lane * 128 + (bi & 7) * 8192), len));
  }
  const int rowbase = ffn ? 128 * g : 128 * g - 16;
  Ring R;
  R.ldw = ffn ? LDW1 : LDQKV;
  R.maxch = ffn ? (j == 15 ? 1 : 5) : 7;
  R.wb = w * 1024;
  R.rot = (j + g) & 7;
  R.lanes(lane);
  R.stepB = 64 * R.ldw;
  R.mlo = (!ffn && w == 0) ? 0xFFFFFF00u : 0xFFFFFFFFu;
  R.xcond = ffn ? 2u : (u32)(w - 4);
  const u32 oA0 = (rowbase + 8 * w < 0 ? 8 * w : rowbase + 8 * w) * LDX, oA1 = (rowbase + 8 * (w + 8)) * LDX;
  const u32 oB = 8 * w * R.ldw + (ffn ? (j - 8) * 352 : j * 128), seg = ffn ? 128 : 1024;
  asm volatile("s_waitcnt lgkmcnt(0)" : "+s"(ka), "+s"(kb)::"memory");
  const Ptrs P = {(const char*)(GAS const char*)ka[0], (const char*)(GAS const char*)ka[1], (const char*)(GAS const char*)ka[2],
                  (const char*)(GAS const char*)ka[3], (const u16*)(GAS const u16*)ka[4], (const char*)(GAS const char*)ka[5],
                  (const char*)(GAS const char*)ka[6], (char*)(GAS char*)ka[7], (char*)(GAS char*)kb};
  STAMP(11);
  R.sA0 = P.x + oA0;
  R.sA1 = P.x + oA1;
  R.sB0 = (const char*)((ffn ? ka[3] : ka[1]) + oB);
  R.sB1 = R.sB0 + seg;
  R.sB2 = R.sB1 + seg;
  sfor<0, 6>([&](auto kc) INL { R.template issue<0, decltype(kc)::v>(); });
  if (!ffn && w == 0 && lane < 8) {
    const u32x4 one = {0x3F803F80u, 0x3F803F80u, 0x3F803F80u, 0x3F803F80u};
#pragma unroll
    for (int s = 0; s < 3; ++s) *(LAS u32x4*)(L + s * SLOT + lane * 16) = one;
  }
  if (TE && w == 6) touch(P.ws + g * WS_GRP);
  STAMP(12);
  if (!ffn) {
    if (w < 4) body<0>(L, P, R, g, j, w STAMP_PASS);
    else body<1>(L, P, R, g, j, w STAMP_PASS);
  } else {
    if (w < 4) body<2>(L, P, R, g, j, w STAMP_PASS);
    else body<3>(L, P, R, g, j, w STAMP_PASS);
  }
}
}

extern "C" void kernel_launch(void* const* d_in, const int* in_sizes, int n_in, void* d_out, int out_size, void* d_ws,
                              size_t ws_size, hipStream_t stream) {
  (void)hipFuncSetAttribute((const void*)fk::fused, hipFuncAttributeMaxDynamicSharedMemorySize, fk::L_TOTAL);
  fk::fused<<<256, 512, fk::L_TOTAL, stream>>>((const char*)d_in[0], (const char*)d_in[1], (const char*)d_in[2], (const char*)d_in[4],
                                               (const u16*)d_in[5], (const char*)d_in[6], (const char*)d_in[7], (char*)d_out,
                                               (char*)d_ws);
}
```

```cpp
#include <hip/hip_runtime.h>
#include <stdint.h>

typedef unsigned u32;
typedef unsigned long long u64;
typedef unsigned short u16;
typedef short s16x4 __attribute__((ext_vector_type(4)));
typedef short s16x8 __attribute__((ext_vector_type(8)));
typedef __bf16 b16x2 __attribute__((ext_vector_type(2)));
typedef float f32x2 __attribute__((ext_vector_type(2)));
typedef float f32x4 __attribute__((ext_vector_type(4)));
typedef float f32x16 __attribute__((ext_vector_type(16)));
typedef u32 u32x2 __attribute__((ext_vector_type(2)));
typedef u32 u32x4 __attribute__((ext_vector_type(4)));
#define LAS __attribute__((address_space(3)))
#define GAS __attribute__((address_space(1)))
#define DEV __device__ __forceinline__
#define INL __attribute__((always_inline))

namespace fk {
constexpr int LDX = 1024, LDQKV = 3072, LDW1 = 2752, LDW2 = 1024;
constexpr int NKS = 118;
constexpr int SLOT = 43008, BOFF = 18432;
constexpr int L_MEAN = 3 * SLOT, L_RSTD = L_MEAN + 576, L_CS = L_RSTD + 576, L_B1 = L_CS + 768, L_OWN = L_B1 + 768,
              L_KV = L_MEAN + 3584, L_JUNK = L_KV + 8192, L_SP = L_JUNK + 2048, L_TOTAL = L_SP + 9 * 2048;
static_assert(L_OWN + 576 + 32 <= L_KV && L_TOTAL <= 163840, "LDS map");
constexpr int WS_GRP = 8192, WS_STAT = 4096;
constexpr int WS_A2 = 16 * WS_GRP, A2_BYTES = 64 * NKS * 1024;
constexpr int AUX_SC0 = 1, AUX_SC1 = 16;
constexpr u32 SPIN_CAP = 1u << 16;

constexpr int NEPI = 12;
constexpr int WIN = 6;
constexpr int RMID = 11;
constexpr int PGAP = 2;

#define STAMP(i) do {} while (0)
#define STAMP_ARG
#define STAMP_PASS
#define STAMP_OUT() do {} while (0)

template <int IMM> DEV void dma16(u32 voff, const void* sbase, u32 lb) {
  asm volatile("s_add_u32 m0, %2, %3\n\ts_nop 0\n\tglobal_load_lds_dwordx4 %0, %1" ::"v"(voff), "s"(sbase), "s"(lb), "i"(IMM) : "memory", "scc");
}
template <int IMM> DEV void dma16_masked(u32 voff, const void* sbase, u32 lb, u32 mlo) {
  asm volatile("s_add_u32 m0, %2, %4\n\ts_mov_b32 exec_lo, %3\n\tglobal_load_lds_dwordx4 %0, %1\n\ts_mov_b32 exec_lo, -1"
               ::"v"(voff), "s"(sbase), "s"(lb), "s"(mlo), "i"(IMM) : "memory", "scc");
}
template <int IMM> DEV void dma16_if(u32 voff, const void* sbase, u32 lb, u32 cond) {
  asm volatile("s_cmp_gt_u32 %3, 1\n\ts_cbranch_scc1 1f\n\ts_add_u32 m0, %2, %4\n\ts_nop 0\n\tglobal_load_lds_dwordx4 %0, %1\n1:"
               ::"v"(voff), "s"(sbase), "s"(lb), "s"(cond), "i"(IMM) : "memory", "scc");
}
DEV void touch(const void* p) {
  asm volatile("s_mov_b32 m0, %1\n\ts_nop 0\n\tglobal_load_lds_dword %0, off sc1" ::"v"(p), "i"(L_JUNK) : "memory");
}
template <int N> DEV void wait_bar() {
  asm volatile("s_waitcnt vmcnt(%0) lgkmcnt(0)\n\ts_barrier" ::"i"(N) : "memory");
}
template <int N> DEV void wait_vm() { asm volatile("s_waitcnt vmcnt(%0)" ::"i"(N) : "memory"); }
DEV void wait_lds() { asm volatile("s_waitcnt lgkmcnt(0)" ::: "memory"); }
DEV void lds_bar() { asm volatile("s_waitcnt lgkmcnt(0)\n\ts_barrier" ::: "memory"); }
#define SB() __builtin_amdgcn_sched_barrier(0)
#define PIN(x) asm volatile("" : "+v"(x))

DEV u32 f2u(float v) { return __builtin_bit_cast(u32, v); }
DEV float u2f(u32 v) { return __builtin_bit_cast(float, v); }
DEV u32 pk_bf16(float a, float b) {
  f32x2 p = {a, b};
  return __builtin_bit_cast(u32, __builtin_convertvector(p, b16x2));
}
DEV u32x2 pk4(f32x4 v) { return u32x2{pk_bf16(v[0], v[1]), pk_bf16(v[2], v[3])}; }
DEV float bf_lo(u32 v) { return u2f(v << 16); }
DEV float bf_hi(u32 v) { return u2f(v & 0xffff0000u); }
DEV float dot2(u32 a, u32 b, float c) {
  return __builtin_amdgcn_fdot2_f32_bf16(__builtin_bit_cast(b16x2, a), __builtin_bit_cast(b16x2, b), c, false);
}
DEV f32x4 mfma16(u32x4 a, u32x4 b, f32x4 c) {
  return __builtin_amdgcn_mfma_f32_16x16x32_bf16(__builtin_bit_cast(s16x8, a), __builtin_bit_cast(s16x8, b), c, 0, 0, 0);
}
DEV f32x4 mfma16k16(u32x2 a, u32x2 b, f32x4 c) {
  return __builtin_amdgcn_mfma_f32_16x16x16bf16_1k(__builtin_bit_cast(s16x4, a), __builtin_bit_cast(s16x4, b), c, 0, 0, 0);
}
DEV f32x16 mfma32(u32x4 a, u32x4 b, f32x16 c) {
  return __builtin_amdgcn_mfma_f32_32x32x16_bf16(__builtin_bit_cast(s16x8, a), __builtin_bit_cast(s16x8, b), c, 0, 0, 0);
}
DEV u32x4 lds128(LAS char* L, u32 off) { return *(LAS u32x4*)(L + off); }
DEV f32x4 ldsf4(LAS char* L, u32 off) { return *(LAS f32x4*)(L + off); }
DEV float ldsf(LAS char* L, u32 off) { return *(LAS float*)(L + off); }
DEV u32x2 ldstr(LAS char* L, u32 off) {
  return __builtin_bit_cast(u32x2, __builtin_amdgcn_ds_read_tr16_b64_v4i16((LAS s16x4*)(L + off)));
}
template <int CTRL> DEV float dpp_add(float v) {
  int t = __builtin_amdgcn_update_dpp(0, (int)f2u(v), CTRL, 0xf, 0xf, true);
  return v + u2f((u32)t);
}
DEV f32x2 swap16(float a, float b) {
  auto q = __builtin_amdgcn_permlane16_swap(f2u(a), f2u(b), false, false);
  return f32x2{u2f(q[0]), u2f(q[1])};
}
DEV f32x2 swap32(float a, float b) {
  auto q = __builtin_amdgcn_permlane32_swap(f2u(a), f2u(b), false, false);
  return f32x2{u2f(q[0]), u2f(q[1])};
}
DEV float vmax(float a, float b) { return __builtin_amdgcn_fmed3f(a, b, __builtin_inff()); }
DEV float sel(bool c, float a, float b) { return c ? a : b; }
DEV u32x4 pair_rows(u32x2 x, u32x2 y) {
  auto a = __builtin_amdgcn_permlane16_swap(x[0], y[0], false, false);
  auto b = __builtin_amdgcn_permlane16_swap(x[1], y[1], false, false);
  return u32x4{a[0], b[0], a[1], b[1]};
}
DEV f32x4 splat4(float v) { return f32x4{v, v, v, v}; }
DEV int fresh_tid() {
  int t = threadIdx.x;
  asm volatile("" : "+v"(t));
  return t;
}
DEV void st128(u32x4 v, __amdgpu_buffer_rsrc_t rs, u32 voff, u32 soff) {
  __builtin_amdgcn_raw_buffer_store_b128(v, rs, voff, soff, AUX_SC1);
  asm volatile("s_nop 1" ::"v"(v));
}
template <int V> struct IC { static constexpr int v = V; };
template <int LO, int HI, class F> DEV void sfor(F&& f) {
  if constexpr (LO < HI) { f(IC<LO>{}); sfor<LO + 1, HI>(f); }
}

struct Ptrs { const char *x, *wq, *wo, *w1; const u16* b1; const char *w2, *b2; char *out, *ws; };
struct Ring {
  u32 voffA, voffB, voffB2, stepB, mlo, xcond, wb;
  int rot, ldw, maxch;
  const char *sA0, *sA1, *sB0, *sB1, *sB2;
  DEV void lanes(int lane) {
    const int r8 = lane >> 3, pos = lane & 7;
    const int swB = (r8 & 2) | ((wb >> 8) & 4);
    voffA = r8 * LDX + ((pos ^ r8) << 4);
    voffB = r8 * ldw + ((pos ^ swB) << 4);
    voffB2 = r8 * ldw + (min(pos ^ swB, maxch) << 4);
  }
  template <int I, int K> DEV void issue() const {
    const int kc = (I + rot) & 7;
    constexpr int slot = (I % 3) * SLOT;
    if constexpr (K == 0) dma16_masked<slot>(voffA + kc * 128, sA0, wb, mlo);
    if constexpr (K == 1) dma16<slot + BOFF>(voffB + kc * stepB, sB0, wb);
    if constexpr (K == 2) dma16<slot + BOFF + 8192>(voffB + kc * stepB, sB1, wb);
    if constexpr (K == 3) dma16<slot + BOFF + 16384>(voffB2 + kc * stepB, sB2, wb);
    if constexpr (K == 4) dma16<slot + 8192>(voffA + kc * 128, sA1, wb);
    if constexpr (K == 5) dma16_if<slot + 12288>(voffA + kc * 128 + 32 * LDX, sA1, wb, xcond);
  }
};

template <int TYPE>
DEV void body(LAS char* L, const Ptrs P, Ring R, int g, int j, int w STAMP_ARG) {
  constexpr bool FFN = TYPE >= 2;
  constexpr int ODD = TYPE & 1;
  constexpr int NT = TYPE == 0 ? 5 : (TYPE == 1 ? 4 : 2);
  constexpr int NC = TYPE == 2 ? 6 : (TYPE == 3 ? 5 : 3);
  constexpr int CB = TYPE == 3 ? 6 : 0;
  constexpr int ND = TYPE == 1 ? 6 : 5;
  constexpr int NM = NT * NC + (FFN ? 2 : 0);
  constexpr int NRL = FFN ? 2 : 4;
  int tid = fresh_tid();
  int lane = tid & 63;
  R.lanes(lane);
  int lg = lane >> 4, lc = lane & 15;
  GAS u64* ctl = (GAS u64*)(P.ws + g * WS_GRP);
  u32x4 r0, r1, r2;
  u64 epoch;
  const int T0 = TYPE == 0 ? 0 : (TYPE == 1 ? 5 : 2 * (w & 3));
  const u32 aA = T0 * 2048 + (lc >> 3) * 1024 + (lc & 7) * 128 + ((lg ^ (lc & 7)) << 4);
  const int tq = lc >> 2, tp = lc & 3;
  const int swR = (tq & 2) | ((lg & 1) << 2);
  const u32 aBl = BOFF + lg * 1024 + tq * 128 + ((tp >> 1) << 4) + (tp & 1) * 8;
  const int crot = min(2 * (w & 3), NC - 2);
  constexpr int NBB = FFN ? NC : 1;
  u32 aB[NBB][2], aAb[2][2];
#pragma unroll
  for (int c = 0; c < NBB; ++c) {
    const int cc = c + crot >= NC ? c + crot - NC : c + crot;
    const int ct = FFN ? CB + cc : (w & 3);
    aB[c][0] = aBl + (ct >> 2) * 8192 + (((2 * (ct & 3)) ^ swR) << 4);
    aB[c][1] = aB[c][0] + 65536;
    asm("" : "+v"(aB[c][0]), "+v"(aB[c][1]));
  }
#pragma unroll
  for (int b = 0; b < 2; ++b) {
    aAb[b][0] = aA ^ (b * 64);
    aAb[b][1] = aAb[b][0] + 65536;
    asm("" : "+v"(aAb[b][0]), "+v"(aAb[b][1]));
  }
  f32x4 acc[NT][NC], acc1[2];
#pragma unroll
  for (int t = 0; t < NT; ++t)
#pragma unroll
    for (int c = 0; c < NC; ++c) acc[t][c] = f32x4{0.f, 0.f, 0.f, 0.f};
  acc1[0] = acc1[1] = f32x4{0.f, 0.f, 0.f, 0.f};
  u32x4 fa[2][NT];
  u32x2 fb[2][NC][2];
  auto rd = [&](int t, int k) INL {
    const int slot = ((t >> 1) % 3) * SLOT, b = t & 1;
    if (k >= 2 && k < 2 + NT) {
      const int o = slot + (k - 2) * 2048, hi = o >= 65536;
      fa[b][k - 2] = lds128(L, aAb[b][hi] + (o - hi * 65536));
    } else {
      const int kk = k < 2 ? k : k - NT, c = kk >> 1;
      const int o = slot + b * 4096 + (kk & 1) * 512 + (FFN ? 0 : c * 8192), hi = o >= 65536;
      fb[b][c][kk & 1] = ldstr(L, aB[FFN ? c : 0][hi] + (o - hi * 65536));
    }
  };
  const int mt = 2 * g + ((j >> 2) & 1), nt = (j & 3) + (j >> 3) * 4;
  const int ks0 = w < 6 ? 15 * w : 90 + 14 * (w - 6), nks = w < 6 ? 15 : 14;
  const u32 voffW = (lane >> 3) * LDW2 + (((lane & 7) ^ ((lane >> 2) & 4)) << 4) + nt * 128;
  const u64 woa = (u64)P.wo, w2a = (u64)P.w2 - 32 * 16 * LDW2;
  const u32 wx0 = w < 5 ? 2 * SLOT + (16 + w) * 2048 : (11 + w) * 2048;
  const u32 wx1 = w < 5 ? SLOT + (16 + w) * 2048 : (w < 7 ? (14 + w) * 2048 : L_SP + 8 * 2048);
  auto wbase = [&](int s) INL -> u32 { const int k = s % 9; return k == 0 ? 2 * R.wb : (k == 5 ? wx0 : (k == 8 ? wx1 : 4 * R.wb)); };
  auto wdma = [&](auto sc_, auto hc_) INL {
    constexpr int s = decltype(sc_)::v, h = decltype(hc_)::v, k = s % 9;
    constexpr int imm = k == 0 ? L_SP : (k <= 2 ? 2 * SLOT + (k - 1) * 2048 : (k <= 4 ? (k - 3) * 2048 : (k == 6 || k == 7 ? SLOT + (k - 6) * 2048 : 0)));
    const int ks = min(ks0 + s, NKS - 1);
    const u64 base = (ks < 32 ? woa : w2a) + (u32)(ks * 16 * LDW2);
    dma16<imm + h * 1024>(voffW + h * 8 * LDW2, (const void*)base, wbase(s));
  };
  u32x4 wf[15][2];
  const u32 tb = (lane >> 5) * 1024 + tq * 128 + (((2 * (lg & 1) + (tp >> 1)) ^ ((tq & 2) << 1)) << 4) + (tp & 1) * 8;
  const __amdgpu_buffer_rsrc_t rsA2 = __builtin_amdgcn_make_buffer_rsrc(P.ws + WS_A2, 0, A2_BYTES, 0x00020000);
  const __amdgpu_buffer_rsrc_t rsG = __builtin_amdgcn_make_buffer_rsrc(P.ws + g * WS_GRP, 0, WS_GRP, 0x00020000);
  const u32x4 ones = {0x3F803F80u, 0x3F803F80u, 0x3F803F80u, 0x3F803F80u};
  u64 tag = 0;
  u32 b1v = 0;
  u32x4 gst;
  u64 tp2 = 0;
  u32 gsto = 0, gldo = 0, b1o = 0, stw = 0, b1w = 0, csw = 0, csw2 = 0;
  float som = 0.f, sor = 0.f;
  f32x4 mP, mP0, mC;
  u32 sel0 = 0, sel1 = 0, wra[4][2];
  auto wread = [&](int s) INL {
    const int k = s % 9;
#pragma unroll
    for (int f = 0; f < 2; ++f) {
      const u32 a = k == 0 ? wra[0][f] : (k <= 2 ? wra[2][f] + (k - 1) * 2048 : (k <= 4 ? wra[1][f] + (k - 3) * 2048 :
                    (k == 6 || k == 7 ? wra[1][f] + SLOT + (k - 6) * 2048 : wra[1][f] - 4 * R.wb + wbase(s))));
      const u32x2 lo = ldstr(L, a), hi = ldstr(L, a + 512);
      wf[s][f] = u32x4{lo[0], lo[1], hi[0], hi[1]};
    }
  };
  constexpr int NK = 5, TK = TYPE == 0 ? 0 : 4;
  float mc1[NK], rc1[NK];
  f32x4 m4[NK], r4[NK], bb[NC];
  auto rdst = [&](int k) INL {
    if (FFN) {
      if (k < 2) mc1[k] = ldsf(L, L_MEAN + ((2 * (w & 3) + k) * 16 + lc) * 4);
      else if (k < 4) rc1[k - 2] = ldsf(L, L_RSTD + ((2 * (w & 3) + k - 2) * 16 + lc) * 4);
      else if (k < 4 + NC) {
        const int c = k - 4, ct = CB + (c + crot >= NC ? c + crot - NC : c + crot);
        bb[c] = ldsf4(L, L_B1 + (ct * 16 + lg * 4) * 4);
      }
    } else {
      const int i = k >> 2, T = TK + i;
      if (i >= NK) return;
      if ((k & 3) == 0) mc1[i] = ldsf(L, L_MEAN + (T * 16 + lc) * 4);
      if ((k & 3) == 1) rc1[i] = ldsf(L, L_RSTD + (T * 16 + lc) * 4);
      if ((k & 3) == 2) m4[i] = ldsf4(L, L_MEAN + (T * 16 + lg * 4) * 4);
      if ((k & 3) == 3) r4[i] = ldsf4(L, L_RSTD + (T * 16 + lg * 4) * 4);
    }
  };
      {
        const int b_ = blockIdx.x, ln = min(b_ * 59 + w * 8 + (lane & 7), 15103);
        tp2 = (ln < 4096 ? woa : w2a) + (u32)(ln * 128);
        const u64 pb = ((lane & 8) ? (u64)P.b1 + (u32)min(max(j - 8, 0) * 352 + (lane & 3) * 128, 2748) : (u64)P.b2 + (u32)(nt * 128));
        const u64 pf = (u64)ctl + (u32)(128 * (lc + 1));
        tp2 = lg == 1 ? pb : tp2;
        tp2 = (lg == 2 && w == 0) ? pf : tp2;
        gsto = (w == 7 && lane < 8) ? WS_STAT + (j * 8 + lane) * 16 : WS_GRP;
        gldo = WS_STAT + (tid & 127) * 16;
        b1o = 2 * (u32)min(max(j - 8, 0) * 176 + tid, 1375);
        stw = (!ODD && w < 2) ? L_MEAN + (tid + (FFN ? 0 : 16)) * 4 : L_JUNK + lane * 4;
        b1w = tid < 192 ? L_B1 + tid * 4 : L_JUNK + lane * 4;
        csw = lc == 0 ? L_CS + ((FFN ? CB + crot : (w & 3)) * 16 + lg * 4) * 4 : L_JUNK + lane * 16;
        csw2 = lg == 0 ? L_CS + (128 + (w & 3) * 16 + lc) * 4 : L_JUNK + lane * 4;
        if (!FFN) {
#pragma unroll
          for (int r = 0; r < 4; ++r) {
            mP[r] = (4 * lg + r >= lc + 2) ? 0.f : -1e30f;
            mP0[r] = (TYPE == 0 && g == 0) ? -1e30f : mP[r];
            mC[r] = (4 * lg + r <= lc) ? 0.f : -1e30f;
          }
          sel0 = (lc & 1) ? 0x01000c0cu : 0x0c0c0100u; sel1 = (lc & 1) ? 0x03020c0cu : 0x0c0c0302u;
          asm volatile("" : "+v"(mP), "+v"(mC), "+v"(sel0), "+v"(sel1));
          if (TYPE == 0) asm volatile("" : "+v"(mP0));
        }
#pragma unroll
        for (int f = 0; f < 2; ++f) {
          wra[0][f] = (tb ^ (f * 64)) + 2 * R.wb + L_SP;
          wra[1][f] = (tb ^ (f * 64)) + 4 * R.wb;
          wra[2][f] = (tb ^ (f * 64)) + 4 * R.wb + 2 * SLOT;
          asm volatile("" : "+v"(wra[0][f]), "+v"(wra[1][f]), "+v"(wra[2][f]));
        }
        asm volatile("" : "+v"(tp2), "+v"(gsto), "+v"(gldo), "+v"(b1o), "+v"(stw), "+v"(b1w), "+v"(csw), "+v"(csw2));
      }
  SB();
  sfor<0, ND>([&](auto kc) INL { R.template issue<1, decltype(kc)::v>(); });
  if (FFN)
    asm volatile("global_load_dwordx2 %0, %2, %3 sc1\n\tglobal_load_dwordx4 %1, %4, %5"
                 : "=&v"(epoch), "=&v"(r0) : "v"(0u), "s"(ctl), "v"(lane * 16), "s"(P.x + (size_t)(128 * g + 8 * j + w) * LDX) : "memory");
  else
    asm volatile("global_load_dwordx2 %0, %4, %5 sc1\n\tglobal_load_dwordx4 %1, %6, %7\n\t"
                 "global_load_dwordx4 %2, %6, %8\n\tglobal_load_dwordx4 %3, %6, %8 offset:1024"
                 : "=&v"(epoch), "=&v"(r0), "=&v"(r1), "=&v"(r2)
                 : "v"(0u), "s"(ctl), "v"(lane * 16), "s"(P.x + (size_t)(128 * g + 8 * j + w) * LDX),
                   "s"(P.x + (size_t)max(128 * g - 16 + 2 * w, 0) * LDX) : "memory");
  STAMP(1);
  wait_bar<5 + NRL>();
  STAMP(2);
#pragma unroll
  for (int k = 0; k < 2 * NC + NT; ++k) rd(0, k);
  SB();
  sfor<0, 16>([&](auto tc) INL {
    constexpr int t = decltype(tc)::v, s = t >> 1, b = t & 1;
    if constexpr (b) {
      if constexpr (s == 1) wait_bar<5 + ODD>();
      else if constexpr (s == 2) wait_bar<6 + ODD>();
      else if constexpr (s == 3) wait_bar<6>();
      else if constexpr (s <= 4) wait_bar<5>();
      else if constexpr (s == 5) wait_bar<7>();
      else if constexpr (s == 6) wait_bar<4>();
    }
    if constexpr (t == 11) {
      if constexpr (!ODD) asm volatile("" : "+v"(gst), "+v"(b1v));
      if constexpr (!ODD) {
        if (__builtin_expect(!__all(gst[1] == (u32)tag && gst[3] == (u32)tag), 0)) {
#pragma nounroll
          for (u32 spins = 0; spins < SPIN_CAP; ++spins) {
            __builtin_amdgcn_s_sleep(1);
            gst = __builtin_amdgcn_raw_buffer_load_b128(rsG, gldo, 0, AUX_SC1);
            if (__all(gst[1] == (u32)tag && gst[3] == (u32)tag)) break;
          }
        }
      }
      SB();
    }
    constexpr int nr = t == 15 ? 0 : 2 * NC + NT;
    constexpr int st = t == 0 ? 2 : (t + 5) / 2;
    constexpr int k0 = (t == 0 || b) ? 0 : 3, k1 = st >= 8 ? 0 : ((t == 0 || !b) ? ND : 3);
    constexpr int nd = k1 > k0 ? k1 - k0 : 0, nw = t >= 10 ? 2 : 0, nv = nd + nw;
    sfor<0, NM>([&](auto qc) INL {
      constexpr int q = decltype(qc)::v;
      constexpr int qq = t < 15 ? q : (FFN ? (q + NT * NC) % NM : (TYPE == 1 ? q : (q < 3 ? q * NT : (q < 5 ? (q - 2) * NT + 4 :
                         (q - 5 < 4 ? q - 4 : (q - 5 < 7 ? q - 3 : q - 1))))));
      if constexpr (qq < NT * NC) {
        constexpr int c = qq / NT, i = qq % NT;
        const u32x4 wfr = {fb[b][c][0][0], fb[b][c][0][1], fb[b][c][1][0], fb[b][c][1][1]};
        if constexpr (!FFN && c == 2) acc[i][c] = mfma16(fa[b][i], wfr, acc[i][c]);
        else acc[i][c] = mfma16(wfr, fa[b][i], acc[i][c]);
        PIN(acc[i][c]);
      } else {
        constexpr int c = qq - NT * NC;
        const u32x4 wfr = {fb[b][c][0][0], fb[b][c][0][1], fb[b][c][1][0], fb[b][c][1][1]};
        acc1[c] = mfma16(wfr, ones, acc1[c]);
        PIN(acc1[c]);
      }
      if constexpr (q < nr) rd(t + 1, q);
      if constexpr (t == 15) { rdst(2 * q); rdst(2 * q + 1); }
      if constexpr (t == 9 && q == 0 && !ODD)
        asm volatile("buffer_load_dwordx4 %0, %2, %3, 0 offen sc1\n\tglobal_load_ushort %1, %4, %5"
                     : "=&v"(gst), "=&v"(b1v) : "v"(gldo), "s"(rsG), "v"(b1o), "s"(P.b1) : "memory");
      if constexpr (t == 11 && !ODD) {
        if constexpr (q == NM - 4) *(LAS u32*)(L + stw) = gst[0];
        if constexpr (q == NM - 3) *(LAS u32*)(L + stw + 576) = gst[2];
        if constexpr (q == NM - 2 && FFN) *(LAS float*)(L + b1w) = bf_lo(b1v);
      }
      if constexpr (t == 1 && ODD && q == NM - 1) { som = ldsf(L, L_OWN + (lane & 7) * 4); sor = ldsf(L, L_OWN + 576 + (lane & 7) * 4); }
      sfor<0, nv>([&](auto uc) INL {
        constexpr int u = decltype(uc)::v;
        if constexpr (((2 * u + ODD) * NM) / (2 * nv) == q) {
          if constexpr (u < nd) R.template issue<st, k0 + u>();
          else wdma(IC<t - 10>{}, IC<u - nd>{});
        }
      });
      if constexpr (t == 4 && q == NM - 1) touch((const void*)tp2);
      if constexpr (t == 2 && q == NM - 1) {
        if (ODD) st128(u32x4{f2u(som), (u32)tag, f2u(sor), (u32)tag}, rsG, gsto, 0);
      }
      if constexpr (t == 15) {
        if constexpr (TYPE == 0) {
          if constexpr (q == 6) *(LAS f32x4*)(L + csw) = acc[0][0];
          if constexpr (q == 7) *(LAS f32x4*)(L + csw + 256) = acc[0][1];
          if constexpr (q == 8) *(LAS float*)(L + csw2) = acc[0][2][0];
          if constexpr (q == 9) *(LAS f32x4*)(L + L_KV + 2 * R.wb + lane * 16) = acc[NT - 1][1];
          if constexpr (q == 10) *(LAS f32x4*)(L + L_KV + 2 * R.wb + 1024 + lane * 16) = acc[NT - 1][2];
        }
        if constexpr (FFN) {
          if constexpr (q == 6) *(LAS f32x4*)(L + csw) = acc1[0];
          if constexpr (q == 7) *(LAS f32x4*)(L + csw + 64) = acc1[1];
        }
      }
      SB();
    });
    if constexpr (t == 0) {
      if (FFN) asm volatile("s_waitcnt vmcnt(%2)" : "+v"(epoch), "+v"(r0) : "i"(5) : "memory");
      else asm volatile("s_waitcnt vmcnt(%4)" : "+v"(epoch), "+v"(r0), "+v"(r1), "+v"(r2) : "i"(5) : "memory");
      tag = (((u64)(u32)__builtin_amdgcn_readfirstlane((u32)(epoch >> 32)) << 32) | (u32)__builtin_amdgcn_readfirstlane((u32)epoch)) + 1;
      float sS[3] = {0.f, 0.f, 0.f}, sQ[3] = {0.f, 0.f, 0.f};
#pragma unroll
      for (int d = 0; d < 4; ++d) {
        sS[0] = dot2(r0[d], 0x3F803F80u, sS[0]); sQ[0] = dot2(r0[d], r0[d], sQ[0]);
        if (!FFN) {
          sS[1] = dot2(r1[d], 0x3F803F80u, sS[1]); sQ[1] = dot2(r1[d], r1[d], sQ[1]);
          sS[2] = dot2(r2[d], 0x3F803F80u, sS[2]); sQ[2] = dot2(r2[d], r2[d], sQ[2]);
        }
      }
      f32x2 a = swap32(sS[0], sS[FFN ? 0 : 1]), c = swap32(sQ[0], sQ[FFN ? 0 : 1]);
      float S = a[0] + a[1], Q = c[0] + c[1], S2 = S, Q2 = Q;
      if (!FFN) {
        a = swap32(sS[2], sS[2]); c = swap32(sQ[2], sQ[2]);
        S2 = a[0] + a[1]; Q2 = c[0] + c[1];
      }
      a = swap16(S, S2); c = swap16(Q, Q2);
      S = a[0] + a[1]; Q = c[0] + c[1];
      S = dpp_add<0xB1>(S); Q = dpp_add<0xB1>(Q);
      S = dpp_add<0x4E>(S); Q = dpp_add<0x4E>(Q);
      S = dpp_add<0x141>(S); Q = dpp_add<0x141>(Q);
      S = dpp_add<0x140>(S); Q = dpp_add<0x140>(Q);
      const float m = S * (1.f / 512.f), e = Q * (1.f / 512.f);
      u32 adr = L_JUNK + lane * 4;
      adr = lane == 0 ? L_OWN + w * 4 : adr;
      if (!FFN) { adr = lane == 32 ? L_MEAN + w * 8 : adr; adr = lane == 16 ? L_MEAN + w * 8 + 4 : adr; }
      *(LAS float*)(L + adr) = -m;
      *(LAS float*)(L + adr + 576) = __builtin_amdgcn_rsqf(fmaxf(e - m * m, 0.f) + 1e-5f);
      SB();
    }
  });
  STAMP(3);
  lds_bar();
  STAMP(4);
  auto slot = [&](auto kc) INL {
    constexpr int k = decltype(kc)::v;
    SB();
    if constexpr (k < NEPI) wdma(IC<6 + k / 2>{}, IC<k % 2>{});
    SB();
  };
  if constexpr (!FFN) {
    const int h = w & 3;
    const f32x4 csq = ldsf4(L, L_CS + (h * 16 + lg * 4) * 4), csk = ldsf4(L, L_CS + (64 + h * 16 + lg * 4) * 4);
    const float csv1 = ldsf(L, L_CS + (128 + h * 16 + lc) * 4);
    f32x4 k4 = acc[0][1], v4 = acc[0][2];
    if (TYPE == 1) { k4 = ldsf4(L, L_KV + h * 2048 + lane * 16); v4 = ldsf4(L, L_KV + h * 2048 + 1024 + lane * 16); }
    slot(IC<0>{});
    wait_vm<7>();
    wread(0); wread(1); wread(2);
    const bool odd = lc & 1;
    const f32x4 csv = splat4(csv1);
    u32x2 Qb[4], Kb[NK], Vb[NK];
    sfor<0, NK>([&](auto ic) INL {
      constexpr int i = decltype(ic)::v, ai = TYPE == 0 ? i : (i > 0 ? i - 1 : 0);
      slot(IC<1 + i>{});
      const f32x4 mc = splat4(mc1[i]);
      Kb[i] = pk4(((i == 0 ? k4 : acc[ai][1]) + mc * csk) * splat4(rc1[i]));
      if constexpr (i > 0) Qb[i - 1] = pk4((acc[ai][0] + mc * csq) * splat4(rc1[i] * (0.25f * 1.4426950408889634f)));
      Vb[i] = pk4(((i == 0 ? v4 : acc[ai][2]) + m4[i] * csv) * r4[i]);
    });
    f32x4 sp[4], sc[4];
#pragma unroll
    for (int q = 0; q < 4; ++q) {
      sp[q] = mfma16k16(Kb[q], Qb[q], q == 0 ? mP0 : mP);
      sc[q] = mfma16k16(Kb[q + 1], Qb[q], mC);
    }
    SB();
    wait_lds();
    wait_vm<6>();
    wread(3); wread(4); wread(5);
    slot(IC<6>{});
    f32x4 a[4];
    float mx[4];
#pragma unroll
    for (int q = 0; q < 4; ++q) {
      a[q] = f32x4{sel(odd, sp[q][1], sp[q][0]), sel(odd, sp[q][3], sp[q][2]), sel(odd, sc[q][1], sc[q][0]), sel(odd, sc[q][3], sc[q][2])};
      mx[q] = vmax(vmax(a[q][0], a[q][1]), vmax(a[q][2], a[q][3]));
    }
    slot(IC<7>{});
    {
      const f32x2 x = swap32(mx[0], mx[1]), y = swap32(mx[2], mx[3]);
      const f32x2 z = swap16(vmax(x[0], x[1]), vmax(y[0], y[1]));
      const float u = vmax(z[0], z[1]);
      const f32x2 p = swap16(u, u);
      const f32x2 p0 = swap32(p[0], p[0]), p1 = swap32(p[1], p[1]);
      mx[0] = p0[0]; mx[1] = p0[1]; mx[2] = p1[0]; mx[3] = p1[1];
    }
    slot(IC<8>{});
#pragma unroll
    for (int q = 0; q < 4; ++q) a[q] -= splat4(mx[q]);
    slot(IC<9>{});
    sfor<0, 4>([&](auto qc) INL {
      constexpr int q = decltype(qc)::v;
#pragma unroll
      for (int r = 0; r < 4; ++r) a[q][r] = __builtin_amdgcn_exp2f(a[q][r]);
      if constexpr (q & 1) slot(IC<10 + q / 2>{});
    });
    wait_lds();
    const f32x4 z4 = {0.f, 0.f, 0.f, 0.f};
    u32x4 pb[4];
    sfor<0, 4>([&](auto qc) INL {
      constexpr int q = decltype(qc)::v;
      const u32x2 u = pk4(a[q]);
      pb[q] = u32x4{__builtin_amdgcn_perm(0, u[0], sel0), __builtin_amdgcn_perm(0, u[0], sel1),
                    __builtin_amdgcn_perm(0, u[1], sel0), __builtin_amdgcn_perm(0, u[1], sel1)};
      if constexpr (q & 1) slot(IC<12 + q / 2>{});
    });
#pragma unroll
    for (int q = 0; q < 4; ++q) {
      sc[q] = mfma16(ones, pb[q], z4);
      sp[q] = mfma16k16(Vb[q], u32x2{pb[q][0], pb[q][1]}, z4);
    }
#pragma unroll
    for (int q = 0; q < 4; ++q) sp[q] = mfma16k16(Vb[q + 1], u32x2{pb[q][2], pb[q][3]}, sp[q]);
    slot(IC<14>{}); slot(IC<15>{}); slot(IC<16>{}); slot(IC<17>{});
    u32x2 Ob[4];
#pragma unroll
    for (int q = 0; q < 4; ++q) Ob[q] = pk4(sp[q] * splat4(__builtin_amdgcn_rcpf(sc[q][0])));
    const int rt = 4 * g + (TYPE == 0 ? 0 : 2), ks = 4 * j + h;
    st128(pair_rows(Ob[0], Ob[1]), rsA2, lane * 16, (rt * NKS + ks) * 1024);
    st128(pair_rows(Ob[2], Ob[3]), rsA2, lane * 16, ((rt + 1) * NKS + ks) * 1024);
  } else {
    f32x4 cs[NC];
#pragma unroll
    for (int c = 0; c < NC; ++c) {
      const int ct = CB + (c + crot >= NC ? c + crot - NC : c + crot);
      cs[c] = ldsf4(L, L_CS + (ct * 16 + lg * 4) * 4);
    }
    wait_vm<6>();
    wread(0); wread(1); wread(2);
    const f32x4 mc[2] = {splat4(mc1[0]), splat4(mc1[1])}, rc[2] = {splat4(rc1[0]), splat4(rc1[1])};
    const int nct = j == 15 ? 9 : 11;
    const int rt = 4 * g + (w & 3), ksb = 32 + 11 * (j - 8);
    const f32x4 C1 = splat4(-2.f * 1.4426950408889634f * 0.7978845608028654f);
    const f32x4 C2 = splat4(-2.f * 1.4426950408889634f * 0.7978845608028654f * 0.044715f);
    const f32x4 ONE = splat4(1.f);
    sfor<0, 3>([&](auto gc) INL {
      constexpr int G = decltype(gc)::v, c0 = 2 * G, n = c0 + 1 < NC ? 4 : 2;
      if constexpr (G == 1) { wait_lds(); wait_vm<8>(); wread(3); wread(4); wread(5); }
      if constexpr (G == 2) wait_lds();
      f32x4 v[4], e[4];
      slot(IC<6 * G>{});
#pragma unroll
      for (int u = 0; u < n; ++u) v[u] = (acc[u & 1][c0 + (u >> 1)] + mc[u & 1] * cs[c0 + (u >> 1)]) * rc[u & 1] + bb[c0 + (u >> 1)];
      slot(IC<6 * G + 1>{});
#pragma unroll
      for (int u = 0; u < n; ++u) e[u] = v[u] * (v[u] * v[u] * C2 + C1);
      slot(IC<6 * G + 2>{});
#pragma unroll
      for (int u = 0; u < n; ++u) {
#pragma unroll
        for (int r = 0; r < 4; ++r) e[u][r] = __builtin_amdgcn_exp2f(e[u][r]);
        if (u == 1) slot(IC<6 * G + 3>{});
      }
#pragma unroll
      for (int u = 0; u < n; ++u) e[u] += ONE;
      slot(IC<6 * G + 4>{});
#pragma unroll
      for (int u = 0; u < n; ++u) {
#pragma unroll
        for (int r = 0; r < 4; ++r) e[u][r] = __builtin_amdgcn_rcpf(e[u][r]);
        if (u == 1) slot(IC<6 * G + 5>{});
      }
#pragma unroll
      for (int cc = 0; cc < n / 2; ++cc) {
        const int c = c0 + cc, ct = CB + (c + crot >= NC ? c + crot - NC : c + crot);
        st128(pair_rows(pk4(v[2 * cc] * e[2 * cc]), pk4(v[2 * cc + 1] * e[2 * cc + 1])), rsA2, lane * 16,
              ct < nct ? (rt * NKS + ksb + ct) * 1024 : A2_BYTES);
      }
      SB();
    });
  }
  STAMP(5);
  wait_vm<0>();
  STAMP(13);
  asm volatile("s_barrier" ::: "memory");
  if (tid == 0) __hip_atomic_store(ctl + 16 * (j + 1), tag, __ATOMIC_RELAXED, __HIP_MEMORY_SCOPE_AGENT);
  STAMP(6);
  sfor<NEPI, 18>([&](auto kc) INL { wdma(IC<6 + decltype(kc)::v / 2>{}, IC<decltype(kc)::v % 2>{}); });
#pragma unroll
  for (int s = 6; s < 9; ++s) wread(s);
  tid = fresh_tid(); lane = tid & 63; lg = lane >> 4; lc = lane & 15;
  const u32 ooff = (u32)((mt * 64 + 4 * w + ((lane >> 3) & 3)) * LDX + (nt * 64 + (lane & 7) * 8) * 2);
  f32x4 xb0, xb1;
#pragma unroll
  for (int s = 0; s < 9; ++s) asm volatile("" : "+v"(wf[s][0]), "+v"(wf[s][1]));
  STAMP(7);
  u64 v0, v1, v2, v3;
  wait_vm<0>();
  {
    const u32 fo = 128 * (lc + 1);
    u32 spins = SPIN_CAP;
    asm volatile(
        "s_cmp_lg_u32 %9, %10\n\ts_cbranch_scc1 2f\n\t"
        "global_load_dwordx2 %0, %5, %6 sc1\n\ts_sleep %8\n\tglobal_load_dwordx2 %1, %5, %6 sc1\n\ts_sleep %8\n\t"
        "global_load_dwordx2 %2, %5, %6 sc1\n\ts_sleep %8\n"
        "1:\n\t"
        "global_load_dwordx2 %3, %5, %6 sc1\n\ts_waitcnt vmcnt(3)\n\tv_cmp_ne_u64 vcc, %7, %0\n\ts_cbranch_vccz 2f\n\ts_sleep %8\n\t"
        "global_load_dwordx2 %0, %5, %6 sc1\n\ts_waitcnt vmcnt(3)\n\tv_cmp_ne_u64 vcc, %7, %1\n\ts_cbranch_vccz 2f\n\ts_sleep %8\n\t"
        "global_load_dwordx2 %1, %5, %6 sc1\n\ts_waitcnt vmcnt(3)\n\tv_cmp_ne_u64 vcc, %7, %2\n\ts_cbranch_vccz 2f\n\ts_sleep %8\n\t"
        "global_load_dwordx2 %2, %5, %6 sc1\n\ts_waitcnt vmcnt(3)\n\tv_cmp_ne_u64 vcc, %7, %3\n\ts_cbranch_vccz 2f\n\ts_sleep %8\n\t"
        "s_sub_u32 %4, %4, 1\n\ts_cmp_lg_u32 %4, 0\n\ts_cbranch_scc1 1b\n"
        "2:\n\ts_barrier"
        : "=&v"(v0), "=&v"(v1), "=&v"(v2), "=&v"(v3), "+s"(spins)
        : "v"(fo), "s"(ctl), "s"(tag), "i"(PGAP), "s"(w), "i"(7) : "memory", "vcc", "scc");
  }
  STAMP(8);
  u32x4 af[2][15];
  auto lda = [&](int r, int s) INL {
    const int so = (s < nks) ? ((2 * mt + r) * NKS + ks0 + s) * 1024 : A2_BYTES;
    af[r][s] = __builtin_amdgcn_raw_buffer_load_b128(rsA2, lane * 16, so, AUX_SC1);
  };
#pragma unroll
  for (int n = 0; n < WIN; ++n) lda(n / 15, n % 15);
  SB();
  u32 pw[4];
#pragma unroll
  for (int y = 0; y < 4; ++y) pw[y] = ((lane + 4 * y) & 63) * 16 + y * 1024 + 4 * R.wb;
  const int blk = lane & 7;
  const u32 prd = (((lane & 32) + 4 * w + ((lane >> 3) & 3) + 4 * (blk & 3)) & 63) * 16 + (blk & 3) * 1024;
  const u32 prd0 = prd + (blk >> 2) * SLOT, prd1 = prd + (blk >> 2) * 4096 + 2 * SLOT;
  const __amdgpu_buffer_rsrc_t rsO = __builtin_amdgcn_make_buffer_rsrc(P.out, 0, 2048 * LDX, 0x00020000);
  auto reduce = [&](int r, f32x4 xb) INL {
    f32x4 p[8];
#pragma unroll
    for (int sw = 0; sw < 8; ++sw) p[sw] = ldsf4(L, r == 0 ? prd0 + sw * 4096 : prd1 + sw * 8192);
    SB();
    const u32x2 o = pk4(((p[0] + p[1]) + (p[2] + p[3])) + ((p[4] + p[5]) + (p[6] + p[7])) + xb);
    auto e0 = __builtin_amdgcn_permlane32_swap(o[0], o[0], false, false), e1 = __builtin_amdgcn_permlane32_swap(o[1], o[1], false, false);
    __builtin_amdgcn_raw_buffer_store_b128(u32x4{e0[0], e1[0], e0[1], e1[1]}, rsO, lane < 32 ? ooff + r * 32 * LDX : 2048 * LDX, 0, AUX_SC0 | AUX_SC1);
  };
  f32x16 pa[2][2];
  auto pwrite = [&](int r, int b8) INL {
    const f32x16& a = pa[r][b8 >> 2];
    const int y = b8 & 3;
    *(LAS f32x4*)(L + pw[y] + (r == 0 ? (b8 >> 2) * SLOT : 2 * SLOT + 4 * R.wb + (b8 >> 2) * 4096)) =
        f32x4{a[4 * y], a[4 * y + 1], a[4 * y + 2], a[4 * y + 3]};
  };
  u32x2 xr0, xr1, bres;
  sfor<0, 2>([&](auto rc_) INL {
    constexpr int r = decltype(rc_)::v;
#pragma unroll
    for (int i = 0; i < 16; ++i) pa[r][0][i] = pa[r][1][i] = 0.f;
    sfor<0, 15>([&](auto sc_) INL {
      constexpr int s = decltype(sc_)::v;
      pa[r][0] = mfma32(wf[s][0], af[r][s], pa[r][0]);
      PIN(pa[r][0]);
      if constexpr (r == 1 && s >= 1 && s < 5) pwrite(0, 2 * s - 2);
      pa[r][1] = mfma32(wf[s][1], af[r][s], pa[r][1]);
      PIN(pa[r][1]);
      if constexpr (r == 1 && s >= 1 && s < 5) pwrite(0, 2 * s - 1);
      if constexpr (r == 0 && s == 2) asm volatile("" ::"v"(v0), "v"(v1), "v"(v2), "v"(v3));
      if constexpr (r == 0 && s == 0) STAMP(14);
      if constexpr (r == 0 && s == 14) STAMP(15);
      if constexpr (r == 0 && s == 1) {
        const u32 o4 = ooff + (lane >> 5) * 8;
        xr0 = *(const GAS u32x2*)(P.x + o4); xr1 = *(const GAS u32x2*)(P.x + (o4 + 32 * LDX));
        bres = *(const GAS u32x2*)(P.b2 + (o4 & 1023));
      }
      if constexpr (r * 15 + s + WIN < 30) lda((r * 15 + s + WIN) / 15, (r * 15 + s + WIN) % 15);
      if constexpr (r == 0 && s >= 7 && s < 13) wread(s + 2);
      if constexpr (r == 0 && s == 14) {
        const f32x4 bv = {bf_lo(bres[0]), bf_hi(bres[0]), bf_lo(bres[1]), bf_hi(bres[1])};
        xb0 = f32x4{bf_lo(xr0[0]), bf_hi(xr0[0]), bf_lo(xr0[1]), bf_hi(xr0[1])} + bv;
        xb1 = f32x4{bf_lo(xr1[0]), bf_hi(xr1[0]), bf_lo(xr1[1]), bf_hi(xr1[1])} + bv;
        PIN(xb0); PIN(xb1);
      }
      if constexpr (r == 1 && s == RMID) { lds_bar(); reduce(0, xb0); }
      SB();
    });
  });
#pragma unroll
  for (int y = 0; y < 8; ++y) pwrite(1, y);
  STAMP(9);
  lds_bar();
  reduce(1, xb1);
  if (j == 0 && tid == 0) __hip_atomic_store(ctl, tag, __ATOMIC_RELAXED, __HIP_MEMORY_SCOPE_AGENT);
  STAMP(10);
  STAMP_OUT();
  asm volatile(".Lfk_end%c0:" ::"i"(TYPE));
  __builtin_amdgcn_endpgm();
}

__global__ __launch_bounds__(512, 2) void fused(const char*, const char*, const char*, const char*, const u16*, const char*, const char*,
                                                char*, char*) {
  extern __shared__ __attribute__((aligned(16))) char smem[];
  LAS char* L = (LAS char*)smem;
  STAMP(0);
  typedef u64 u64x8 __attribute__((ext_vector_type(8)));
  u64x8 ka;
  u64 kb;
  asm volatile("s_load_dwordx16 %0, %2, 0x0\n\ts_load_dwordx2 %1, %2, 0x40"
               : "=&s"(ka), "=&s"(kb) : "s"(__builtin_amdgcn_kernarg_segment_ptr()) : "memory");
  const int lane = threadIdx.x & 63, w = __builtin_amdgcn_readfirstlane(threadIdx.x >> 6);
  const int b = blockIdx.x, bi = b >> 3;
  const int g = (b & 3) * 4 + (bi & 3), j = ((bi >> 2) & 3) + ((b >> 2) & 1) * 4 + (bi >> 4) * 8;
  const bool ffn = j >= 8;
  if (w == 7) {
    u64 pc;
    u32 len;
    asm volatile("s_getpc_b64 %0\n.Lfk_here:\n\ts_mov_b32 %1, .Lfk_end0-.Lfk_here\n\ts_max_u32 %1, %1, .Lfk_end1-.Lfk_here\n\t"
                 "s_max_u32 %1, %1, .Lfk_end2-.Lfk_here\n\ts_max_u32 %1, %1, .Lfk_end3-.Lfk_here" : "=s"(pc), "=s"(len)::"scc");
    touch((const char*)pc + min((u32)(lane * 128 + (bi & 7) * 8192), len));
  }
  const int rowbase = ffn ? 128 * g : 128 * g - 16;
  Ring R;
  R.ldw = ffn ? LDW1 : LDQKV;
  R.maxch = ffn ? (j == 15 ? 1 : 5) : 7;
  R.wb = w * 1024;
  R.rot = (j + g) & 7;
  R.lanes(lane);
  R.stepB = 64 * R.ldw;
  R.mlo = (!ffn && w == 0) ? 0xFFFFFF00u : 0xFFFFFFFFu;
  R.xcond = ffn ? 2u : (u32)(w - 4);
  const u32 oA0 = (rowbase + 8 * w < 0 ? 8 * w : rowbase + 8 * w) * LDX, oA1 = (rowbase + 8 * (w + 8)) * LDX;
  const u32 oB = 8 * w * R.ldw + (ffn ? (j - 8) * 352 : j * 128), seg = ffn ? 128 : 1024;
  asm volatile("s_waitcnt lgkmcnt(0)" : "+s"(ka), "+s"(kb)::"memory");
  const Ptrs P = {(const char*)(GAS const char*)ka[0], (const char*)(GAS const char*)ka[1], (const char*)(GAS const char*)ka[2],
                  (const char*)(GAS const char*)ka[3], (const u16*)(GAS const u16*)ka[4], (const char*)(GAS const char*)ka[5],
                  (const char*)(GAS const char*)ka[6], (char*)(GAS char*)ka[7], (char*)(GAS char*)kb};
  STAMP(11);
  R.sA0 = P.x + oA0;
  R.sA1 = P.x + oA1;
  R.sB0 = (const char*)((ffn ? ka[3] : ka[1]) + oB);
  R.sB1 = R.sB0 + seg;
  R.sB2 = R.sB1 + seg;
  sfor<0, 6>([&](auto kc) INL { R.template issue<0, decltype(kc)::v>(); });
  if (!ffn && w == 0 && lane < 8) {
    const u32x4 one = {0x3F803F80u, 0x3F803F80u, 0x3F803F80u, 0x3F803F80u};
#pragma unroll
    for (int s = 0; s < 3; ++s) *(LAS u32x4*)(L + s * SLOT + lane * 16) = one;
  }
  STAMP(12);
  if (!ffn) {
    if (w < 4) body<0>(L, P, R, g, j, w STAMP_PASS);
    else body<1>(L, P, R, g, j, w STAMP_PASS);
  } else {
    if (w < 4) body<2>(L, P, R, g, j, w STAMP_PASS);
    else body<3>(L, P, R, g, j, w STAMP_PASS);
  }
}
}

extern "C" void kernel_launch(void* const* d_in, const int* in_sizes, int n_in, void* d_out, int out_size, void* d_ws,
                              size_t ws_size, hipStream_t stream) {
  (void)hipFuncSetAttribute((const void*)fk::fused, hipFuncAttributeMaxDynamicSharedMemorySize, fk::L_TOTAL);
  fk::fused<<<256, 512, fk::L_TOTAL, stream>>>((const char*)d_in[0], (const char*)d_in[1], (const char*)d_in[2], (const char*)d_in[4],
                                               (const u16*)d_in[5], (const char*)d_in[6], (const char*)d_in[7], (char*)d_out,
                                               (char*)d_ws);
}
```

```cpp
#include <hip/hip_runtime.h>
#include <stdint.h>

typedef unsigned u32;
typedef unsigned long long u64;
typedef unsigned short u16;
typedef short s16x4 __attribute__((ext_vector_type(4)));
typedef short s16x8 __attribute__((ext_vector_type(8)));
typedef __bf16 b16x2 __attribute__((ext_vector_type(2)));
typedef float f32x2 __attribute__((ext_vector_type(2)));
typedef float f32x4 __attribute__((ext_vector_type(4)));
typedef float f32x16 __attribute__((ext_vector_type(16)));
typedef u32 u32x2 __attribute__((ext_vector_type(2)));
typedef u32 u32x4 __attribute__((ext_vector_type(4)));
#define LAS __attribute__((address_space(3)))
#define GAS __attribute__((address_space(1)))
#define DEV __device__ __forceinline__
#define INL __attribute__((always_inline))

namespace fk {
constexpr int LDX = 1024, LDQKV = 3072, LDW1 = 2752, LDW2 = 1024;
constexpr int NKS = 118;
constexpr int SLOT = 43008, BOFF = 18432;
constexpr int L_MEAN = 3 * SLOT, L_RSTD = L_MEAN + 576, L_CS = L_RSTD + 576, L_B1 = L_CS + 768, L_OWN = L_B1 + 768,
              L_KV = L_MEAN + 3584, L_JUNK = L_KV + 8192, L_SP = L_JUNK + 2048, L_TOTAL = L_SP + 9 * 2048;
static_assert(L_OWN + 576 + 32 <= L_KV && L_TOTAL <= 163840, "LDS map");
constexpr int WS_GRP = 8192, WS_STAT = 4096;
constexpr int WS_A2 = 16 * WS_GRP, A2_BYTES = 64 * NKS * 1024;
constexpr int AUX_SC0 = 1, AUX_SC1 = 16;
constexpr u32 SPIN_CAP = 1u << 16;

constexpr int NEPI = 12;
constexpr int TT = 4;
constexpr int WIN = 5;
constexpr int RMID = 11;
constexpr int PGAP = 2;

#define STAMP(i) do {} while (0)
#define STAMP_ARG
#define STAMP_PASS
#define STAMP_OUT() do {} while (0)

template <int IMM> DEV void dma16(u32 voff, const void* sbase, u32 lb) {
  asm volatile("s_add_u32 m0, %2, %3\n\ts_nop 0\n\tglobal_load_lds_dwordx4 %0, %1" ::"v"(voff), "s"(sbase), "s"(lb), "i"(IMM) : "memory", "scc");
}
template <int IMM> DEV void dma16_masked(u32 voff, const void* sbase, u32 lb, u32 mlo) {
  asm volatile("s_add_u32 m0, %2, %4\n\ts_mov_b32 exec_lo, %3\n\tglobal_load_lds_dwordx4 %0, %1\n\ts_mov_b32 exec_lo, -1"
               ::"v"(voff), "s"(sbase), "s"(lb), "s"(mlo), "i"(IMM) : "memory", "scc");
}
template <int IMM> DEV void dma16_if(u32 voff, const void* sbase, u32 lb, u32 cond) {
  asm volatile("s_cmp_gt_u32 %3, 1\n\ts_cbranch_scc1 1f\n\ts_add_u32 m0, %2, %4\n\ts_nop 0\n\tglobal_load_lds_dwordx4 %0, %1\n1:"
               ::"v"(voff), "s"(sbase), "s"(lb), "s"(cond), "i"(IMM) : "memory", "scc");
}
DEV void touch(const void* p) {
  asm volatile("s_mov_b32 m0, %1\n\ts_nop 0\n\tglobal_load_lds_dword %0, off sc1" ::"v"(p), "i"(L_JUNK) : "memory");
}
template <int N> DEV void wait_bar() {
  asm volatile("s_waitcnt vmcnt(%0) lgkmcnt(0)\n\ts_barrier" ::"i"(N) : "memory");
}
template <int N> DEV void wait_vm() { asm volatile("s_waitcnt vmcnt(%0)" ::"i"(N) : "memory"); }
DEV void wait_lds() { asm volatile("s_waitcnt lgkmcnt(0)" ::: "memory"); }
DEV void lds_bar() { asm volatile("s_waitcnt lgkmcnt(0)\n\ts_barrier" ::: "memory"); }
#define SB() __builtin_amdgcn_sched_barrier(0)
#define PIN(x) asm volatile("" : "+v"(x))

DEV u32 f2u(float v) { return __builtin_bit_cast(u32, v); }
DEV float u2f(u32 v) { return __builtin_bit_cast(float, v); }
DEV u32 pk_bf16(float a, float b) {
  f32x2 p = {a, b};
  return __builtin_bit_cast(u32, __builtin_convertvector(p, b16x2));
}
DEV u32x2 pk4(f32x4 v) { return u32x2{pk_bf16(v[0], v[1]), pk_bf16(v[2], v[3])}; }
DEV float bf_lo(u32 v) { return u2f(v << 16); }
DEV float bf_hi(u32 v) { return u2f(v & 0xffff0000u); }
DEV float dot2(u32 a, u32 b, float c) {
  return __builtin_amdgcn_fdot2_f32_bf16(__builtin_bit_cast(b16x2, a), __builtin_bit_cast(b16x2, b), c, false);
}
DEV f32x4 mfma16(u32x4 a, u32x4 b, f32x4 c) {
  return __builtin_amdgcn_mfma_f32_16x16x32_bf16(__builtin_bit_cast(s16x8, a), __builtin_bit_cast(s16x8, b), c, 0, 0, 0);
}
DEV f32x4 mfma16k16(u32x2 a, u32x2 b, f32x4 c) {
  return __builtin_amdgcn_mfma_f32_16x16x16bf16_1k(__builtin_bit_cast(s16x4, a), __builtin_bit_cast(s16x4, b), c, 0, 0, 0);
}
DEV f32x16 mfma32(u32x4 a, u32x4 b, f32x16 c) {
  return __builtin_amdgcn_mfma_f32_32x32x16_bf16(__builtin_bit_cast(s16x8, a), __builtin_bit_cast(s16x8, b), c, 0, 0, 0);
}
DEV u32x4 lds128(LAS char* L, u32 off) { return *(LAS u32x4*)(L + off); }
DEV f32x4 ldsf4(LAS char* L, u32 off) { return *(LAS f32x4*)(L + off); }
DEV float ldsf(LAS char* L, u32 off) { return *(LAS float*)(L + off); }
DEV u32x2 ldstr(LAS char* L, u32 off) {
  return __builtin_bit_cast(u32x2, __builtin_amdgcn_ds_read_tr16_b64_v4i16((LAS s16x4*)(L + off)));
}
template <int CTRL> DEV float dpp_add(float v) {
  int t = __builtin_amdgcn_update_dpp(0, (int)f2u(v), CTRL, 0xf, 0xf, true);
  return v + u2f((u32)t);
}
DEV f32x2 swap16(float a, float b) {
  auto q = __builtin_amdgcn_permlane16_swap(f2u(a), f2u(b), false, false);
  return f32x2{u2f(q[0]), u2f(q[1])};
}
DEV f32x2 swap32(float a, float b) {
  auto q = __builtin_amdgcn_permlane32_swap(f2u(a), f2u(b), false, false);
  return f32x2{u2f(q[0]), u2f(q[1])};
}
DEV float vmax(float a, float b) { return __builtin_amdgcn_fmed3f(a, b, __builtin_inff()); }
DEV float sel(bool c, float a, float b) { return c ? a : b; }
DEV u32x4 pair_rows(u32x2 x, u32x2 y) {
  auto a = __builtin_amdgcn_permlane16_swap(x[0], y[0], false, false);
  auto b = __builtin_amdgcn_permlane16_swap(x[1], y[1], false, false);
  return u32x4{a[0], b[0], a[1], b[1]};
}
DEV f32x4 splat4(float v) { return f32x4{v, v, v, v}; }
DEV int fresh_tid() {
  int t = threadIdx.x;
  asm volatile("" : "+v"(t));
  return t;
}
DEV void st128(u32x4 v, __amdgpu_buffer_rsrc_t rs, u32 voff, u32 soff) {
  __builtin_amdgcn_raw_buffer_store_b128(v, rs, voff, soff, AUX_SC1);
  asm volatile("s_nop 1" ::"v"(v));
}
template <int V> struct IC { static constexpr int v = V; };
template <int LO, int HI, class F> DEV void sfor(F&& f) {
  if constexpr (LO < HI) { f(IC<LO>{}); sfor<LO + 1, HI>(f); }
}

struct Ptrs { const char *x, *wq, *wo, *w1; const u16* b1; const char *w2, *b2; char *out, *ws; };
struct Ring {
  u32 voffA, voffB, voffB2, stepB, mlo, xcond, wb;
  int rot, ldw, maxch;
  const char *sA0, *sA1, *sB0, *sB1, *sB2;
  DEV void lanes(int lane) {
    const int r8 = lane >> 3, pos = lane & 7;
    const int swB = (r8 & 2) | ((wb >> 8) & 4);
    voffA = r8 * LDX + ((pos ^ r8) << 4);
    voffB = r8 * ldw + ((pos ^ swB) << 4);
    voffB2 = r8 * ldw + (min(pos ^ swB, maxch) << 4);
  }
  template <int I, int K> DEV void issue() const {
    const int kc = (I + rot) & 7;
    constexpr int slot = (I % 3) * SLOT;
    if constexpr (K == 0) dma16_masked<slot>(voffA + kc * 128, sA0, wb, mlo);
    if constexpr (K == 1) dma16<slot + BOFF>(voffB + kc * stepB, sB0, wb);
    if constexpr (K == 2) dma16<slot + BOFF + 8192>(voffB + kc * stepB, sB1, wb);
    if constexpr (K == 3) dma16<slot + BOFF + 16384>(voffB2 + kc * stepB, sB2, wb);
    if constexpr (K == 4) dma16<slot + 8192>(voffA + kc * 128, sA1, wb);
    if constexpr (K == 5) dma16_if<slot + 12288>(voffA + kc * 128 + 32 * LDX, sA1, wb, xcond);
  }
};

template <int TYPE>
DEV void body(LAS char* L, const Ptrs P, Ring R, int g, int j, int w STAMP_ARG) {
  constexpr bool FFN = TYPE >= 2;
  constexpr int ODD = TYPE & 1;
  constexpr int NT = TYPE == 0 ? 5 : (TYPE == 1 ? 4 : 2);
  constexpr int NC = TYPE == 2 ? 6 : (TYPE == 3 ? 5 : 3);
  constexpr int CB = TYPE == 3 ? 6 : 0;
  constexpr int ND = TYPE == 1 ? 6 : 5;
  constexpr int NM = NT * NC + (FFN ? 2 : 0);
  constexpr int NRL = FFN ? 2 : 4;
  int tid = fresh_tid();
  int lane = tid & 63;
  R.lanes(lane);
  int lg = lane >> 4, lc = lane & 15;
  GAS u64* ctl = (GAS u64*)(P.ws + g * WS_GRP);
  u32x4 r0, r1, r2;
  u64 epoch;
  const int T0 = TYPE == 0 ? 0 : (TYPE == 1 ? 5 : 2 * (w & 3));
  const u32 aA = T0 * 2048 + (lc >> 3) * 1024 + (lc & 7) * 128 + ((lg ^ (lc & 7)) << 4);
  const int tq = lc >> 2, tp = lc & 3;
  const int swR = (tq & 2) | ((lg & 1) << 2);
  const u32 aBl = BOFF + lg * 1024 + tq * 128 + ((tp >> 1) << 4) + (tp & 1) * 8;
  const int crot = min(2 * (w & 3), NC - 2);
  constexpr int NBB = FFN ? NC : 1;
  u32 aB[NBB][2], aAb[2][2];
#pragma unroll
  for (int c = 0; c < NBB; ++c) {
    const int cc = c + crot >= NC ? c + crot - NC : c + crot;
    const int ct = FFN ? CB + cc : (w & 3);
    aB[c][0] = aBl + (ct >> 2) * 8192 + (((2 * (ct & 3)) ^ swR) << 4);
    aB[c][1] = aB[c][0] + 65536;
    asm("" : "+v"(aB[c][0]), "+v"(aB[c][1]));
  }
#pragma unroll
  for (int b = 0; b < 2; ++b) {
    aAb[b][0] = aA ^ (b * 64);
    aAb[b][1] = aAb[b][0] + 65536;
    asm("" : "+v"(aAb[b][0]), "+v"(aAb[b][1]));
  }
  f32x4 acc[NT][NC], acc1[2];
#pragma unroll
  for (int t = 0; t < NT; ++t)
#pragma unroll
    for (int c = 0; c < NC; ++c) acc[t][c] = f32x4{0.f, 0.f, 0.f, 0.f};
  acc1[0] = acc1[1] = f32x4{0.f, 0.f, 0.f, 0.f};
  u32x4 fa[2][NT];
  u32x2 fb[2][NC][2];
  auto rd = [&](int t, int k) INL {
    const int slot = ((t >> 1) % 3) * SLOT, b = t & 1;
    if (k >= 2 && k < 2 + NT) {
      const int o = slot + (k - 2) * 2048, hi = o >= 65536;
      fa[b][k - 2] = lds128(L, aAb[b][hi] + (o - hi * 65536));
    } else {
      const int kk = k < 2 ? k : k - NT, c = kk >> 1;
      const int o = slot + b * 4096 + (kk & 1) * 512 + (FFN ? 0 : c * 8192), hi = o >= 65536;
      fb[b][c][kk & 1] = ldstr(L, aB[FFN ? c : 0][hi] + (o - hi * 65536));
    }
  };
  const int mt = 2 * g + ((j >> 2) & 1), nt = (j & 3) + (j >> 3) * 4;
  const int ks0 = w < 6 ? 15 * w : 90 + 14 * (w - 6), nks = w < 6 ? 15 : 14;
  const u32 voffW = (lane >> 3) * LDW2 + (((lane & 7) ^ ((lane >> 2) & 4)) << 4) + nt * 128;
  const u64 woa = (u64)P.wo, w2a = (u64)P.w2 - 32 * 16 * LDW2;
  const u32 wx0 = w < 5 ? 2 * SLOT + (16 + w) * 2048 : (11 + w) * 2048;
  const u32 wx1 = w < 5 ? SLOT + (16 + w) * 2048 : (w < 7 ? (14 + w) * 2048 : L_SP + 8 * 2048);
  auto wbase = [&](int s) INL -> u32 { const int k = s % 9; return k == 0 ? 2 * R.wb : (k == 5 ? wx0 : (k == 8 ? wx1 : 4 * R.wb)); };
  auto wdma = [&](auto sc_, auto hc_) INL {
    constexpr int s = decltype(sc_)::v, h = decltype(hc_)::v, k = s % 9;
    constexpr int imm = k == 0 ? L_SP : (k <= 2 ? 2 * SLOT + (k - 1) * 2048 : (k <= 4 ? (k - 3) * 2048 : (k == 6 || k == 7 ? SLOT + (k - 6) * 2048 : 0)));
    const int ks = min(ks0 + s, NKS - 1);
    const u64 base = (ks < 32 ? woa : w2a) + (u32)(ks * 16 * LDW2);
    dma16<imm + h * 1024>(voffW + h * 8 * LDW2, (const void*)base, wbase(s));
  };
  u32x4 wf[15][2];
  const u32 tb = (lane >> 5) * 1024 + tq * 128 + (((2 * (lg & 1) + (tp >> 1)) ^ ((tq & 2) << 1)) << 4) + (tp & 1) * 8;
  const __amdgpu_buffer_rsrc_t rsA2 = __builtin_amdgcn_make_buffer_rsrc(P.ws + WS_A2, 0, A2_BYTES, 0x00020000);
  const __amdgpu_buffer_rsrc_t rsG = __builtin_amdgcn_make_buffer_rsrc(P.ws + g * WS_GRP, 0, WS_GRP, 0x00020000);
  const u32x4 ones = {0x3F803F80u, 0x3F803F80u, 0x3F803F80u, 0x3F803F80u};
  u64 tag = 0;
  u32 b1v = 0;
  u32x4 gst;
  u64 tp2 = 0;
  u32 gsto = 0, gldo = 0, b1o = 0, stw = 0, b1w = 0, csw = 0, csw2 = 0;
  float som = 0.f, sor = 0.f;
  f32x4 mP, mP0, mC;
  u32 sel0 = 0, sel1 = 0, wra[4][2];
  auto wread = [&](int s) INL {
    const int k = s % 9;
#pragma unroll
    for (int f = 0; f < 2; ++f) {
      const u32 a = k == 0 ? wra[0][f] : (k <= 2 ? wra[2][f] + (k - 1) * 2048 : (k <= 4 ? wra[1][f] + (k - 3) * 2048 :
                    (k == 6 || k == 7 ? wra[1][f] + SLOT + (k - 6) * 2048 : wra[1][f] - 4 * R.wb + wbase(s))));
      const u32x2 lo = ldstr(L, a), hi = ldstr(L, a + 512);
      wf[s][f] = u32x4{lo[0], lo[1], hi[0], hi[1]};
    }
  };
  constexpr int NK = 5, TK = TYPE == 0 ? 0 : 4;
  float mc1[NK], rc1[NK];
  f32x4 m4[NK], r4[NK], bb[NC];
  auto rdst = [&](int k) INL {
    if (FFN) {
      if (k < 2) mc1[k] = ldsf(L, L_MEAN + ((2 * (w & 3) + k) * 16 + lc) * 4);
      else if (k < 4) rc1[k - 2] = ldsf(L, L_RSTD + ((2 * (w & 3) + k - 2) * 16 + lc) * 4);
      else if (k < 4 + NC) {
        const int c = k - 4, ct = CB + (c + crot >= NC ? c + crot - NC : c + crot);
        bb[c] = ldsf4(L, L_B1 + (ct * 16 + lg * 4) * 4);
      }
    } else {
      const int i = k >> 2, T = TK + i;
      if (i >= NK) return;
      if ((k & 3) == 0) mc1[i] = ldsf(L, L_MEAN + (T * 16 + lc) * 4);
      if ((k & 3) == 1) rc1[i] = ldsf(L, L_RSTD + (T * 16 + lc) * 4);
      if ((k & 3) == 2) m4[i] = ldsf4(L, L_MEAN + (T * 16 + lg * 4) * 4);
      if ((k & 3) == 3) r4[i] = ldsf4(L, L_RSTD + (T * 16 + lg * 4) * 4);
    }
  };
      {
        const int b_ = blockIdx.x, ln = min(b_ * 59 + w * 8 + (lane & 7), 15103);
        tp2 = (ln < 4096 ? woa : w2a) + (u32)(ln * 128);
        const u64 pb = ((lane & 8) ? (u64)P.b1 + (u32)min(max(j - 8, 0) * 352 + (lane & 3) * 128, 2748) : (u64)P.b2 + (u32)(nt * 128));
        const u64 pf = (u64)ctl + (u32)(128 * (lc + 1));
        tp2 = lg == 1 ? pb : tp2;
        tp2 = (lg == 2 && w == 0) ? pf : tp2;
        gsto = (w == 7 && lane < 8) ? WS_STAT + (j * 8 + lane) * 16 : WS_GRP;
        gldo = WS_STAT + (tid & 127) * 16;
        b1o = 2 * (u32)min(max(j - 8, 0) * 176 + tid, 1375);
        stw = (!ODD && w < 2) ? L_MEAN + (tid + (FFN ? 0 : 16)) * 4 : L_JUNK + lane * 4;
        b1w = tid < 192 ? L_B1 + tid * 4 : L_JUNK + lane * 4;
        csw = lc == 0 ? L_CS + ((FFN ? CB + crot : (w & 3)) * 16 + lg * 4) * 4 : L_JUNK + lane * 16;
        csw2 = lg == 0 ? L_CS + (128 + (w & 3) * 16 + lc) * 4 : L_JUNK + lane * 4;
        if (!FFN) {
#pragma unroll
          for (int r = 0; r < 4; ++r) {
            mP[r] = (4 * lg + r >= lc + 2) ? 0.f : -1e30f;
            mP0[r] = (TYPE == 0 && g == 0) ? -1e30f : mP[r];
            mC[r] = (4 * lg + r <= lc) ? 0.f : -1e30f;
          }
          sel0 = (lc & 1) ? 0x01000c0cu : 0x0c0c0100u; sel1 = (lc & 1) ? 0x03020c0cu : 0x0c0c0302u;
          asm volatile("" : "+v"(mP), "+v"(mC), "+v"(sel0), "+v"(sel1));
          if (TYPE == 0) asm volatile("" : "+v"(mP0));
        }
#pragma unroll
        for (int f = 0; f < 2; ++f) {
          wra[0][f] = (tb ^ (f * 64)) + 2 * R.wb + L_SP;
          wra[1][f] = (tb ^ (f * 64)) + 4 * R.wb;
          wra[2][f] = (tb ^ (f * 64)) + 4 * R.wb + 2 * SLOT;
          asm volatile("" : "+v"(wra[0][f]), "+v"(wra[1][f]), "+v"(wra[2][f]));
        }
        asm volatile("" : "+v"(tp2), "+v"(gsto), "+v"(gldo), "+v"(b1o), "+v"(stw), "+v"(b1w), "+v"(csw), "+v"(csw2));
      }
  SB();
  sfor<0, ND>([&](auto kc) INL { R.template issue<1, decltype(kc)::v>(); });
  if (FFN)
    asm volatile("global_load_dwordx2 %0, %2, %3 sc1\n\tglobal_load_dwordx4 %1, %4, %5"
                 : "=&v"(epoch), "=&v"(r0) : "v"(0u), "s"(ctl), "v"(lane * 16), "s"(P.x + (size_t)(128 * g + 8 * j + w) * LDX) : "memory");
  else
    asm volatile("global_load_dwordx2 %0, %4, %5 sc1\n\tglobal_load_dwordx4 %1, %6, %7\n\t"
                 "global_load_dwordx4 %2, %6, %8\n\tglobal_load_dwordx4 %3, %6, %8 offset:1024"
                 : "=&v"(epoch), "=&v"(r0), "=&v"(r1), "=&v"(r2)
                 : "v"(0u), "s"(ctl), "v"(lane * 16), "s"(P.x + (size_t)(128 * g + 8 * j + w) * LDX),
                   "s"(P.x + (size_t)max(128 * g - 16 + 2 * w, 0) * LDX) : "memory");
  STAMP(1);
  wait_bar<5 + NRL>();
  STAMP(2);
#pragma unroll
  for (int k = 0; k < 2 * NC + NT; ++k) rd(0, k);
  SB();
  sfor<0, 16>([&](auto tc) INL {
    constexpr int t = decltype(tc)::v, s = t >> 1, b = t & 1;
    if constexpr (b) {
      if constexpr (s <= 4) wait_bar<5 + ((s == 1 || s == 2) ? ODD : 0) + ((t == TT + 1 || t == TT + 3) ? 1 : 0)>();
      else if constexpr (s == 5) wait_bar<7>();
      else if constexpr (s == 6) wait_bar<4>();
    }
    if constexpr (t == 11) {
      if constexpr (!ODD) asm volatile("" : "+v"(gst), "+v"(b1v));
      if constexpr (!ODD) {
        if (__builtin_expect(!__all(gst[1] == (u32)tag && gst[3] == (u32)tag), 0)) {
#pragma nounroll
          for (u32 spins = 0; spins < SPIN_CAP; ++spins) {
            __builtin_amdgcn_s_sleep(1);
            gst = __builtin_amdgcn_raw_buffer_load_b128(rsG, gldo, 0, AUX_SC1);
            if (__all(gst[1] == (u32)tag && gst[3] == (u32)tag)) break;
          }
        }
      }
      SB();
    }
    constexpr int nr = t == 15 ? 0 : 2 * NC + NT;
    constexpr int st = t == 0 ? 2 : (t + 5) / 2;
    constexpr int k0 = (t == 0 || b) ? 0 : 3, k1 = st >= 8 ? 0 : ((t == 0 || !b) ? ND : 3);
    constexpr int nd = k1 > k0 ? k1 - k0 : 0, nw = t >= 10 ? 2 : 0, nv = nd + nw;
    sfor<0, NM>([&](auto qc) INL {
      constexpr int q = decltype(qc)::v;
      constexpr int qq = t < 15 ? q : (FFN ? (q + NT * NC) % NM : (TYPE == 1 ? q : (q < 3 ? q * NT : (q < 5 ? (q - 2) * NT + 4 :
                         (q - 5 < 4 ? q - 4 : (q - 5 < 7 ? q - 3 : q - 1))))));
      if constexpr (qq < NT * NC) {
        constexpr int c = qq / NT, i = qq % NT;
        const u32x4 wfr = {fb[b][c][0][0], fb[b][c][0][1], fb[b][c][1][0], fb[b][c][1][1]};
        if constexpr (!FFN && c == 2) acc[i][c] = mfma16(fa[b][i], wfr, acc[i][c]);
        else acc[i][c] = mfma16(wfr, fa[b][i], acc[i][c]);
        PIN(acc[i][c]);
      } else {
        constexpr int c = qq - NT * NC;
        const u32x4 wfr = {fb[b][c][0][0], fb[b][c][0][1], fb[b][c][1][0], fb[b][c][1][1]};
        acc1[c] = mfma16(wfr, ones, acc1[c]);
        PIN(acc1[c]);
      }
      if constexpr (q < nr) rd(t + 1, q);
      if constexpr (t == 15) { rdst(2 * q); rdst(2 * q + 1); }
      if constexpr (t == 9 && q == 0 && !ODD)
        asm volatile("buffer_load_dwordx4 %0, %2, %3, 0 offen sc1\n\tglobal_load_ushort %1, %4, %5"
                     : "=&v"(gst), "=&v"(b1v) : "v"(gldo), "s"(rsG), "v"(b1o), "s"(P.b1) : "memory");
      if constexpr (t == 11 && !ODD) {
        if constexpr (q == NM - 4) *(LAS u32*)(L + stw) = gst[0];
        if constexpr (q == NM - 3) *(LAS u32*)(L + stw + 576) = gst[2];
        if constexpr (q == NM - 2 && FFN) *(LAS float*)(L + b1w) = bf_lo(b1v);
      }
      if constexpr (t == 1 && ODD && q == NM - 1) { som = ldsf(L, L_OWN + (lane & 7) * 4); sor = ldsf(L, L_OWN + 576 + (lane & 7) * 4); }
      sfor<0, nv>([&](auto uc) INL {
        constexpr int u = decltype(uc)::v;
        if constexpr (((2 * u + ODD) * NM) / (2 * nv) == q) {
          if constexpr (u < nd) R.template issue<st, k0 + u>();
          else wdma(IC<t - 10>{}, IC<u - nd>{});
        }
      });
      if constexpr (t == TT && q == NM - 1) touch((const void*)tp2);
      if constexpr (t == 2 && q == NM - 1) {
        if (ODD) st128(u32x4{f2u(som), (u32)tag, f2u(sor), (u32)tag}, rsG, gsto, 0);
      }
      if constexpr (t == 15) {
        if constexpr (TYPE == 0) {
          if constexpr (q == 6) *(LAS f32x4*)(L + csw) = acc[0][0];
          if constexpr (q == 7) *(LAS f32x4*)(L + csw + 256) = acc[0][1];
          if constexpr (q == 8) *(LAS float*)(L + csw2) = acc[0][2][0];
          if constexpr (q == 9) *(LAS f32x4*)(L + L_KV + 2 * R.wb + lane * 16) = acc[NT - 1][1];
          if constexpr (q == 10) *(LAS f32x4*)(L + L_KV + 2 * R.wb + 1024 + lane * 16) = acc[NT - 1][2];
        }
        if constexpr (FFN) {
          if constexpr (q == 6) *(LAS f32x4*)(L + csw) = acc1[0];
          if constexpr (q == 7) *(LAS f32x4*)(L + csw + 64) = acc1[1];
        }
      }
      SB();
    });
    if constexpr (t == 0) {
      if (FFN) asm volatile("s_waitcnt vmcnt(%2)" : "+v"(epoch), "+v"(r0) : "i"(5) : "memory");
      else asm volatile("s_waitcnt vmcnt(%4)" : "+v"(epoch), "+v"(r0), "+v"(r1), "+v"(r2) : "i"(5) : "memory");
      tag = (((u64)(u32)__builtin_amdgcn_readfirstlane((u32)(epoch >> 32)) << 32) | (u32)__builtin_amdgcn_readfirstlane((u32)epoch)) + 1;
      float sS[3] = {0.f, 0.f, 0.f}, sQ[3] = {0.f, 0.f, 0.f};
#pragma unroll
      for (int d = 0; d < 4; ++d) {
        sS[0] = dot2(r0[d], 0x3F803F80u, sS[0]); sQ[0] = dot2(r0[d], r0[d], sQ[0]);
        if (!FFN) {
          sS[1] = dot2(r1[d], 0x3F803F80u, sS[1]); sQ[1] = dot2(r1[d], r1[d], sQ[1]);
          sS[2] = dot2(r2[d], 0x3F803F80u, sS[2]); sQ[2] = dot2(r2[d], r2[d], sQ[2]);
        }
      }
      f32x2 a = swap32(sS[0], sS[FFN ? 0 : 1]), c = swap32(sQ[0], sQ[FFN ? 0 : 1]);
      float S = a[0] + a[1], Q = c[0] + c[1], S2 = S, Q2 = Q;
      if (!FFN) {
        a = swap32(sS[2], sS[2]); c = swap32(sQ[2], sQ[2]);
        S2 = a[0] + a[1]; Q2 = c[0] + c[1];
      }
      a = swap16(S, S2); c = swap16(Q, Q2);
      S = a[0] + a[1]; Q = c[0] + c[1];
      S = dpp_add<0xB1>(S); Q = dpp_add<0xB1>(Q);
      S = dpp_add<0x4E>(S); Q = dpp_add<0x4E>(Q);
      S = dpp_add<0x141>(S); Q = dpp_add<0x141>(Q);
      S = dpp_add<0x140>(S); Q = dpp_add<0x140>(Q);
      const float m = S * (1.f / 512.f), e = Q * (1.f / 512.f);
      u32 adr = L_JUNK + lane * 4;
      adr = lane == 0 ? L_OWN + w * 4 : adr;
      if (!FFN) { adr = lane == 32 ? L_MEAN + w * 8 : adr; adr = lane == 16 ? L_MEAN + w * 8 + 4 : adr; }
      *(LAS float*)(L + adr) = -m;
      *(LAS float*)(L + adr + 576) = __builtin_amdgcn_rsqf(fmaxf(e - m * m, 0.f) + 1e-5f);
      SB();
    }
  });
  STAMP(3);
  lds_bar();
  STAMP(4);
  auto slot = [&](auto kc) INL {
    constexpr int k = decltype(kc)::v;
    SB();
    if constexpr (k < NEPI) wdma(IC<6 + k / 2>{}, IC<k % 2>{});
    SB();
  };
  if constexpr (!FFN) {
    const int h = w & 3;
    const f32x4 csq = ldsf4(L, L_CS + (h * 16 + lg * 4) * 4), csk = ldsf4(L, L_CS + (64 + h * 16 + lg * 4) * 4);
    const float csv1 = ldsf(L, L_CS + (128 + h * 16 + lc) * 4);
    f32x4 k4 = acc[0][1], v4 = acc[0][2];
    if (TYPE == 1) { k4 = ldsf4(L, L_KV + h * 2048 + lane * 16); v4 = ldsf4(L, L_KV + h * 2048 + 1024 + lane * 16); }
    slot(IC<0>{});
    wait_vm<7>();
    wread(0); wread(1); wread(2);
    const bool odd = lc & 1;
    const f32x4 csv = splat4(csv1);
    u32x2 Qb[4], Kb[NK], Vb[NK];
    sfor<0, NK>([&](auto ic) INL {
      constexpr int i = decltype(ic)::v, ai = TYPE == 0 ? i : (i > 0 ? i - 1 : 0);
      slot(IC<1 + i>{});
      const f32x4 mc = splat4(mc1[i]);
      Kb[i] = pk4(((i == 0 ? k4 : acc[ai][1]) + mc * csk) * splat4(rc1[i]));
      if constexpr (i > 0) Qb[i - 1] = pk4((acc[ai][0] + mc * csq) * splat4(rc1[i] * (0.25f * 1.4426950408889634f)));
      Vb[i] = pk4(((i == 0 ? v4 : acc[ai][2]) + m4[i] * csv) * r4[i]);
    });
    f32x4 sp[4], sc[4];
#pragma unroll
    for (int q = 0; q < 4; ++q) {
      sp[q] = mfma16k16(Kb[q], Qb[q], q == 0 ? mP0 : mP);
      sc[q] = mfma16k16(Kb[q + 1], Qb[q], mC);
    }
    SB();
    wait_lds();
    wait_vm<6>();
    wread(3); wread(4); wread(5);
    slot(IC<6>{});
    f32x4 a[4];
    float mx[4];
#pragma unroll
    for (int q = 0; q < 4; ++q) {
      a[q] = f32x4{sel(odd, sp[q][1], sp[q][0]), sel(odd, sp[q][3], sp[q][2]), sel(odd, sc[q][1], sc[q][0]), sel(odd, sc[q][3], sc[q][2])};
      mx[q] = vmax(vmax(a[q][0], a[q][1]), vmax(a[q][2], a[q][3]));
    }
    slot(IC<7>{});
    {
      const f32x2 x = swap32(mx[0], mx[1]), y = swap32(mx[2], mx[3]);
      const f32x2 z = swap16(vmax(x[0], x[1]), vmax(y[0], y[1]));
      const float u = vmax(z[0], z[1]);
      const f32x2 p = swap16(u, u);
      const f32x2 p0 = swap32(p[0], p[0]), p1 = swap32(p[1], p[1]);
      mx[0] = p0[0]; mx[1] = p0[1]; mx[2] = p1[0]; mx[3] = p1[1];
    }
    slot(IC<8>{});
#pragma unroll
    for (int q = 0; q < 4; ++q) a[q] -= splat4(mx[q]);
    slot(IC<9>{});
    sfor<0, 4>([&](auto qc) INL {
      constexpr int q = decltype(qc)::v;
#pragma unroll
      for (int r = 0; r < 4; ++r) a[q][r] = __builtin_amdgcn_exp2f(a[q][r]);
      if constexpr (q & 1) slot(IC<10 + q / 2>{});
    });
    wait_lds();
    const f32x4 z4 = {0.f, 0.f, 0.f, 0.f};
    u32x4 pb[4];
    sfor<0, 4>([&](auto qc) INL {
      constexpr int q = decltype(qc)::v;
      const u32x2 u = pk4(a[q]);
      pb[q] = u32x4{__builtin_amdgcn_perm(0, u[0], sel0), __builtin_amdgcn_perm(0, u[0], sel1),
                    __builtin_amdgcn_perm(0, u[1], sel0), __builtin_amdgcn_perm(0, u[1], sel1)};
      if constexpr (q & 1) slot(IC<12 + q / 2>{});
    });
#pragma unroll
    for (int q = 0; q < 4; ++q) {
      sc[q] = mfma16(ones, pb[q], z4);
      sp[q] = mfma16k16(Vb[q], u32x2{pb[q][0], pb[q][1]}, z4);
    }
#pragma unroll
    for (int q = 0; q < 4; ++q) sp[q] = mfma16k16(Vb[q + 1], u32x2{pb[q][2], pb[q][3]}, sp[q]);
    slot(IC<14>{}); slot(IC<15>{}); slot(IC<16>{}); slot(IC<17>{});
    u32x2 Ob[4];
#pragma unroll
    for (int q = 0; q < 4; ++q) Ob[q] = pk4(sp[q] * splat4(__builtin_amdgcn_rcpf(sc[q][0])));
    const int rt = 4 * g + (TYPE == 0 ? 0 : 2), ks = 4 * j + h;
    st128(pair_rows(Ob[0], Ob[1]), rsA2, lane * 16, (rt * NKS + ks) * 1024);
    st128(pair_rows(Ob[2], Ob[3]), rsA2, lane * 16, ((rt + 1) * NKS + ks) * 1024);
  } else {
    f32x4 cs[NC];
#pragma unroll
    for (int c = 0; c < NC; ++c) {
      const int ct = CB + (c + crot >= NC ? c + crot - NC : c + crot);
      cs[c] = ldsf4(L, L_CS + (ct * 16 + lg * 4) * 4);
    }
    wait_vm<6>();
    wread(0); wread(1); wread(2);
    const f32x4 mc[2] = {splat4(mc1[0]), splat4(mc1[1])}, rc[2] = {splat4(rc1[0]), splat4(rc1[1])};
    const int nct = j == 15 ? 9 : 11;
    const int rt = 4 * g + (w & 3), ksb = 32 + 11 * (j - 8);
    const f32x4 C1 = splat4(-2.f * 1.4426950408889634f * 0.7978845608028654f);
    const f32x4 C2 = splat4(-2.f * 1.4426950408889634f * 0.7978845608028654f * 0.044715f);
    const f32x4 ONE = splat4(1.f);
    sfor<0, 3>([&](auto gc) INL {
      constexpr int G = decltype(gc)::v, c0 = 2 * G, n = c0 + 1 < NC ? 4 : 2;
      if constexpr (G == 1) { wait_lds(); wait_vm<8>(); wread(3); wread(4); wread(5); }
      if constexpr (G == 2) wait_lds();
      f32x4 v[4], e[4];
      slot(IC<6 * G>{});
#pragma unroll
      for (int u = 0; u < n; ++u) v[u] = (acc[u & 1][c0 + (u >> 1)] + mc[u & 1] * cs[c0 + (u >> 1)]) * rc[u & 1] + bb[c0 + (u >> 1)];
      slot(IC<6 * G + 1>{});
#pragma unroll
      for (int u = 0; u < n; ++u) e[u] = v[u] * (v[u] * v[u] * C2 + C1);
      slot(IC<6 * G + 2>{});
#pragma unroll
      for (int u = 0; u < n; ++u) {
#pragma unroll
        for (int r = 0; r < 4; ++r) e[u][r] = __builtin_amdgcn_exp2f(e[u][r]);
        if (u == 1) slot(IC<6 * G + 3>{});
      }
#pragma unroll
      for (int u = 0; u < n; ++u) e[u] += ONE;
      slot(IC<6 * G + 4>{});
#pragma unroll
      for (int u = 0; u < n; ++u) {
#pragma unroll
        for (int r = 0; r < 4; ++r) e[u][r] = __builtin_amdgcn_rcpf(e[u][r]);
        if (u == 1) slot(IC<6 * G + 5>{});
      }
#pragma unroll
      for (int cc = 0; cc < n / 2; ++cc) {
        const int c = c0 + cc, ct = CB + (c + crot >= NC ? c + crot - NC : c + crot);
        st128(pair_rows(pk4(v[2 * cc] * e[2 * cc]), pk4(v[2 * cc + 1] * e[2 * cc + 1])), rsA2, lane * 16,
              ct < nct ? (rt * NKS + ksb + ct) * 1024 : A2_BYTES);
      }
      SB();
    });
  }
  STAMP(5);
  wait_vm<0>();
  STAMP(13);
  asm volatile("s_barrier" ::: "memory");
  if (tid == 0) __hip_atomic_store(ctl + 16 * (j + 1), tag, __ATOMIC_RELAXED, __HIP_MEMORY_SCOPE_AGENT);
  STAMP(6);
  sfor<NEPI, 18>([&](auto kc) INL { wdma(IC<6 + decltype(kc)::v / 2>{}, IC<decltype(kc)::v % 2>{}); });
#pragma unroll
  for (int s = 6; s < 9; ++s) wread(s);
  tid = fresh_tid(); lane = tid & 63; lg = lane >> 4; lc = lane & 15;
  const u32 ooff = (u32)((mt * 64 + 4 * w + ((lane >> 3) & 3)) * LDX + (nt * 64 + (lane & 7) * 8) * 2);
  f32x4 xb0, xb1;
#pragma unroll
  for (int s = 0; s < 9; ++s) asm volatile("" : "+v"(wf[s][0]), "+v"(wf[s][1]));
  STAMP(7);
  u64 v0, v1, v2, v3;
  wait_vm<0>();
  {
    const u32 fo = 128 * (lc + 1);
    u32 spins = SPIN_CAP;
    asm volatile(
        "s_cmp_lg_u32 %9, %10\n\ts_cbranch_scc1 2f\n\t"
        "global_load_dwordx2 %0, %5, %6 sc1\n\ts_sleep %8\n\tglobal_load_dwordx2 %1, %5, %6 sc1\n\ts_sleep %8\n\t"
        "global_load_dwordx2 %2, %5, %6 sc1\n\ts_sleep %8\n"
        "1:\n\t"
        "global_load_dwordx2 %3, %5, %6 sc1\n\ts_waitcnt vmcnt(3)\n\tv_cmp_ne_u64 vcc, %7, %0\n\ts_cbranch_vccz 2f\n\ts_sleep %8\n\t"
        "global_load_dwordx2 %0, %5, %6 sc1\n\ts_waitcnt vmcnt(3)\n\tv_cmp_ne_u64 vcc, %7, %1\n\ts_cbranch_vccz 2f\n\ts_sleep %8\n\t"
        "global_load_dwordx2 %1, %5, %6 sc1\n\ts_waitcnt vmcnt(3)\n\tv_cmp_ne_u64 vcc, %7, %2\n\ts_cbranch_vccz 2f\n\ts_sleep %8\n\t"
        "global_load_dwordx2 %2, %5, %6 sc1\n\ts_waitcnt vmcnt(3)\n\tv_cmp_ne_u64 vcc, %7, %3\n\ts_cbranch_vccz 2f\n\ts_sleep %8\n\t"
        "s_sub_u32 %4, %4, 1\n\ts_cmp_lg_u32 %4, 0\n\ts_cbranch_scc1 1b\n"
        "2:\n\ts_barrier"
        : "=&v"(v0), "=&v"(v1), "=&v"(v2), "=&v"(v3), "+s"(spins)
        : "v"(fo), "s"(ctl), "s"(tag), "i"(PGAP), "s"(w), "i"(7) : "memory", "vcc", "scc");
  }
  STAMP(8);
  u32x4 af[2][15];
  auto lda = [&](int r, int s) INL {
    const int so = (s < nks) ? ((2 * mt + r) * NKS + ks0 + s) * 1024 : A2_BYTES;
    af[r][s] = __builtin_amdgcn_raw_buffer_load_b128(rsA2, lane * 16, so, AUX_SC1);
  };
#pragma unroll
  for (int n = 0; n < WIN; ++n) lda(n / 15, n % 15);
  SB();
  u32 pw[4];
#pragma unroll
  for (int y = 0; y < 4; ++y) pw[y] = ((lane + 4 * y) & 63) * 16 + y * 1024 + 4 * R.wb;
  const int blk = lane & 7;
  const u32 prd = (((lane & 32) + 4 * w + ((lane >> 3) & 3) + 4 * (blk & 3)) & 63) * 16 + (blk & 3) * 1024;
  const u32 prd0 = prd + (blk >> 2) * SLOT, prd1 = prd + (blk >> 2) * 4096 + 2 * SLOT;
  const __amdgpu_buffer_rsrc_t rsO = __builtin_amdgcn_make_buffer_rsrc(P.out, 0, 2048 * LDX, 0x00020000);
  auto reduce = [&](int r, f32x4 xb) INL {
    f32x4 p[8];
#pragma unroll
    for (int sw = 0; sw < 8; ++sw) p[sw] = ldsf4(L, r == 0 ? prd0 + sw * 4096 : prd1 + sw * 8192);
    SB();
    const u32x2 o = pk4(((p[0] + p[1]) + (p[2] + p[3])) + ((p[4] + p[5]) + (p[6] + p[7])) + xb);
    auto e0 = __builtin_amdgcn_permlane32_swap(o[0], o[0], false, false), e1 = __builtin_amdgcn_permlane32_swap(o[1], o[1], false, false);
    __builtin_amdgcn_raw_buffer_store_b128(u32x4{e0[0], e1[0], e0[1], e1[1]}, rsO, lane < 32 ? ooff + r * 32 * LDX : 2048 * LDX, 0, AUX_SC0 | AUX_SC1);
  };
  f32x16 pa[2][2];
  auto pwrite = [&](int r, int b8) INL {
    const f32x16& a = pa[r][b8 >> 2];
    const int y = b8 & 3;
    *(LAS f32x4*)(L + pw[y] + (r == 0 ? (b8 >> 2) * SLOT : 2 * SLOT + 4 * R.wb + (b8 >> 2) * 4096)) =
        f32x4{a[4 * y], a[4 * y + 1], a[4 * y + 2], a[4 * y + 3]};
  };
  u32x2 xr0, xr1, bres;
  sfor<0, 2>([&](auto rc_) INL {
    constexpr int r = decltype(rc_)::v;
#pragma unroll
    for (int i = 0; i < 16; ++i) pa[r][0][i] = pa[r][1][i] = 0.f;
    sfor<0, 15>([&](auto sc_) INL {
      constexpr int s = decltype(sc_)::v;
      pa[r][0] = mfma32(wf[s][0], af[r][s], pa[r][0]);
      PIN(pa[r][0]);
      if constexpr (r == 1 && s >= 1 && s < 5) pwrite(0, 2 * s - 2);
      pa[r][1] = mfma32(wf[s][1], af[r][s], pa[r][1]);
      PIN(pa[r][1]);
      if constexpr (r == 1 && s >= 1 && s < 5) pwrite(0, 2 * s - 1);
      if constexpr (r == 0 && s == 2) asm volatile("" ::"v"(v0), "v"(v1), "v"(v2), "v"(v3));
      if constexpr (r == 0 && s == 0) STAMP(14);
      if constexpr (r == 0 && s == 14) STAMP(15);
      if constexpr (r == 0 && s == 1) {
        const u32 o4 = ooff + (lane >> 5) * 8;
        xr0 = *(const GAS u32x2*)(P.x + o4); xr1 = *(const GAS u32x2*)(P.x + (o4 + 32 * LDX));
        bres = *(const GAS u32x2*)(P.b2 + (o4 & 1023));
      }
      if constexpr (r * 15 + s + WIN < 30) lda((r * 15 + s + WIN) / 15, (r * 15 + s + WIN) % 15);
      if constexpr (r == 0 && s >= 7 && s < 13) wread(s + 2);
      if constexpr (r == 0 && s == 14) {
        const f32x4 bv = {bf_lo(bres[0]), bf_hi(bres[0]), bf_lo(bres[1]), bf_hi(bres[1])};
        xb0 = f32x4{bf_lo(xr0[0]), bf_hi(xr0[0]), bf_lo(xr0[1]), bf_hi(xr0[1])} + bv;
        xb1 = f32x4{bf_lo(xr1[0]), bf_hi(xr1[0]), bf_lo(xr1[1]), bf_hi(xr1[1])} + bv;
        PIN(xb0); PIN(xb1);
      }
      if constexpr (r == 1 && s == RMID) { lds_bar(); reduce(0, xb0); }
      SB();
    });
  });
#pragma unroll
  for (int y = 0; y < 8; ++y) pwrite(1, y);
  STAMP(9);
  lds_bar();
  reduce(1, xb1);
  if (j == 0 && tid == 0) __hip_atomic_store(ctl, tag, __ATOMIC_RELAXED, __HIP_MEMORY_SCOPE_AGENT);
  STAMP(10);
  STAMP_OUT();
  asm volatile(".Lfk_end%c0:" ::"i"(TYPE));
  __builtin_amdgcn_endpgm();
}

__global__ __launch_bounds__(512, 2) void fused(const char*, const char*, const char*, const char*, const u16*, const char*, const char*,
                                                char*, char*) {
  extern __shared__ __attribute__((aligned(16))) char smem[];
  LAS char* L = (LAS char*)smem;
  STAMP(0);
  typedef u64 u64x8 __attribute__((ext_vector_type(8)));
  u64x8 ka;
  u64 kb;
  asm volatile("s_load_dwordx16 %0, %2, 0x0\n\ts_load_dwordx2 %1, %2, 0x40"
               : "=&s"(ka), "=&s"(kb) : "s"(__builtin_amdgcn_kernarg_segment_ptr()) : "memory");
  const int lane = threadIdx.x & 63, w = __builtin_amdgcn_readfirstlane(threadIdx.x >> 6);
  const int b = blockIdx.x, bi = b >> 3;
  const int g = (b & 3) * 4 + (bi & 3), j = ((bi >> 2) & 3) + ((b >> 2) & 1) * 4 + (bi >> 4) * 8;
  const bool ffn = j >= 8;
  if (w == 7) {
    u64 pc;
    u32 len;
    asm volatile("s_getpc_b64 %0\n.Lfk_here:\n\ts_mov_b32 %1, .Lfk_end0-.Lfk_here\n\ts_max_u32 %1, %1, .Lfk_end1-.Lfk_here\n\t"
                 "s_max_u32 %1, %1, .Lfk_end2-.Lfk_here\n\ts_max_u32 %1, %1, .Lfk_end3-.Lfk_here" : "=s"(pc), "=s"(len)::"scc");
    touch((const char*)pc + min((u32)(lane * 128 + (bi & 7) * 8192), len));
  }
  const int rowbase = ffn ? 128 * g : 128 * g - 16;
  Ring R;
  R.ldw = ffn ? LDW1 : LDQKV;
  R.maxch = ffn ? (j == 15 ? 1 : 5) : 7;
  R.wb = w * 1024;
  R.rot = (j + 2 * (g >> 2) + ((g >> 1) & 1)) & 7;
  R.lanes(lane);
  R.stepB = 64 * R.ldw;
  R.mlo = (!ffn && w == 0) ? 0xFFFFFF00u : 0xFFFFFFFFu;
  R.xcond = ffn ? 2u : (u32)(w - 4);
  const u32 oA0 = (rowbase + 8 * w < 0 ? 8 * w : rowbase + 8 * w) * LDX, oA1 = (rowbase + 8 * (w + 8)) * LDX;
  const u32 oB = 8 * w * R.ldw + (ffn ? (j - 8) * 352 : j * 128), seg = ffn ? 128 : 1024;
  asm volatile("s_waitcnt lgkmcnt(0)" : "+s"(ka), "+s"(kb)::"memory");
  const Ptrs P = {(const char*)(GAS const char*)ka[0], (const char*)(GAS const char*)ka[1], (const char*)(GAS const char*)ka[2],
                  (const char*)(GAS const char*)ka[3], (const u16*)(GAS const u16*)ka[4], (const char*)(GAS const char*)ka[5],
                  (const char*)(GAS const char*)ka[6], (char*)(GAS char*)ka[7], (char*)(GAS char*)kb};
  STAMP(11);
  R.sA0 = P.x + oA0;
  R.sA1 = P.x + oA1;
  R.sB0 = (const char*)((ffn ? ka[3] : ka[1]) + oB);
  R.sB1 = R.sB0 + seg;
  R.sB2 = R.sB1 + seg;
  sfor<0, 6>([&](auto kc) INL { R.template issue<0, decltype(kc)::v>(); });
  if (!ffn && w == 0 && lane < 8) {
    const u32x4 one = {0x3F803F80u, 0x3F803F80u, 0x3F803F80u, 0x3F803F80u};
#pragma unroll
    for (int s = 0; s < 3; ++s) *(LAS u32x4*)(L + s * SLOT + lane * 16) = one;
  }
  STAMP(12);
  if (!ffn) {
    if (w < 4) body<0>(L, P, R, g, j, w STAMP_PASS);
    else body<1>(L, P, R, g, j, w STAMP_PASS);
  } else {
    if (w < 4) body<2>(L, P, R, g, j, w STAMP_PASS);
    else body<3>(L, P, R, g, j, w STAMP_PASS);
  }
}
}

extern "C" void kernel_launch(void* const* d_in, const int* in_sizes, int n_in, void* d_out, int out_size, void* d_ws,
                              size_t ws_size, hipStream_t stream) {
  (void)hipFuncSetAttribute((const void*)fk::fused, hipFuncAttributeMaxDynamicSharedMemorySize, fk::L_TOTAL);
  fk::fused<<<256, 512, fk::L_TOTAL, stream>>>((const char*)d_in[0], (const char*)d_in[1], (const char*)d_in[2], (const char*)d_in[4],
                                               (const u16*)d_in[5], (const char*)d_in[6], (const char*)d_in[7], (char*)d_out,
                                               (char*)d_ws);
}
```

```cpp
#include <hip/hip_runtime.h>
#include <stdint.h>

typedef unsigned u32;
typedef unsigned long long u64;
typedef unsigned short u16;
typedef short s16x4 __attribute__((ext_vector_type(4)));
typedef short s16x8 __attribute__((ext_vector_type(8)));
typedef __bf16 b16x2 __attribute__((ext_vector_type(2)));
typedef float f32x2 __attribute__((ext_vector_type(2)));
typedef float f32x4 __attribute__((ext_vector_type(4)));
typedef float f32x16 __attribute__((ext_vector_type(16)));
typedef u32 u32x2 __attribute__((ext_vector_type(2)));
typedef u32 u32x4 __attribute__((ext_vector_type(4)));
#define LAS __attribute__((address_space(3)))
#define GAS __attribute__((address_space(1)))
#define DEV __device__ __forceinline__
#define INL __attribute__((always_inline))

namespace fk {
constexpr int LDX = 1024, LDQKV = 3072, LDW1 = 2752, LDW2 = 1024;
constexpr int NKS = 118;
constexpr int SLOT = 43008, BOFF = 18432;
constexpr int L_MEAN = 3 * SLOT, L_RSTD = L_MEAN + 576, L_CS = L_RSTD + 576, L_B1 = L_CS + 768, L_OWN = L_B1 + 768,
              L_KV = L_MEAN + 3584, L_JUNK = L_KV + 8192, L_SP = L_JUNK + 2048, L_TOTAL = L_SP + 9 * 2048;
static_assert(L_OWN + 576 + 32 <= L_KV && L_TOTAL <= 163840, "LDS map");
constexpr int WS_GRP = 8192, WS_STAT = 4096;
constexpr int WS_A2 = 16 * WS_GRP, A2_BYTES = 64 * NKS * 1024;
constexpr int AUX_SC0 = 1, AUX_SC1 = 16;
constexpr u32 SPIN_CAP = 1u << 16;

constexpr int NEPI = 12;
constexpr int TT = 4;
constexpr int WIN = 5;
constexpr int RMID = 11;
constexpr int PGAP = 2;

template <int IMM> DEV void dma16(u32 voff, const void* sbase, u32 lb) {
  asm volatile("s_add_u32 m0, %2, %3\n\ts_nop 0\n\tglobal_load_lds_dwordx4 %0, %1" ::"v"(voff), "s"(sbase), "s"(lb), "i"(IMM) : "memory", "scc");
}
template <int IMM> DEV void dma16_masked(u32 voff, const void* sbase, u32 lb, u32 mlo) {
  asm volatile("s_add_u32 m0, %2, %4\n\ts_mov_b32 exec_lo, %3\n\tglobal_load_lds_dwordx4 %0, %1\n\ts_mov_b32 exec_lo, -1"
               ::"v"(voff), "s"(sbase), "s"(lb), "s"(mlo), "i"(IMM) : "memory", "scc");
}
template <int IMM> DEV void dma16_if(u32 voff, const void* sbase, u32 lb, u32 cond) {
  asm volatile("s_cmp_gt_u32 %3, 1\n\ts_cbranch_scc1 1f\n\ts_add_u32 m0, %2, %4\n\ts_nop 0\n\tglobal_load_lds_dwordx4 %0, %1\n1:"
               ::"v"(voff), "s"(sbase), "s"(lb), "s"(cond), "i"(IMM) : "memory", "scc");
}
DEV void touch(const void* p) {
  asm volatile("s_mov_b32 m0, %1\n\ts_nop 0\n\tglobal_load_lds_dword %0, off sc1" ::"v"(p), "i"(L_JUNK) : "memory");
}
template <int N> DEV void wait_bar() {
  asm volatile("s_waitcnt vmcnt(%0) lgkmcnt(0)\n\ts_barrier" ::"i"(N) : "memory");
}
template <int N> DEV void wait_vm() { asm volatile("s_waitcnt vmcnt(%0)" ::"i"(N) : "memory"); }
DEV void wait_lds() { asm volatile("s_waitcnt lgkmcnt(0)" ::: "memory"); }
DEV void lds_bar() { asm volatile("s_waitcnt lgkmcnt(0)\n\ts_barrier" ::: "memory"); }
#define SB() __builtin_amdgcn_sched_barrier(0)
#define PIN(x) asm volatile("" : "+v"(x))

DEV u32 f2u(float v) { return __builtin_bit_cast(u32, v); }
DEV float u2f(u32 v) { return __builtin_bit_cast(float, v); }
DEV u32 pk_bf16(float a, float b) {
  f32x2 p = {a, b};
  return __builtin_bit_cast(u32, __builtin_convertvector(p, b16x2));
}
DEV u32x2 pk4(f32x4 v) { return u32x2{pk_bf16(v[0], v[1]), pk_bf16(v[2], v[3])}; }
DEV float bf_lo(u32 v) { return u2f(v << 16); }
DEV float bf_hi(u32 v) { return u2f(v & 0xffff0000u); }
DEV float dot2(u32 a, u32 b, float c) {
  return __builtin_amdgcn_fdot2_f32_bf16(__builtin_bit_cast(b16x2, a), __builtin_bit_cast(b16x2, b), c, false);
}
DEV f32x4 mfma16(u32x4 a, u32x4 b, f32x4 c) {
  return __builtin_amdgcn_mfma_f32_16x16x32_bf16(__builtin_bit_cast(s16x8, a), __builtin_bit_cast(s16x8, b), c, 0, 0, 0);
}
DEV f32x4 mfma16k16(u32x2 a, u32x2 b, f32x4 c) {
  return __builtin_amdgcn_mfma_f32_16x16x16bf16_1k(__builtin_bit_cast(s16x4, a), __builtin_bit_cast(s16x4, b), c, 0, 0, 0);
}
DEV f32x16 mfma32(u32x4 a, u32x4 b, f32x16 c) {
  return __builtin_amdgcn_mfma_f32_32x32x16_bf16(__builtin_bit_cast(s16x8, a), __builtin_bit_cast(s16x8, b), c, 0, 0, 0);
}
DEV u32x4 lds128(LAS char* L, u32 off) { return *(LAS u32x4*)(L + off); }
DEV f32x4 ldsf4(LAS char* L, u32 off) { return *(LAS f32x4*)(L + off); }
DEV float ldsf(LAS char* L, u32 off) { return *(LAS float*)(L + off); }
DEV u32x2 ldstr(LAS char* L, u32 off) {
  return __builtin_bit_cast(u32x2, __builtin_amdgcn_ds_read_tr16_b64_v4i16((LAS s16x4*)(L + off)));
}
template <int CTRL> DEV float dpp_add(float v) {
  int t = __builtin_amdgcn_update_dpp(0, (int)f2u(v), CTRL, 0xf, 0xf, true);
  return v + u2f((u32)t);
}
DEV f32x2 swap16(float a, float b) {
  auto q = __builtin_amdgcn_permlane16_swap(f2u(a), f2u(b), false, false);
  return f32x2{u2f(q[0]), u2f(q[1])};
}
DEV f32x2 swap32(float a, float b) {
  auto q = __builtin_amdgcn_permlane32_swap(f2u(a), f2u(b), false, false);
  return f32x2{u2f(q[0]), u2f(q[1])};
}
DEV float vmax(float a, float b) { return __builtin_amdgcn_fmed3f(a, b, __builtin_inff()); }
DEV float sel(bool c, float a, float b) { return c ? a : b; }
DEV u32x4 pair_rows(u32x2 x, u32x2 y) {
  auto a = __builtin_amdgcn_permlane16_swap(x[0], y[0], false, false);
  auto b = __builtin_amdgcn_permlane16_swap(x[1], y[1], false, false);
  return u32x4{a[0], b[0], a[1], b[1]};
}
DEV f32x4 splat4(float v) { return f32x4{v, v, v, v}; }
DEV int fresh_tid() {
  int t = threadIdx.x;
  asm volatile("" : "+v"(t));
  return t;
}
DEV void st128(u32x4 v, __amdgpu_buffer_rsrc_t rs, u32 voff, u32 soff) {
  __builtin_amdgcn_raw_buffer_store_b128(v, rs, voff, soff, AUX_SC1);
  asm volatile("s_nop 1" ::"v"(v));
}
template <int V> struct IC { static constexpr int v = V; };
template <int LO, int HI, class F> DEV void sfor(F&& f) {
  if constexpr (LO < HI) { f(IC<LO>{}); sfor<LO + 1, HI>(f); }
}

struct Ptrs { const char *x, *wq, *wo, *w1; const u16* b1; const char *w2, *b2; char *out, *ws; };
struct Ring {
  u32 voffA, voffB, voffB2, stepB, mlo, xcond, wb;
  int rot, ldw, maxch;
  const char *sA0, *sA1, *sB0, *sB1, *sB2;
  DEV void lanes(int lane) {
    const int r8 = lane >> 3, pos = lane & 7;
    const int swB = (r8 & 2) | ((wb >> 8) & 4);
    voffA = r8 * LDX + ((pos ^ r8) << 4);
    voffB = r8 * ldw + ((pos ^ swB) << 4);
    voffB2 = r8 * ldw + (min(pos ^ swB, maxch) << 4);
  }
  template <int I, int K> DEV void issue() const {
    const int kc = (I + rot) & 7;
    constexpr int slot = (I % 3) * SLOT;
    if constexpr (K == 0) dma16_masked<slot>(voffA + kc * 128, sA0, wb, mlo);
    if constexpr (K == 1) dma16<slot + BOFF>(voffB + kc * stepB, sB0, wb);
    if constexpr (K == 2) dma16<slot + BOFF + 8192>(voffB + kc * stepB, sB1, wb);
    if constexpr (K == 3) dma16<slot + BOFF + 16384>(voffB2 + kc * stepB, sB2, wb);
    if constexpr (K == 4) dma16<slot + 8192>(voffA + kc * 128, sA1, wb);
    if constexpr (K == 5) dma16_if<slot + 12288>(voffA + kc * 128 + 32 * LDX, sA1, wb, xcond);
  }
};

template <int TYPE>
DEV void body(LAS char* L, const Ptrs P, Ring R, int g, int j, int w) {
  constexpr bool FFN = TYPE >= 2;
  constexpr int ODD = TYPE & 1;
  constexpr int NT = TYPE == 0 ? 5 : (TYPE == 1 ? 4 : 2);
  constexpr int NC = TYPE == 2 ? 6 : (TYPE == 3 ? 5 : 3);
  constexpr int CB = TYPE == 3 ? 6 : 0;
  constexpr int ND = TYPE == 1 ? 6 : 5;
  constexpr int NM = NT * NC + (FFN ? 2 : 0);
  constexpr int NRL = FFN ? 2 : 4;
  int tid = fresh_tid();
  int lane = tid & 63;
  R.lanes(lane);
  int lg = lane >> 4, lc = lane & 15;
  GAS u64* ctl = (GAS u64*)(P.ws + g * WS_GRP);
  u32x4 r0, r1, r2;
  u64 epoch;
  const int T0 = TYPE == 0 ? 0 : (TYPE == 1 ? 5 : 2 * (w & 3));
  const u32 aA = T0 * 2048 + (lc >> 3) * 1024 + (lc & 7) * 128 + ((lg ^ (lc & 7)) << 4);
  const int tq = lc >> 2, tp = lc & 3;
  const int swR = (tq & 2) | ((lg & 1) << 2);
  const u32 aBl = BOFF + lg * 1024 + tq * 128 + ((tp >> 1) << 4) + (tp & 1) * 8;
  const int crot = min(2 * (w & 3), NC - 2);
  constexpr int NBB = FFN ? NC : 1;
  u32 aB[NBB][2], aAb[2][2];
#pragma unroll
  for (int c = 0; c < NBB; ++c) {
    const int cc = c + crot >= NC ? c + crot - NC : c + crot;
    const int ct = FFN ? CB + cc : (w & 3);
    aB[c][0] = aBl + (ct >> 2) * 8192 + (((2 * (ct & 3)) ^ swR) << 4);
    aB[c][1] = aB[c][0] + 65536;
    asm("" : "+v"(aB[c][0]), "+v"(aB[c][1]));
  }
#pragma unroll
  for (int b = 0; b < 2; ++b) {
    aAb[b][0] = aA ^ (b * 64);
    aAb[b][1] = aAb[b][0] + 65536;
    asm("" : "+v"(aAb[b][0]), "+v"(aAb[b][1]));
  }
  f32x4 acc[NT][NC], acc1[2];
#pragma unroll
  for (int t = 0; t < NT; ++t)
#pragma unroll
    for (int c = 0; c < NC; ++c) acc[t][c] = f32x4{0.f, 0.f, 0.f, 0.f};
  acc1[0] = acc1[1] = f32x4{0.f, 0.f, 0.f, 0.f};
  u32x4 fa[2][NT];
  u32x2 fb[2][NC][2];
  auto rd = [&](int t, int k) INL {
    const int slot = ((t >> 1) % 3) * SLOT, b = t & 1;
    if (k >= 2 && k < 2 + NT) {
      const int o = slot + (k - 2) * 2048, hi = o >= 65536;
      fa[b][k - 2] = lds128(L, aAb[b][hi] + (o - hi * 65536));
    } else {
      const int kk = k < 2 ? k : k - NT, c = kk >> 1;
      const int o = slot + b * 4096 + (kk & 1) * 512 + (FFN ? 0 : c * 8192), hi = o >= 65536;
      fb[b][c][kk & 1] = ldstr(L, aB[FFN ? c : 0][hi] + (o - hi * 65536));
    }
  };
  const int mt = 2 * g + ((j >> 2) & 1), nt = (j & 3) + (j >> 3) * 4;
  const int ks0 = w < 6 ? 15 * w : 90 + 14 * (w - 6), nks = w < 6 ? 15 : 14;
  const u32 voffW = (lane >> 3) * LDW2 + (((lane & 7) ^ ((lane >> 2) & 4)) << 4) + nt * 128;
  const u64 woa = (u64)P.wo, w2a = (u64)P.w2 - 32 * 16 * LDW2;
  const u32 wx0 = w < 5 ? 2 * SLOT + (16 + w) * 2048 : (11 + w) * 2048;
  const u32 wx1 = w < 5 ? SLOT + (16 + w) * 2048 : (w < 7 ? (14 + w) * 2048 : L_SP + 8 * 2048);
  auto wbase = [&](int s) INL -> u32 { const int k = s % 9; return k == 0 ? 2 * R.wb : (k == 5 ? wx0 : (k == 8 ? wx1 : 4 * R.wb)); };
  auto wdma = [&](auto sc_, auto hc_) INL {
    constexpr int s = decltype(sc_)::v, h = decltype(hc_)::v, k = s % 9;
    constexpr int imm = k == 0 ? L_SP : (k <= 2 ? 2 * SLOT + (k - 1) * 2048 : (k <= 4 ? (k - 3) * 2048 : (k == 6 || k == 7 ? SLOT + (k - 6) * 2048 : 0)));
    const int ks = min(ks0 + s, NKS - 1);
    const u64 base = (ks < 32 ? woa : w2a) + (u32)(ks * 16 * LDW2);
    dma16<imm + h * 1024>(voffW + h * 8 * LDW2, (const void*)base, wbase(s));
  };
  u32x4 wf[15][2];
  const u32 tb = (lane >> 5) * 1024 + tq * 128 + (((2 * (lg & 1) + (tp >> 1)) ^ ((tq & 2) << 1)) << 4) + (tp & 1) * 8;
  const __amdgpu_buffer_rsrc_t rsA2 = __builtin_amdgcn_make_buffer_rsrc(P.ws + WS_A2, 0, A2_BYTES, 0x00020000);
  const __amdgpu_buffer_rsrc_t rsG = __builtin_amdgcn_make_buffer_rsrc(P.ws + g * WS_GRP, 0, WS_GRP, 0x00020000);
  const u32x4 ones = {0x3F803F80u, 0x3F803F80u, 0x3F803F80u, 0x3F803F80u};
  u64 tag = 0;
  u32 b1v = 0;
  u32x4 gst;
  u64 tp2 = 0;
  u32 gsto = 0, gldo = 0, b1o = 0, stw = 0, b1w = 0, csw = 0, csw2 = 0;
  float som = 0.f, sor = 0.f;
  f32x4 mP, mP0, mC;
  u32 sel0 = 0, sel1 = 0, wra[4][2];
  auto wread = [&](int s) INL {
    const int k = s % 9;
#pragma unroll
    for (int f = 0; f < 2; ++f) {
      const u32 a = k == 0 ? wra[0][f] : (k <= 2 ? wra[2][f] + (k - 1) * 2048 : (k <= 4 ? wra[1][f] + (k - 3) * 2048 :
                    (k == 6 || k == 7 ? wra[1][f] + SLOT + (k - 6) * 2048 : wra[1][f] - 4 * R.wb + wbase(s))));
      const u32x2 lo = ldstr(L, a), hi = ldstr(L, a + 512);
      wf[s][f] = u32x4{lo[0], lo[1], hi[0], hi[1]};
    }
  };
  constexpr int NK = 5, TK = TYPE == 0 ? 0 : 4;
  float mc1[NK], rc1[NK];
  f32x4 m4[NK], r4[NK], bb[NC];
  auto rdst = [&](int k) INL {
    if (FFN) {
      if (k < 2) mc1[k] = ldsf(L, L_MEAN + ((2 * (w & 3) + k) * 16 + lc) * 4);
      else if (k < 4) rc1[k - 2] = ldsf(L, L_RSTD + ((2 * (w & 3) + k - 2) * 16 + lc) * 4);
      else if (k < 4 + NC) {
        const int c = k - 4, ct = CB + (c + crot >= NC ? c + crot - NC : c + crot);
        bb[c] = ldsf4(L, L_B1 + (ct * 16 + lg * 4) * 4);
      }
    } else {
      const int i = k >> 2, T = TK + i;
      if (i >= NK) return;
      if ((k & 3) == 0) mc1[i] = ldsf(L, L_MEAN + (T * 16 + lc) * 4);
      if ((k & 3) == 1) rc1[i] = ldsf(L, L_RSTD + (T * 16 + lc) * 4);
      if ((k & 3) == 2) m4[i] = ldsf4(L, L_MEAN + (T * 16 + lg * 4) * 4);
      if ((k & 3) == 3) r4[i] = ldsf4(L, L_RSTD + (T * 16 + lg * 4) * 4);
    }
  };
  {
        const int b_ = blockIdx.x, ln = min(b_ * 59 + w * 8 + (lane & 7), 15103);
        tp2 = (ln < 4096 ? woa : w2a) + (u32)(ln * 128);
        const u64 pb = ((lane & 8) ? (u64)P.b1 + (u32)min(max(j - 8, 0) * 352 + (lane & 3) * 128, 2748) : (u64)P.b2 + (u32)(nt * 128));
        const u64 pf = (u64)ctl + (u32)(128 * (lc + 1));
        tp2 = lg == 1 ? pb : tp2;
        tp2 = (lg == 2 && w == 0) ? pf : tp2;
        gsto = (w == 7 && lane < 8) ? WS_STAT + (j * 8 + lane) * 16 : WS_GRP;
        gldo = WS_STAT + (tid & 127) * 16;
        b1o = 2 * (u32)min(max(j - 8, 0) * 176 + tid, 1375);
        stw = (!ODD && w < 2) ? L_MEAN + (tid + (FFN ? 0 : 16)) * 4 : L_JUNK + lane * 4;
        b1w = tid < 192 ? L_B1 + tid * 4 : L_JUNK + lane * 4;
        csw = lc == 0 ? L_CS + ((FFN ? CB + crot : (w & 3)) * 16 + lg * 4) * 4 : L_JUNK + lane * 16;
        csw2 = lg == 0 ? L_CS + (128 + (w & 3) * 16 + lc) * 4 : L_JUNK + lane * 4;
        if (!FFN) {
#pragma unroll
          for (int r = 0; r < 4; ++r) {
            mP[r] = (4 * lg + r >= lc + 2) ? 0.f : -1e30f;
            mP0[r] = (TYPE == 0 && g == 0) ? -1e30f : mP[r];
            mC[r] = (4 * lg + r <= lc) ? 0.f : -1e30f;
          }
          sel0 = (lc & 1) ? 0x01000c0cu : 0x0c0c0100u; sel1 = (lc & 1) ? 0x03020c0cu : 0x0c0c0302u;
          asm volatile("" : "+v"(mP), "+v"(mC), "+v"(sel0), "+v"(sel1));
          if (TYPE == 0) asm volatile("" : "+v"(mP0));
        }
#pragma unroll
        for (int f = 0; f < 2; ++f) {
          wra[0][f] = (tb ^ (f * 64)) + 2 * R.wb + L_SP;
          wra[1][f] = (tb ^ (f * 64)) + 4 * R.wb;
          wra[2][f] = (tb ^ (f * 64)) + 4 * R.wb + 2 * SLOT;
          asm volatile("" : "+v"(wra[0][f]), "+v"(wra[1][f]), "+v"(wra[2][f]));
        }
        asm volatile("" : "+v"(tp2), "+v"(gsto), "+v"(gldo), "+v"(b1o), "+v"(stw), "+v"(b1w), "+v"(csw), "+v"(csw2));
      }
  SB();
  sfor<0, ND>([&](auto kc) INL { R.template issue<1, decltype(kc)::v>(); });
  if (FFN)
    asm volatile("global_load_dwordx2 %0, %2, %3 sc1\n\tglobal_load_dwordx4 %1, %4, %5"
                 : "=&v"(epoch), "=&v"(r0) : "v"(0u), "s"(ctl), "v"(lane * 16), "s"(P.x + (size_t)(128 * g + 8 * j + w) * LDX) : "memory");
  else
    asm volatile("global_load_dwordx2 %0, %4, %5 sc1\n\tglobal_load_dwordx4 %1, %6, %7\n\t"
                 "global_load_dwordx4 %2, %6, %8\n\tglobal_load_dwordx4 %3, %6, %8 offset:1024"
                 : "=&v"(epoch), "=&v"(r0), "=&v"(r1), "=&v"(r2)
                 : "v"(0u), "s"(ctl), "v"(lane * 16), "s"(P.x + (size_t)(128 * g + 8 * j + w) * LDX),
                   "s"(P.x + (size_t)max(128 * g - 16 + 2 * w, 0) * LDX) : "memory");
  wait_bar<5 + NRL>();
#pragma unroll
  for (int k = 0; k < 2 * NC + NT; ++k) rd(0, k);
  SB();
  sfor<0, 16>([&](auto tc) INL {
    constexpr int t = decltype(tc)::v, s = t >> 1, b = t & 1;
    if constexpr (b) {
      if constexpr (s <= 4) wait_bar<5 + ((s == 1 || s == 2) ? ODD : 0) + ((t == TT + 1 || t == TT + 3) ? 1 : 0)>();
      else if constexpr (s == 5) wait_bar<7>();
      else if constexpr (s == 6) wait_bar<4>();
    }
    if constexpr (t == 11) {
      if constexpr (!ODD) asm volatile("" : "+v"(gst), "+v"(b1v));
      if constexpr (!ODD) {
        if (__builtin_expect(!__all(gst[1] == (u32)tag && gst[3] == (u32)tag), 0)) {
#pragma nounroll
          for (u32 spins = 0; spins < SPIN_CAP; ++spins) {
            __builtin_amdgcn_s_sleep(1);
            gst = __builtin_amdgcn_raw_buffer_load_b128(rsG, gldo, 0, AUX_SC1);
            if (__all(gst[1] == (u32)tag && gst[3] == (u32)tag)) break;
          }
        }
      }
      SB();
    }
    constexpr int nr = t == 15 ? 0 : 2 * NC + NT;
    constexpr int st = t == 0 ? 2 : (t + 5) / 2;
    constexpr int k0 = (t == 0 || b) ? 0 : 3, k1 = st >= 8 ? 0 : ((t == 0 || !b) ? ND : 3);
    constexpr int nd = k1 > k0 ? k1 - k0 : 0, nw = t >= 10 ? 2 : 0, nv = nd + nw;
    sfor<0, NM>([&](auto qc) INL {
      constexpr int q = decltype(qc)::v;
      constexpr int qq = t < 15 ? q : (FFN ? (q + NT * NC) % NM : (TYPE == 1 ? q : (q < 3 ? q * NT : (q < 5 ? (q - 2) * NT + 4 :
                         (q - 5 < 4 ? q - 4 : (q - 5 < 7 ? q - 3 : q - 1))))));
      if constexpr (qq < NT * NC) {
        constexpr int c = qq / NT, i = qq % NT;
        const u32x4 wfr = {fb[b][c][0][0], fb[b][c][0][1], fb[b][c][1][0], fb[b][c][1][1]};
        if constexpr (!FFN && c == 2) acc[i][c] = mfma16(fa[b][i], wfr, acc[i][c]);
        else acc[i][c] = mfma16(wfr, fa[b][i], acc[i][c]);
        PIN(acc[i][c]);
      } else {
        constexpr int c = qq - NT * NC;
        const u32x4 wfr = {fb[b][c][0][0], fb[b][c][0][1], fb[b][c][1][0], fb[b][c][1][1]};
        acc1[c] = mfma16(wfr, ones, acc1[c]);
        PIN(acc1[c]);
      }
      if constexpr (q < nr) rd(t + 1, q);
      if constexpr (t == 15) { rdst(2 * q); rdst(2 * q + 1); }
      if constexpr (t == 9 && q == 0 && !ODD)
        asm volatile("buffer_load_dwordx4 %0, %2, %3, 0 offen sc1\n\tglobal_load_ushort %1, %4, %5"
                     : "=&v"(gst), "=&v"(b1v) : "v"(gldo), "s"(rsG), "v"(b1o), "s"(P.b1) : "memory");
      if constexpr (t == 11 && !ODD) {
        if constexpr (q == NM - 4) *(LAS u32*)(L + stw) = gst[0];
        if constexpr (q == NM - 3) *(LAS u32*)(L + stw + 576) = gst[2];
        if constexpr (q == NM - 2 && FFN) *(LAS float*)(L + b1w) = bf_lo(b1v);
      }
      if constexpr (t == 1 && ODD && q == NM - 1) { som = ldsf(L, L_OWN + (lane & 7) * 4); sor = ldsf(L, L_OWN + 576 + (lane & 7) * 4); }
      sfor<0, nv>([&](auto uc) INL {
        constexpr int u = decltype(uc)::v;
        if constexpr (((2 * u + ODD) * NM) / (2 * nv) == q) {
          if constexpr (u < nd) R.template issue<st, k0 + u>();
          else wdma(IC<t - 10>{}, IC<u - nd>{});
        }
      });
      if constexpr (t == TT && q == NM - 1) touch((const void*)tp2);
      if constexpr (t == 2 && q == NM - 1) {
        if (ODD) st128(u32x4{f2u(som), (u32)tag, f2u(sor), (u32)tag}, rsG, gsto, 0);
      }
      if constexpr (t == 15) {
        if constexpr (TYPE == 0) {
          if constexpr (q == 6) *(LAS f32x4*)(L + csw) = acc[0][0];
          if constexpr (q == 7) *(LAS f32x4*)(L + csw + 256) = acc[0][1];
          if constexpr (q == 8) *(LAS float*)(L + csw2) = acc[0][2][0];
          if constexpr (q == 9) *(LAS f32x4*)(L + L_KV + 2 * R.wb + lane * 16) = acc[NT - 1][1];
          if constexpr (q == 10) *(LAS f32x4*)(L + L_KV + 2 * R.wb + 1024 + lane * 16) = acc[NT - 1][2];
        }
        if constexpr (FFN) {
          if constexpr (q == 6) *(LAS f32x4*)(L + csw) = acc1[0];
          if constexpr (q == 7) *(LAS f32x4*)(L + csw + 64) = acc1[1];
        }
      }
      SB();
    });
    if constexpr (t == 0) {
      if (FFN) asm volatile("s_waitcnt vmcnt(%2)" : "+v"(epoch), "+v"(r0) : "i"(5) : "memory");
      else asm volatile("s_waitcnt vmcnt(%4)" : "+v"(epoch), "+v"(r0), "+v"(r1), "+v"(r2) : "i"(5) : "memory");
      tag = (((u64)(u32)__builtin_amdgcn_readfirstlane((u32)(epoch >> 32)) << 32) | (u32)__builtin_amdgcn_readfirstlane((u32)epoch)) + 1;
      float sS[3] = {0.f, 0.f, 0.f}, sQ[3] = {0.f, 0.f, 0.f};
#pragma unroll
      for (int d = 0; d < 4; ++d) {
        sS[0] = dot2(r0[d], 0x3F803F80u, sS[0]); sQ[0] = dot2(r0[d], r0[d], sQ[0]);
        if (!FFN) {
          sS[1] = dot2(r1[d], 0x3F803F80u, sS[1]); sQ[1] = dot2(r1[d], r1[d], sQ[1]);
          sS[2] = dot2(r2[d], 0x3F803F80u, sS[2]); sQ[2] = dot2(r2[d], r2[d], sQ[2]);
        }
      }
      f32x2 a = swap32(sS[0], sS[FFN ? 0 : 1]), c = swap32(sQ[0], sQ[FFN ? 0 : 1]);
      float S = a[0] + a[1], Q = c[0] + c[1], S2 = S, Q2 = Q;
      if (!FFN) {
        a = swap32(sS[2], sS[2]); c = swap32(sQ[2], sQ[2]);
        S2 = a[0] + a[1]; Q2 = c[0] + c[1];
      }
      a = swap16(S, S2); c = swap16(Q, Q2);
      S = a[0] + a[1]; Q = c[0] + c[1];
      S = dpp_add<0xB1>(S); Q = dpp_add<0xB1>(Q);
      S = dpp_add<0x4E>(S); Q = dpp_add<0x4E>(Q);
      S = dpp_add<0x141>(S); Q = dpp_add<0x141>(Q);
      S = dpp_add<0x140>(S); Q = dpp_add<0x140>(Q);
      const float m = S * (1.f / 512.f), e = Q * (1.f / 512.f);
      u32 adr = L_JUNK + lane * 4;
      adr = lane == 0 ? L_OWN + w * 4 : adr;
      if (!FFN) { adr = lane == 32 ? L_MEAN + w * 8 : adr; adr = lane == 16 ? L_MEAN + w * 8 + 4 : adr; }
      *(LAS float*)(L + adr) = -m;
      *(LAS float*)(L + adr + 576) = __builtin_amdgcn_rsqf(fmaxf(e - m * m, 0.f) + 1e-5f);
      SB();
    }
  });
  lds_bar();
  auto slot = [&](auto kc) INL {
    constexpr int k = decltype(kc)::v;
    SB();
    if constexpr (k < NEPI) wdma(IC<6 + k / 2>{}, IC<k % 2>{});
    SB();
  };
  if constexpr (!FFN) {
    const int h = w & 3;
    const f32x4 csq = ldsf4(L, L_CS + (h * 16 + lg * 4) * 4), csk = ldsf4(L, L_CS + (64 + h * 16 + lg * 4) * 4);
    const float csv1 = ldsf(L, L_CS + (128 + h * 16 + lc) * 4);
    f32x4 k4 = acc[0][1], v4 = acc[0][2];
    if (TYPE == 1) { k4 = ldsf4(L, L_KV + h * 2048 + lane * 16); v4 = ldsf4(L, L_KV + h * 2048 + 1024 + lane * 16); }
    slot(IC<0>{});
    wait_vm<7>();
    wread(0); wread(1); wread(2);
    const bool odd = lc & 1;
    const f32x4 csv = splat4(csv1);
    u32x2 Qb[4], Kb[NK], Vb[NK];
    sfor<0, NK>([&](auto ic) INL {
      constexpr int i = decltype(ic)::v, ai = TYPE == 0 ? i : (i > 0 ? i - 1 : 0);
      slot(IC<1 + i>{});
      const f32x4 mc = splat4(mc1[i]);
      Kb[i] = pk4(((i == 0 ? k4 : acc[ai][1]) + mc * csk) * splat4(rc1[i]));
      if constexpr (i > 0) Qb[i - 1] = pk4((acc[ai][0] + mc * csq) * splat4(rc1[i] * (0.25f * 1.4426950408889634f)));
      Vb[i] = pk4(((i == 0 ? v4 : acc[ai][2]) + m4[i] * csv) * r4[i]);
    });
    f32x4 sp[4], sc[4];
#pragma unroll
    for (int q = 0; q < 4; ++q) {
      sp[q] = mfma16k16(Kb[q], Qb[q], q == 0 ? mP0 : mP);
      sc[q] = mfma16k16(Kb[q + 1], Qb[q], mC);
    }
    SB();
    wait_lds();
    wait_vm<6>();
    wread(3); wread(4); wread(5);
    slot(IC<6>{});
    f32x4 a[4];
    float mx[4];
#pragma unroll
    for (int q = 0; q < 4; ++q) {
      a[q] = f32x4{sel(odd, sp[q][1], sp[q][0]), sel(odd, sp[q][3], sp[q][2]), sel(odd, sc[q][1], sc[q][0]), sel(odd, sc[q][3], sc[q][2])};
      mx[q] = vmax(vmax(a[q][0], a[q][1]), vmax(a[q][2], a[q][3]));
    }
    slot(IC<7>{});
    {
      const f32x2 x = swap32(mx[0], mx[1]), y = swap32(mx[2], mx[3]);
      const f32x2 z = swap16(vmax(x[0], x[1]), vmax(y[0], y[1]));
      const float u = vmax(z[0], z[1]);
      const f32x2 p = swap16(u, u);
      const f32x2 p0 = swap32(p[0], p[0]), p1 = swap32(p[1], p[1]);
      mx[0] = p0[0]; mx[1] = p0[1]; mx[2] = p1[0]; mx[3] = p1[1];
    }
    slot(IC<8>{});
#pragma unroll
    for (int q = 0; q < 4; ++q) a[q] -= splat4(mx[q]);
    slot(IC<9>{});
    sfor<0, 4>([&](auto qc) INL {
      constexpr int q = decltype(qc)::v;
#pragma unroll
      for (int r = 0; r < 4; ++r) a[q][r] = __builtin_amdgcn_exp2f(a[q][r]);
      if constexpr (q & 1) slot(IC<10 + q / 2>{});
    });
    wait_lds();
    const f32x4 z4 = {0.f, 0.f, 0.f, 0.f};
    u32x4 pb[4];
    sfor<0, 4>([&](auto qc) INL {
      constexpr int q = decltype(qc)::v;
      const u32x2 u = pk4(a[q]);
      pb[q] = u32x4{__builtin_amdgcn_perm(0, u[0], sel0), __builtin_amdgcn_perm(0, u[0], sel1),
                    __builtin_amdgcn_perm(0, u[1], sel0), __builtin_amdgcn_perm(0, u[1], sel1)};
      if constexpr (q & 1) slot(IC<12 + q / 2>{});
    });
#pragma unroll
    for (int q = 0; q < 4; ++q) {
      sc[q] = mfma16(ones, pb[q], z4);
      sp[q] = mfma16k16(Vb[q], u32x2{pb[q][0], pb[q][1]}, z4);
    }
#pragma unroll
    for (int q = 0; q < 4; ++q) sp[q] = mfma16k16(Vb[q + 1], u32x2{pb[q][2], pb[q][3]}, sp[q]);
    slot(IC<14>{}); slot(IC<15>{}); slot(IC<16>{}); slot(IC<17>{});
    u32x2 Ob[4];
#pragma unroll
    for (int q = 0; q < 4; ++q) Ob[q] = pk4(sp[q] * splat4(__builtin_amdgcn_rcpf(sc[q][0])));
    const int rt = 4 * g + (TYPE == 0 ? 0 : 2), ks = 4 * j + h;
    st128(pair_rows(Ob[0], Ob[1]), rsA2, lane * 16, (rt * NKS + ks) * 1024);
    st128(pair_rows(Ob[2], Ob[3]), rsA2, lane * 16, ((rt + 1) * NKS + ks) * 1024);
  } else {
    f32x4 cs[NC];
#pragma unroll
    for (int c = 0; c < NC; ++c) {
      const int ct = CB + (c + crot >= NC ? c + crot - NC : c + crot);
      cs[c] = ldsf4(L, L_CS + (ct * 16 + lg * 4) * 4);
    }
    wait_vm<6>();
    wread(0); wread(1); wread(2);
    const f32x4 mc[2] = {splat4(mc1[0]), splat4(mc1[1])}, rc[2] = {splat4(rc1[0]), splat4(rc1[1])};
    const int nct = j == 15 ? 9 : 11;
    const int rt = 4 * g + (w & 3), ksb = 32 + 11 * (j - 8);
    const f32x4 C1 = splat4(-2.f * 1.4426950408889634f * 0.7978845608028654f);
    const f32x4 C2 = splat4(-2.f * 1.4426950408889634f * 0.7978845608028654f * 0.044715f);
    const f32x4 ONE = splat4(1.f);
    sfor<0, 3>([&](auto gc) INL {
      constexpr int G = decltype(gc)::v, c0 = 2 * G, n = c0 + 1 < NC ? 4 : 2;
      if constexpr (G == 1) { wait_lds(); wait_vm<8>(); wread(3); wread(4); wread(5); }
      if constexpr (G == 2) wait_lds();
      f32x4 v[4], e[4];
      slot(IC<6 * G>{});
#pragma unroll
      for (int u = 0; u < n; ++u) v[u] = (acc[u & 1][c0 + (u >> 1)] + mc[u & 1] * cs[c0 + (u >> 1)]) * rc[u & 1] + bb[c0 + (u >> 1)];
      slot(IC<6 * G + 1>{});
#pragma unroll
      for (int u = 0; u < n; ++u) e[u] = v[u] * (v[u] * v[u] * C2 + C1);
      slot(IC<6 * G + 2>{});
#pragma unroll
      for (int u = 0; u < n; ++u) {
#pragma unroll
        for (int r = 0; r < 4; ++r) e[u][r] = __builtin_amdgcn_exp2f(e[u][r]);
        if (u == 1) slot(IC<6 * G + 3>{});
      }
#pragma unroll
      for (int u = 0; u < n; ++u) e[u] += ONE;
      slot(IC<6 * G + 4>{});
#pragma unroll
      for (int u = 0; u < n; ++u) {
#pragma unroll
        for (int r = 0; r < 4; ++r) e[u][r] = __builtin_amdgcn_rcpf(e[u][r]);
        if (u == 1) slot(IC<6 * G + 5>{});
      }
#pragma unroll
      for (int cc = 0; cc < n / 2; ++cc) {
        const int c = c0 + cc, ct = CB + (c + crot >= NC ? c + crot - NC : c + crot);
        st128(pair_rows(pk4(v[2 * cc] * e[2 * cc]), pk4(v[2 * cc + 1] * e[2 * cc + 1])), rsA2, lane * 16,
              ct < nct ? (rt * NKS + ksb + ct) * 1024 : A2_BYTES);
      }
      SB();
    });
  }
  wait_vm<0>();
  asm volatile("s_barrier" ::: "memory");
  if (tid == 0) __hip_atomic_store(ctl + 16 * (j + 1), tag, __ATOMIC_RELAXED, __HIP_MEMORY_SCOPE_AGENT);
  sfor<NEPI, 18>([&](auto kc) INL { wdma(IC<6 + decltype(kc)::v / 2>{}, IC<decltype(kc)::v % 2>{}); });
#pragma unroll
  for (int s = 6; s < 9; ++s) wread(s);
  tid = fresh_tid(); lane = tid & 63; lg = lane >> 4; lc = lane & 15;
  const u32 ooff = (u32)((mt * 64 + 4 * w + ((lane >> 3) & 3)) * LDX + (nt * 64 + (lane & 7) * 8) * 2);
  f32x4 xb0, xb1;
#pragma unroll
  for (int s = 0; s < 9; ++s) asm volatile("" : "+v"(wf[s][0]), "+v"(wf[s][1]));
  u64 v0, v1, v2, v3;
  wait_vm<0>();
  {
    const u32 fo = 128 * (lc + 1);
    u32 spins = SPIN_CAP;
    asm volatile(
        "s_cmp_lg_u32 %9, %10\n\ts_cbranch_scc1 2f\n\t"
        "global_load_dwordx2 %0, %5, %6 sc1\n\ts_sleep %8\n\tglobal_load_dwordx2 %1, %5, %6 sc1\n\ts_sleep %8\n\t"
        "global_load_dwordx2 %2, %5, %6 sc1\n\ts_sleep %8\n"
        "1:\n\t"
        "global_load_dwordx2 %3, %5, %6 sc1\n\ts_waitcnt vmcnt(3)\n\tv_cmp_ne_u64 vcc, %7, %0\n\ts_cmp_eq_u64 vcc, 0\n\ts_cbranch_scc1 2f\n\ts_sleep %8\n\t"
        "global_load_dwordx2 %0, %5, %6 sc1\n\ts_waitcnt vmcnt(3)\n\tv_cmp_ne_u64 vcc, %7, %1\n\ts_cmp_eq_u64 vcc, 0\n\ts_cbranch_scc1 2f\n\ts_sleep %8\n\t"
        "global_load_dwordx2 %1, %5, %6 sc1\n\ts_waitcnt vmcnt(3)\n\tv_cmp_ne_u64 vcc, %7, %2\n\ts_cmp_eq_u64 vcc, 0\n\ts_cbranch_scc1 2f\n\ts_sleep %8\n\t"
        "global_load_dwordx2 %2, %5, %6 sc1\n\ts_waitcnt vmcnt(3)\n\tv_cmp_ne_u64 vcc, %7, %3\n\ts_cmp_eq_u64 vcc, 0\n\ts_cbranch_scc1 2f\n\ts_sleep %8\n\t"
        "s_sub_u32 %4, %4, 1\n\ts_cmp_lg_u32 %4, 0\n\ts_cbranch_scc1 1b\n"
        "2:\n\ts_barrier"
        : "=&v"(v0), "=&v"(v1), "=&v"(v2), "=&v"(v3), "+s"(spins)
        : "v"(fo), "s"(ctl), "s"(tag), "i"(PGAP), "s"(w), "i"(7) : "memory", "vcc", "scc");
  }
  u32x4 af[2][15];
  auto lda = [&](int r, int s) INL {
    const int so = (s < nks) ? ((2 * mt + r) * NKS + ks0 + s) * 1024 : A2_BYTES;
    af[r][s] = __builtin_amdgcn_raw_buffer_load_b128(rsA2, lane * 16, so, AUX_SC1);
  };
#pragma unroll
  for (int n = 0; n < WIN; ++n) lda(n / 15, n % 15);
  SB();
  u32 pw[4];
#pragma unroll
  for (int y = 0; y < 4; ++y) pw[y] = ((lane + 4 * y) & 63) * 16 + y * 1024 + 4 * R.wb;
  const int blk = lane & 7;
  const u32 prd = (((lane & 32) + 4 * w + ((lane >> 3) & 3) + 4 * (blk & 3)) & 63) * 16 + (blk & 3) * 1024;
  const u32 prd0 = prd + (blk >> 2) * SLOT, prd1 = prd + (blk >> 2) * 4096 + 2 * SLOT;
  const __amdgpu_buffer_rsrc_t rsO = __builtin_amdgcn_make_buffer_rsrc(P.out, 0, 2048 * LDX, 0x00020000);
  auto reduce = [&](int r, f32x4 xb) INL {
    f32x4 p[8];
#pragma unroll
    for (int sw = 0; sw < 8; ++sw) p[sw] = ldsf4(L, r == 0 ? prd0 + sw * 4096 : prd1 + sw * 8192);
    SB();
    const u32x2 o = pk4(((p[0] + p[1]) + (p[2] + p[3])) + ((p[4] + p[5]) + (p[6] + p[7])) + xb);
    auto e0 = __builtin_amdgcn_permlane32_swap(o[0], o[0], false, false), e1 = __builtin_amdgcn_permlane32_swap(o[1], o[1], false, false);
    __builtin_amdgcn_raw_buffer_store_b128(u32x4{e0[0], e1[0], e0[1], e1[1]}, rsO, lane < 32 ? ooff + r * 32 * LDX : 2048 * LDX, 0, AUX_SC0 | AUX_SC1);
  };
  f32x16 pa[2][2];
  auto pwrite = [&](int r, int b8) INL {
    const f32x16& a = pa[r][b8 >> 2];
    const int y = b8 & 3;
    *(LAS f32x4*)(L + pw[y] + (r == 0 ? (b8 >> 2) * SLOT : 2 * SLOT + 4 * R.wb + (b8 >> 2) * 4096)) =
        f32x4{a[4 * y], a[4 * y + 1], a[4 * y + 2], a[4 * y + 3]};
  };
  u32x2 xr0, xr1, bres;
  sfor<0, 2>([&](auto rc_) INL {
    constexpr int r = decltype(rc_)::v;
#pragma unroll
    for (int i = 0; i < 16; ++i) pa[r][0][i] = pa[r][1][i] = 0.f;
    sfor<0, 15>([&](auto sc_) INL {
      constexpr int s = decltype(sc_)::v;
      pa[r][0] = mfma32(wf[s][0], af[r][s], pa[r][0]);
      PIN(pa[r][0]);
      if constexpr (r == 1 && s >= 1 && s < 5) pwrite(0, 2 * s - 2);
      pa[r][1] = mfma32(wf[s][1], af[r][s], pa[r][1]);
      PIN(pa[r][1]);
      if constexpr (r == 1 && s >= 1 && s < 5) pwrite(0, 2 * s - 1);
      if constexpr (r == 0 && s == 2) asm volatile("" ::"v"(v0), "v"(v1), "v"(v2), "v"(v3));
      if constexpr (r == 0 && s == 1) {
        const u32 o4 = ooff + (lane >> 5) * 8;
        xr0 = *(const GAS u32x2*)(P.x + o4); xr1 = *(const GAS u32x2*)(P.x + (o4 + 32 * LDX));
        bres = *(const GAS u32x2*)(P.b2 + (o4 & 1023));
      }
      if constexpr (r * 15 + s + WIN < 30) lda((r * 15 + s + WIN) / 15, (r * 15 + s + WIN) % 15);
      if constexpr (r == 0 && s >= 7 && s < 13) wread(s + 2);
      if constexpr (r == 0 && s == 14) {
        const f32x4 bv = {bf_lo(bres[0]), bf_hi(bres[0]), bf_lo(bres[1]), bf_hi(bres[1])};
        xb0 = f32x4{bf_lo(xr0[0]), bf_hi(xr0[0]), bf_lo(xr0[1]), bf_hi(xr0[1])} + bv;
        xb1 = f32x4{bf_lo(xr1[0]), bf_hi(xr1[0]), bf_lo(xr1[1]), bf_hi(xr1[1])} + bv;
        PIN(xb0); PIN(xb1);
      }
      if constexpr (r == 1 && s == RMID) { lds_bar(); reduce(0, xb0); }
      SB();
    });
  });
#pragma unroll
  for (int y = 0; y < 8; ++y) pwrite(1, y);
  lds_bar();
  reduce(1, xb1);
  if (j == 0 && tid == 0) __hip_atomic_store(ctl, tag, __ATOMIC_RELAXED, __HIP_MEMORY_SCOPE_AGENT);
  asm volatile(".Lfk_end%c0:" ::"i"(TYPE));
  __builtin_amdgcn_endpgm();
}

__global__ __launch_bounds__(512, 2) void fused(const char*, const char*, const char*, const char*, const u16*, const char*, const char*,
                                                char*, char*) {
  extern __shared__ __attribute__((aligned(16))) char smem[];
  LAS char* L = (LAS char*)smem;
  typedef u64 u64x8 __attribute__((ext_vector_type(8)));
  u64x8 ka;
  u64 kb;
  asm volatile("s_load_dwordx16 %0, %2, 0x0\n\ts_load_dwordx2 %1, %2, 0x40"
               : "=&s"(ka), "=&s"(kb) : "s"(__builtin_amdgcn_kernarg_segment_ptr()) : "memory");
  const int lane = threadIdx.x & 63, w = __builtin_amdgcn_readfirstlane(threadIdx.x >> 6);
  const int b = blockIdx.x, bi = b >> 3;
  const int g = (b & 3) * 4 + (bi & 3), j = ((bi >> 2) & 3) + ((b >> 2) & 1) * 4 + (bi >> 4) * 8;
  const bool ffn = j >= 8;
  if (w == 7) {
    u64 pc;
    u32 len;
    asm volatile("s_getpc_b64 %0\n.Lfk_here:\n\ts_mov_b32 %1, .Lfk_end0-.Lfk_here\n\ts_max_u32 %1, %1, .Lfk_end1-.Lfk_here\n\t"
                 "s_max_u32 %1, %1, .Lfk_end2-.Lfk_here\n\ts_max_u32 %1, %1, .Lfk_end3-.Lfk_here" : "=s"(pc), "=s"(len)::"scc");
    touch((const char*)pc + min((u32)(lane * 128 + (bi & 7) * 8192), len));
  }
  const int rowbase = ffn ? 128 * g : 128 * g - 16;
  Ring R;
  R.ldw = ffn ? LDW1 : LDQKV;
  R.maxch = ffn ? (j == 15 ? 1 : 5) : 7;
  R.wb = w * 1024;
  R.rot = (j + g) & 7;
  R.lanes(lane);
  R.stepB = 64 * R.ldw;
  R.mlo = (!ffn && w == 0) ? 0xFFFFFF00u : 0xFFFFFFFFu;
  R.xcond = ffn ? 2u : (u32)(w - 4);
  const u32 oA0 = (rowbase + 8 * w < 0 ? 8 * w : rowbase + 8 * w) * LDX, oA1 = (rowbase + 8 * (w + 8)) * LDX;
  const u32 oB = 8 * w * R.ldw + (ffn ? (j - 8) * 352 : j * 128), seg = ffn ? 128 : 1024;
  asm volatile("s_waitcnt lgkmcnt(0)" : "+s"(ka), "+s"(kb)::"memory");
  const Ptrs P = {(const char*)(GAS const char*)ka[0], (const char*)(GAS const char*)ka[1], (const char*)(GAS const char*)ka[2],
                  (const char*)(GAS const char*)ka[3], (const u16*)(GAS const u16*)ka[4], (const char*)(GAS const char*)ka[5],
                  (const char*)(GAS const char*)ka[6], (char*)(GAS char*)ka[7], (char*)(GAS char*)kb};
  R.sA0 = P.x + oA0;
  R.sA1 = P.x + oA1;
  R.sB0 = (const char*)((ffn ? ka[3] : ka[1]) + oB);
  R.sB1 = R.sB0 + seg;
  R.sB2 = R.sB1 + seg;
  sfor<0, 6>([&](auto kc) INL { R.template issue<0, decltype(kc)::v>(); });
  if (!ffn && w == 0 && lane < 8) {
    const u32x4 one = {0x3F803F80u, 0x3F803F80u, 0x3F803F80u, 0x3F803F80u};
#pragma unroll
    for (int s = 0; s < 3; ++s) *(LAS u32x4*)(L + s * SLOT + lane * 16) = one;
  }
  if (!ffn) {
    if (w < 4) body<0>(L, P, R, g, j, w);
    else body<1>(L, P, R, g, j, w);
  } else {
    if (w < 4) body<2>(L, P, R, g, j, w);
    else body<3>(L, P, R, g, j, w);
  }
}
}

extern "C" void kernel_launch(void* const* d_in, const int* in_sizes, int n_in, void* d_out, int out_size, void* d_ws,
                              size_t ws_size, hipStream_t stream) {
  (void)hipFuncSetAttribute((const void*)fk::fused, hipFuncAttributeMaxDynamicSharedMemorySize, fk::L_TOTAL);
  fk::fused<<<256, 512, fk::L_TOTAL, stream>>>((const char*)d_in[0], (const char*)d_in[1], (const char*)d_in[2], (const char*)d_in[4],
                                               (const u16*)d_in[5], (const char*)d_in[6], (const char*)d_in[7], (char*)d_out,
                                               (char*)d_ws);
}
```

```cpp
#include <hip/hip_runtime.h>
#include <stdint.h>

typedef unsigned u32;
typedef unsigned long long u64;
typedef unsigned short u16;
typedef short s16x4 __attribute__((ext_vector_type(4)));
typedef short s16x8 __attribute__((ext_vector_type(8)));
typedef __bf16 b16x2 __attribute__((ext_vector_type(2)));
typedef float f32x2 __attribute__((ext_vector_type(2)));
typedef float f32x4 __attribute__((ext_vector_type(4)));
typedef float f32x16 __attribute__((ext_vector_type(16)));
typedef u32 u32x2 __attribute__((ext_vector_type(2)));
typedef u32 u32x4 __attribute__((ext_vector_type(4)));
#define LAS __attribute__((address_space(3)))
#define GAS __attribute__((address_space(1)))
#define DEV __device__ __forceinline__
#define INL __attribute__((always_inline))

namespace fk {
constexpr int LDX = 1024, LDQKV = 3072, LDW1 = 2752, LDW2 = 1024;
constexpr int NKS = 118;
constexpr int SLOT = 43008, BOFF = 18432;
constexpr int L_MEAN = 3 * SLOT, L_RSTD = L_MEAN + 576, L_CS = L_RSTD + 576, L_B1 = L_CS + 768, L_OWN = L_B1 + 768,
              L_KV = L_MEAN + 3584, L_JUNK = L_KV + 8192, L_SP = L_JUNK + 2048, L_TOTAL = L_SP + 9 * 2048;
static_assert(L_OWN + 576 + 32 <= L_KV && L_TOTAL <= 163840, "LDS map");
constexpr int WS_GRP = 8192, WS_STAT = 4096;
constexpr int WS_A2 = 16 * WS_GRP, A2_BYTES = 64 * NKS * 1024;
constexpr int AUX_SC0 = 1, AUX_SC1 = 16;
constexpr u32 SPIN_CAP = 1u << 16;

constexpr int NEPI = 12;
constexpr int WIN = 6;
constexpr int RMID = 11;
constexpr int PGAP = 2;

#define STAMP(i) do {} while (0)
#define STAMP_ARG
#define STAMP_PASS
#define STAMP_OUT() do {} while (0)

template <int IMM> DEV void dma16(u32 voff, const void* sbase, u32 lb) {
  asm volatile("s_add_u32 m0, %2, %3\n\ts_nop 0\n\tglobal_load_lds_dwordx4 %0, %1" ::"v"(voff), "s"(sbase), "s"(lb), "i"(IMM) : "memory", "scc");
}
template <int IMM> DEV void dma16_masked(u32 voff, const void* sbase, u32 lb, u32 mlo) {
  asm volatile("s_add_u32 m0, %2, %4\n\ts_mov_b32 exec_lo, %3\n\tglobal_load_lds_dwordx4 %0, %1\n\ts_mov_b32 exec_lo, -1"
               ::"v"(voff), "s"(sbase), "s"(lb), "s"(mlo), "i"(IMM) : "memory", "scc");
}
template <int IMM> DEV void dma16_if(u32 voff, const void* sbase, u32 lb, u32 cond) {
  asm volatile("s_cmp_gt_u32 %3, 1\n\ts_cbranch_scc1 1f\n\ts_add_u32 m0, %2, %4\n\ts_nop 0\n\tglobal_load_lds_dwordx4 %0, %1\n1:"
               ::"v"(voff), "s"(sbase), "s"(lb), "s"(cond), "i"(IMM) : "memory", "scc");
}
DEV void touch(const void* p) {
  asm volatile("s_mov_b32 m0, %1\n\ts_nop 0\n\tglobal_load_lds_dword %0, off sc1" ::"v"(p), "i"(L_JUNK) : "memory");
}
template <int N> DEV void wait_bar() {
  asm volatile("s_waitcnt vmcnt(%0) lgkmcnt(0)\n\ts_barrier" ::"i"(N) : "memory");
}
template <int N> DEV void wait_vm() { asm volatile("s_waitcnt vmcnt(%0)" ::"i"(N) : "memory"); }
DEV void wait_lds() { asm volatile("s_waitcnt lgkmcnt(0)" ::: "memory"); }
DEV void lds_bar() { asm volatile("s_waitcnt lgkmcnt(0)\n\ts_barrier" ::: "memory"); }
#define SB() __builtin_amdgcn_sched_barrier(0)
#define PIN(x) asm volatile("" : "+v"(x))

DEV u32 f2u(float v) { return __builtin_bit_cast(u32, v); }
DEV float u2f(u32 v) { return __builtin_bit_cast(float, v); }
DEV u32 pk_bf16(float a, float b) {
  f32x2 p = {a, b};
  return __builtin_bit_cast(u32, __builtin_convertvector(p, b16x2));
}
DEV u32x2 pk4(f32x4 v) { return u32x2{pk_bf16(v[0], v[1]), pk_bf16(v[2], v[3])}; }
DEV float bf_lo(u32 v) { return u2f(v << 16); }
DEV float bf_hi(u32 v) { return u2f(v & 0xffff0000u); }
DEV float dot2(u32 a, u32 b, float c) {
  return __builtin_amdgcn_fdot2_f32_bf16(__builtin_bit_cast(b16x2, a), __builtin_bit_cast(b16x2, b), c, false);
}
DEV f32x4 mfma16(u32x4 a, u32x4 b, f32x4 c) {
  return __builtin_amdgcn_mfma_f32_16x16x32_bf16(__builtin_bit_cast(s16x8, a), __builtin_bit_cast(s16x8, b), c, 0, 0, 0);
}
DEV f32x4 mfma16k16(u32x2 a, u32x2 b, f32x4 c) {
  return __builtin_amdgcn_mfma_f32_16x16x16bf16_1k(__builtin_bit_cast(s16x4, a), __builtin_bit_cast(s16x4, b), c, 0, 0, 0);
}
DEV f32x16 mfma32(u32x4 a, u32x4 b, f32x16 c) {
  return __builtin_amdgcn_mfma_f32_32x32x16_bf16(__builtin_bit_cast(s16x8, a), __builtin_bit_cast(s16x8, b), c, 0, 0, 0);
}
DEV u32x4 lds128(LAS char* L, u32 off) { return *(LAS u32x4*)(L + off); }
DEV f32x4 ldsf4(LAS char* L, u32 off) { return *(LAS f32x4*)(L + off); }
DEV float ldsf(LAS char* L, u32 off) { return *(LAS float*)(L + off); }
DEV u32x2 ldstr(LAS char* L, u32 off) {
  return __builtin_bit_cast(u32x2, __builtin_amdgcn_ds_read_tr16_b64_v4i16((LAS s16x4*)(L + off)));
}
template <int CTRL> DEV float dpp_add(float v) {
  int t = __builtin_amdgcn_update_dpp(0, (int)f2u(v), CTRL, 0xf, 0xf, true);
  return v + u2f((u32)t);
}
DEV f32x2 swap16(float a, float b) {
  auto q = __builtin_amdgcn_permlane16_swap(f2u(a), f2u(b), false, false);
  return f32x2{u2f(q[0]), u2f(q[1])};
}
DEV f32x2 swap32(float a, float b) {
  auto q = __builtin_amdgcn_permlane32_swap(f2u(a), f2u(b), false, false);
  return f32x2{u2f(q[0]), u2f(q[1])};
}
DEV float vmax(float a, float b) { return __builtin_amdgcn_fmed3f(a, b, __builtin_inff()); }
DEV float sel(bool c, float a, float b) { return c ? a : b; }
DEV u32x4 pair_rows(u32x2 x, u32x2 y) {
  auto a = __builtin_amdgcn_permlane16_swap(x[0], y[0], false, false);
  auto b = __builtin_amdgcn_permlane16_swap(x[1], y[1], false, false);
  return u32x4{a[0], b[0], a[1], b[1]};
}
DEV f32x4 splat4(float v) { return f32x4{v, v, v, v}; }
DEV int fresh_tid() {
  int t = threadIdx.x;
  asm volatile("" : "+v"(t));
  return t;
}
DEV void st128(u32x4 v, __amdgpu_buffer_rsrc_t rs, u32 voff, u32 soff) {
  __builtin_amdgcn_raw_buffer_store_b128(v, rs, voff, soff, AUX_SC1);
  asm volatile("s_nop 1" ::"v"(v));
}
template <int V> struct IC { static constexpr int v = V; };
template <int LO, int HI, class F> DEV void sfor(F&& f) {
  if constexpr (LO < HI) { f(IC<LO>{}); sfor<LO + 1, HI>(f); }
}

struct Ptrs { const char *x, *wq, *wo, *w1; const u16* b1; const char *w2, *b2; char *out, *ws; };
struct Ring {
  u32 voffA, voffB, voffB2, stepB, mlo, xcond, wb;
  int rot, ldw, maxch;
  const char *sA0, *sA1, *sB0, *sB1, *sB2;
  DEV void lanes(int lane) {
    const int r8 = lane >> 3, pos = lane & 7;
    const int swB = (r8 & 2) | ((wb >> 8) & 4);
    voffA = r8 * LDX + ((pos ^ r8) << 4);
    voffB = r8 * ldw + ((pos ^ swB) << 4);
    voffB2 = r8 * ldw + (min(pos ^ swB, maxch) << 4);
  }
  template <int I, int K> DEV void issue() const {
    const int kc = (I + rot) & 7;
    constexpr int slot = (I % 3) * SLOT;
    if constexpr (K == 0) dma16_masked<slot>(voffA + kc * 128, sA0, wb, mlo);
    if constexpr (K == 1) dma16<slot + BOFF>(voffB + kc * stepB, sB0, wb);
    if constexpr (K == 2) dma16<slot + BOFF + 8192>(voffB + kc * stepB, sB1, wb);
    if constexpr (K == 3) dma16<slot + BOFF + 16384>(voffB2 + kc * stepB, sB2, wb);
    if constexpr (K == 4) dma16<slot + 8192>(voffA + kc * 128, sA1, wb);
    if constexpr (K == 5) dma16_if<slot + 12288>(voffA + kc * 128 + 32 * LDX, sA1, wb, xcond);
  }
};

template <int TYPE>
DEV void body(LAS char* L, const Ptrs P, Ring R, int g, int j, int w STAMP_ARG) {
  constexpr bool FFN = TYPE >= 2;
  constexpr int ODD = TYPE & 1;
  constexpr int NT = TYPE == 0 ? 5 : (TYPE == 1 ? 4 : 2);
  constexpr int NC = TYPE == 2 ? 6 : (TYPE == 3 ? 5 : 3);
  constexpr int CB = TYPE == 3 ? 6 : 0;
  constexpr int ND = TYPE == 1 ? 6 : 5;
  constexpr int NM = NT * NC + (FFN ? 2 : 0);
  constexpr int NRL = FFN ? 2 : 4;
  int tid = fresh_tid();
  int lane = tid & 63;
  R.lanes(lane);
  int lg = lane >> 4, lc = lane & 15;
  GAS u64* ctl = (GAS u64*)(P.ws + g * WS_GRP);
  u32x4 r0, r1, r2;
  u64 epoch;
  const int T0 = TYPE == 0 ? 0 : (TYPE == 1 ? 5 : 2 * (w & 3));
  const u32 aA = T0 * 2048 + (lc >> 3) * 1024 + (lc & 7) * 128 + ((lg ^ (lc & 7)) << 4);
  const int tq = lc >> 2, tp = lc & 3;
  const int swR = (tq & 2) | ((lg & 1) << 2);
  const u32 aBl = BOFF + lg * 1024 + tq * 128 + ((tp >> 1) << 4) + (tp & 1) * 8;
  const int crot = min(2 * (w & 3), NC - 2);
  constexpr int NBB = FFN ? NC : 1;
  u32 aB[NBB][2], aAb[2][2];
#pragma unroll
  for (int c = 0; c < NBB; ++c) {
    const int cc = c + crot >= NC ? c + crot - NC : c + crot;
    const int ct = FFN ? CB + cc : (w & 3);
    aB[c][0] = aBl + (ct >> 2) * 8192 + (((2 * (ct & 3)) ^ swR) << 4);
    aB[c][1] = aB[c][0] + 65536;
    asm("" : "+v"(aB[c][0]), "+v"(aB[c][1]));
  }
#pragma unroll
  for (int b = 0; b < 2; ++b) {
    aAb[b][0] = aA ^ (b * 64);
    aAb[b][1] = aAb[b][0] + 65536;
    asm("" : "+v"(aAb[b][0]), "+v"(aAb[b][1]));
  }
  f32x4 acc[NT][NC], acc1[2];
#pragma unroll
  for (int t = 0; t < NT; ++t)
#pragma unroll
    for (int c = 0; c < NC; ++c) acc[t][c] = f32x4{0.f, 0.f, 0.f, 0.f};
  acc1[0] = acc1[1] = f32x4{0.f, 0.f, 0.f, 0.f};
  u32x4 fa[2][NT];
  u32x2 fb[2][NC][2];
  auto rd = [&](int t, int k) INL {
    const int slot = ((t >> 1) % 3) * SLOT, b = t & 1;
    if (k >= 2 && k < 2 + NT) {
      const int o = slot + (k - 2) * 2048, hi = o >= 65536;
      fa[b][k - 2] = lds128(L, aAb[b][hi] + (o - hi * 65536));
    } else {
      const int kk = k < 2 ? k : k - NT, c = kk >> 1;
      const int o = slot + b * 4096 + (kk & 1) * 512 + (FFN ? 0 : c * 8192), hi = o >= 65536;
      fb[b][c][kk & 1] = ldstr(L, aB[FFN ? c : 0][hi] + (o - hi * 65536));
    }
  };
  const int mt = 2 * g + ((j >> 2) & 1), nt = (j & 3) + (j >> 3) * 4;
  const int ks0 = w < 6 ? 15 * w : 90 + 14 * (w - 6), nks = w < 6 ? 15 : 14;
  const u32 voffW = (lane >> 3) * LDW2 + (((lane & 7) ^ ((lane >> 2) & 4)) << 4) + nt * 128;
  const u64 woa = (u64)P.wo, w2a = (u64)P.w2 - 32 * 16 * LDW2;
  const u32 wx0 = w < 5 ? 2 * SLOT + (16 + w) * 2048 : (11 + w) * 2048;
  const u32 wx1 = w < 5 ? SLOT + (16 + w) * 2048 : (w < 7 ? (14 + w) * 2048 : L_SP + 8 * 2048);
  auto wbase = [&](int s) INL -> u32 { const int k = s % 9; return k == 0 ? 2 * R.wb : (k == 5 ? wx0 : (k == 8 ? wx1 : 4 * R.wb)); };
  auto wdma = [&](auto sc_, auto hc_) INL {
    constexpr int s = decltype(sc_)::v, h = decltype(hc_)::v, k = s % 9;
    constexpr int imm = k == 0 ? L_SP : (k <= 2 ? 2 * SLOT + (k - 1) * 2048 : (k <= 4 ? (k - 3) * 2048 : (k == 6 || k == 7 ? SLOT + (k - 6) * 2048 : 0)));
    const int ks = min(ks0 + s, NKS - 1);
    const u64 base = (ks < 32 ? woa : w2a) + (u32)(ks * 16 * LDW2);
    dma16<imm + h * 1024>(voffW + h * 8 * LDW2, (const void*)base, wbase(s));
  };
  u32x4 wf[15][2];
  const u32 tb = (lane >> 5) * 1024 + tq * 128 + (((2 * (lg & 1) + (tp >> 1)) ^ ((tq & 2) << 1)) << 4) + (tp & 1) * 8;
  const __amdgpu_buffer_rsrc_t rsA2 = __builtin_amdgcn_make_buffer_rsrc(P.ws + WS_A2, 0, A2_BYTES, 0x00020000);
  const __amdgpu_buffer_rsrc_t rsG = __builtin_amdgcn_make_buffer_rsrc(P.ws + g * WS_GRP, 0, WS_GRP, 0x00020000);
  const u32x4 ones = {0x3F803F80u, 0x3F803F80u, 0x3F803F80u, 0x3F803F80u};
  u64 tag = 0;
  u32 b1v = 0;
  u32x4 gst;
  u64 tp2 = 0;
  u32 gsto = 0, gldo = 0, b1o = 0, stw = 0, b1w = 0, csw = 0, csw2 = 0;
  float som = 0.f, sor = 0.f;
  f32x4 mP, mP0, mC;
  u32 sel0 = 0, sel1 = 0, wra[4][2];
  auto wread = [&](int s) INL {
    const int k = s % 9;
#pragma unroll
    for (int f = 0; f < 2; ++f) {
      const u32 a = k == 0 ? wra[0][f] : (k <= 2 ? wra[2][f] + (k - 1) * 2048 : (k <= 4 ? wra[1][f] + (k - 3) * 2048 :
                    (k == 6 || k == 7 ? wra[1][f] + SLOT + (k - 6) * 2048 : wra[1][f] - 4 * R.wb + wbase(s))));
      const u32x2 lo = ldstr(L, a), hi = ldstr(L, a + 512);
      wf[s][f] = u32x4{lo[0], lo[1], hi[0], hi[1]};
    }
  };
  constexpr int NK = 5, TK = TYPE == 0 ? 0 : 4;
  float mc1[NK], rc1[NK];
  f32x4 m4[NK], r4[NK], bb[NC];
  auto rdst = [&](int k) INL {
    if (FFN) {
      if (k < 2) mc1[k] = ldsf(L, L_MEAN + ((2 * (w & 3) + k) * 16 + lc) * 4);
      else if (k < 4) rc1[k - 2] = ldsf(L, L_RSTD + ((2 * (w & 3) + k - 2) * 16 + lc) * 4);
      else if (k < 4 + NC) {
        const int c = k - 4, ct = CB + (c + crot >= NC ? c + crot - NC : c + crot);
        bb[c] = ldsf4(L, L_B1 + (ct * 16 + lg * 4) * 4);
      }
    } else {
      const int i = k >> 2, T = TK + i;
      if (i >= NK) return;
      if ((k & 3) == 0) mc1[i] = ldsf(L, L_MEAN + (T * 16 + lc) * 4);
      if ((k & 3) == 1) rc1[i] = ldsf(L, L_RSTD + (T * 16 + lc) * 4);
      if ((k & 3) == 2) m4[i] = ldsf4(L, L_MEAN + (T * 16 + lg * 4) * 4);
      if ((k & 3) == 3) r4[i] = ldsf4(L, L_RSTD + (T * 16 + lg * 4) * 4);
    }
  };
      {
        const int b_ = blockIdx.x, ln = min(b_ * 59 + w * 8 + (lane & 7), 15103);
        tp2 = (ln < 4096 ? woa : w2a) + (u32)(ln * 128);
        const u64 pa = (u64)P.ws + (u32)(WS_A2 + (lane & 3) * (2u << 20) + (b_ & 63) * 4096), po = (u64)P.out + (u32)(b_ * 8192);
        tp2 = (lane >= 8 && lane < 12) ? pa : tp2;
        tp2 = lane == 12 ? po : tp2;
        gsto = (w == 7 && lane < 8) ? WS_STAT + (j * 8 + lane) * 16 : WS_GRP;
        gldo = WS_STAT + (tid & 127) * 16;
        b1o = 2 * (u32)min(max(j - 8, 0) * 176 + tid, 1375);
        stw = (!ODD && w < 2) ? L_MEAN + (tid + (FFN ? 0 : 16)) * 4 : L_JUNK + lane * 4;
        b1w = tid < 192 ? L_B1 + tid * 4 : L_JUNK + lane * 4;
        csw = lc == 0 ? L_CS + ((FFN ? CB + crot : (w & 3)) * 16 + lg * 4) * 4 : L_JUNK + lane * 16;
        csw2 = lg == 0 ? L_CS + (128 + (w & 3) * 16 + lc) * 4 : L_JUNK + lane * 4;
        if (!FFN) {
#pragma unroll
          for (int r = 0; r < 4; ++r) {
            mP[r] = (4 * lg + r >= lc + 2) ? 0.f : -1e30f;
            mP0[r] = (TYPE == 0 && g == 0) ? -1e30f : mP[r];
            mC[r] = (4 * lg + r <= lc) ? 0.f : -1e30f;
          }
          sel0 = (lc & 1) ? 0x01000c0cu : 0x0c0c0100u; sel1 = (lc & 1) ? 0x03020c0cu : 0x0c0c0302u;
          asm volatile("" : "+v"(mP), "+v"(mC), "+v"(sel0), "+v"(sel1));
          if (TYPE == 0) asm volatile("" : "+v"(mP0));
        }
#pragma unroll
        for (int f = 0; f < 2; ++f) {
          wra[0][f] = (tb ^ (f * 64)) + 2 * R.wb + L_SP;
          wra[1][f] = (tb ^ (f * 64)) + 4 * R.wb;
          wra[2][f] = (tb ^ (f * 64)) + 4 * R.wb + 2 * SLOT;
          asm volatile("" : "+v"(wra[0][f]), "+v"(wra[1][f]), "+v"(wra[2][f]));
        }
        asm volatile("" : "+v"(tp2), "+v"(gsto), "+v"(gldo), "+v"(b1o), "+v"(stw), "+v"(b1w), "+v"(csw), "+v"(csw2));
      }
  SB();
  sfor<0, ND>([&](auto kc) INL { R.template issue<1, decltype(kc)::v>(); });
  if (FFN)
    asm volatile("global_load_dwordx2 %0, %2, %3 sc1\n\tglobal_load_dwordx4 %1, %4, %5"
                 : "=&v"(epoch), "=&v"(r0) : "v"(0u), "s"(ctl), "v"(lane * 16), "s"(P.x + (size_t)(128 * g + 8 * j + w) * LDX) : "memory");
  else
    asm volatile("global_load_dwordx2 %0, %4, %5 sc1\n\tglobal_load_dwordx4 %1, %6, %7\n\t"
                 "global_load_dwordx4 %2, %6, %8\n\tglobal_load_dwordx4 %3, %6, %8 offset:1024"
                 : "=&v"(epoch), "=&v"(r0), "=&v"(r1), "=&v"(r2)
                 : "v"(0u), "s"(ctl), "v"(lane * 16), "s"(P.x + (size_t)(128 * g + 8 * j + w) * LDX),
                   "s"(P.x + (size_t)max(128 * g - 16 + 2 * w, 0) * LDX) : "memory");
  STAMP(1);
  wait_bar<5 + NRL>();
  STAMP(2);
#pragma unroll
  for (int k = 0; k < 2 * NC + NT; ++k) rd(0, k);
  SB();
  sfor<0, 16>([&](auto tc) INL {
    constexpr int t = decltype(tc)::v, s = t >> 1, b = t & 1;
    if constexpr (b) {
      if constexpr (s == 1 || s == 2) wait_bar<6 + ODD>();
      else if constexpr (s <= 4) wait_bar<5>();
      else if constexpr (s == 5) wait_bar<7>();
      else if constexpr (s == 6) wait_bar<4>();
    }
    if constexpr (t == 11) {
      if constexpr (!ODD) asm volatile("" : "+v"(gst), "+v"(b1v));
      if constexpr (!ODD) {
        if (__builtin_expect(!__all(gst[1] == (u32)tag && gst[3] == (u32)tag), 0)) {
#pragma nounroll
          for (u32 spins = 0; spins < SPIN_CAP; ++spins) {
            __builtin_amdgcn_s_sleep(1);
            gst = __builtin_amdgcn_raw_buffer_load_b128(rsG, gldo, 0, AUX_SC1);
            if (__all(gst[1] == (u32)tag && gst[3] == (u32)tag)) break;
          }
        }
      }
      SB();
    }
    constexpr int nr = t == 15 ? 0 : 2 * NC + NT;
    constexpr int st = t == 0 ? 2 : (t + 5) / 2;
    constexpr int k0 = (t == 0 || b) ? 0 : 3, k1 = st >= 8 ? 0 : ((t == 0 || !b) ? ND : 3);
    constexpr int nd = k1 > k0 ? k1 - k0 : 0, nw = t >= 10 ? 2 : 0, nv = nd + nw;
    sfor<0, NM>([&](auto qc) INL {
      constexpr int q = decltype(qc)::v;
      constexpr int qq = t < 15 ? q : (FFN ? (q + NT * NC) % NM : (TYPE == 1 ? q : (q < 3 ? q * NT : (q < 5 ? (q - 2) * NT + 4 :
                         (q - 5 < 4 ? q - 4 : (q - 5 < 7 ? q - 3 : q - 1))))));
      if constexpr (qq < NT * NC) {
        constexpr int c = qq / NT, i = qq % NT;
        const u32x4 wfr = {fb[b][c][0][0], fb[b][c][0][1], fb[b][c][1][0], fb[b][c][1][1]};
        if constexpr (!FFN && c == 2) acc[i][c] = mfma16(fa[b][i], wfr, acc[i][c]);
        else acc[i][c] = mfma16(wfr, fa[b][i], acc[i][c]);
        PIN(acc[i][c]);
      } else {
        constexpr int c = qq - NT * NC;
        const u32x4 wfr = {fb[b][c][0][0], fb[b][c][0][1], fb[b][c][1][0], fb[b][c][1][1]};
        acc1[c] = mfma16(wfr, ones, acc1[c]);
        PIN(acc1[c]);
      }
      if constexpr (q < nr) rd(t + 1, q);
      if constexpr (t == 15) { rdst(2 * q); rdst(2 * q + 1); }
      if constexpr (t == 9 && q == 0 && !ODD)
        asm volatile("buffer_load_dwordx4 %0, %2, %3, 0 offen sc1\n\tglobal_load_ushort %1, %4, %5"
                     : "=&v"(gst), "=&v"(b1v) : "v"(gldo), "s"(rsG), "v"(b1o), "s"(P.b1) : "memory");
      if constexpr (t == 11 && !ODD) {
        if constexpr (q == NM - 4) *(LAS u32*)(L + stw) = gst[0];
        if constexpr (q == NM - 3) *(LAS u32*)(L + stw + 576) = gst[2];
        if constexpr (q == NM - 2 && FFN) *(LAS float*)(L + b1w) = bf_lo(b1v);
      }
      if constexpr (t == 1 && ODD && q == NM - 1) { som = ldsf(L, L_OWN + (lane & 7) * 4); sor = ldsf(L, L_OWN + 576 + (lane & 7) * 4); }
      sfor<0, nv>([&](auto uc) INL {
        constexpr int u = decltype(uc)::v;
        if constexpr (((2 * u + ODD) * NM) / (2 * nv) == q) {
          if constexpr (u < nd) R.template issue<st, k0 + u>();
          else wdma(IC<t - 10>{}, IC<u - nd>{});
        }
      });
      if constexpr (t == 2 && q == NM - 1) {
        touch((const void*)tp2);
        if (ODD) st128(u32x4{f2u(som), (u32)tag, f2u(sor), (u32)tag}, rsG, gsto, 0);
      }
      if constexpr (t == 15) {
        if constexpr (TYPE == 0) {
          if constexpr (q == 6) *(LAS f32x4*)(L + csw) = acc[0][0];
          if constexpr (q == 7) *(LAS f32x4*)(L + csw + 256) = acc[0][1];
          if constexpr (q == 8) *(LAS float*)(L + csw2) = acc[0][2][0];
          if constexpr (q == 9) *(LAS f32x4*)(L + L_KV + 2 * R.wb + lane * 16) = acc[NT - 1][1];
          if constexpr (q == 10) *(LAS f32x4*)(L + L_KV + 2 * R.wb + 1024 + lane * 16) = acc[NT - 1][2];
        }
        if constexpr (FFN) {
          if constexpr (q == 6) *(LAS f32x4*)(L + csw) = acc1[0];
          if constexpr (q == 7) *(LAS f32x4*)(L + csw + 64) = acc1[1];
        }
      }
      SB();
    });
    if constexpr (t == 0) {
      if (FFN) asm volatile("s_waitcnt vmcnt(%2)" : "+v"(epoch), "+v"(r0) : "i"(5) : "memory");
      else asm volatile("s_waitcnt vmcnt(%4)" : "+v"(epoch), "+v"(r0), "+v"(r1), "+v"(r2) : "i"(5) : "memory");
      tag = (((u64)(u32)__builtin_amdgcn_readfirstlane((u32)(epoch >> 32)) << 32) | (u32)__builtin_amdgcn_readfirstlane((u32)epoch)) + 1;
      float sS[3] = {0.f, 0.f, 0.f}, sQ[3] = {0.f, 0.f, 0.f};
#pragma unroll
      for (int d = 0; d < 4; ++d) {
        sS[0] = dot2(r0[d], 0x3F803F80u, sS[0]); sQ[0] = dot2(r0[d], r0[d], sQ[0]);
        if (!FFN) {
          sS[1] = dot2(r1[d], 0x3F803F80u, sS[1]); sQ[1] = dot2(r1[d], r1[d], sQ[1]);
          sS[2] = dot2(r2[d], 0x3F803F80u, sS[2]); sQ[2] = dot2(r2[d], r2[d], sQ[2]);
        }
      }
      f32x2 a = swap32(sS[0], sS[FFN ? 0 : 1]), c = swap32(sQ[0], sQ[FFN ? 0 : 1]);
      float S = a[0] + a[1], Q = c[0] + c[1], S2 = S, Q2 = Q;
      if (!FFN) {
        a = swap32(sS[2], sS[2]); c = swap32(sQ[2], sQ[2]);
        S2 = a[0] + a[1]; Q2 = c[0] + c[1];
      }
      a = swap16(S, S2); c = swap16(Q, Q2);
      S = a[0] + a[1]; Q = c[0] + c[1];
      S = dpp_add<0xB1>(S); Q = dpp_add<0xB1>(Q);
      S = dpp_add<0x4E>(S); Q = dpp_add<0x4E>(Q);
      S = dpp_add<0x141>(S); Q = dpp_add<0x141>(Q);
      S = dpp_add<0x140>(S); Q = dpp_add<0x140>(Q);
      const float m = S * (1.f / 512.f), e = Q * (1.f / 512.f);
      u32 adr = L_JUNK + lane * 4;
      adr = lane == 0 ? L_OWN + w * 4 : adr;
      if (!FFN) { adr = lane == 32 ? L_MEAN + w * 8 : adr; adr = lane == 16 ? L_MEAN + w * 8 + 4 : adr; }
      *(LAS float*)(L + adr) = -m;
      *(LAS float*)(L + adr + 576) = __builtin_amdgcn_rsqf(fmaxf(e - m * m, 0.f) + 1e-5f);
      SB();
    }
  });
  STAMP(3);
  lds_bar();
  STAMP(4);
  auto slot = [&](auto kc) INL {
    constexpr int k = decltype(kc)::v;
    SB();
    if constexpr (k < NEPI) wdma(IC<6 + k / 2>{}, IC<k % 2>{});
    SB();
  };
  if constexpr (!FFN) {
    const int h = w & 3;
    const f32x4 csq = ldsf4(L, L_CS + (h * 16 + lg * 4) * 4), csk = ldsf4(L, L_CS + (64 + h * 16 + lg * 4) * 4);
    const float csv1 = ldsf(L, L_CS + (128 + h * 16 + lc) * 4);
    f32x4 k4 = acc[0][1], v4 = acc[0][2];
    if (TYPE == 1) { k4 = ldsf4(L, L_KV + h * 2048 + lane * 16); v4 = ldsf4(L, L_KV + h * 2048 + 1024 + lane * 16); }
    slot(IC<0>{});
    wait_vm<7>();
    wread(0); wread(1); wread(2);
    const bool odd = lc & 1;
    const f32x4 csv = splat4(csv1);
    u32x2 Qb[4], Kb[NK], Vb[NK];
    sfor<0, NK>([&](auto ic) INL {
      constexpr int i = decltype(ic)::v, ai = TYPE == 0 ? i : (i > 0 ? i - 1 : 0);
      slot(IC<1 + i>{});
      const f32x4 mc = splat4(mc1[i]);
      Kb[i] = pk4(((i == 0 ? k4 : acc[ai][1]) + mc * csk) * splat4(rc1[i]));
      if constexpr (i > 0) Qb[i - 1] = pk4((acc[ai][0] + mc * csq) * splat4(rc1[i] * (0.25f * 1.4426950408889634f)));
      Vb[i] = pk4(((i == 0 ? v4 : acc[ai][2]) + m4[i] * csv) * r4[i]);
    });
    f32x4 sp[4], sc[4];
#pragma unroll
    for (int q = 0; q < 4; ++q) {
      sp[q] = mfma16k16(Kb[q], Qb[q], q == 0 ? mP0 : mP);
      sc[q] = mfma16k16(Kb[q + 1], Qb[q], mC);
    }
    SB();
    wait_lds();
    wait_vm<6>();
    wread(3); wread(4); wread(5);
    slot(IC<6>{});
    f32x4 a[4];
    float mx[4];
#pragma unroll
    for (int q = 0; q < 4; ++q) {
      a[q] = f32x4{sel(odd, sp[q][1], sp[q][0]), sel(odd, sp[q][3], sp[q][2]), sel(odd, sc[q][1], sc[q][0]), sel(odd, sc[q][3], sc[q][2])};
      mx[q] = vmax(vmax(a[q][0], a[q][1]), vmax(a[q][2], a[q][3]));
    }
    slot(IC<7>{});
    {
      const f32x2 x = swap32(mx[0], mx[1]), y = swap32(mx[2], mx[3]);
      const f32x2 z = swap16(vmax(x[0], x[1]), vmax(y[0], y[1]));
      const float u = vmax(z[0], z[1]);
      const f32x2 p = swap16(u, u);
      const f32x2 p0 = swap32(p[0], p[0]), p1 = swap32(p[1], p[1]);
      mx[0] = p0[0]; mx[1] = p0[1]; mx[2] = p1[0]; mx[3] = p1[1];
    }
    slot(IC<8>{});
#pragma unroll
    for (int q = 0; q < 4; ++q) a[q] -= splat4(mx[q]);
    slot(IC<9>{});
    sfor<0, 4>([&](auto qc) INL {
      constexpr int q = decltype(qc)::v;
#pragma unroll
      for (int r = 0; r < 4; ++r) a[q][r] = __builtin_amdgcn_exp2f(a[q][r]);
      if constexpr (q & 1) slot(IC<10 + q / 2>{});
    });
    wait_lds();
    const f32x4 z4 = {0.f, 0.f, 0.f, 0.f};
    u32x4 pb[4];
    sfor<0, 4>([&](auto qc) INL {
      constexpr int q = decltype(qc)::v;
      const u32x2 u = pk4(a[q]);
      pb[q] = u32x4{__builtin_amdgcn_perm(0, u[0], sel0), __builtin_amdgcn_perm(0, u[0], sel1),
                    __builtin_amdgcn_perm(0, u[1], sel0), __builtin_amdgcn_perm(0, u[1], sel1)};
      if constexpr (q & 1) slot(IC<12 + q / 2>{});
    });
#pragma unroll
    for (int q = 0; q < 4; ++q) {
      sc[q] = mfma16(ones, pb[q], z4);
      sp[q] = mfma16k16(Vb[q], u32x2{pb[q][0], pb[q][1]}, z4);
    }
#pragma unroll
    for (int q = 0; q < 4; ++q) sp[q] = mfma16k16(Vb[q + 1], u32x2{pb[q][2], pb[q][3]}, sp[q]);
    slot(IC<14>{}); slot(IC<15>{}); slot(IC<16>{}); slot(IC<17>{});
    u32x2 Ob[4];
#pragma unroll
    for (int q = 0; q < 4; ++q) Ob[q] = pk4(sp[q] * splat4(__builtin_amdgcn_rcpf(sc[q][0])));
    const int rt = 4 * g + (TYPE == 0 ? 0 : 2), ks = 4 * j + h;
    st128(pair_rows(Ob[0], Ob[1]), rsA2, lane * 16, (rt * NKS + ks) * 1024);
    st128(pair_rows(Ob[2], Ob[3]), rsA2, lane * 16, ((rt + 1) * NKS + ks) * 1024);
  } else {
    f32x4 cs[NC];
#pragma unroll
    for (int c = 0; c < NC; ++c) {
      const int ct = CB + (c + crot >= NC ? c + crot - NC : c + crot);
      cs[c] = ldsf4(L, L_CS + (ct * 16 + lg * 4) * 4);
    }
    wait_vm<6>();
    wread(0); wread(1); wread(2);
    const f32x4 mc[2] = {splat4(mc1[0]), splat4(mc1[1])}, rc[2] = {splat4(rc1[0]), splat4(rc1[1])};
    const int nct = j == 15 ? 9 : 11;
    const int rt = 4 * g + (w & 3), ksb = 32 + 11 * (j - 8);
    const f32x4 C1 = splat4(-2.f * 1.4426950408889634f * 0.7978845608028654f);
    const f32x4 C2 = splat4(-2.f * 1.4426950408889634f * 0.7978845608028654f * 0.044715f);
    const f32x4 ONE = splat4(1.f);
    sfor<0, 3>([&](auto gc) INL {
      constexpr int G = decltype(gc)::v, c0 = 2 * G, n = c0 + 1 < NC ? 4 : 2;
      if constexpr (G == 1) { wait_lds(); wait_vm<8>(); wread(3); wread(4); wread(5); }
      if constexpr (G == 2) wait_lds();
      f32x4 v[4], e[4];
      slot(IC<6 * G>{});
#pragma unroll
      for (int u = 0; u < n; ++u) v[u] = (acc[u & 1][c0 + (u >> 1)] + mc[u & 1] * cs[c0 + (u >> 1)]) * rc[u & 1] + bb[c0 + (u >> 1)];
      slot(IC<6 * G + 1>{});
#pragma unroll
      for (int u = 0; u < n; ++u) e[u] = v[u] * (v[u] * v[u] * C2 + C1);
      slot(IC<6 * G + 2>{});
#pragma unroll
      for (int u = 0; u < n; ++u) {
#pragma unroll
        for (int r = 0; r < 4; ++r) e[u][r] = __builtin_amdgcn_exp2f(e[u][r]);
        if (u == 1) slot(IC<6 * G + 3>{});
      }
#pragma unroll
      for (int u = 0; u < n; ++u) e[u] += ONE;
      slot(IC<6 * G + 4>{});
#pragma unroll
      for (int u = 0; u < n; ++u) {
#pragma unroll
        for (int r = 0; r < 4; ++r) e[u][r] = __builtin_amdgcn_rcpf(e[u][r]);
        if (u == 1) slot(IC<6 * G + 5>{});
      }
#pragma unroll
      for (int cc = 0; cc < n / 2; ++cc) {
        const int c = c0 + cc, ct = CB + (c + crot >= NC ? c + crot - NC : c + crot);
        st128(pair_rows(pk4(v[2 * cc] * e[2 * cc]), pk4(v[2 * cc + 1] * e[2 * cc + 1])), rsA2, lane * 16,
              ct < nct ? (rt * NKS + ksb + ct) * 1024 : A2_BYTES);
      }
      SB();
    });
  }
  STAMP(5);
  wait_vm<0>();
  STAMP(13);
  asm volatile("s_barrier" ::: "memory");
  if (tid == 0) __hip_atomic_store(ctl + 16 * (j + 1), tag, __ATOMIC_RELAXED, __HIP_MEMORY_SCOPE_AGENT);
  STAMP(6);
  sfor<NEPI, 18>([&](auto kc) INL { wdma(IC<6 + decltype(kc)::v / 2>{}, IC<decltype(kc)::v % 2>{}); });
#pragma unroll
  for (int s = 6; s < 9; ++s) wread(s);
  tid = fresh_tid(); lane = tid & 63; lg = lane >> 4; lc = lane & 15;
  const u32 ooff = (u32)((mt * 64 + 4 * w + ((lane >> 3) & 3)) * LDX + (nt * 64 + (lane & 7) * 8) * 2);
  f32x4 xb0, xb1;
#pragma unroll
  for (int s = 0; s < 9; ++s) asm volatile("" : "+v"(wf[s][0]), "+v"(wf[s][1]));
  STAMP(7);
  u64 v0, v1, v2, v3;
  wait_vm<0>();
  {
    const u32 fo = 128 * (lc + 1);
    u32 spins = SPIN_CAP;
    asm volatile(
        "s_cmp_lg_u32 %9, %10\n\ts_cbranch_scc1 2f\n\t"
        "global_load_dwordx2 %0, %5, %6 sc1\n\ts_sleep %8\n\tglobal_load_dwordx2 %1, %5, %6 sc1\n\ts_sleep %8\n\t"
        "global_load_dwordx2 %2, %5, %6 sc1\n\ts_sleep %8\n"
        "1:\n\t"
        "global_load_dwordx2 %3, %5, %6 sc1\n\ts_waitcnt vmcnt(3)\n\tv_cmp_ne_u64 vcc, %7, %0\n\ts_cbranch_vccz 2f\n\ts_sleep %8\n\t"
        "global_load_dwordx2 %0, %5, %6 sc1\n\ts_waitcnt vmcnt(3)\n\tv_cmp_ne_u64 vcc, %7, %1\n\ts_cbranch_vccz 2f\n\ts_sleep %8\n\t"
        "global_load_dwordx2 %1, %5, %6 sc1\n\ts_waitcnt vmcnt(3)\n\tv_cmp_ne_u64 vcc, %7, %2\n\ts_cbranch_vccz 2f\n\ts_sleep %8\n\t"
        "global_load_dwordx2 %2, %5, %6 sc1\n\ts_waitcnt vmcnt(3)\n\tv_cmp_ne_u64 vcc, %7, %3\n\ts_cbranch_vccz 2f\n\ts_sleep %8\n\t"
        "s_sub_u32 %4, %4, 1\n\ts_cmp_lg_u32 %4, 0\n\ts_cbranch_scc1 1b\n"
        "2:\n\ts_barrier"
        : "=&v"(v0), "=&v"(v1), "=&v"(v2), "=&v"(v3), "+s"(spins)
        : "v"(fo), "s"(ctl), "s"(tag), "i"(PGAP), "s"(w), "i"(7) : "memory", "vcc", "scc");
  }
  STAMP(8);
  u32x4 af[2][15];
  auto lda = [&](int r, int s) INL {
    const int so = (s < nks) ? ((2 * mt + r) * NKS + ks0 + s) * 1024 : A2_BYTES;
    af[r][s] = __builtin_amdgcn_raw_buffer_load_b128(rsA2, lane * 16, so, AUX_SC1);
  };
#pragma unroll
  for (int n = 0; n < WIN; ++n) lda(n / 15, n % 15);
  SB();
  u32 pw[4];
#pragma unroll
  for (int y = 0; y < 4; ++y) pw[y] = ((lane + 4 * y) & 63) * 16 + y * 1024 + 4 * R.wb;
  const int blk = lane & 7;
  const u32 prd = (((lane & 32) + 4 * w + ((lane >> 3) & 3) + 4 * (blk & 3)) & 63) * 16 + (blk & 3) * 1024;
  const u32 prd0 = prd + (blk >> 2) * SLOT, prd1 = prd + (blk >> 2) * 4096 + 2 * SLOT;
  const __amdgpu_buffer_rsrc_t rsO = __builtin_amdgcn_make_buffer_rsrc(P.out, 0, 2048 * LDX, 0x00020000);
  auto reduce = [&](int r, f32x4 xb) INL {
    f32x4 p[8];
#pragma unroll
    for (int sw = 0; sw < 8; ++sw) p[sw] = ldsf4(L, r == 0 ? prd0 + sw * 4096 : prd1 + sw * 8192);
    SB();
    const u32x2 o = pk4(((p[0] + p[1]) + (p[2] + p[3])) + ((p[4] + p[5]) + (p[6] + p[7])) + xb);
    auto e0 = __builtin_amdgcn_permlane32_swap(o[0], o[0], false, false), e1 = __builtin_amdgcn_permlane32_swap(o[1], o[1], false, false);
    __builtin_amdgcn_raw_buffer_store_b128(u32x4{e0[0], e1[0], e0[1], e1[1]}, rsO, lane < 32 ? ooff + r * 32 * LDX : 2048 * LDX, 0, AUX_SC0 | AUX_SC1);
  };
  f32x16 pa[2][2];
  auto pwrite = [&](int r, int b8) INL {
    const f32x16& a = pa[r][b8 >> 2];
    const int y = b8 & 3;
    *(LAS f32x4*)(L + pw[y] + (r == 0 ? (b8 >> 2) * SLOT : 2 * SLOT + 4 * R.wb + (b8 >> 2) * 4096)) =
        f32x4{a[4 * y], a[4 * y + 1], a[4 * y + 2], a[4 * y + 3]};
  };
  u32x2 xr0, xr1, bres;
  sfor<0, 2>([&](auto rc_) INL {
    constexpr int r = decltype(rc_)::v;
#pragma unroll
    for (int i = 0; i < 16; ++i) pa[r][0][i] = pa[r][1][i] = 0.f;
    sfor<0, 15>([&](auto sc_) INL {
      constexpr int s = decltype(sc_)::v;
      pa[r][0] = mfma32(wf[s][0], af[r][s], pa[r][0]);
      PIN(pa[r][0]);
      if constexpr (r == 1 && s >= 1 && s < 5) pwrite(0, 2 * s - 2);
      pa[r][1] = mfma32(wf[s][1], af[r][s], pa[r][1]);
      PIN(pa[r][1]);
      if constexpr (r == 1 && s >= 1 && s < 5) pwrite(0, 2 * s - 1);
      if constexpr (r == 0 && s == 2) asm volatile("" ::"v"(v0), "v"(v1), "v"(v2), "v"(v3));
      if constexpr (r == 0 && s == 0) STAMP(14);
      if constexpr (r == 0 && s == 14) STAMP(15);
      if constexpr (r == 0 && s == 1) {
        const u32 o4 = ooff + (lane >> 5) * 8;
        xr0 = *(const GAS u32x2*)(P.x + o4); xr1 = *(const GAS u32x2*)(P.x + (o4 + 32 * LDX));
        bres = *(const GAS u32x2*)(P.b2 + (o4 & 1023));
      }
      if constexpr (r * 15 + s + WIN < 30) lda((r * 15 + s + WIN) / 15, (r * 15 + s + WIN) % 15);
      if constexpr (r == 0 && s >= 7 && s < 13) wread(s + 2);
      if constexpr (r == 0 && s == 14) {
        const f32x4 bv = {bf_lo(bres[0]), bf_hi(bres[0]), bf_lo(bres[1]), bf_hi(bres[1])};
        xb0 = f32x4{bf_lo(xr0[0]), bf_hi(xr0[0]), bf_lo(xr0[1]), bf_hi(xr0[1])} + bv;
        xb1 = f32x4{bf_lo(xr1[0]), bf_hi(xr1[0]), bf_lo(xr1[1]), bf_hi(xr1[1])} + bv;
        PIN(xb0); PIN(xb1);
      }
      if constexpr (r == 1 && s == RMID) { lds_bar(); reduce(0, xb0); }
      SB();
    });
  });
#pragma unroll
  for (int y = 0; y < 8; ++y) pwrite(1, y);
  STAMP(9);
  lds_bar();
  reduce(1, xb1);
  if (j == 0 && tid == 0) __hip_atomic_store(ctl, tag, __ATOMIC_RELAXED, __HIP_MEMORY_SCOPE_AGENT);
  STAMP(10);
  STAMP_OUT();
  asm volatile(".Lfk_end%c0:" ::"i"(TYPE));
  __builtin_amdgcn_endpgm();
}

__global__ __launch_bounds__(512, 2) void fused(const char*, const char*, const char*, const char*, const u16*, const char*, const char*,
                                                char*, char*) {
  extern __shared__ __attribute__((aligned(16))) char smem[];
  LAS char* L = (LAS char*)smem;
  STAMP(0);
  typedef u64 u64x8 __attribute__((ext_vector_type(8)));
  u64x8 ka;
  u64 kb;
  asm volatile("s_load_dwordx16 %0, %2, 0x0\n\ts_load_dwordx2 %1, %2, 0x40"
               : "=&s"(ka), "=&s"(kb) : "s"(__builtin_amdgcn_kernarg_segment_ptr()) : "memory");
  const int lane = threadIdx.x & 63, w = __builtin_amdgcn_readfirstlane(threadIdx.x >> 6);
  const int b = blockIdx.x, bi = b >> 3;
  const int g = (b & 3) * 4 + (bi & 3), j = ((bi >> 2) & 3) + ((b >> 2) & 1) * 4 + (bi >> 4) * 8;
  const bool ffn = j >= 8;
  if (w == 7) {
    u64 pc;
    u32 len;
    asm volatile("s_getpc_b64 %0\n.Lfk_here:\n\ts_mov_b32 %1, .Lfk_end0-.Lfk_here\n\ts_max_u32 %1, %1, .Lfk_end1-.Lfk_here\n\t"
                 "s_max_u32 %1, %1, .Lfk_end2-.Lfk_here\n\ts_max_u32 %1, %1, .Lfk_end3-.Lfk_here" : "=s"(pc), "=s"(len)::"scc");
    touch((const char*)pc + min((u32)(lane * 128 + (bi & 7) * 8192), len));
  }
  const int rowbase = ffn ? 128 * g : 128 * g - 16;
  Ring R;
  R.ldw = ffn ? LDW1 : LDQKV;
  R.maxch = ffn ? (j == 15 ? 1 : 5) : 7;
  R.wb = w * 1024;
  R.rot = (j + g) & 7;
  R.lanes(lane);
  R.stepB = 64 * R.ldw;
  R.mlo = (!ffn && w == 0) ? 0xFFFFFF00u : 0xFFFFFFFFu;
  R.xcond = ffn ? 2u : (u32)(w - 4);
  const u32 oA0 = (rowbase + 8 * w < 0 ? 8 * w : rowbase + 8 * w) * LDX, oA1 = (rowbase + 8 * (w + 8)) * LDX;
  const u32 oB = 8 * w * R.ldw + (ffn ? (j - 8) * 352 : j * 128), seg = ffn ? 128 : 1024;
  asm volatile("s_waitcnt lgkmcnt(0)" : "+s"(ka), "+s"(kb)::"memory");
  const Ptrs P = {(const char*)(GAS const char*)ka[0], (const char*)(GAS const char*)ka[1], (const char*)(GAS const char*)ka[2],
                  (const char*)(GAS const char*)ka[3], (const u16*)(GAS const u16*)ka[4], (const char*)(GAS const char*)ka[5],
                  (const char*)(GAS const char*)ka[6], (char*)(GAS char*)ka[7], (char*)(GAS char*)kb};
  STAMP(11);
  R.sA0 = P.x + oA0;
  R.sA1 = P.x + oA1;
  R.sB0 = (const char*)((ffn ? ka[3] : ka[1]) + oB);
  R.sB1 = R.sB0 + seg;
  R.sB2 = R.sB1 + seg;
  sfor<0, 6>([&](auto kc) INL { R.template issue<0, decltype(kc)::v>(); });
  if (!ffn && w == 0 && lane < 8) {
    const u32x4 one = {0x3F803F80u, 0x3F803F80u, 0x3F803F80u, 0x3F803F80u};
#pragma unroll
    for (int s = 0; s < 3; ++s) *(LAS u32x4*)(L + s * SLOT + lane * 16) = one;
  }
  STAMP(12);
  if (!ffn) {
    if (w < 4) body<0>(L, P, R, g, j, w STAMP_PASS);
    else body<1>(L, P, R, g, j, w STAMP_PASS);
  } else {
    if (w < 4) body<2>(L, P, R, g, j, w STAMP_PASS);
    else body<3>(L, P, R, g, j, w STAMP_PASS);
  }
}
}

extern "C" void kernel_launch(void* const* d_in, const int* in_sizes, int n_in, void* d_out, int out_size, void* d_ws,
                              size_t ws_size, hipStream_t stream) {
  (void)hipFuncSetAttribute((const void*)fk::fused, hipFuncAttributeMaxDynamicSharedMemorySize, fk::L_TOTAL);
  fk::fused<<<256, 512, fk::L_TOTAL, stream>>>((const char*)d_in[0], (const char*)d_in[1], (const char*)d_in[2], (const char*)d_in[4],
                                               (const u16*)d_in[5], (const char*)d_in[6], (const char*)d_in[7], (char*)d_out,
                                               (char*)d_ws);
}
```

```cpp
#include <hip/hip_runtime.h>
#include <stdint.h>

typedef unsigned u32;
typedef unsigned long long u64;
typedef unsigned short u16;
typedef short s16x4 __attribute__((ext_vector_type(4)));
typedef short s16x8 __attribute__((ext_vector_type(8)));
typedef __bf16 b16x2 __attribute__((ext_vector_type(2)));
typedef float f32x2 __attribute__((ext_vector_type(2)));
typedef float f32x4 __attribute__((ext_vector_type(4)));
typedef float f32x16 __attribute__((ext_vector_type(16)));
typedef u32 u32x2 __attribute__((ext_vector_type(2)));
typedef u32 u32x4 __attribute__((ext_vector_type(4)));
#define LAS __attribute__((address_space(3)))
#define GAS __attribute__((address_space(1)))
#define DEV __device__ __forceinline__
#define INL __attribute__((always_inline))

namespace fk {
constexpr int LDX = 1024, LDQKV = 3072, LDW1 = 2752, LDW2 = 1024;
constexpr int NKS = 118;
constexpr int SLOT = 43008, BOFF = 18432;
constexpr int L_MEAN = 3 * SLOT, L_RSTD = L_MEAN + 576, L_CS = L_RSTD + 576, L_B1 = L_CS + 768, L_OWN = L_B1 + 768,
              L_KV = L_MEAN + 3584, L_JUNK = L_KV + 8192, L_SP = L_JUNK + 2048, L_TOTAL = L_SP + 9 * 2048;
static_assert(L_OWN + 576 + 32 <= L_KV && L_TOTAL <= 163840, "LDS map");
constexpr int WS_GRP = 8192, WS_STAT = 4096;
constexpr int WS_A2 = 16 * WS_GRP, A2_BYTES = 64 * NKS * 1024;
constexpr int AUX_SC0 = 1, AUX_SC1 = 16;
constexpr u32 SPIN_CAP = 1u << 16;

constexpr int NEPI = 12;
constexpr int TT = 4;
constexpr int WIN = 5;
constexpr int RMID = 11;
constexpr int PGAP = 2;

template <int IMM> DEV void dma16(u32 voff, const void* sbase, u32 lb) {
  asm volatile("s_add_u32 m0, %2, %3\n\ts_nop 0\n\tglobal_load_lds_dwordx4 %0, %1" ::"v"(voff), "s"(sbase), "s"(lb), "i"(IMM) : "memory", "scc");
}
template <int IMM> DEV void dma16_masked(u32 voff, const void* sbase, u32 lb, u32 mlo) {
  asm volatile("s_add_u32 m0, %2, %4\n\ts_mov_b32 exec_lo, %3\n\tglobal_load_lds_dwordx4 %0, %1\n\ts_mov_b32 exec_lo, -1"
               ::"v"(voff), "s"(sbase), "s"(lb), "s"(mlo), "i"(IMM) : "memory", "scc");
}
template <int IMM> DEV void dma16_if(u32 voff, const void* sbase, u32 lb, u32 cond) {
  asm volatile("s_cmp_gt_u32 %3, 1\n\ts_cbranch_scc1 1f\n\ts_add_u32 m0, %2, %4\n\ts_nop 0\n\tglobal_load_lds_dwordx4 %0, %1\n1:"
               ::"v"(voff), "s"(sbase), "s"(lb), "s"(cond), "i"(IMM) : "memory", "scc");
}
DEV void touch(const void* p) {
  asm volatile("s_mov_b32 m0, %1\n\ts_nop 0\n\tglobal_load_lds_dword %0, off sc1" ::"v"(p), "i"(L_JUNK) : "memory");
}
template <int N> DEV void wait_bar() {
  asm volatile("s_waitcnt vmcnt(%0) lgkmcnt(0)\n\ts_barrier" ::"i"(N) : "memory");
}
template <int N> DEV void wait_vm() { asm volatile("s_waitcnt vmcnt(%0)" ::"i"(N) : "memory"); }
DEV void wait_lds() { asm volatile("s_waitcnt lgkmcnt(0)" ::: "memory"); }
DEV void lds_bar() { asm volatile("s_waitcnt lgkmcnt(0)\n\ts_barrier" ::: "memory"); }
#define SB() __builtin_amdgcn_sched_barrier(0)
#define PIN(x) asm volatile("" : "+v"(x))

DEV u32 f2u(float v) { return __builtin_bit_cast(u32, v); }
DEV float u2f(u32 v) { return __builtin_bit_cast(float, v); }
DEV u32 pk_bf16(float a, float b) {
  f32x2 p = {a, b};
  return __builtin_bit_cast(u32, __builtin_convertvector(p, b16x2));
}
DEV u32x2 pk4(f32x4 v) { return u32x2{pk_bf16(v[0], v[1]), pk_bf16(v[2], v[3])}; }
DEV float bf_lo(u32 v) { return u2f(v << 16); }
DEV float bf_hi(u32 v) { return u2f(v & 0xffff0000u); }
DEV float dot2(u32 a, u32 b, float c) {
  return __builtin_amdgcn_fdot2_f32_bf16(__builtin_bit_cast(b16x2, a), __builtin_bit_cast(b16x2, b), c, false);
}
DEV f32x4 mfma16(u32x4 a, u32x4 b, f32x4 c) {
  return __builtin_amdgcn_mfma_f32_16x16x32_bf16(__builtin_bit_cast(s16x8, a), __builtin_bit_cast(s16x8, b), c, 0, 0, 0);
}
DEV void mfma16_if(f32x4& acc, u32x4 a, u32x4 b, u32 cond) {
  asm volatile("s_cmp_lg_u32 %3, 0\n\ts_cbranch_scc1 1f\n\tv_mfma_f32_16x16x32_bf16 %0, %1, %2, %0\n1:" : "+v"(acc) : "v"(a), "v"(b), "s"(cond) : "scc");
}
DEV f32x4 mfma16k16(u32x2 a, u32x2 b, f32x4 c) {
  return __builtin_amdgcn_mfma_f32_16x16x16bf16_1k(__builtin_bit_cast(s16x4, a), __builtin_bit_cast(s16x4, b), c, 0, 0, 0);
}
DEV f32x16 mfma32(u32x4 a, u32x4 b, f32x16 c) {
  return __builtin_amdgcn_mfma_f32_32x32x16_bf16(__builtin_bit_cast(s16x8, a), __builtin_bit_cast(s16x8, b), c, 0, 0, 0);
}
DEV u32x4 lds128(LAS char* L, u32 off) { return *(LAS u32x4*)(L + off); }
DEV f32x4 ldsf4(LAS char* L, u32 off) { return *(LAS f32x4*)(L + off); }
DEV float ldsf(LAS char* L, u32 off) { return *(LAS float*)(L + off); }
DEV u32x2 ldstr(LAS char* L, u32 off) {
  return __builtin_bit_cast(u32x2, __builtin_amdgcn_ds_read_tr16_b64_v4i16((LAS s16x4*)(L + off)));
}
template <int CTRL> DEV float dpp_add(float v) {
  int t = __builtin_amdgcn_update_dpp(0, (int)f2u(v), CTRL, 0xf, 0xf, true);
  return v + u2f((u32)t);
}
DEV f32x2 swap16(float a, float b) {
  auto q = __builtin_amdgcn_permlane16_swap(f2u(a), f2u(b), false, false);
  return f32x2{u2f(q[0]), u2f(q[1])};
}
DEV f32x2 swap32(float a, float b) {
  auto q = __builtin_amdgcn_permlane32_swap(f2u(a), f2u(b), false, false);
  return f32x2{u2f(q[0]), u2f(q[1])};
}
DEV float vmax(float a, float b) { return __builtin_amdgcn_fmed3f(a, b, __builtin_inff()); }
DEV float sel(bool c, float a, float b) { return c ? a : b; }
DEV u32x4 pair_rows(u32x2 x, u32x2 y) {
  auto a = __builtin_amdgcn_permlane16_swap(x[0], y[0], false, false);
  auto b = __builtin_amdgcn_permlane16_swap(x[1], y[1], false, false);
  return u32x4{a[0], b[0], a[1], b[1]};
}
DEV f32x4 splat4(float v) { return f32x4{v, v, v, v}; }
DEV int fresh_tid() {
  int t = threadIdx.x;
  asm volatile("" : "+v"(t));
  return t;
}
DEV void st128(u32x4 v, __amdgpu_buffer_rsrc_t rs, u32 voff, u32 soff) {
  __builtin_amdgcn_raw_buffer_store_b128(v, rs, voff, soff, AUX_SC1);
  asm volatile("s_nop 1" ::"v"(v));
}
template <int V> struct IC { static constexpr int v = V; };
template <int LO, int HI, class F> DEV void sfor(F&& f) {
  if constexpr (LO < HI) { f(IC<LO>{}); sfor<LO + 1, HI>(f); }
}

struct Ptrs { const char *x, *wq, *wo, *w1; const u16* b1; const char *w2, *b2; char *out, *ws; };
struct Ring {
  u32 voffA, voffB, voffB2, stepB, mlo, xcond, wb;
  int rot, ldw, maxch;
  const char *sA0, *sA1, *sB0, *sB1, *sB2;
  DEV void lanes(int lane) {
    const int r8 = lane >> 3, pos = lane & 7;
    const int swB = (r8 & 2) | ((wb >> 8) & 4);
    voffA = r8 * LDX + ((pos ^ r8) << 4);
    voffB = r8 * ldw + ((pos ^ swB) << 4);
    voffB2 = r8 * ldw + (min(pos ^ swB, maxch) << 4);
  }
  template <int I, int K> DEV void issue() const {
    const int kc = (I + rot) & 7;
    constexpr int slot = (I % 3) * SLOT;
    if constexpr (K == 0) dma16_masked<slot>(voffA + kc * 128, sA0, wb, mlo);
    if constexpr (K == 1) dma16<slot + BOFF>(voffB + kc * stepB, sB0, wb);
    if constexpr (K == 2) dma16<slot + BOFF + 8192>(voffB + kc * stepB, sB1, wb);
    if constexpr (K == 3) dma16<slot + BOFF + 16384>(voffB2 + kc * stepB, sB2, wb);
    if constexpr (K == 4) dma16<slot + 8192>(voffA + kc * 128, sA1, wb);
    if constexpr (K == 5) dma16_if<slot + 12288>(voffA + kc * 128 + 32 * LDX, sA1, wb, xcond);
  }
};

template <int TYPE>
DEV void body(LAS char* L, const Ptrs P, Ring R, int g, int j, int w) {
  constexpr bool FFN = TYPE >= 2;
  constexpr int ODD = TYPE & 1;
  constexpr int NT = TYPE == 0 ? 5 : (TYPE == 1 ? 4 : 2);
  constexpr int NC = TYPE == 2 ? 6 : (TYPE == 3 ? 5 : 3);
  constexpr int CB = TYPE == 3 ? 6 : 0;
  constexpr int ND = TYPE == 1 ? 6 : 5;
  constexpr int NM = NT * NC + (FFN ? 2 : 0);
  constexpr int NRL = FFN ? 2 : 4;
  int tid = fresh_tid();
  int lane = tid & 63;
  R.lanes(lane);
  int lg = lane >> 4, lc = lane & 15;
  GAS u64* ctl = (GAS u64*)(P.ws + g * WS_GRP);
  u32x4 r0, r1, r2;
  u64 epoch;
  const int T0 = TYPE == 0 ? 0 : (TYPE == 1 ? 5 : 2 * (w & 3));
  const u32 aA = T0 * 2048 + (lc >> 3) * 1024 + (lc & 7) * 128 + ((lg ^ (lc & 7)) << 4);
  const int tq = lc >> 2, tp = lc & 3;
  const int swR = (tq & 2) | ((lg & 1) << 2);
  const u32 aBl = BOFF + lg * 1024 + tq * 128 + ((tp >> 1) << 4) + (tp & 1) * 8;
  const int crot = TYPE == 2 ? ((w & 3) < 2 ? 2 * (w & 3) : (w & 3) + 2) : ((w & 3) < 3 ? (w & 3) : 4);
  const u32 two = TYPE == 2 ? (u32)w >> 1 : (u32)w - 6;
  constexpr int NBB = FFN ? NC : 1;
  u32 aB[NBB][2], aAb[2][2];
#pragma unroll
  for (int c = 0; c < NBB; ++c) {
    const int cc = c + crot >= NC ? c + crot - NC : c + crot;
    const int ct = FFN ? CB + cc : (w & 3);
    aB[c][0] = aBl + (ct >> 2) * 8192 + (((2 * (ct & 3)) ^ swR) << 4);
    aB[c][1] = aB[c][0] + 65536;
    asm("" : "+v"(aB[c][0]), "+v"(aB[c][1]));
  }
#pragma unroll
  for (int b = 0; b < 2; ++b) {
    aAb[b][0] = aA ^ (b * 64);
    aAb[b][1] = aAb[b][0] + 65536;
    asm("" : "+v"(aAb[b][0]), "+v"(aAb[b][1]));
  }
  f32x4 acc[NT][NC], acc1[2];
#pragma unroll
  for (int t = 0; t < NT; ++t)
#pragma unroll
    for (int c = 0; c < NC; ++c) acc[t][c] = f32x4{0.f, 0.f, 0.f, 0.f};
  acc1[0] = acc1[1] = f32x4{0.f, 0.f, 0.f, 0.f};
  u32x4 fa[2][NT];
  u32x2 fb[2][NC][2];
  auto rd = [&](int t, int k) INL {
    const int slot = ((t >> 1) % 3) * SLOT, b = t & 1;
    if (k >= 2 && k < 2 + NT) {
      const int o = slot + (k - 2) * 2048, hi = o >= 65536;
      fa[b][k - 2] = lds128(L, aAb[b][hi] + (o - hi * 65536));
    } else {
      const int kk = k < 2 ? k : k - NT, c = kk >> 1;
      const int o = slot + b * 4096 + (kk & 1) * 512 + (FFN ? 0 : c * 8192), hi = o >= 65536;
      fb[b][c][kk & 1] = ldstr(L, aB[FFN ? c : 0][hi] + (o - hi * 65536));
    }
  };
  const int mt = 2 * g + ((j >> 2) & 1), nt = (j & 3) + (j >> 3) * 4;
  const int ks0 = w < 6 ? 15 * w : 90 + 14 * (w - 6), nks = w < 6 ? 15 : 14;
  const u32 voffW = (lane >> 3) * LDW2 + (((lane & 7) ^ ((lane >> 2) & 4)) << 4) + nt * 128;
  const u64 woa = (u64)P.wo, w2a = (u64)P.w2 - 32 * 16 * LDW2;
  const u32 wx0 = w < 5 ? 2 * SLOT + (16 + w) * 2048 : (11 + w) * 2048;
  const u32 wx1 = w < 5 ? SLOT + (16 + w) * 2048 : (w < 7 ? (14 + w) * 2048 : L_SP + 8 * 2048);
  auto wbase = [&](int s) INL -> u32 { const int k = s % 9; return k == 0 ? 2 * R.wb : (k == 5 ? wx0 : (k == 8 ? wx1 : 4 * R.wb)); };
  auto wdma = [&](auto sc_, auto hc_) INL {
    constexpr int s = decltype(sc_)::v, h = decltype(hc_)::v, k = s % 9;
    constexpr int imm = k == 0 ? L_SP : (k <= 2 ? 2 * SLOT + (k - 1) * 2048 : (k <= 4 ? (k - 3) * 2048 : (k == 6 || k == 7 ? SLOT + (k - 6) * 2048 : 0)));
    const int ks = min(ks0 + s, NKS - 1);
    const u64 base = (ks < 32 ? woa : w2a) + (u32)(ks * 16 * LDW2);
    dma16<imm + h * 1024>(voffW + h * 8 * LDW2, (const void*)base, wbase(s));
  };
  u32x4 wf[15][2];
  const u32 tb = (lane >> 5) * 1024 + tq * 128 + (((2 * (lg & 1) + (tp >> 1)) ^ ((tq & 2) << 1)) << 4) + (tp & 1) * 8;
  const __amdgpu_buffer_rsrc_t rsA2 = __builtin_amdgcn_make_buffer_rsrc(P.ws + WS_A2, 0, A2_BYTES, 0x00020000);
  const __amdgpu_buffer_rsrc_t rsG = __builtin_amdgcn_make_buffer_rsrc(P.ws + g * WS_GRP, 0, WS_GRP, 0x00020000);
  const u32x4 ones = {0x3F803F80u, 0x3F803F80u, 0x3F803F80u, 0x3F803F80u};
  u32x4 onesv = ones;
  if (FFN) { PIN(onesv); PIN(acc1[1]); }
  u64 tag = 0;
  u32 b1v = 0;
  u32x4 gst;
  u64 tp2 = 0;
  u32 gsto = 0, gldo = 0, b1o = 0, stw = 0, b1w = 0, csw = 0, csw2 = 0;
  float som = 0.f, sor = 0.f;
  f32x4 mP, mP0, mC;
  u32 sel0 = 0, sel1 = 0, wra[4][2];
  auto wread = [&](int s) INL {
    const int k = s % 9;
#pragma unroll
    for (int f = 0; f < 2; ++f) {
      const u32 a = k == 0 ? wra[0][f] : (k <= 2 ? wra[2][f] + (k - 1) * 2048 : (k <= 4 ? wra[1][f] + (k - 3) * 2048 :
                    (k == 6 || k == 7 ? wra[1][f] + SLOT + (k - 6) * 2048 : wra[1][f] - 4 * R.wb + wbase(s))));
      const u32x2 lo = ldstr(L, a), hi = ldstr(L, a + 512);
      wf[s][f] = u32x4{lo[0], lo[1], hi[0], hi[1]};
    }
  };
  constexpr int NK = 5, TK = TYPE == 0 ? 0 : 4;
  float mc1[NK], rc1[NK];
  f32x4 m4[NK], r4[NK], bb[NC];
  auto rdst = [&](int k) INL {
    if (FFN) {
      if (k < 2) mc1[k] = ldsf(L, L_MEAN + ((2 * (w & 3) + k) * 16 + lc) * 4);
      else if (k < 4) rc1[k - 2] = ldsf(L, L_RSTD + ((2 * (w & 3) + k - 2) * 16 + lc) * 4);
      else if (k < 4 + NC) {
        const int c = k - 4, ct = CB + (c + crot >= NC ? c + crot - NC : c + crot);
        bb[c] = ldsf4(L, L_B1 + (ct * 16 + lg * 4) * 4);
      }
    } else {
      const int i = k >> 2, T = TK + i;
      if (i >= NK) return;
      if ((k & 3) == 0) mc1[i] = ldsf(L, L_MEAN + (T * 16 + lc) * 4);
      if ((k & 3) == 1) rc1[i] = ldsf(L, L_RSTD + (T * 16 + lc) * 4);
      if ((k & 3) == 2) m4[i] = ldsf4(L, L_MEAN + (T * 16 + lg * 4) * 4);
      if ((k & 3) == 3) r4[i] = ldsf4(L, L_RSTD + (T * 16 + lg * 4) * 4);
    }
  };
  {
    const int b_ = blockIdx.x, ln = min(b_ * 59 + w * 8 + (lane & 7), 15103);
    tp2 = (ln < 4096 ? woa : w2a) + (u32)(ln * 128);
    const u64 pb = ((lane & 8) ? (u64)P.b1 + (u32)min(max(j - 8, 0) * 352 + (lane & 3) * 128, 2748) : (u64)P.b2 + (u32)(nt * 128));
    const u64 pf = (u64)ctl + (u32)(128 * (lc + 1));
    tp2 = lg == 1 ? pb : tp2;
    tp2 = (lg == 2 && w == 0) ? pf : tp2;
    gsto = (w == 7 && lane < 8) ? WS_STAT + (j * 8 + lane) * 16 : WS_GRP;
    gldo = WS_STAT + (tid & 127) * 16;
    b1o = 2 * (u32)min(max(j - 8, 0) * 176 + tid, 1375);
    stw = (!ODD && w < 2) ? L_MEAN + (tid + (FFN ? 0 : 16)) * 4 : L_JUNK + lane * 4;
    b1w = tid < 192 ? L_B1 + tid * 4 : L_JUNK + lane * 4;
    csw = lc == 0 ? L_CS + ((FFN ? CB + crot : (w & 3)) * 16 + lg * 4) * 4 : L_JUNK + lane * 16;
    csw2 = FFN ? (two == 0 ? csw + 64 : L_JUNK + lane * 16) : (lg == 0 ? L_CS + (128 + (w & 3) * 16 + lc) * 4 : L_JUNK + lane * 4);
    if (!FFN) {
#pragma unroll
      for (int r = 0; r < 4; ++r) {
        mP[r] = (4 * lg + r >= lc + 2) ? 0.f : -1e30f;
        mP0[r] = (TYPE == 0 && g == 0) ? -1e30f : mP[r];
        mC[r] = (4 * lg + r <= lc) ? 0.f : -1e30f;
      }
      sel0 = (lc & 1) ? 0x01000c0cu : 0x0c0c0100u; sel1 = (lc & 1) ? 0x03020c0cu : 0x0c0c0302u;
      asm volatile("" : "+v"(mP), "+v"(mC), "+v"(sel0), "+v"(sel1));
      if (TYPE == 0) asm volatile("" : "+v"(mP0));
    }
#pragma unroll
    for (int f = 0; f < 2; ++f) {
      wra[0][f] = (tb ^ (f * 64)) + 2 * R.wb + L_SP;
      wra[1][f] = (tb ^ (f * 64)) + 4 * R.wb;
      wra[2][f] = (tb ^ (f * 64)) + 4 * R.wb + 2 * SLOT;
      asm volatile("" : "+v"(wra[0][f]), "+v"(wra[1][f]), "+v"(wra[2][f]));
    }
    asm volatile("" : "+v"(tp2), "+v"(gsto), "+v"(gldo), "+v"(b1o), "+v"(stw), "+v"(b1w), "+v"(csw), "+v"(csw2));
  }
  SB();
  sfor<0, ND>([&](auto kc) INL { R.template issue<1, decltype(kc)::v>(); });
  if (FFN)
    asm volatile("global_load_dwordx2 %0, %2, %3 sc1\n\tglobal_load_dwordx4 %1, %4, %5"
                 : "=&v"(epoch), "=&v"(r0) : "v"(0u), "s"(ctl), "v"(lane * 16), "s"(P.x + (size_t)(128 * g + 8 * j + w) * LDX) : "memory");
  else
    asm volatile("global_load_dwordx2 %0, %4, %5 sc1\n\tglobal_load_dwordx4 %1, %6, %7\n\t"
                 "global_load_dwordx4 %2, %6, %8\n\tglobal_load_dwordx4 %3, %6, %8 offset:1024"
                 : "=&v"(epoch), "=&v"(r0), "=&v"(r1), "=&v"(r2)
                 : "v"(0u), "s"(ctl), "v"(lane * 16), "s"(P.x + (size_t)(128 * g + 8 * j + w) * LDX),
                   "s"(P.x + (size_t)max(128 * g - 16 + 2 * w, 0) * LDX) : "memory");
  wait_bar<5 + NRL>();
#pragma unroll
  for (int k = 0; k < 2 * NC + NT; ++k) rd(0, k);
  SB();
  sfor<0, 16>([&](auto tc) INL {
    constexpr int t = decltype(tc)::v, s = t >> 1, b = t & 1;
    if constexpr (b) {
      if constexpr (s <= 4) wait_bar<5 + ((s == 1 || s == 2) ? ODD : 0) + ((t == TT + 1 || t == TT + 3) ? 1 : 0)>();
      else if constexpr (s == 5) wait_bar<7>();
      else if constexpr (s == 6) wait_bar<4>();
    }
    if constexpr (t == 11) {
      if constexpr (!ODD) asm volatile("" : "+v"(gst), "+v"(b1v));
      if constexpr (!ODD) {
        if (__builtin_expect(!__all(gst[1] == (u32)tag && gst[3] == (u32)tag), 0)) {
#pragma nounroll
          for (u32 spins = 0; spins < SPIN_CAP; ++spins) {
            __builtin_amdgcn_s_sleep(1);
            gst = __builtin_amdgcn_raw_buffer_load_b128(rsG, gldo, 0, AUX_SC1);
            if (__all(gst[1] == (u32)tag && gst[3] == (u32)tag)) break;
          }
        }
      }
      SB();
    }
    constexpr int nr = t == 15 ? 0 : 2 * NC + NT;
    constexpr int st = t == 0 ? 2 : (t + 5) / 2;
    constexpr int k0 = (t == 0 || b) ? 0 : 3, k1 = st >= 8 ? 0 : ((t == 0 || !b) ? ND : 3);
    constexpr int nd = k1 > k0 ? k1 - k0 : 0, nw = t >= 10 ? 2 : 0, nv = nd + nw;
    sfor<0, NM>([&](auto qc) INL {
      constexpr int q = decltype(qc)::v;
      constexpr int qq = t < 15 ? q : (FFN ? (q + NT * NC) % NM : (TYPE == 1 ? q : (q < 3 ? q * NT : (q < 5 ? (q - 2) * NT + 4 :
                         (q - 5 < 4 ? q - 4 : (q - 5 < 7 ? q - 3 : q - 1))))));
      if constexpr (qq < NT * NC) {
        constexpr int c = qq / NT, i = qq % NT;
        const u32x4 wfr = {fb[b][c][0][0], fb[b][c][0][1], fb[b][c][1][0], fb[b][c][1][1]};
        if constexpr (!FFN && c == 2) acc[i][c] = mfma16(fa[b][i], wfr, acc[i][c]);
        else acc[i][c] = mfma16(wfr, fa[b][i], acc[i][c]);
        PIN(acc[i][c]);
      } else {
        constexpr int c = qq - NT * NC;
        const u32x4 wfr = {fb[b][c][0][0], fb[b][c][0][1], fb[b][c][1][0], fb[b][c][1][1]};
        if constexpr (c == 0) { acc1[0] = mfma16(wfr, ones, acc1[0]); PIN(acc1[0]); }
        else mfma16_if(acc1[1], wfr, onesv, two);
      }
      if constexpr (q < nr) rd(t + 1, q);
      if constexpr (t == 15) { rdst(2 * q); rdst(2 * q + 1); }
      if constexpr (t == 9 && q == 0 && !ODD)
        asm volatile("buffer_load_dwordx4 %0, %2, %3, 0 offen sc1\n\tglobal_load_ushort %1, %4, %5"
                     : "=&v"(gst), "=&v"(b1v) : "v"(gldo), "s"(rsG), "v"(b1o), "s"(P.b1) : "memory");
      if constexpr (t == 11 && !ODD) {
        if constexpr (q == NM - 4) *(LAS u32*)(L + stw) = gst[0];
        if constexpr (q == NM - 3) *(LAS u32*)(L + stw + 576) = gst[2];
        if constexpr (q == NM - 2 && FFN) *(LAS float*)(L + b1w) = bf_lo(b1v);
      }
      if constexpr (t == 1 && ODD && q == NM - 1) { som = ldsf(L, L_OWN + (lane & 7) * 4); sor = ldsf(L, L_OWN + 576 + (lane & 7) * 4); }
      sfor<0, nv>([&](auto uc) INL {
        constexpr int u = decltype(uc)::v;
        if constexpr (((2 * u + ODD) * NM) / (2 * nv) == q) {
          if constexpr (u < nd) R.template issue<st, k0 + u>();
          else wdma(IC<t - 10>{}, IC<u - nd>{});
        }
      });
      if constexpr (t == TT && q == NM - 1) touch((const void*)tp2);
      if constexpr (t == 2 && q == NM - 1) {
        if (ODD) st128(u32x4{f2u(som), (u32)tag, f2u(sor), (u32)tag}, rsG, gsto, 0);
      }
      if constexpr (t == 15) {
        if constexpr (TYPE == 0) {
          if constexpr (q == 6) *(LAS f32x4*)(L + csw) = acc[0][0];
          if constexpr (q == 7) *(LAS f32x4*)(L + csw + 256) = acc[0][1];
          if constexpr (q == 8) *(LAS float*)(L + csw2) = acc[0][2][0];
          if constexpr (q == 9) *(LAS f32x4*)(L + L_KV + 2 * R.wb + lane * 16) = acc[NT - 1][1];
          if constexpr (q == 10) *(LAS f32x4*)(L + L_KV + 2 * R.wb + 1024 + lane * 16) = acc[NT - 1][2];
        }
        if constexpr (FFN) {
          if constexpr (q == 6) *(LAS f32x4*)(L + csw) = acc1[0];
          if constexpr (q == 9) *(LAS f32x4*)(L + csw2) = acc1[1];
        }
      }
      SB();
    });
    if constexpr (t == 0) {
      if (FFN) asm volatile("s_waitcnt vmcnt(%2)" : "+v"(epoch), "+v"(r0) : "i"(5) : "memory");
      else asm volatile("s_waitcnt vmcnt(%4)" : "+v"(epoch), "+v"(r0), "+v"(r1), "+v"(r2) : "i"(5) : "memory");
      tag = (((u64)(u32)__builtin_amdgcn_readfirstlane((u32)(epoch >> 32)) << 32) | (u32)__builtin_amdgcn_readfirstlane((u32)epoch)) + 1;
      float sS[3] = {0.f, 0.f, 0.f}, sQ[3] = {0.f, 0.f, 0.f};
#pragma unroll
      for (int d = 0; d < 4; ++d) {
        sS[0] = dot2(r0[d], 0x3F803F80u, sS[0]); sQ[0] = dot2(r0[d], r0[d], sQ[0]);
        if (!FFN) {
          sS[1] = dot2(r1[d], 0x3F803F80u, sS[1]); sQ[1] = dot2(r1[d], r1[d], sQ[1]);
          sS[2] = dot2(r2[d], 0x3F803F80u, sS[2]); sQ[2] = dot2(r2[d], r2[d], sQ[2]);
        }
      }
      f32x2 a = swap32(sS[0], sS[FFN ? 0 : 1]), c = swap32(sQ[0], sQ[FFN ? 0 : 1]);
      float S = a[0] + a[1], Q = c[0] + c[1], S2 = S, Q2 = Q;
      if (!FFN) {
        a = swap32(sS[2], sS[2]); c = swap32(sQ[2], sQ[2]);
        S2 = a[0] + a[1]; Q2 = c[0] + c[1];
      }
      a = swap16(S, S2); c = swap16(Q, Q2);
      S = a[0] + a[1]; Q = c[0] + c[1];
      S = dpp_add<0xB1>(S); Q = dpp_add<0xB1>(Q);
      S = dpp_add<0x4E>(S); Q = dpp_add<0x4E>(Q);
      S = dpp_add<0x141>(S); Q = dpp_add<0x141>(Q);
      S = dpp_add<0x140>(S); Q = dpp_add<0x140>(Q);
      const float m = S * (1.f / 512.f), e = Q * (1.f / 512.f);
      u32 adr = L_JUNK + lane * 4;
      adr = lane == 0 ? L_OWN + w * 4 : adr;
      if (!FFN) { adr = lane == 32 ? L_MEAN + w * 8 : adr; adr = lane == 16 ? L_MEAN + w * 8 + 4 : adr; }
      *(LAS float*)(L + adr) = -m;
      *(LAS float*)(L + adr + 576) = __builtin_amdgcn_rsqf(fmaxf(e - m * m, 0.f) + 1e-5f);
      SB();
    }
  });
  lds_bar();
  auto slot = [&](auto kc) INL {
    constexpr int k = decltype(kc)::v;
    SB();
    if constexpr (k < NEPI) wdma(IC<6 + k / 2>{}, IC<k % 2>{});
    SB();
  };
  if constexpr (!FFN) {
    const int h = w & 3;
    const f32x4 csq = ldsf4(L, L_CS + (h * 16 + lg * 4) * 4), csk = ldsf4(L, L_CS + (64 + h * 16 + lg * 4) * 4);
    const float csv1 = ldsf(L, L_CS + (128 + h * 16 + lc) * 4);
    f32x4 k4 = acc[0][1], v4 = acc[0][2];
    if (TYPE == 1) { k4 = ldsf4(L, L_KV + h * 2048 + lane * 16); v4 = ldsf4(L, L_KV + h * 2048 + 1024 + lane * 16); }
    slot(IC<0>{});
    wait_vm<7>();
    wread(0); wread(1); wread(2);
    const bool odd = lc & 1;
    const f32x4 csv = splat4(csv1);
    u32x2 Qb[4], Kb[NK], Vb[NK];
    sfor<0, NK>([&](auto ic) INL {
      constexpr int i = decltype(ic)::v, ai = TYPE == 0 ? i : (i > 0 ? i - 1 : 0);
      slot(IC<1 + i>{});
      const f32x4 mc = splat4(mc1[i]);
      Kb[i] = pk4(((i == 0 ? k4 : acc[ai][1]) + mc * csk) * splat4(rc1[i]));
      if constexpr (i > 0) Qb[i - 1] = pk4((acc[ai][0] + mc * csq) * splat4(rc1[i] * (0.25f * 1.4426950408889634f)));
      Vb[i] = pk4(((i == 0 ? v4 : acc[ai][2]) + m4[i] * csv) * r4[i]);
    });
    f32x4 sp[4], sc[4];
#pragma unroll
    for (int q = 0; q < 4; ++q) {
      sp[q] = mfma16k16(Kb[q], Qb[q], q == 0 ? mP0 : mP);
      sc[q] = mfma16k16(Kb[q + 1], Qb[q], mC);
    }
    SB();
    wait_lds();
    wait_vm<6>();
    wread(3); wread(4); wread(5);
    slot(IC<6>{});
    f32x4 a[4];
    float mx[4];
#pragma unroll
    for (int q = 0; q < 4; ++q) {
      a[q] = f32x4{sel(odd, sp[q][1], sp[q][0]), sel(odd, sp[q][3], sp[q][2]), sel(odd, sc[q][1], sc[q][0]), sel(odd, sc[q][3], sc[q][2])};
      mx[q] = vmax(vmax(a[q][0], a[q][1]), vmax(a[q][2], a[q][3]));
    }
    slot(IC<7>{});
    {
      const f32x2 x = swap32(mx[0], mx[1]), y = swap32(mx[2], mx[3]);
      const f32x2 z = swap16(vmax(x[0], x[1]), vmax(y[0], y[1]));
      const float u = vmax(z[0], z[1]);
      const f32x2 p = swap16(u, u);
      const f32x2 p0 = swap32(p[0], p[0]), p1 = swap32(p[1], p[1]);
      mx[0] = p0[0]; mx[1] = p0[1]; mx[2] = p1[0]; mx[3] = p1[1];
    }
    slot(IC<8>{});
#pragma unroll
    for (int q = 0; q < 4; ++q) a[q] -= splat4(mx[q]);
    slot(IC<9>{});
    sfor<0, 4>([&](auto qc) INL {
      constexpr int q = decltype(qc)::v;
#pragma unroll
      for (int r = 0; r < 4; ++r) a[q][r] = __builtin_amdgcn_exp2f(a[q][r]);
      if constexpr (q & 1) slot(IC<10 + q / 2>{});
    });
    wait_lds();
    const f32x4 z4 = {0.f, 0.f, 0.f, 0.f};
    u32x4 pb[4];
    sfor<0, 4>([&](auto qc) INL {
      constexpr int q = decltype(qc)::v;
      const u32x2 u = pk4(a[q]);
      pb[q] = u32x4{__builtin_amdgcn_perm(0, u[0], sel0), __builtin_amdgcn_perm(0, u[0], sel1),
                    __builtin_amdgcn_perm(0, u[1], sel0), __builtin_amdgcn_perm(0, u[1], sel1)};
      if constexpr (q & 1) slot(IC<12 + q / 2>{});
    });
#pragma unroll
    for (int q = 0; q < 4; ++q) {
      sc[q] = mfma16(ones, pb[q], z4);
      sp[q] = mfma16k16(Vb[q], u32x2{pb[q][0], pb[q][1]}, z4);
    }
#pragma unroll
    for (int q = 0; q < 4; ++q) sp[q] = mfma16k16(Vb[q + 1], u32x2{pb[q][2], pb[q][3]}, sp[q]);
    slot(IC<14>{}); slot(IC<15>{}); slot(IC<16>{}); slot(IC<17>{});
    u32x2 Ob[4];
#pragma unroll
    for (int q = 0; q < 4; ++q) Ob[q] = pk4(sp[q] * splat4(__builtin_amdgcn_rcpf(sc[q][0])));
    const int rt = 4 * g + (TYPE == 0 ? 0 : 2), ks = 4 * j + h;
    st128(pair_rows(Ob[0], Ob[1]), rsA2, lane * 16, (rt * NKS + ks) * 1024);
    st128(pair_rows(Ob[2], Ob[3]), rsA2, lane * 16, ((rt + 1) * NKS + ks) * 1024);
  } else {
    f32x4 cs[NC];
#pragma unroll
    for (int c = 0; c < NC; ++c) {
      const int ct = CB + (c + crot >= NC ? c + crot - NC : c + crot);
      cs[c] = ldsf4(L, L_CS + (ct * 16 + lg * 4) * 4);
    }
    wait_vm<6>();
    wread(0); wread(1); wread(2);
    const f32x4 mc[2] = {splat4(mc1[0]), splat4(mc1[1])}, rc[2] = {splat4(rc1[0]), splat4(rc1[1])};
    const int nct = j == 15 ? 9 : 11;
    const int rt = 4 * g + (w & 3), ksb = 32 + 11 * (j - 8);
    const f32x4 C1 = splat4(-2.f * 1.4426950408889634f * 0.7978845608028654f);
    const f32x4 C2 = splat4(-2.f * 1.4426950408889634f * 0.7978845608028654f * 0.044715f);
    const f32x4 ONE = splat4(1.f);
    sfor<0, 3>([&](auto gc) INL {
      constexpr int G = decltype(gc)::v, c0 = 2 * G, n = c0 + 1 < NC ? 4 : 2;
      if constexpr (G == 1) { wait_lds(); wait_vm<8>(); wread(3); wread(4); wread(5); }
      if constexpr (G == 2) wait_lds();
      f32x4 v[4], e[4];
      slot(IC<6 * G>{});
#pragma unroll
      for (int u = 0; u < n; ++u) v[u] = (acc[u & 1][c0 + (u >> 1)] + mc[u & 1] * cs[c0 + (u >> 1)]) * rc[u & 1] + bb[c0 + (u >> 1)];
      slot(IC<6 * G + 1>{});
#pragma unroll
      for (int u = 0; u < n; ++u) e[u] = v[u] * (v[u] * v[u] * C2 + C1);
      slot(IC<6 * G + 2>{});
#pragma unroll
      for (int u = 0; u < n; ++u) {
#pragma unroll
        for (int r = 0; r < 4; ++r) e[u][r] = __builtin_amdgcn_exp2f(e[u][r]);
        if (u == 1) slot(IC<6 * G + 3>{});
      }
#pragma unroll
      for (int u = 0; u < n; ++u) e[u] += ONE;
      slot(IC<6 * G + 4>{});
#pragma unroll
      for (int u = 0; u < n; ++u) {
#pragma unroll
        for (int r = 0; r < 4; ++r) e[u][r] = __builtin_amdgcn_rcpf(e[u][r]);
        if (u == 1) slot(IC<6 * G + 5>{});
      }
#pragma unroll
      for (int cc = 0; cc < n / 2; ++cc) {
        const int c = c0 + cc, ct = CB + (c + crot >= NC ? c + crot - NC : c + crot);
        st128(pair_rows(pk4(v[2 * cc] * e[2 * cc]), pk4(v[2 * cc + 1] * e[2 * cc + 1])), rsA2, lane * 16,
              ct < nct ? (rt * NKS + ksb + ct) * 1024 : A2_BYTES);
      }
      SB();
    });
  }
  wait_vm<0>();
  asm volatile("s_barrier" ::: "memory");
  if (tid == 0) __hip_atomic_store(ctl + 16 * (j + 1), tag, __ATOMIC_RELAXED, __HIP_MEMORY_SCOPE_AGENT);
  sfor<NEPI, 18>([&](auto kc) INL { wdma(IC<6 + decltype(kc)::v / 2>{}, IC<decltype(kc)::v % 2>{}); });
#pragma unroll
  for (int s = 6; s < 9; ++s) wread(s);
  tid = fresh_tid(); lane = tid & 63; lg = lane >> 4; lc = lane & 15;
  const u32 ooff = (u32)((mt * 64 + 4 * w + ((lane >> 3) & 3)) * LDX + (nt * 64 + (lane & 7) * 8) * 2);
  f32x4 xb0, xb1;
#pragma unroll
  for (int s = 0; s < 9; ++s) asm volatile("" : "+v"(wf[s][0]), "+v"(wf[s][1]));
  u64 v0, v1, v2, v3;
  wait_vm<0>();
  {
    const u32 fo = 128 * (lc + 1);
    u32 spins = SPIN_CAP;
    asm volatile(
        "s_cmp_lg_u32 %9, %10\n\ts_cbranch_scc1 2f\n\t"
        "global_load_dwordx2 %0, %5, %6 sc1\n\ts_sleep %8\n\tglobal_load_dwordx2 %1, %5, %6 sc1\n\ts_sleep %8\n\t"
        "global_load_dwordx2 %2, %5, %6 sc1\n\ts_sleep %8\n"
        "1:\n\t"
        "global_load_dwordx2 %3, %5, %6 sc1\n\ts_waitcnt vmcnt(3)\n\tv_cmp_ne_u64 vcc, %7, %0\n\ts_cmp_eq_u64 vcc, 0\n\ts_cbranch_scc1 2f\n\ts_sleep %8\n\t"
        "global_load_dwordx2 %0, %5, %6 sc1\n\ts_waitcnt vmcnt(3)\n\tv_cmp_ne_u64 vcc, %7, %1\n\ts_cmp_eq_u64 vcc, 0\n\ts_cbranch_scc1 2f\n\ts_sleep %8\n\t"
        "global_load_dwordx2 %1, %5, %6 sc1\n\ts_waitcnt vmcnt(3)\n\tv_cmp_ne_u64 vcc, %7, %2\n\ts_cmp_eq_u64 vcc, 0\n\ts_cbranch_scc1 2f\n\ts_sleep %8\n\t"
        "global_load_dwordx2 %2, %5, %6 sc1\n\ts_waitcnt vmcnt(3)\n\tv_cmp_ne_u64 vcc, %7, %3\n\ts_cmp_eq_u64 vcc, 0\n\ts_cbranch_scc1 2f\n\ts_sleep %8\n\t"
        "s_sub_u32 %4, %4, 1\n\ts_cmp_lg_u32 %4, 0\n\ts_cbranch_scc1 1b\n"
        "2:\n\ts_barrier"
        : "=&v"(v0), "=&v"(v1), "=&v"(v2), "=&v"(v3), "+s"(spins)
        : "v"(fo), "s"(ctl), "s"(tag), "i"(PGAP), "s"(w), "i"(7) : "memory", "vcc", "scc");
  }
  u32x4 af[2][15];
  auto lda = [&](int r, int s) INL {
    const int so = (s < nks) ? ((2 * mt + r) * NKS + ks0 + s) * 1024 : A2_BYTES;
    af[r][s] = __builtin_amdgcn_raw_buffer_load_b128(rsA2, lane * 16, so, AUX_SC1);
  };
#pragma unroll
  for (int n = 0; n < WIN; ++n) lda(n / 15, n % 15);
  SB();
  u32 pw[4];
#pragma unroll
  for (int y = 0; y < 4; ++y) pw[y] = ((lane + 4 * y) & 63) * 16 + y * 1024 + 4 * R.wb;
  const int blk = lane & 7;
  const u32 prd = (((lane & 32) + 4 * w + ((lane >> 3) & 3) + 4 * (blk & 3)) & 63) * 16 + (blk & 3) * 1024;
  const u32 prd0 = prd + (blk >> 2) * SLOT;
  u32 prd1 = prd + (blk >> 2) * 4096 + 2 * SLOT, pw1[4], oo1 = lane < 32 ? ooff + 32 * LDX : 2048 * LDX;
#pragma unroll
  for (int y = 0; y < 4; ++y) { pw1[y] = pw[y] + 2 * SLOT + 4 * R.wb; PIN(pw1[y]); }
  PIN(prd1); PIN(oo1);
  const __amdgpu_buffer_rsrc_t rsO = __builtin_amdgcn_make_buffer_rsrc(P.out, 0, 2048 * LDX, 0x00020000);
  auto reduce = [&](int r, f32x4 xb) INL {
    f32x4 p[8];
#pragma unroll
    for (int sw = 0; sw < 8; ++sw) p[sw] = ldsf4(L, r == 0 ? prd0 + sw * 4096 : prd1 + sw * 8192);
    SB();
    const u32x2 o = pk4(((p[0] + p[1]) + (p[2] + p[3])) + ((p[4] + p[5]) + (p[6] + p[7])) + xb);
    auto e0 = __builtin_amdgcn_permlane32_swap(o[0], o[0], false, false), e1 = __builtin_amdgcn_permlane32_swap(o[1], o[1], false, false);
    __builtin_amdgcn_raw_buffer_store_b128(u32x4{e0[0], e1[0], e0[1], e1[1]}, rsO, r == 1 ? oo1 : (lane < 32 ? ooff : 2048 * LDX), 0, AUX_SC0 | AUX_SC1);
  };
  f32x16 pa[2][2];
  auto pwrite = [&](int r, int b8) INL {
    const f32x16& a = pa[r][b8 >> 2];
    const int y = b8 & 3;
    *(LAS f32x4*)(L + (r == 0 ? pw[y] + (b8 >> 2) * SLOT : pw1[y] + (b8 >> 2) * 4096)) =
        f32x4{a[4 * y], a[4 * y + 1], a[4 * y + 2], a[4 * y + 3]};
  };
  u32x2 xr0, xr1, bres;
  sfor<0, 2>([&](auto rc_) INL {
    constexpr int r = decltype(rc_)::v;
#pragma unroll
    for (int i = 0; i < 16; ++i) pa[r][0][i] = pa[r][1][i] = 0.f;
    sfor<0, 15>([&](auto sc_) INL {
      constexpr int s = decltype(sc_)::v;
      pa[r][0] = mfma32(wf[s][0], af[r][s], pa[r][0]);
      PIN(pa[r][0]);
      if constexpr (r == 1 && s >= 1 && s < 5) pwrite(0, 2 * s - 2);
      pa[r][1] = mfma32(wf[s][1], af[r][s], pa[r][1]);
      PIN(pa[r][1]);
      if constexpr (r == 1 && s >= 1 && s < 5) pwrite(0, 2 * s - 1);
      if constexpr (r == 0 && s == 2) asm volatile("" ::"v"(v0), "v"(v1), "v"(v2), "v"(v3));
      if constexpr (r == 0 && s == 1) {
        const u32 o4 = ooff + (lane >> 5) * 8;
        xr0 = *(const GAS u32x2*)(P.x + o4); xr1 = *(const GAS u32x2*)(P.x + (o4 + 32 * LDX));
        bres = *(const GAS u32x2*)(P.b2 + (o4 & 1023));
      }
      if constexpr (r * 15 + s + WIN < 30) lda((r * 15 + s + WIN) / 15, (r * 15 + s + WIN) % 15);
      if constexpr (r == 0 && s >= 7 && s < 13) wread(s + 2);
      if constexpr (r == 0 && s == 14) {
        const f32x4 bv = {bf_lo(bres[0]), bf_hi(bres[0]), bf_lo(bres[1]), bf_hi(bres[1])};
        xb0 = f32x4{bf_lo(xr0[0]), bf_hi(xr0[0]), bf_lo(xr0[1]), bf_hi(xr0[1])} + bv;
        xb1 = f32x4{bf_lo(xr1[0]), bf_hi(xr1[0]), bf_lo(xr1[1]), bf_hi(xr1[1])} + bv;
        PIN(xb0); PIN(xb1);
      }
      if constexpr (r == 1 && s == RMID) { lds_bar(); reduce(0, xb0); }
      SB();
    });
  });
#pragma unroll
  for (int y = 0; y < 8; ++y) pwrite(1, y);
  lds_bar();
  reduce(1, xb1);
  if (j == 0 && tid == 0) __hip_atomic_store(ctl, tag, __ATOMIC_RELAXED, __HIP_MEMORY_SCOPE_AGENT);
  asm volatile(".Lfk_end%c0:" ::"i"(TYPE));
  __builtin_amdgcn_endpgm();
}

__global__ __launch_bounds__(512, 2) void fused(const char*, const char*, const char*, const char*, const u16*, const char*, const char*,
                                                char*, char*) {
  extern __shared__ __attribute__((aligned(16))) char smem[];
  LAS char* L = (LAS char*)smem;
  typedef u64 u64x8 __attribute__((ext_vector_type(8)));
  u64x8 ka;
  u64 kb;
  asm volatile("s_load_dwordx16 %0, %2, 0x0\n\ts_load_dwordx2 %1, %2, 0x40"
               : "=&s"(ka), "=&s"(kb) : "s"(__builtin_amdgcn_kernarg_segment_ptr()) : "memory");
  const int lane = threadIdx.x & 63, w = __builtin_amdgcn_readfirstlane(threadIdx.x >> 6);
  const int b = blockIdx.x, bi = b >> 3;
  const int g = (b & 3) * 4 + (bi & 3), j = ((bi >> 2) & 3) + ((b >> 2) & 1) * 4 + (bi >> 4) * 8;
  const bool ffn = j >= 8;
  if (w == 7) {
    u64 pc;
    u32 len;
    asm volatile("s_getpc_b64 %0\n.Lfk_here:\n\ts_mov_b32 %1, .Lfk_end0-.Lfk_here\n\ts_max_u32 %1, %1, .Lfk_end1-.Lfk_here\n\t"
                 "s_max_u32 %1, %1, .Lfk_end2-.Lfk_here\n\ts_max_u32 %1, %1, .Lfk_end3-.Lfk_here" : "=s"(pc), "=s"(len)::"scc");
    touch((const char*)pc + min((u32)(lane * 128 + (bi & 7) * 8192), len));
  }
  const int rowbase = ffn ? 128 * g : 128 * g - 16;
  Ring R;
  R.ldw = ffn ? LDW1 : LDQKV;
  R.maxch = ffn ? (j == 15 ? 1 : 5) : 7;
  R.wb = w * 1024;
  R.rot = (j + g) & 7;
  R.lanes(lane);
  R.stepB = 64 * R.ldw;
  R.mlo = (!ffn && w == 0) ? 0xFFFFFF00u : 0xFFFFFFFFu;
  R.xcond = ffn ? 2u : (u32)(w - 4);
  const u32 oA0 = (rowbase + 8 * w < 0 ? 8 * w : rowbase + 8 * w) * LDX, oA1 = (rowbase + 8 * (w + 8)) * LDX;
  const u32 oB = 8 * w * R.ldw + (ffn ? (j - 8) * 352 : j * 128), seg = ffn ? 128 : 1024;
  asm volatile("s_waitcnt lgkmcnt(0)" : "+s"(ka), "+s"(kb)::"memory");
  const Ptrs P = {(const char*)(GAS const char*)ka[0], (const char*)(GAS const char*)ka[1], (const char*)(GAS const char*)ka[2],
                  (const char*)(GAS const char*)ka[3], (const u16*)(GAS const u16*)ka[4], (const char*)(GAS const char*)ka[5],
                  (const char*)(GAS const char*)ka[6], (char*)(GAS char*)ka[7], (char*)(GAS char*)kb};
  R.sA0 = P.x + oA0;
  R.sA1 = P.x + oA1;
  R.sB0 = (const char*)((ffn ? ka[3] : ka[1]) + oB);
  R.sB1 = R.sB0 + seg;
  R.sB2 = R.sB1 + seg;
  sfor<0, 6>([&](auto kc) INL { R.template issue<0, decltype(kc)::v>(); });
  if (!ffn && w == 0 && lane < 8) {
    const u32x4 one = {0x3F803F80u, 0x3F803F80u, 0x3F803F80u, 0x3F803F80u};
#pragma unroll
    for (int s = 0; s < 3; ++s) *(LAS u32x4*)(L + s * SLOT + lane * 16) = one;
  }
  if (!ffn) {
    if (w < 4) body<0>(L, P, R, g, j, w);
    else body<1>(L, P, R, g, j, w);
  } else {
    if (w < 4) body<2>(L, P, R, g, j, w);
    else body<3>(L, P, R, g, j, w);
  }
}
}

extern "C" void kernel_launch(void* const* d_in, const int* in_sizes, int n_in, void* d_out, int out_size, void* d_ws,
                              size_t ws_size, hipStream_t stream) {
  (void)hipFuncSetAttribute((const void*)fk::fused, hipFuncAttributeMaxDynamicSharedMemorySize, fk::L_TOTAL);
  fk::fused<<<256, 512, fk::L_TOTAL, stream>>>((const char*)d_in[0], (const char*)d_in[1], (const char*)d_in[2], (const char*)d_in[4],
                                               (const u16*)d_in[5], (const char*)d_in[6], (const char*)d_in[7], (char*)d_out,
                                               (char*)d_ws);
}
```

```cpp
#define STAMPS
#define NSTAMP 16
#define AMP 0
#include <hip/hip_runtime.h>
#include <stdint.h>

typedef unsigned u32;
typedef unsigned long long u64;
typedef unsigned short u16;
typedef short s16x4 __attribute__((ext_vector_type(4)));
typedef short s16x8 __attribute__((ext_vector_type(8)));
typedef __bf16 b16x2 __attribute__((ext_vector_type(2)));
typedef float f32x2 __attribute__((ext_vector_type(2)));
typedef float f32x4 __attribute__((ext_vector_type(4)));
typedef float f32x16 __attribute__((ext_vector_type(16)));
typedef u32 u32x2 __attribute__((ext_vector_type(2)));
typedef u32 u32x4 __attribute__((ext_vector_type(4)));
#define LAS __attribute__((address_space(3)))
#define GAS __attribute__((address_space(1)))
#define DEV __device__ __forceinline__
#define INL __attribute__((always_inline))

namespace fk {
constexpr int LDX = 1024, LDQKV = 3072, LDW1 = 2752, LDW2 = 1024;
constexpr int NKS = 118;
constexpr int SLOT = 43008, BOFF = 18432;
constexpr int L_MEAN = 3 * SLOT, L_RSTD = L_MEAN + 576, L_CS = L_RSTD + 576, L_B1 = L_CS + 768, L_OWN = L_B1 + 768,
              L_KV = L_MEAN + 3584, L_JUNK = L_KV + 8192, L_SP = L_JUNK + 2048, L_TOTAL = L_SP + 9 * 2048;
static_assert(L_OWN + 576 + 48 <= L_KV && L_TOTAL <= 163840, "LDS map");
constexpr int WS_GRP = 8192, WS_STAT = 4096;
constexpr int WS_A2 = 16 * WS_GRP, A2_BYTES = 64 * NKS * 1024;
constexpr int AUX_SC0 = 1, AUX_SC1 = 16;
constexpr u32 SPIN_CAP = 1u << 16;

constexpr int NEPI = 12;
constexpr int TT = 4;
constexpr int WIN = 5;
constexpr int RMID = 11;
constexpr int PGAP = 2;

struct Stamps { u32 t[16]; };
#define STAMP(i) do { u64 t_; asm volatile("s_memrealtime %0\n\ts_waitcnt lgkmcnt(0)" : "=s"(t_) :: "memory"); ST.t[i] = (u32)t_; } while (0)
#define STAMP_ARG , Stamps& ST
#define STAMP_PASS , ST
#define STAMP_OUT() do { if ((threadIdx.x & 63) == 0) { _Pragma("unroll") for (int i_ = 0; i_ < NSTAMP; ++i_) \
  ((u64*)(P.ws + (64u << 20)))[(blockIdx.x * 8 + (threadIdx.x >> 6)) * 16 + i_] = ST.t[i_]; } } while (0)

#undef STAMP_OUT
#define STAMP_OUT() do { const u32 lim_ = 4000u + 10u * (ST.t[AMP] - ST.t[0]); \
  while ((u32)__builtin_amdgcn_s_memrealtime() - ST.t[0] < lim_) __builtin_amdgcn_s_sleep(2); } while (0)
template <int IMM> DEV void dma16(u32 voff, const void* sbase, u32 lb) {
  asm volatile("s_add_u32 m0, %2, %3\n\ts_nop 0\n\tglobal_load_lds_dwordx4 %0, %1" ::"v"(voff), "s"(sbase), "s"(lb), "i"(IMM) : "memory", "scc");
}
template <int IMM> DEV void dma16_masked(u32 voff, const void* sbase, u32 lb, u32 mlo) {
  asm volatile("s_add_u32 m0, %2, %4\n\ts_mov_b32 exec_lo, %3\n\tglobal_load_lds_dwordx4 %0, %1\n\ts_mov_b32 exec_lo, -1"
               ::"v"(voff), "s"(sbase), "s"(lb), "s"(mlo), "i"(IMM) : "memory", "scc");
}
template <int IMM> DEV void dma16_if(u32 voff, const void* sbase, u32 lb, u32 cond) {
  asm volatile("s_cmp_gt_u32 %3, 1\n\ts_cbranch_scc1 1f\n\ts_add_u32 m0, %2, %4\n\ts_nop 0\n\tglobal_load_lds_dwordx4 %0, %1\n1:"
               ::"v"(voff), "s"(sbase), "s"(lb), "s"(cond), "i"(IMM) : "memory", "scc");
}
DEV void touch(const void* p) {
  asm volatile("s_mov_b32 m0, %1\n\ts_nop 0\n\tglobal_load_lds_dword %0, off sc1" ::"v"(p), "i"(L_JUNK) : "memory");
}
DEV void ld3x2(u32x2& a, u32x2& b, u32x2& c, u32 oa, u32 ob, u32 oc, const char* pab, const char* pc) {
  asm volatile("global_load_dwordx2 %0, %3, %6\n\tglobal_load_dwordx2 %1, %4, %6\n\tglobal_load_dwordx2 %2, %5, %7"
               : "=&v"(a), "=&v"(b), "=&v"(c) : "v"(oa), "v"(ob), "v"(oc), "s"(pab), "s"(pc) : "memory");
}
DEV void tie3(u32x2& a, u32x2& b, u32x2& c) { asm volatile("" : "+v"(a), "+v"(b), "+v"(c)); }
template <int N> DEV void wait_bar() {
  asm volatile("s_waitcnt vmcnt(%0) lgkmcnt(0)\n\ts_barrier" ::"i"(N) : "memory");
}
template <int N> DEV void wait_vm() { asm volatile("s_waitcnt vmcnt(%0)" ::"i"(N) : "memory"); }
DEV void wait_lds() { asm volatile("s_waitcnt lgkmcnt(0)" ::: "memory"); }
DEV void lds_bar() { asm volatile("s_waitcnt lgkmcnt(0)\n\ts_barrier" ::: "memory"); }
#define SB() __builtin_amdgcn_sched_barrier(0)
#define PIN(x) asm volatile("" : "+v"(x))

DEV u32 f2u(float v) { return __builtin_bit_cast(u32, v); }
DEV float u2f(u32 v) { return __builtin_bit_cast(float, v); }
DEV u32 pk_bf16(float a, float b) {
  f32x2 p = {a, b};
  return __builtin_bit_cast(u32, __builtin_convertvector(p, b16x2));
}
DEV u32x2 pk4(f32x4 v) { return u32x2{pk_bf16(v[0], v[1]), pk_bf16(v[2], v[3])}; }
DEV float bf_lo(u32 v) { return u2f(v << 16); }
DEV float bf_hi(u32 v) { return u2f(v & 0xffff0000u); }
DEV float dot2(u32 a, u32 b, float c) {
  return __builtin_amdgcn_fdot2_f32_bf16(__builtin_bit_cast(b16x2, a), __builtin_bit_cast(b16x2, b), c, false);
}
DEV f32x4 mfma16(u32x4 a, u32x4 b, f32x4 c) {
  return __builtin_amdgcn_mfma_f32_16x16x32_bf16(__builtin_bit_cast(s16x8, a), __builtin_bit_cast(s16x8, b), c, 0, 0, 0);
}
DEV void mfma16_if(f32x4& acc, u32x4 a, u32x4 b, u32 cond) {
  asm volatile("s_cmp_lg_u32 %3, 0\n\ts_cbranch_scc1 1f\n\tv_mfma_f32_16x16x32_bf16 %0, %1, %2, %0\n1:" : "+v"(acc) : "v"(a), "v"(b), "s"(cond) : "scc");
}
DEV f32x4 mfma16k16(u32x2 a, u32x2 b, f32x4 c) {
  return __builtin_amdgcn_mfma_f32_16x16x16bf16_1k(__builtin_bit_cast(s16x4, a), __builtin_bit_cast(s16x4, b), c, 0, 0, 0);
}
DEV f32x16 mfma32(u32x4 a, u32x4 b, f32x16 c) {
  return __builtin_amdgcn_mfma_f32_32x32x16_bf16(__builtin_bit_cast(s16x8, a), __builtin_bit_cast(s16x8, b), c, 0, 0, 0);
}
DEV u32x4 lds128(LAS char* L, u32 off) { return *(LAS u32x4*)(L + off); }
DEV f32x4 ldsf4(LAS char* L, u32 off) { return *(LAS f32x4*)(L + off); }
DEV float ldsf(LAS char* L, u32 off) { return *(LAS float*)(L + off); }
DEV u32x2 ldstr(LAS char* L, u32 off) {
  return __builtin_bit_cast(u32x2, __builtin_amdgcn_ds_read_tr16_b64_v4i16((LAS s16x4*)(L + off)));
}
template <int CTRL> DEV float dpp_add(float v) {
  int t = __builtin_amdgcn_update_dpp(0, (int)f2u(v), CTRL, 0xf, 0xf, true);
  return v + u2f((u32)t);
}
DEV f32x2 swap16(float a, float b) {
  auto q = __builtin_amdgcn_permlane16_swap(f2u(a), f2u(b), false, false);
  return f32x2{u2f(q[0]), u2f(q[1])};
}
DEV f32x2 swap32(float a, float b) {
  auto q = __builtin_amdgcn_permlane32_swap(f2u(a), f2u(b), false, false);
  return f32x2{u2f(q[0]), u2f(q[1])};
}
DEV float vmax(float a, float b) { return __builtin_amdgcn_fmed3f(a, b, __builtin_inff()); }
DEV float sel(bool c, float a, float b) { return c ? a : b; }
DEV u32x4 pair_rows(u32x2 x, u32x2 y) {
  auto a = __builtin_amdgcn_permlane16_swap(x[0], y[0], false, false);
  auto b = __builtin_amdgcn_permlane16_swap(x[1], y[1], false, false);
  return u32x4{a[0], b[0], a[1], b[1]};
}
DEV f32x4 splat4(float v) { return f32x4{v, v, v, v}; }
DEV int fresh_tid() {
  int t = threadIdx.x;
  asm volatile("" : "+v"(t));
  return t;
}
DEV void st128(u32x4 v, __amdgpu_buffer_rsrc_t rs, u32 voff, u32 soff) {
  __builtin_amdgcn_raw_buffer_store_b128(v, rs, voff, soff, AUX_SC1);
  asm volatile("s_nop 1" ::"v"(v));
}
template <int V> struct IC { static constexpr int v = V; };
template <int LO, int HI, class F> DEV void sfor(F&& f) {
  if constexpr (LO < HI) { f(IC<LO>{}); sfor<LO + 1, HI>(f); }
}

struct Ptrs { const char *x, *wq, *wo, *w1; const u16* b1; const char *w2, *b2; char *out, *ws; };
struct Ring {
  u32 voffA, voffB, voffB2, stepB, mlo, xcond, wb;
  int rot, ldw, maxch;
  const char *sA0, *sA1, *sB0, *sB1, *sB2;
  DEV void lanes(int lane) {
    const int r8 = lane >> 3, pos = lane & 7;
    const int swB = (r8 & 2) | ((wb >> 8) & 4);
    voffA = r8 * LDX + ((pos ^ r8) << 4);
    voffB = r8 * ldw + ((pos ^ swB) << 4);
    voffB2 = r8 * ldw + (min(pos ^ swB, maxch) << 4);
  }
  template <int I, int K> DEV void issue() const {
    const int kc = (I + rot) & 7;
    constexpr int slot = (I % 3) * SLOT;
    if constexpr (K == 0) dma16_masked<slot>(voffA + kc * 128, sA0, wb, mlo);
    if constexpr (K == 1) dma16<slot + BOFF>(voffB + kc * stepB, sB0, wb);
    if constexpr (K == 2) dma16<slot + BOFF + 8192>(voffB + kc * stepB, sB1, wb);
    if constexpr (K == 3) dma16<slot + BOFF + 16384>(voffB2 + kc * stepB, sB2, wb);
    if constexpr (K == 4) dma16<slot + 8192>(voffA + kc * 128, sA1, wb);
    if constexpr (K == 5) dma16_if<slot + 12288>(voffA + kc * 128 + 32 * LDX, sA1, wb, xcond);
  }
};

template <int TYPE>
DEV void body(LAS char* L, const Ptrs P, Ring R, int g, int j, int w STAMP_ARG) {
  constexpr bool FFN = TYPE >= 2;
  constexpr int ODD = TYPE & 1;
  constexpr int NT = TYPE == 0 ? 5 : (TYPE == 1 ? 4 : 2);
  constexpr int NC = TYPE == 2 ? 6 : (TYPE == 3 ? 5 : 3);
  constexpr int CB = TYPE == 3 ? 6 : 0;
  constexpr int ND = TYPE == 1 ? 6 : 5;
  constexpr int NM = NT * NC + (FFN ? 2 : 0);
  constexpr int NRL = TYPE == 0 ? 3 : 2;
  int tid = fresh_tid();
  int lane = tid & 63;
  R.lanes(lane);
  int lg = lane >> 4, lc = lane & 15;
  GAS u64* ctl = (GAS u64*)(P.ws + g * WS_GRP);
  u32x4 r0, r1;
  u64 epoch;
  const int T0 = TYPE == 0 ? 0 : (TYPE == 1 ? 5 : 2 * (w & 3));
  const u32 aA = T0 * 2048 + (lc >> 3) * 1024 + (lc & 7) * 128 + ((lg ^ (lc & 7)) << 4);
  const int tq = lc >> 2, tp = lc & 3;
  const int swR = (tq & 2) | ((lg & 1) << 2);
  const u32 aBl = BOFF + lg * 1024 + tq * 128 + ((tp >> 1) << 4) + (tp & 1) * 8;
  const int crot = TYPE == 2 ? ((w & 3) < 2 ? 2 * (w & 3) : (w & 3) + 2) : ((w & 3) < 3 ? (w & 3) : 4);
  const u32 two = TYPE == 2 ? (u32)w >> 1 : (u32)w - 6;
  constexpr int NBB = FFN ? NC : 1;
  u32 aB[NBB][2], aAb[2][2];
#pragma unroll
  for (int c = 0; c < NBB; ++c) {
    const int cc = c + crot >= NC ? c + crot - NC : c + crot;
    const int ct = FFN ? CB + cc : (w & 3);
    aB[c][0] = aBl + (ct >> 2) * 8192 + (((2 * (ct & 3)) ^ swR) << 4);
    aB[c][1] = aB[c][0] + 65536;
    asm("" : "+v"(aB[c][0]), "+v"(aB[c][1]));
  }
#pragma unroll
  for (int b = 0; b < 2; ++b) {
    aAb[b][0] = aA ^ (b * 64);
    aAb[b][1] = aAb[b][0] + 65536;
    asm("" : "+v"(aAb[b][0]), "+v"(aAb[b][1]));
  }
  f32x4 acc[NT][NC], acc1[2];
#pragma unroll
  for (int t = 0; t < NT; ++t)
#pragma unroll
    for (int c = 0; c < NC; ++c) acc[t][c] = f32x4{0.f, 0.f, 0.f, 0.f};
  acc1[0] = acc1[1] = f32x4{0.f, 0.f, 0.f, 0.f};
  u32x4 fa[2][NT];
  u32x2 fb[2][NC][2];
  auto rd = [&](int t, int k) INL {
    const int slot = ((t >> 1) % 3) * SLOT, b = t & 1;
    if (k >= 2 && k < 2 + NT) {
      const int o = slot + (k - 2) * 2048, hi = o >= 65536;
      fa[b][k - 2] = lds128(L, aAb[b][hi] + (o - hi * 65536));
    } else {
      const int kk = k < 2 ? k : k - NT, c = kk >> 1;
      const int o = slot + b * 4096 + (kk & 1) * 512 + (FFN ? 0 : c * 8192), hi = o >= 65536;
      fb[b][c][kk & 1] = ldstr(L, aB[FFN ? c : 0][hi] + (o - hi * 65536));
    }
  };
  const int mt = 2 * g + ((j >> 2) & 1), nt = (j & 3) + (j >> 3) * 4;
  const int ks0 = w < 6 ? 15 * w : 90 + 14 * (w - 6), nks = w < 6 ? 15 : 14;
  const u32 voffW = (lane >> 3) * LDW2 + (((lane & 7) ^ ((lane >> 2) & 4)) << 4) + nt * 128;
  const u64 woa = (u64)P.wo, w2a = (u64)P.w2 - 32 * 16 * LDW2;
  const u32 wx0 = w < 5 ? 2 * SLOT + (16 + w) * 2048 : (11 + w) * 2048;
  const u32 wx1 = w < 5 ? SLOT + (16 + w) * 2048 : (w < 7 ? (14 + w) * 2048 : L_SP + 8 * 2048);
  auto wbase = [&](int s) INL -> u32 { const int k = s % 9; return k == 0 ? 2 * R.wb : (k == 5 ? wx0 : (k == 8 ? wx1 : 4 * R.wb)); };
  auto wdma = [&](auto sc_, auto hc_) INL {
    constexpr int s = decltype(sc_)::v, h = decltype(hc_)::v, k = s % 9;
    constexpr int imm = k == 0 ? L_SP : (k <= 2 ? 2 * SLOT + (k - 1) * 2048 : (k <= 4 ? (k - 3) * 2048 : (k == 6 || k == 7 ? SLOT + (k - 6) * 2048 : 0)));
    const int ks = min(ks0 + s, NKS - 1);
    const u64 base = (ks < 32 ? woa : w2a) + (u32)(ks * 16 * LDW2);
    if (ODD && s == 14) dma16_if<imm + h * 1024>(voffW + h * 8 * LDW2, (const void*)base, wbase(s), (u32)w - 4);
    else dma16<imm + h * 1024>(voffW + h * 8 * LDW2, (const void*)base, wbase(s));
  };
  u32x4 wf[15][2];
  const u32 tb = (lane >> 5) * 1024 + tq * 128 + (((2 * (lg & 1) + (tp >> 1)) ^ ((tq & 2) << 1)) << 4) + (tp & 1) * 8;
  const __amdgpu_buffer_rsrc_t rsA2 = __builtin_amdgcn_make_buffer_rsrc(P.ws + WS_A2, 0, A2_BYTES, 0x00020000);
  const __amdgpu_buffer_rsrc_t rsG = __builtin_amdgcn_make_buffer_rsrc(P.ws + g * WS_GRP, 0, WS_GRP, 0x00020000);
  const u32x4 ones = {0x3F803F80u, 0x3F803F80u, 0x3F803F80u, 0x3F803F80u};
  u32x4 onesv = ones;
  if (FFN) { PIN(onesv); PIN(acc1[1]); }
  u64 tag = 0;
  u32 b1v = 0;
  u32x4 gst;
  u32x2 xr0, xr1, bres;
  u64 tp2 = 0;
  u32 gsto = 0, gldo = 0, b1o = 0, stw = 0, b1w = 0, csw = 0, csw2 = 0;
  float som = 0.f, sor = 0.f;
  f32x4 mP, mP0, mC;
  u32 sel0 = 0, sel1 = 0, wra[4][2];
  auto wread = [&](int s) INL {
    const int k = s % 9;
#pragma unroll
    for (int f = 0; f < 2; ++f) {
      const u32 a = k == 0 ? wra[0][f] : (k <= 2 ? wra[2][f] + (k - 1) * 2048 : (k <= 4 ? wra[1][f] + (k - 3) * 2048 :
                    (k == 6 || k == 7 ? wra[1][f] + SLOT + (k - 6) * 2048 : wra[1][f] - 4 * R.wb + wbase(s))));
      const u32x2 lo = ldstr(L, a), hi = ldstr(L, a + 512);
      wf[s][f] = u32x4{lo[0], lo[1], hi[0], hi[1]};
    }
  };
  constexpr int NK = 5, TK = TYPE == 0 ? 0 : 4;
  float mc1[NK], rc1[NK];
  f32x4 m4[NK], r4[NK], bb[NC];
  auto rdst = [&](int k) INL {
    if (FFN) {
      if (k < 2) mc1[k] = ldsf(L, L_MEAN + ((2 * (w & 3) + k) * 16 + lc) * 4);
      else if (k < 4) rc1[k - 2] = ldsf(L, L_RSTD + ((2 * (w & 3) + k - 2) * 16 + lc) * 4);
      else if (k < 4 + NC) {
        const int c = k - 4, ct = CB + (c + crot >= NC ? c + crot - NC : c + crot);
        bb[c] = ldsf4(L, L_B1 + (ct * 16 + lg * 4) * 4);
      }
    } else {
      const int i = k >> 2, T = TK + i;
      if (i >= NK) return;
      if ((k & 3) == 0) mc1[i] = ldsf(L, L_MEAN + (T * 16 + lc) * 4);
      if ((k & 3) == 1) rc1[i] = ldsf(L, L_RSTD + (T * 16 + lc) * 4);
      if ((k & 3) == 2) m4[i] = ldsf4(L, L_MEAN + (T * 16 + lg * 4) * 4);
      if ((k & 3) == 3) r4[i] = ldsf4(L, L_RSTD + (T * 16 + lg * 4) * 4);
    }
  };
      {
        const int b_ = blockIdx.x, ln = min(b_ * 59 + w * 8 + (lane & 7), 15103);
        tp2 = (ln < 4096 ? woa : w2a) + (u32)(ln * 128);
        const u64 pb = ((lane & 8) ? (u64)P.b1 + (u32)min(max(j - 8, 0) * 352 + (lane & 3) * 128, 2748) : (u64)P.b2 + (u32)(nt * 128));
        const u64 pf = (u64)ctl + (u32)(128 * (lc + 1));
        tp2 = lg == 1 ? pb : tp2;
        tp2 = (lg == 2 && w == 0) ? pf : tp2;
        gsto = lane < 8 ? WS_STAT + (16 + j * 8 + lane) * 16 : WS_STAT + (2 * j - 8 + lane) * 16;
        gsto = (w == 7 && lane < (FFN ? 8 : 10)) ? gsto : WS_GRP;
        gldo = WS_STAT + (FFN ? 16 + (tid & 127) : min(tid, 143)) * 16;
        b1o = 2 * (u32)min(max(j - 8, 0) * 176 + tid, 1375);
        stw = tid < (FFN ? 128 : 144) ? L_MEAN + tid * 4 : L_JUNK + lane * 4;
        b1w = tid < 192 ? L_B1 + tid * 4 : L_JUNK + lane * 4;
        csw = lc == 0 ? L_CS + ((FFN ? CB + crot : (w & 3)) * 16 + lg * 4) * 4 : L_JUNK + lane * 16;
        csw2 = FFN ? (two == 0 ? csw + 64 : L_JUNK + lane * 16) : (lg == 0 ? L_CS + (128 + (w & 3) * 16 + lc) * 4 : L_JUNK + lane * 4);
        if (!FFN) {
#pragma unroll
          for (int r = 0; r < 4; ++r) {
            mP[r] = (4 * lg + r >= lc + 2) ? 0.f : -1e30f;
            mP0[r] = (TYPE == 0 && g == 0) ? -1e30f : mP[r];
            mC[r] = (4 * lg + r <= lc) ? 0.f : -1e30f;
          }
          sel0 = (lc & 1) ? 0x01000c0cu : 0x0c0c0100u; sel1 = (lc & 1) ? 0x03020c0cu : 0x0c0c0302u;
          asm volatile("" : "+v"(mP), "+v"(mC), "+v"(sel0), "+v"(sel1));
          if (TYPE == 0) asm volatile("" : "+v"(mP0));
        }
#pragma unroll
        for (int f = 0; f < 2; ++f) {
          wra[0][f] = (tb ^ (f * 64)) + 2 * R.wb + L_SP;
          wra[1][f] = (tb ^ (f * 64)) + 4 * R.wb;
          wra[2][f] = (tb ^ (f * 64)) + 4 * R.wb + 2 * SLOT;
          asm volatile("" : "+v"(wra[0][f]), "+v"(wra[1][f]), "+v"(wra[2][f]));
        }
        asm volatile("" : "+v"(tp2), "+v"(gsto), "+v"(gldo), "+v"(b1o), "+v"(stw), "+v"(b1w), "+v"(csw), "+v"(csw2));
      }
  SB();
  sfor<0, ND>([&](auto kc) INL { R.template issue<1, decltype(kc)::v>(); });
  if (TYPE != 0)
    asm volatile("global_load_dwordx2 %0, %2, %3 sc1\n\tglobal_load_dwordx4 %1, %4, %5"
                 : "=&v"(epoch), "=&v"(r0) : "v"(0u), "s"(ctl), "v"(lane * 16), "s"(P.x + (size_t)(128 * g + 8 * j + w) * LDX) : "memory");
  else
    asm volatile("global_load_dwordx2 %0, %3, %4 sc1\n\tglobal_load_dwordx4 %1, %5, %6\n\tglobal_load_dwordx4 %2, %5, %7"
                 : "=&v"(epoch), "=&v"(r0), "=&v"(r1)
                 : "v"(0u), "s"(ctl), "v"(lane * 16), "s"(P.x + (size_t)(128 * g + 8 * j + w) * LDX),
                   "s"(P.x + (size_t)max(128 * g - 16 + 2 * j + (w & 1), 0) * LDX) : "memory");
  STAMP(1);
  wait_bar<5 + NRL>();
  STAMP(2);
#pragma unroll
  for (int k = 0; k < 2 * NC + NT; ++k) rd(0, k);
  SB();
  sfor<0, 16>([&](auto tc) INL {
    constexpr int t = decltype(tc)::v, s = t >> 1, b = t & 1;
    if constexpr (b) {
      if constexpr (s <= 4) wait_bar<5 + ((s == 1 || s == 2) ? ODD : 0) + ((t == TT + 1 || t == TT + 3) ? 1 : 0)>();
      else if constexpr (s == 5) wait_bar<7>();
      else if constexpr (s == 6) wait_bar<4>();
    }
    if constexpr (t == 11) {
      if constexpr (!ODD) asm volatile("" : "+v"(gst), "+v"(b1v));
      if constexpr (!ODD) {
        if (__builtin_expect(!__all(gst[1] == (u32)tag && gst[3] == (u32)tag), 0)) {
#pragma nounroll
          for (u32 spins = 0; spins < SPIN_CAP; ++spins) {
            __builtin_amdgcn_s_sleep(1);
            gst = __builtin_amdgcn_raw_buffer_load_b128(rsG, gldo, 0, AUX_SC1);
            if (__all(gst[1] == (u32)tag && gst[3] == (u32)tag)) break;
          }
        }
      }
      SB();
    }
    constexpr int nr = t == 15 ? 0 : 2 * NC + NT;
    constexpr int st = t == 0 ? 2 : (t + 5) / 2;
    constexpr int k0 = (t == 0 || b) ? 0 : 3, k1 = st >= 8 ? 0 : ((t == 0 || !b) ? ND : 3);
    constexpr int nd = k1 > k0 ? k1 - k0 : 0, nw = t >= 10 ? 2 : 0, nv = nd + nw;
    sfor<0, NM>([&](auto qc) INL {
      constexpr int q = decltype(qc)::v;
      constexpr int qq = t < 15 ? q : (FFN ? (q + NT * NC) % NM : (TYPE == 1 ? q : (q < 3 ? q * NT : (q < 5 ? (q - 2) * NT + 4 :
                         (q - 5 < 4 ? q - 4 : (q - 5 < 7 ? q - 3 : q - 1))))));
      if constexpr (qq < NT * NC) {
        constexpr int c = qq / NT, i = qq % NT;
        const u32x4 wfr = {fb[b][c][0][0], fb[b][c][0][1], fb[b][c][1][0], fb[b][c][1][1]};
        if constexpr (!FFN && c == 2) acc[i][c] = mfma16(fa[b][i], wfr, acc[i][c]);
        else acc[i][c] = mfma16(wfr, fa[b][i], acc[i][c]);
        PIN(acc[i][c]);
      } else {
        constexpr int c = qq - NT * NC;
        const u32x4 wfr = {fb[b][c][0][0], fb[b][c][0][1], fb[b][c][1][0], fb[b][c][1][1]};
        if constexpr (c == 0) { acc1[0] = mfma16(wfr, ones, acc1[0]); PIN(acc1[0]); }
        else mfma16_if(acc1[1], wfr, onesv, two);
      }
      if constexpr (q < nr) rd(t + 1, q);
      if constexpr (t == 15) { rdst(2 * q); rdst(2 * q + 1); }
      if constexpr (t == 9 && q == 0 && !ODD)
        asm volatile("buffer_load_dwordx4 %0, %2, %3, 0 offen sc1\n\tglobal_load_ushort %1, %4, %5"
                     : "=&v"(gst), "=&v"(b1v) : "v"(gldo), "s"(rsG), "v"(b1o), "s"(P.b1) : "memory");
      if constexpr (t == 11 && q == 0) {
        const u32 o4 = (u32)((mt * 64 + 4 * w + ((lane >> 3) & 3)) * LDX + (nt * 64 + (lane & 7) * 8) * 2 + (lane >> 5) * 8);
        ld3x2(xr0, xr1, bres, o4, o4 + 32 * LDX, o4 & 1023, P.x, P.b2);
      }
      if constexpr (t == 13 && q == 0) tie3(xr0, xr1, bres);
      if constexpr (t == 11 && !ODD) {
        if constexpr (q == NM - 4) *(LAS u32*)(L + stw) = gst[0];
        if constexpr (q == NM - 3) *(LAS u32*)(L + stw + 576) = gst[2];
        if constexpr (q == NM - 2 && FFN) *(LAS float*)(L + b1w) = bf_lo(b1v);
      }
      if constexpr (t == 1 && ODD && q == NM - 1) { som = ldsf(L, L_OWN + min(lane, 9) * 4); sor = ldsf(L, L_OWN + 576 + min(lane, 9) * 4); }
      sfor<0, nv>([&](auto uc) INL {
        constexpr int u = decltype(uc)::v;
        if constexpr (((2 * u + ODD) * NM) / (2 * nv) == q) {
          if constexpr (u < nd) R.template issue<st, k0 + u>();
          else wdma(IC<t - 10>{}, IC<u - nd>{});
        }
      });
      if constexpr (t == TT && q == NM - 1) touch((const void*)tp2);
      if constexpr (t == 2 && q == NM - 1) {
        if (ODD) st128(u32x4{f2u(som), (u32)tag, f2u(sor), (u32)tag}, rsG, gsto, 0);
      }
      if constexpr (t == 15) {
        if constexpr (TYPE == 0) {
          if constexpr (q == 6) *(LAS f32x4*)(L + csw) = acc[0][0];
          if constexpr (q == 7) *(LAS f32x4*)(L + csw + 256) = acc[0][1];
          if constexpr (q == 8) *(LAS float*)(L + csw2) = acc[0][2][0];
          if constexpr (q == 9) *(LAS f32x4*)(L + L_KV + 2 * R.wb + lane * 16) = acc[NT - 1][1];
          if constexpr (q == 10) *(LAS f32x4*)(L + L_KV + 2 * R.wb + 1024 + lane * 16) = acc[NT - 1][2];
        }
        if constexpr (FFN) {
          if constexpr (q == 6) *(LAS f32x4*)(L + csw) = acc1[0];
          if constexpr (q == 9) *(LAS f32x4*)(L + csw2) = acc1[1];
        }
      }
      SB();
    });
    if constexpr (t == 0) {
      if (TYPE != 0) asm volatile("s_waitcnt vmcnt(5)" : "+v"(epoch), "+v"(r0)::"memory");
      else asm volatile("s_waitcnt vmcnt(5)" : "+v"(epoch), "+v"(r0), "+v"(r1)::"memory");
      tag = (((u64)(u32)__builtin_amdgcn_readfirstlane((u32)(epoch >> 32)) << 32) | (u32)__builtin_amdgcn_readfirstlane((u32)epoch)) + 1;
      float s0 = 0.f, q0 = 0.f, s1 = 0.f, q1 = 0.f;
#pragma unroll
      for (int d = 0; d < 4; ++d) {
        s0 = dot2(r0[d], 0x3F803F80u, s0); q0 = dot2(r0[d], r0[d], q0);
        if (TYPE == 0) { s1 = dot2(r1[d], 0x3F803F80u, s1); q1 = dot2(r1[d], r1[d], q1); }
      }
      f32x2 a = swap32(s0, TYPE == 0 ? s1 : s0), c = swap32(q0, TYPE == 0 ? q1 : q0);
      float S = a[0] + a[1], Q = c[0] + c[1];
      a = swap16(S, S); c = swap16(Q, Q);
      S = a[0] + a[1]; Q = c[0] + c[1];
      S = dpp_add<0xB1>(S); Q = dpp_add<0xB1>(Q);
      S = dpp_add<0x4E>(S); Q = dpp_add<0x4E>(Q);
      S = dpp_add<0x141>(S); Q = dpp_add<0x141>(Q);
      S = dpp_add<0x140>(S); Q = dpp_add<0x140>(Q);
      const float m = S * (1.f / 512.f), e = Q * (1.f / 512.f);
      u32 adr = L_JUNK + lane * 4;
      adr = lane == 0 ? L_OWN + w * 4 : adr;
      if (TYPE == 0) adr = (lane == 32 && w < 2) ? L_OWN + 32 + w * 4 : adr;
      *(LAS float*)(L + adr) = -m;
      *(LAS float*)(L + adr + 576) = __builtin_amdgcn_rsqf(fmaxf(e - m * m, 0.f) + 1e-5f);
      SB();
    }
  });
  STAMP(3);
  lds_bar();
  STAMP(4);
  auto slot = [&](auto kc) INL {
    constexpr int k = decltype(kc)::v;
    SB();
    if constexpr (k < NEPI) wdma(IC<6 + k / 2>{}, IC<k % 2>{});
    SB();
  };
  if constexpr (!FFN) {
    const int h = w & 3;
    const f32x4 csq = ldsf4(L, L_CS + (h * 16 + lg * 4) * 4), csk = ldsf4(L, L_CS + (64 + h * 16 + lg * 4) * 4);
    const float csv1 = ldsf(L, L_CS + (128 + h * 16 + lc) * 4);
    f32x4 k4 = acc[0][1], v4 = acc[0][2];
    if (TYPE == 1) { k4 = ldsf4(L, L_KV + h * 2048 + lane * 16); v4 = ldsf4(L, L_KV + h * 2048 + 1024 + lane * 16); }
    slot(IC<0>{});
    wait_vm<7>();
    wread(0); wread(1); wread(2);
    const bool odd = lc & 1;
    const f32x4 csv = splat4(csv1);
    u32x2 Qb[4], Kb[NK], Vb[NK];
    sfor<0, NK>([&](auto ic) INL {
      constexpr int i = decltype(ic)::v, ai = TYPE == 0 ? i : (i > 0 ? i - 1 : 0);
      slot(IC<1 + i>{});
      const f32x4 mc = splat4(mc1[i]);
      Kb[i] = pk4(((i == 0 ? k4 : acc[ai][1]) + mc * csk) * splat4(rc1[i]));
      if constexpr (i > 0) Qb[i - 1] = pk4((acc[ai][0] + mc * csq) * splat4(rc1[i] * (0.25f * 1.4426950408889634f)));
      Vb[i] = pk4(((i == 0 ? v4 : acc[ai][2]) + m4[i] * csv) * r4[i]);
    });
    f32x4 sp[4], sc[4];
#pragma unroll
    for (int q = 0; q < 4; ++q) {
      sp[q] = mfma16k16(Kb[q], Qb[q], q == 0 ? mP0 : mP);
      sc[q] = mfma16k16(Kb[q + 1], Qb[q], mC);
    }
    SB();
    wait_lds();
    wait_vm<6>();
    wread(3); wread(4); wread(5);
    slot(IC<6>{});
    f32x4 a[4];
    float mx[4];
#pragma unroll
    for (int q = 0; q < 4; ++q) {
      a[q] = f32x4{sel(odd, sp[q][1], sp[q][0]), sel(odd, sp[q][3], sp[q][2]), sel(odd, sc[q][1], sc[q][0]), sel(odd, sc[q][3], sc[q][2])};
      mx[q] = vmax(vmax(a[q][0], a[q][1]), vmax(a[q][2], a[q][3]));
    }
    slot(IC<7>{});
    {
      const f32x2 x = swap32(mx[0], mx[1]), y = swap32(mx[2], mx[3]);
      const f32x2 z = swap16(vmax(x[0], x[1]), vmax(y[0], y[1]));
      const float u = vmax(z[0], z[1]);
      const f32x2 p = swap16(u, u);
      const f32x2 p0 = swap32(p[0], p[0]), p1 = swap32(p[1], p[1]);
      mx[0] = p0[0]; mx[1] = p0[1]; mx[2] = p1[0]; mx[3] = p1[1];
    }
    slot(IC<8>{});
#pragma unroll
    for (int q = 0; q < 4; ++q) a[q] -= splat4(mx[q]);
    slot(IC<9>{});
    sfor<0, 4>([&](auto qc) INL {
      constexpr int q = decltype(qc)::v;
#pragma unroll
      for (int r = 0; r < 4; ++r) a[q][r] = __builtin_amdgcn_exp2f(a[q][r]);
      if constexpr (q & 1) slot(IC<10 + q / 2>{});
    });
    wait_lds();
    const f32x4 z4 = {0.f, 0.f, 0.f, 0.f};
    u32x4 pb[4];
    sfor<0, 4>([&](auto qc) INL {
      constexpr int q = decltype(qc)::v;
      const u32x2 u = pk4(a[q]);
      pb[q] = u32x4{__builtin_amdgcn_perm(0, u[0], sel0), __builtin_amdgcn_perm(0, u[0], sel1),
                    __builtin_amdgcn_perm(0, u[1], sel0), __builtin_amdgcn_perm(0, u[1], sel1)};
      if constexpr (q & 1) slot(IC<12 + q / 2>{});
    });
#pragma unroll
    for (int q = 0; q < 4; ++q) {
      sc[q] = mfma16(ones, pb[q], z4);
      sp[q] = mfma16k16(Vb[q], u32x2{pb[q][0], pb[q][1]}, z4);
    }
#pragma unroll
    for (int q = 0; q < 4; ++q) sp[q] = mfma16k16(Vb[q + 1], u32x2{pb[q][2], pb[q][3]}, sp[q]);
    slot(IC<14>{}); slot(IC<15>{}); slot(IC<16>{}); slot(IC<17>{});
    u32x2 Ob[4];
#pragma unroll
    for (int q = 0; q < 4; ++q) Ob[q] = pk4(sp[q] * splat4(__builtin_amdgcn_rcpf(sc[q][0])));
    const int rt = 4 * g + (TYPE == 0 ? 0 : 2), ks = 4 * j + h;
    st128(pair_rows(Ob[0], Ob[1]), rsA2, lane * 16, (rt * NKS + ks) * 1024);
    st128(pair_rows(Ob[2], Ob[3]), rsA2, lane * 16, ((rt + 1) * NKS + ks) * 1024);
  } else {
    f32x4 cs[NC];
#pragma unroll
    for (int c = 0; c < NC; ++c) {
      const int ct = CB + (c + crot >= NC ? c + crot - NC : c + crot);
      cs[c] = ldsf4(L, L_CS + (ct * 16 + lg * 4) * 4);
    }
    wait_vm<6>();
    wread(0); wread(1); wread(2);
    const f32x4 mc[2] = {splat4(mc1[0]), splat4(mc1[1])}, rc[2] = {splat4(rc1[0]), splat4(rc1[1])};
    const int nct = j == 15 ? 9 : 11;
    const int rt = 4 * g + (w & 3), ksb = 32 + 11 * (j - 8);
    const f32x4 C1 = splat4(-2.f * 1.4426950408889634f * 0.7978845608028654f);
    const f32x4 C2 = splat4(-2.f * 1.4426950408889634f * 0.7978845608028654f * 0.044715f);
    const f32x4 ONE = splat4(1.f);
    sfor<0, 3>([&](auto gc) INL {
      constexpr int G = decltype(gc)::v, c0 = 2 * G, n = c0 + 1 < NC ? 4 : 2;
      if constexpr (G == 1) { wait_lds(); wait_vm<8>(); wread(3); wread(4); wread(5); }
      if constexpr (G == 2) wait_lds();
      f32x4 v[4], e[4];
      slot(IC<6 * G>{});
#pragma unroll
      for (int u = 0; u < n; ++u) v[u] = (acc[u & 1][c0 + (u >> 1)] + mc[u & 1] * cs[c0 + (u >> 1)]) * rc[u & 1] + bb[c0 + (u >> 1)];
      slot(IC<6 * G + 1>{});
#pragma unroll
      for (int u = 0; u < n; ++u) e[u] = v[u] * (v[u] * v[u] * C2 + C1);
      slot(IC<6 * G + 2>{});
#pragma unroll
      for (int u = 0; u < n; ++u) {
#pragma unroll
        for (int r = 0; r < 4; ++r) e[u][r] = __builtin_amdgcn_exp2f(e[u][r]);
        if (u == 1) slot(IC<6 * G + 3>{});
      }
#pragma unroll
      for (int u = 0; u < n; ++u) e[u] += ONE;
      slot(IC<6 * G + 4>{});
#pragma unroll
      for (int u = 0; u < n; ++u) {
#pragma unroll
        for (int r = 0; r < 4; ++r) e[u][r] = __builtin_amdgcn_rcpf(e[u][r]);
        if (u == 1) slot(IC<6 * G + 5>{});
      }
#pragma unroll
      for (int cc = 0; cc < n / 2; ++cc) {
        const int c = c0 + cc, ct = CB + (c + crot >= NC ? c + crot - NC : c + crot);
        st128(pair_rows(pk4(v[2 * cc] * e[2 * cc]), pk4(v[2 * cc + 1] * e[2 * cc + 1])), rsA2, lane * 16,
              ct < nct ? (rt * NKS + ksb + ct) * 1024 : A2_BYTES);
      }
      SB();
    });
  }
  STAMP(5);
  wait_vm<0>();
  STAMP(13);
  asm volatile("s_barrier" ::: "memory");
  if (tid == 0) __hip_atomic_store(ctl + 16 * (j + 1), tag, __ATOMIC_RELAXED, __HIP_MEMORY_SCOPE_AGENT);
  STAMP(6);
  sfor<NEPI, 18>([&](auto kc) INL { wdma(IC<6 + decltype(kc)::v / 2>{}, IC<decltype(kc)::v % 2>{}); });
#pragma unroll
  for (int s = 6; s < 9; ++s) wread(s);
  tid = fresh_tid(); lane = tid & 63; lg = lane >> 4; lc = lane & 15;
  const u32 ooff = (u32)((mt * 64 + 4 * w + ((lane >> 3) & 3)) * LDX + (nt * 64 + (lane & 7) * 8) * 2);
  f32x4 xb0, xb1;
#pragma unroll
  for (int s = 0; s < 9; ++s) asm volatile("" : "+v"(wf[s][0]), "+v"(wf[s][1]));
  STAMP(7);
  u64 v0, v1, v2, v3;
  wait_vm<0>();
  {
    const u32 fo = 128 * (lc + 1);
    u32 spins = SPIN_CAP;
    asm volatile(
        "s_cmp_lg_u32 %9, %10\n\ts_cbranch_scc1 2f\n\t"
        "global_load_dwordx2 %0, %5, %6 sc1\n\ts_sleep %8\n\tglobal_load_dwordx2 %1, %5, %6 sc1\n\ts_sleep %8\n\t"
        "global_load_dwordx2 %2, %5, %6 sc1\n\ts_sleep %8\n"
        "1:\n\t"
        "global_load_dwordx2 %3, %5, %6 sc1\n\ts_waitcnt vmcnt(3)\n\tv_cmp_ne_u64 vcc, %7, %0\n\ts_cmp_eq_u64 vcc, 0\n\ts_cbranch_scc1 2f\n\ts_sleep %8\n\t"
        "global_load_dwordx2 %0, %5, %6 sc1\n\ts_waitcnt vmcnt(3)\n\tv_cmp_ne_u64 vcc, %7, %1\n\ts_cmp_eq_u64 vcc, 0\n\ts_cbranch_scc1 2f\n\ts_sleep %8\n\t"
        "global_load_dwordx2 %1, %5, %6 sc1\n\ts_waitcnt vmcnt(3)\n\tv_cmp_ne_u64 vcc, %7, %2\n\ts_cmp_eq_u64 vcc, 0\n\ts_cbranch_scc1 2f\n\ts_sleep %8\n\t"
        "global_load_dwordx2 %2, %5, %6 sc1\n\ts_waitcnt vmcnt(3)\n\tv_cmp_ne_u64 vcc, %7, %3\n\ts_cmp_eq_u64 vcc, 0\n\ts_cbranch_scc1 2f\n\ts_sleep %8\n\t"
        "s_sub_u32 %4, %4, 1\n\ts_cmp_lg_u32 %4, 0\n\ts_cbranch_scc1 1b\n"
        "2:\n\ts_barrier"
        : "=&v"(v0), "=&v"(v1), "=&v"(v2), "=&v"(v3), "+s"(spins)
        : "v"(fo), "s"(ctl), "s"(tag), "i"(PGAP), "s"(w), "i"(7) : "memory", "vcc", "scc");
  }
  STAMP(8);
  u32x4 af[2][15];
  auto lda = [&](int r, int s) INL {
    const int so = (s < nks) ? ((2 * mt + r) * NKS + ks0 + s) * 1024 : A2_BYTES;
    af[r][s] = __builtin_amdgcn_raw_buffer_load_b128(rsA2, lane * 16, so, AUX_SC1);
  };
#pragma unroll
  for (int n = 0; n < WIN; ++n) lda(n / 15, n % 15);
  SB();
  u32 pw[4];
#pragma unroll
  for (int y = 0; y < 4; ++y) pw[y] = ((lane + 4 * y) & 63) * 16 + y * 1024 + 4 * R.wb;
  const int blk = lane & 7;
  const u32 prd = (((lane & 32) + 4 * w + ((lane >> 3) & 3) + 4 * (blk & 3)) & 63) * 16 + (blk & 3) * 1024;
  const u32 prd0 = prd + (blk >> 2) * SLOT;
  u32 prd1 = prd + (blk >> 2) * 4096 + 2 * SLOT, pw1[4], oo1 = lane < 32 ? ooff + 32 * LDX : 2048 * LDX;
#pragma unroll
  for (int y = 0; y < 4; ++y) { pw1[y] = pw[y] + 2 * SLOT + 4 * R.wb; PIN(pw1[y]); }
  PIN(prd1); PIN(oo1);
  const __amdgpu_buffer_rsrc_t rsO = __builtin_amdgcn_make_buffer_rsrc(P.out, 0, 2048 * LDX, 0x00020000);
  auto reduce = [&](int r, f32x4 xb) INL {
    f32x4 p[8];
#pragma unroll
    for (int sw = 0; sw < 8; ++sw) p[sw] = ldsf4(L, r == 0 ? prd0 + sw * 4096 : prd1 + sw * 8192);
    SB();
    const u32x2 o = pk4(((p[0] + p[1]) + (p[2] + p[3])) + ((p[4] + p[5]) + (p[6] + p[7])) + xb);
    auto e0 = __builtin_amdgcn_permlane32_swap(o[0], o[0], false, false), e1 = __builtin_amdgcn_permlane32_swap(o[1], o[1], false, false);
    __builtin_amdgcn_raw_buffer_store_b128(u32x4{e0[0], e1[0], e0[1], e1[1]}, rsO, r == 1 ? oo1 : (lane < 32 ? ooff : 2048 * LDX), 0, AUX_SC0 | AUX_SC1);
  };
  f32x16 pa[2][2];
  auto pwrite = [&](int r, int b8) INL {
    const f32x16& a = pa[r][b8 >> 2];
    const int y = b8 & 3;
    *(LAS f32x4*)(L + (r == 0 ? pw[y] + (b8 >> 2) * SLOT : pw1[y] + (b8 >> 2) * 4096)) =
        f32x4{a[4 * y], a[4 * y + 1], a[4 * y + 2], a[4 * y + 3]};
  };
  sfor<0, 2>([&](auto rc_) INL {
    constexpr int r = decltype(rc_)::v;
#pragma unroll
    for (int i = 0; i < 16; ++i) pa[r][0][i] = pa[r][1][i] = 0.f;
    sfor<0, 15>([&](auto sc_) INL {
      constexpr int s = decltype(sc_)::v;
      pa[r][0] = mfma32(wf[s][0], af[r][s], pa[r][0]);
      PIN(pa[r][0]);
      if constexpr (r == 1 && s >= 1 && s < 5) pwrite(0, 2 * s - 2);
      pa[r][1] = mfma32(wf[s][1], af[r][s], pa[r][1]);
      PIN(pa[r][1]);
      if constexpr (r == 1 && s >= 1 && s < 5) pwrite(0, 2 * s - 1);
      if constexpr (r == 0 && s == 2) asm volatile("" ::"v"(v0), "v"(v1), "v"(v2), "v"(v3));
      if constexpr (r == 0 && s == 0) STAMP(14);
      if constexpr (r == 0 && s == 14) STAMP(15);
      if constexpr (r * 15 + s + WIN < 30) lda((r * 15 + s + WIN) / 15, (r * 15 + s + WIN) % 15);
      if constexpr (r == 0 && s >= 7 && s < 13) wread(s + 2);
      if constexpr (r == 0 && s == 3) {
        const f32x4 bv = {bf_lo(bres[0]), bf_hi(bres[0]), bf_lo(bres[1]), bf_hi(bres[1])};
        xb0 = f32x4{bf_lo(xr0[0]), bf_hi(xr0[0]), bf_lo(xr0[1]), bf_hi(xr0[1])} + bv;
        xb1 = f32x4{bf_lo(xr1[0]), bf_hi(xr1[0]), bf_lo(xr1[1]), bf_hi(xr1[1])} + bv;
        PIN(xb0); PIN(xb1);
      }
      if constexpr (r == 1 && s == RMID) { lds_bar(); reduce(0, xb0); }
      SB();
    });
  });
#pragma unroll
  for (int y = 0; y < 8; ++y) pwrite(1, y);
  STAMP(9);
  lds_bar();
  reduce(1, xb1);
  if (j == 0 && tid == 0) __hip_atomic_store(ctl, tag, __ATOMIC_RELAXED, __HIP_MEMORY_SCOPE_AGENT);
  STAMP(10);
  STAMP_OUT();
  asm volatile(".Lfk_end%c0:" ::"i"(TYPE));
  __builtin_amdgcn_endpgm();
}

__global__ __launch_bounds__(512, 2) void fused(const char*, const char*, const char*, const char*, const u16*, const char*, const char*,
                                                char*, char*) {
  extern __shared__ __attribute__((aligned(16))) char smem[];
  LAS char* L = (LAS char*)smem;
  Stamps ST;
  STAMP(0);
  typedef u64 u64x8 __attribute__((ext_vector_type(8)));
  u64x8 ka;
  u64 kb;
  asm volatile("s_load_dwordx16 %0, %2, 0x0\n\ts_load_dwordx2 %1, %2, 0x40"
               : "=&s"(ka), "=&s"(kb) : "s"(__builtin_amdgcn_kernarg_segment_ptr()) : "memory");
  const int lane = threadIdx.x & 63, w = __builtin_amdgcn_readfirstlane(threadIdx.x >> 6);
  const int b = blockIdx.x, bi = b >> 3;
  const int g = (b & 3) * 4 + (bi & 3), j = ((bi >> 2) & 3) + ((b >> 2) & 1) * 4 + (bi >> 4) * 8;
  const bool ffn = j >= 8;
  if (w == 7) {
    u64 pc;
    u32 len;
    asm volatile("s_getpc_b64 %0\n.Lfk_here:\n\ts_mov_b32 %1, .Lfk_end0-.Lfk_here\n\ts_max_u32 %1, %1, .Lfk_end1-.Lfk_here\n\t"
                 "s_max_u32 %1, %1, .Lfk_end2-.Lfk_here\n\ts_max_u32 %1, %1, .Lfk_end3-.Lfk_here" : "=s"(pc), "=s"(len)::"scc");
    touch((const char*)pc + min((u32)(lane * 128 + (bi & 7) * 8192), len));
  }
  const int rowbase = ffn ? 128 * g : 128 * g - 16;
  Ring R;
  R.ldw = ffn ? LDW1 : LDQKV;
  R.maxch = ffn ? (j == 15 ? 1 : 5) : 7;
  R.wb = w * 1024;
  R.rot = (j + g) & 7;
  R.lanes(lane);
  R.stepB = 64 * R.ldw;
  R.mlo = (!ffn && w == 0) ? 0xFFFFFF00u : 0xFFFFFFFFu;
  R.xcond = ffn ? 2u : (u32)(w - 4);
  const u32 oA0 = (rowbase + 8 * w < 0 ? 8 * w : rowbase + 8 * w) * LDX, oA1 = (rowbase + 8 * (w + 8)) * LDX;
  const u32 oB = 8 * w * R.ldw + (ffn ? (j - 8) * 352 : j * 128), seg = ffn ? 128 : 1024;
  asm volatile("s_waitcnt lgkmcnt(0)" : "+s"(ka), "+s"(kb)::"memory");
  const Ptrs P = {(const char*)(GAS const char*)ka[0], (const char*)(GAS const char*)ka[1], (const char*)(GAS const char*)ka[2],
                  (const char*)(GAS const char*)ka[3], (const u16*)(GAS const u16*)ka[4], (const char*)(GAS const char*)ka[5],
                  (const char*)(GAS const char*)ka[6], (char*)(GAS char*)ka[7], (char*)(GAS char*)kb};
  STAMP(11);
  R.sA0 = P.x + oA0;
  R.sA1 = P.x + oA1;
  R.sB0 = (const char*)((ffn ? ka[3] : ka[1]) + oB);
  R.sB1 = R.sB0 + seg;
  R.sB2 = R.sB1 + seg;
  sfor<0, 6>([&](auto kc) INL { R.template issue<0, decltype(kc)::v>(); });
  if (!ffn && w == 0 && lane < 8) {
    const u32x4 one = {0x3F803F80u, 0x3F803F80u, 0x3F803F80u, 0x3F803F80u};
#pragma unroll
    for (int s = 0; s < 3; ++s) *(LAS u32x4*)(L + s * SLOT + lane * 16) = one;
  }
  STAMP(12);
  if (!ffn) {
    if (w < 4) body<0>(L, P, R, g, j, w STAMP_PASS);
    else body<1>(L, P, R, g, j, w STAMP_PASS);
  } else {
    if (w < 4) body<2>(L, P, R, g, j, w STAMP_PASS);
    else body<3>(L, P, R, g, j, w STAMP_PASS);
  }
}
}

extern "C" void kernel_launch(void* const* d_in, const int* in_sizes, int n_in, void* d_out, int out_size, void* d_ws,
                              size_t ws_size, hipStream_t stream) {
  (void)hipFuncSetAttribute((const void*)fk::fused, hipFuncAttributeMaxDynamicSharedMemorySize, fk::L_TOTAL);
  fk::fused<<<256, 512, fk::L_TOTAL, stream>>>((const char*)d_in[0], (const char*)d_in[1], (const char*)d_in[2], (const char*)d_in[4],
                                               (const u16*)d_in[5], (const char*)d_in[6], (const char*)d_in[7], (char*)d_out,
                                               (char*)d_ws);
}
```

```cpp
#include <hip/hip_runtime.h>
#include <stdint.h>

typedef unsigned u32;
typedef unsigned long long u64;
typedef unsigned short u16;
typedef short s16x4 __attribute__((ext_vector_type(4)));
typedef short s16x8 __attribute__((ext_vector_type(8)));
typedef __bf16 b16x2 __attribute__((ext_vector_type(2)));
typedef float f32x2 __attribute__((ext_vector_type(2)));
typedef float f32x4 __attribute__((ext_vector_type(4)));
typedef float f32x16 __attribute__((ext_vector_type(16)));
typedef u32 u32x2 __attribute__((ext_vector_type(2)));
typedef u32 u32x4 __attribute__((ext_vector_type(4)));
#define LAS __attribute__((address_space(3)))
#define GAS __attribute__((address_space(1)))
#define DEV __device__ __forceinline__
#define INL __attribute__((always_inline))

namespace fk {
constexpr int LDX = 1024, LDQKV = 3072, LDW1 = 2752, LDW2 = 1024;
constexpr int NKS = 118;
constexpr int SLOT = 43008, BOFF = 18432;
constexpr int L_MEAN = 3 * SLOT, L_RSTD = L_MEAN + 576, L_CS = L_RSTD + 576, L_B1 = L_CS + 768, L_OWN = L_B1 + 768,
              L_KV = L_MEAN + 3584, L_JUNK = L_KV + 8192, L_SP = L_JUNK + 2048, L_TOTAL = L_SP + 9 * 2048;
static_assert(L_OWN + 576 + 48 <= L_KV && L_TOTAL <= 163840, "LDS map");
constexpr int WS_GRP = 8192, WS_STAT = 4096;
constexpr int WS_A2 = 16 * WS_GRP, A2_BYTES = 64 * NKS * 1024;
constexpr int AUX_SC0 = 1, AUX_SC1 = 16;
constexpr u32 SPIN_CAP = 1u << 16;

constexpr int NEPI = 12;
constexpr int TT = 4;
constexpr int WIN = 5;
constexpr int RMID = 11;
constexpr int PGAP = 2;

template <int IMM> DEV void dma16(u32 voff, const void* sbase, u32 lb) {
  asm volatile("s_add_u32 m0, %2, %3\n\ts_nop 0\n\tglobal_load_lds_dwordx4 %0, %1" ::"v"(voff), "s"(sbase), "s"(lb), "i"(IMM) : "memory", "scc");
}
template <int IMM> DEV void dma16_masked(u32 voff, const void* sbase, u32 lb, u32 mlo) {
  asm volatile("s_add_u32 m0, %2, %4\n\ts_mov_b32 exec_lo, %3\n\tglobal_load_lds_dwordx4 %0, %1\n\ts_mov_b32 exec_lo, -1"
               ::"v"(voff), "s"(sbase), "s"(lb), "s"(mlo), "i"(IMM) : "memory", "scc");
}
template <int IMM> DEV void dma16_if(u32 voff, const void* sbase, u32 lb, u32 cond) {
  asm volatile("s_cmp_gt_u32 %3, 1\n\ts_cbranch_scc1 1f\n\ts_add_u32 m0, %2, %4\n\ts_nop 0\n\tglobal_load_lds_dwordx4 %0, %1\n1:"
               ::"v"(voff), "s"(sbase), "s"(lb), "s"(cond), "i"(IMM) : "memory", "scc");
}
DEV void touch(const void* p) {
  asm volatile("s_mov_b32 m0, %1\n\ts_nop 0\n\tglobal_load_lds_dword %0, off sc1" ::"v"(p), "i"(L_JUNK) : "memory");
}
DEV void ld3x2(u32x2& a, u32x2& b, u32x2& c, u32 oa, u32 ob, u32 oc, const char* pab, const char* pc) {
  asm volatile("global_load_dwordx2 %0, %3, %6\n\tglobal_load_dwordx2 %1, %4, %6\n\tglobal_load_dwordx2 %2, %5, %7"
               : "=&v"(a), "=&v"(b), "=&v"(c) : "v"(oa), "v"(ob), "v"(oc), "s"(pab), "s"(pc) : "memory");
}
DEV void tie3(u32x2& a, u32x2& b, u32x2& c) { asm volatile("" : "+v"(a), "+v"(b), "+v"(c)); }
template <int N> DEV void wait_bar() {
  asm volatile("s_waitcnt vmcnt(%0) lgkmcnt(0)\n\ts_barrier" ::"i"(N) : "memory");
}
template <int N> DEV void wait_vm() { asm volatile("s_waitcnt vmcnt(%0)" ::"i"(N) : "memory"); }
DEV void wait_lds() { asm volatile("s_waitcnt lgkmcnt(0)" ::: "memory"); }
DEV void lds_bar() { asm volatile("s_waitcnt lgkmcnt(0)\n\ts_barrier" ::: "memory"); }
#define SB() __builtin_amdgcn_sched_barrier(0)
#define PIN(x) asm volatile("" : "+v"(x))

DEV u32 f2u(float v) { return __builtin_bit_cast(u32, v); }
DEV float u2f(u32 v) { return __builtin_bit_cast(float, v); }
DEV u32 pk_bf16(float a, float b) {
  f32x2 p = {a, b};
  return __builtin_bit_cast(u32, __builtin_convertvector(p, b16x2));
}
DEV u32x2 pk4(f32x4 v) { return u32x2{pk_bf16(v[0], v[1]), pk_bf16(v[2], v[3])}; }
DEV float bf_lo(u32 v) { return u2f(v << 16); }
DEV float bf_hi(u32 v) { return u2f(v & 0xffff0000u); }
DEV float dot2(u32 a, u32 b, float c) {
  return __builtin_amdgcn_fdot2_f32_bf16(__builtin_bit_cast(b16x2, a), __builtin_bit_cast(b16x2, b), c, false);
}
DEV f32x4 mfma16(u32x4 a, u32x4 b, f32x4 c) {
  return __builtin_amdgcn_mfma_f32_16x16x32_bf16(__builtin_bit_cast(s16x8, a), __builtin_bit_cast(s16x8, b), c, 0, 0, 0);
}
DEV void mfma16_if(f32x4& acc, u32x4 a, u32x4 b, u32 cond) {
  asm volatile("s_cmp_lg_u32 %3, 0\n\ts_cbranch_scc1 1f\n\tv_mfma_f32_16x16x32_bf16 %0, %1, %2, %0\n1:" : "+v"(acc) : "v"(a), "v"(b), "s"(cond) : "scc");
}
DEV f32x4 mfma16k16(u32x2 a, u32x2 b, f32x4 c) {
  return __builtin_amdgcn_mfma_f32_16x16x16bf16_1k(__builtin_bit_cast(s16x4, a), __builtin_bit_cast(s16x4, b), c, 0, 0, 0);
}
DEV f32x16 mfma32(u32x4 a, u32x4 b, f32x16 c) {
  return __builtin_amdgcn_mfma_f32_32x32x16_bf16(__builtin_bit_cast(s16x8, a), __builtin_bit_cast(s16x8, b), c, 0, 0, 0);
}
DEV u32x4 lds128(LAS char* L, u32 off) { return *(LAS u32x4*)(L + off); }
DEV f32x4 ldsf4(LAS char* L, u32 off) { return *(LAS f32x4*)(L + off); }
DEV float ldsf(LAS char* L, u32 off) { return *(LAS float*)(L + off); }
DEV u32x2 ldstr(LAS char* L, u32 off) {
  return __builtin_bit_cast(u32x2, __builtin_amdgcn_ds_read_tr16_b64_v4i16((LAS s16x4*)(L + off)));
}
template <int CTRL> DEV float dpp_add(float v) {
  int t = __builtin_amdgcn_update_dpp(0, (int)f2u(v), CTRL, 0xf, 0xf, true);
  return v + u2f((u32)t);
}
DEV f32x2 swap16(float a, float b) {
  auto q = __builtin_amdgcn_permlane16_swap(f2u(a), f2u(b), false, false);
  return f32x2{u2f(q[0]), u2f(q[1])};
}
DEV f32x2 swap32(float a, float b) {
  auto q = __builtin_amdgcn_permlane32_swap(f2u(a), f2u(b), false, false);
  return f32x2{u2f(q[0]), u2f(q[1])};
}
DEV float vmax(float a, float b) { return __builtin_amdgcn_fmed3f(a, b, __builtin_inff()); }
DEV float sel(bool c, float a, float b) { return c ? a : b; }
DEV u32x4 pair_rows(u32x2 x, u32x2 y) {
  auto a = __builtin_amdgcn_permlane16_swap(x[0], y[0], false, false);
  auto b = __builtin_amdgcn_permlane16_swap(x[1], y[1], false, false);
  return u32x4{a[0], b[0], a[1], b[1]};
}
DEV f32x4 splat4(float v) { return f32x4{v, v, v, v}; }
DEV int fresh_tid() {
  int t = threadIdx.x;
  asm volatile("" : "+v"(t));
  return t;
}
DEV void st128(u32x4 v, __amdgpu_buffer_rsrc_t rs, u32 voff, u32 soff) {
  __builtin_amdgcn_raw_buffer_store_b128(v, rs, voff, soff, AUX_SC1);
  asm volatile("s_nop 1" ::"v"(v));
}
template <int V> struct IC { static constexpr int v = V; };
template <int LO, int HI, class F> DEV void sfor(F&& f) {
  if constexpr (LO < HI) { f(IC<LO>{}); sfor<LO + 1, HI>(f); }
}

struct Ptrs { const char *x, *wq, *wo, *w1; const u16* b1; const char *w2, *b2; char *out, *ws; };
struct Ring {
  u32 voffA, voffB, voffB2, stepB, mlo, xcond, wb;
  int rot, ldw, maxch;
  const char *sA0, *sA1, *sB0, *sB1, *sB2;
  DEV void lanes(int lane) {
    const int r8 = lane >> 3, pos = lane & 7;
    const int swB = (r8 & 2) | ((wb >> 8) & 4);
    voffA = r8 * LDX + ((pos ^ r8) << 4);
    voffB = r8 * ldw + ((pos ^ swB) << 4);
    voffB2 = r8 * ldw + (min(pos ^ swB, maxch) << 4);
  }
  template <int I, int K> DEV void issue() const {
    const int kc = (I + rot) & 7;
    constexpr int slot = (I % 3) * SLOT;
    if constexpr (K == 0) dma16_masked<slot>(voffA + kc * 128, sA0, wb, mlo);
    if constexpr (K == 1) dma16<slot + BOFF>(voffB + kc * stepB, sB0, wb);
    if constexpr (K == 2) dma16<slot + BOFF + 8192>(voffB + kc * stepB, sB1, wb);
    if constexpr (K == 3) dma16<slot + BOFF + 16384>(voffB2 + kc * stepB, sB2, wb);
    if constexpr (K == 4) dma16<slot + 8192>(voffA + kc * 128, sA1, wb);
    if constexpr (K == 5) dma16_if<slot + 12288>(voffA + kc * 128 + 32 * LDX, sA1, wb, xcond);
  }
};

template <int TYPE>
DEV void body(LAS char* L, const Ptrs P, Ring R, int g, int j, int w) {
  constexpr bool FFN = TYPE >= 2;
  constexpr int ODD = TYPE & 1;
  constexpr int NT = TYPE == 0 ? 5 : (TYPE == 1 ? 4 : 2);
  constexpr int NC = TYPE == 2 ? 6 : (TYPE == 3 ? 5 : 3);
  constexpr int CB = TYPE == 3 ? 6 : 0;
  constexpr int ND = TYPE == 1 ? 6 : 5;
  constexpr int NM = NT * NC + (FFN ? 2 : 0);
  constexpr int NRL = TYPE == 0 ? 3 : 2;
  int tid = fresh_tid();
  int lane = tid & 63;
  R.lanes(lane);
  int lg = lane >> 4, lc = lane & 15;
  GAS u64* ctl = (GAS u64*)(P.ws + g * WS_GRP);
  u32x4 r0, r1;
  u64 epoch;
  const int T0 = TYPE == 0 ? 0 : (TYPE == 1 ? 5 : 2 * (w & 3));
  const u32 aA = T0 * 2048 + (lc >> 3) * 1024 + (lc & 7) * 128 + ((lg ^ (lc & 7)) << 4);
  const int tq = lc >> 2, tp = lc & 3;
  const int swR = (tq & 2) | ((lg & 1) << 2);
  const u32 aBl = BOFF + lg * 1024 + tq * 128 + ((tp >> 1) << 4) + (tp & 1) * 8;
  const int crot = TYPE == 2 ? ((w & 3) < 2 ? 2 * (w & 3) : (w & 3) + 2) : ((w & 3) < 3 ? (w & 3) : 4);
  const u32 two = TYPE == 2 ? (u32)w >> 1 : (u32)w - 6;
  constexpr int NBB = FFN ? NC : 1;
  u32 aB[NBB][2], aAb[2][2];
#pragma unroll
  for (int c = 0; c < NBB; ++c) {
    const int cc = c + crot >= NC ? c + crot - NC : c + crot;
    const int ct = FFN ? CB + cc : (w & 3);
    aB[c][0] = aBl + (ct >> 2) * 8192 + (((2 * (ct & 3)) ^ swR) << 4);
    aB[c][1] = aB[c][0] + 65536;
    asm("" : "+v"(aB[c][0]), "+v"(aB[c][1]));
  }
#pragma unroll
  for (int b = 0; b < 2; ++b) {
    aAb[b][0] = aA ^ (b * 64);
    aAb[b][1] = aAb[b][0] + 65536;
    asm("" : "+v"(aAb[b][0]), "+v"(aAb[b][1]));
  }
  f32x4 acc[NT][NC], acc1[2];
#pragma unroll
  for (int t = 0; t < NT; ++t)
#pragma unroll
    for (int c = 0; c < NC; ++c) acc[t][c] = f32x4{0.f, 0.f, 0.f, 0.f};
  acc1[0] = acc1[1] = f32x4{0.f, 0.f, 0.f, 0.f};
  u32x4 fa[2][NT];
  u32x2 fb[2][NC][2];
  auto rd = [&](int t, int k) INL {
    const int slot = ((t >> 1) % 3) * SLOT, b = t & 1;
    if (k >= 2 && k < 2 + NT) {
      const int o = slot + (k - 2) * 2048, hi = o >= 65536;
      fa[b][k - 2] = lds128(L, aAb[b][hi] + (o - hi * 65536));
    } else {
      const int kk = k < 2 ? k : k - NT, c = kk >> 1;
      const int o = slot + b * 4096 + (kk & 1) * 512 + (FFN ? 0 : c * 8192), hi = o >= 65536;
      fb[b][c][kk & 1] = ldstr(L, aB[FFN ? c : 0][hi] + (o - hi * 65536));
    }
  };
  const int mt = 2 * g + ((j >> 2) & 1), nt = (j & 3) + (j >> 3) * 4;
  const int ks0 = w < 6 ? 15 * w : 90 + 14 * (w - 6), nks = w < 6 ? 15 : 14;
  const u32 voffW = (lane >> 3) * LDW2 + (((lane & 7) ^ ((lane >> 2) & 4)) << 4) + nt * 128;
  const u64 woa = (u64)P.wo, w2a = (u64)P.w2 - 32 * 16 * LDW2;
  const u32 wx0 = w < 5 ? 2 * SLOT + (16 + w) * 2048 : (11 + w) * 2048;
  const u32 wx1 = w < 5 ? SLOT + (16 + w) * 2048 : (w < 7 ? (14 + w) * 2048 : L_SP + 8 * 2048);
  auto wbase = [&](int s) INL -> u32 { const int k = s % 9; return k == 0 ? 2 * R.wb : (k == 5 ? wx0 : (k == 8 ? wx1 : 4 * R.wb)); };
  auto wdma = [&](auto sc_, auto hc_) INL {
    constexpr int s = decltype(sc_)::v, h = decltype(hc_)::v, k = s % 9;
    constexpr int imm = k == 0 ? L_SP : (k <= 2 ? 2 * SLOT + (k - 1) * 2048 : (k <= 4 ? (k - 3) * 2048 : (k == 6 || k == 7 ? SLOT + (k - 6) * 2048 : 0)));
    const int ks = min(ks0 + s, NKS - 1);
    const u64 base = (ks < 32 ? woa : w2a) + (u32)(ks * 16 * LDW2);
    if (ODD && s == 14) dma16_if<imm + h * 1024>(voffW + h * 8 * LDW2, (const void*)base, wbase(s), (u32)w - 4);
    else dma16<imm + h * 1024>(voffW + h * 8 * LDW2, (const void*)base, wbase(s));
  };
  u32x4 wf[15][2];
  const u32 tb = (lane >> 5) * 1024 + tq * 128 + (((2 * (lg & 1) + (tp >> 1)) ^ ((tq & 2) << 1)) << 4) + (tp & 1) * 8;
  const __amdgpu_buffer_rsrc_t rsA2 = __builtin_amdgcn_make_buffer_rsrc(P.ws + WS_A2, 0, A2_BYTES, 0x00020000);
  const __amdgpu_buffer_rsrc_t rsG = __builtin_amdgcn_make_buffer_rsrc(P.ws + g * WS_GRP, 0, WS_GRP, 0x00020000);
  const u32x4 ones = {0x3F803F80u, 0x3F803F80u, 0x3F803F80u, 0x3F803F80u};
  u32x4 onesv = ones;
  if (FFN) { PIN(onesv); PIN(acc1[1]); }
  u64 tag = 0;
  u32 b1v = 0;
  u32x4 gst;
  u32x2 xr0, xr1, bres;
  u64 tp2 = 0;
  u32 gsto = 0, gldo = 0, b1o = 0, stw = 0, b1w = 0, csw = 0, csw2 = 0;
  float som = 0.f, sor = 0.f;
  f32x4 mP, mP0, mC;
  u32 sel0 = 0, sel1 = 0, wra[4][2];
  auto wread = [&](int s) INL {
    const int k = s % 9;
#pragma unroll
    for (int f = 0; f < 2; ++f) {
      const u32 a = k == 0 ? wra[0][f] : (k <= 2 ? wra[2][f] + (k - 1) * 2048 : (k <= 4 ? wra[1][f] + (k - 3) * 2048 :
                    (k == 6 || k == 7 ? wra[1][f] + SLOT + (k - 6) * 2048 : wra[1][f] - 4 * R.wb + wbase(s))));
      const u32x2 lo = ldstr(L, a), hi = ldstr(L, a + 512);
      wf[s][f] = u32x4{lo[0], lo[1], hi[0], hi[1]};
    }
  };
  constexpr int NK = 5, TK = TYPE == 0 ? 0 : 4;
  float mc1[NK], rc1[NK];
  f32x4 m4[NK], r4[NK], bb[NC];
  auto rdst = [&](int k) INL {
    if (FFN) {
      if (k < 2) mc1[k] = ldsf(L, L_MEAN + ((2 * (w & 3) + k) * 16 + lc) * 4);
      else if (k < 4) rc1[k - 2] = ldsf(L, L_RSTD + ((2 * (w & 3) + k - 2) * 16 + lc) * 4);
      else if (k < 4 + NC) {
        const int c = k - 4, ct = CB + (c + crot >= NC ? c + crot - NC : c + crot);
        bb[c] = ldsf4(L, L_B1 + (ct * 16 + lg * 4) * 4);
      }
    } else {
      const int i = k >> 2, T = TK + i;
      if (i >= NK) return;
      if ((k & 3) == 0) mc1[i] = ldsf(L, L_MEAN + (T * 16 + lc) * 4);
      if ((k & 3) == 1) rc1[i] = ldsf(L, L_RSTD + (T * 16 + lc) * 4);
      if ((k & 3) == 2) m4[i] = ldsf4(L, L_MEAN + (T * 16 + lg * 4) * 4);
      if ((k & 3) == 3) r4[i] = ldsf4(L, L_RSTD + (T * 16 + lg * 4) * 4);
    }
  };
  {
    const int b_ = blockIdx.x, ln = min(b_ * 59 + w * 8 + (lane & 7), 15103);
    tp2 = (ln < 4096 ? woa : w2a) + (u32)(ln * 128);
    const u64 pb = ((lane & 8) ? (u64)P.b1 + (u32)min(max(j - 8, 0) * 352 + (lane & 3) * 128, 2748) : (u64)P.b2 + (u32)(nt * 128));
    const u64 pf = (u64)ctl + (u32)(128 * (lc + 1));
    tp2 = lg == 1 ? pb : tp2;
    tp2 = (lg == 2 && w == 0) ? pf : tp2;
    gsto = lane < 8 ? WS_STAT + (16 + j * 8 + lane) * 16 : WS_STAT + (2 * j - 8 + lane) * 16;
    gsto = (w == 7 && lane < (FFN ? 8 : 10)) ? gsto : WS_GRP;
    gldo = WS_STAT + (FFN ? 16 + (tid & 127) : min(tid, 143)) * 16;
    b1o = 2 * (u32)min(max(j - 8, 0) * 176 + tid, 1375);
    stw = tid < (FFN ? 128 : 144) ? L_MEAN + tid * 4 : L_JUNK + lane * 4;
    b1w = tid < 192 ? L_B1 + tid * 4 : L_JUNK + lane * 4;
    csw = lc == 0 ? L_CS + ((FFN ? CB + crot : (w & 3)) * 16 + lg * 4) * 4 : L_JUNK + lane * 16;
    csw2 = FFN ? (two == 0 ? csw + 64 : L_JUNK + lane * 16) : (lg == 0 ? L_CS + (128 + (w & 3) * 16 + lc) * 4 : L_JUNK + lane * 4);
    if (!FFN) {
#pragma unroll
      for (int r = 0; r < 4; ++r) {
        mP[r] = (4 * lg + r >= lc + 2) ? 0.f : -1e30f;
        mP0[r] = (TYPE == 0 && g == 0) ? -1e30f : mP[r];
        mC[r] = (4 * lg + r <= lc) ? 0.f : -1e30f;
      }
      sel0 = (lc & 1) ? 0x01000c0cu : 0x0c0c0100u; sel1 = (lc & 1) ? 0x03020c0cu : 0x0c0c0302u;
      asm volatile("" : "+v"(mP), "+v"(mC), "+v"(sel0), "+v"(sel1));
      if (TYPE == 0) asm volatile("" : "+v"(mP0));
    }
#pragma unroll
    for (int f = 0; f < 2; ++f) {
      wra[0][f] = (tb ^ (f * 64)) + 2 * R.wb + L_SP;
      wra[1][f] = (tb ^ (f * 64)) + 4 * R.wb;
      wra[2][f] = (tb ^ (f * 64)) + 4 * R.wb + 2 * SLOT;
      asm volatile("" : "+v"(wra[0][f]), "+v"(wra[1][f]), "+v"(wra[2][f]));
    }
    asm volatile("" : "+v"(tp2), "+v"(gsto), "+v"(gldo), "+v"(b1o), "+v"(stw), "+v"(b1w), "+v"(csw), "+v"(csw2));
  }
  SB();
  sfor<0, ND>([&](auto kc) INL { R.template issue<1, decltype(kc)::v>(); });
  if (TYPE != 0)
    asm volatile("global_load_dwordx2 %0, %2, %3 sc1\n\tglobal_load_dwordx4 %1, %4, %5"
                 : "=&v"(epoch), "=&v"(r0) : "v"(0u), "s"(ctl), "v"(lane * 16), "s"(P.x + (size_t)(128 * g + 8 * j + w) * LDX) : "memory");
  else
    asm volatile("global_load_dwordx2 %0, %3, %4 sc1\n\tglobal_load_dwordx4 %1, %5, %6\n\tglobal_load_dwordx4 %2, %5, %7"
                 : "=&v"(epoch), "=&v"(r0), "=&v"(r1)
                 : "v"(0u), "s"(ctl), "v"(lane * 16), "s"(P.x + (size_t)(128 * g + 8 * j + w) * LDX),
                   "s"(P.x + (size_t)max(128 * g - 16 + 2 * j + (w & 1), 0) * LDX) : "memory");
  wait_bar<5 + NRL>();
#pragma unroll
  for (int k = 0; k < 2 * NC + NT; ++k) rd(0, k);
  SB();
  sfor<0, 16>([&](auto tc) INL {
    constexpr int t = decltype(tc)::v, s = t >> 1, b = t & 1;
    if constexpr (b) {
      if constexpr (s <= 4) wait_bar<5 + ((s == 1 || s == 2) ? ODD : 0) + ((t == TT + 1 || t == TT + 3) ? 1 : 0)>();
      else if constexpr (s == 5) wait_bar<7>();
      else if constexpr (s == 6) wait_bar<4>();
    }
    if constexpr (t == 11) {
      if constexpr (!ODD) asm volatile("" : "+v"(gst), "+v"(b1v));
      if constexpr (!ODD) {
        if (__builtin_expect(!__all(gst[1] == (u32)tag && gst[3] == (u32)tag), 0)) {
#pragma nounroll
          for (u32 spins = 0; spins < SPIN_CAP; ++spins) {
            __builtin_amdgcn_s_sleep(1);
            gst = __builtin_amdgcn_raw_buffer_load_b128(rsG, gldo, 0, AUX_SC1);
            if (__all(gst[1] == (u32)tag && gst[3] == (u32)tag)) break;
          }
        }
      }
      SB();
    }
    constexpr int nr = t == 15 ? 0 : 2 * NC + NT;
    constexpr int st = t == 0 ? 2 : (t + 5) / 2;
    constexpr int k0 = (t == 0 || b) ? 0 : 3, k1 = st >= 8 ? 0 : ((t == 0 || !b) ? ND : 3);
    constexpr int nd = k1 > k0 ? k1 - k0 : 0, nw = t >= 10 ? 2 : 0, nv = nd + nw;
    sfor<0, NM>([&](auto qc) INL {
      constexpr int q = decltype(qc)::v;
      constexpr int qq = t < 15 ? q : (FFN ? (q + NT * NC) % NM : (TYPE == 1 ? q : (q < 3 ? q * NT : (q < 5 ? (q - 2) * NT + 4 :
                         (q - 5 < 4 ? q - 4 : (q - 5 < 7 ? q - 3 : q - 1))))));
      if constexpr (qq < NT * NC) {
        constexpr int c = qq / NT, i = qq % NT;
        const u32x4 wfr = {fb[b][c][0][0], fb[b][c][0][1], fb[b][c][1][0], fb[b][c][1][1]};
        if constexpr (!FFN && c == 2) acc[i][c] = mfma16(fa[b][i], wfr, acc[i][c]);
        else acc[i][c] = mfma16(wfr, fa[b][i], acc[i][c]);
        PIN(acc[i][c]);
      } else {
        constexpr int c = qq - NT * NC;
        const u32x4 wfr = {fb[b][c][0][0], fb[b][c][0][1], fb[b][c][1][0], fb[b][c][1][1]};
        if constexpr (c == 0) { acc1[0] = mfma16(wfr, ones, acc1[0]); PIN(acc1[0]); }
        else mfma16_if(acc1[1], wfr, onesv, two);
      }
      if constexpr (q < nr) rd(t + 1, q);
      if constexpr (t == 15) { rdst(2 * q); rdst(2 * q + 1); }
      if constexpr (t == 9 && q == 0 && !ODD)
        asm volatile("buffer_load_dwordx4 %0, %2, %3, 0 offen sc1\n\tglobal_load_ushort %1, %4, %5"
                     : "=&v"(gst), "=&v"(b1v) : "v"(gldo), "s"(rsG), "v"(b1o), "s"(P.b1) : "memory");
      if constexpr (t == 11 && q == 0) {
        const u32 o4 = (u32)((mt * 64 + 4 * w + ((lane >> 3) & 3)) * LDX + (nt * 64 + (lane & 7) * 8) * 2 + (lane >> 5) * 8);
        ld3x2(xr0, xr1, bres, o4, o4 + 32 * LDX, o4 & 1023, P.x, P.b2);
      }
      if constexpr (t == 13 && q == 0) tie3(xr0, xr1, bres);
      if constexpr (t == 11 && !ODD) {
        if constexpr (q == NM - 4) *(LAS u32*)(L + stw) = gst[0];
        if constexpr (q == NM - 3) *(LAS u32*)(L + stw + 576) = gst[2];
        if constexpr (q == NM - 2 && FFN) *(LAS float*)(L + b1w) = bf_lo(b1v);
      }
      if constexpr (t == 1 && ODD && q == NM - 1) { som = ldsf(L, L_OWN + min(lane, 9) * 4); sor = ldsf(L, L_OWN + 576 + min(lane, 9) * 4); }
      sfor<0, nv>([&](auto uc) INL {
        constexpr int u = decltype(uc)::v;
        if constexpr (((2 * u + ODD) * NM) / (2 * nv) == q) {
          if constexpr (u < nd) R.template issue<st, k0 + u>();
          else wdma(IC<t - 10>{}, IC<u - nd>{});
        }
      });
      if constexpr (t == TT && q == NM - 1) touch((const void*)tp2);
      if constexpr (t == 2 && q == NM - 1) {
        if (ODD) st128(u32x4{f2u(som), (u32)tag, f2u(sor), (u32)tag}, rsG, gsto, 0);
      }
      if constexpr (t == 15) {
        if constexpr (TYPE == 0) {
          if constexpr (q == 6) *(LAS f32x4*)(L + csw) = acc[0][0];
          if constexpr (q == 7) *(LAS f32x4*)(L + csw + 256) = acc[0][1];
          if constexpr (q == 8) *(LAS float*)(L + csw2) = acc[0][2][0];
          if constexpr (q == 9) *(LAS f32x4*)(L + L_KV + 2 * R.wb + lane * 16) = acc[NT - 1][1];
          if constexpr (q == 10) *(LAS f32x4*)(L + L_KV + 2 * R.wb + 1024 + lane * 16) = acc[NT - 1][2];
        }
        if constexpr (FFN) {
          if constexpr (q == 6) *(LAS f32x4*)(L + csw) = acc1[0];
          if constexpr (q == 9) *(LAS f32x4*)(L + csw2) = acc1[1];
        }
      }
      SB();
    });
    if constexpr (t == 0) {
      if (TYPE != 0) asm volatile("s_waitcnt vmcnt(5)" : "+v"(epoch), "+v"(r0)::"memory");
      else asm volatile("s_waitcnt vmcnt(5)" : "+v"(epoch), "+v"(r0), "+v"(r1)::"memory");
      tag = (((u64)(u32)__builtin_amdgcn_readfirstlane((u32)(epoch >> 32)) << 32) | (u32)__builtin_amdgcn_readfirstlane((u32)epoch)) + 1;
      float s0 = 0.f, q0 = 0.f, s1 = 0.f, q1 = 0.f;
#pragma unroll
      for (int d = 0; d < 4; ++d) {
        s0 = dot2(r0[d], 0x3F803F80u, s0); q0 = dot2(r0[d], r0[d], q0);
        if (TYPE == 0) { s1 = dot2(r1[d], 0x3F803F80u, s1); q1 = dot2(r1[d], r1[d], q1); }
      }
      f32x2 a = swap32(s0, TYPE == 0 ? s1 : s0), c = swap32(q0, TYPE == 0 ? q1 : q0);
      float S = a[0] + a[1], Q = c[0] + c[1];
      a = swap16(S, S); c = swap16(Q, Q);
      S = a[0] + a[1]; Q = c[0] + c[1];
      S = dpp_add<0xB1>(S); Q = dpp_add<0xB1>(Q);
      S = dpp_add<0x4E>(S); Q = dpp_add<0x4E>(Q);
      S = dpp_add<0x141>(S); Q = dpp_add<0x141>(Q);
      S = dpp_add<0x140>(S); Q = dpp_add<0x140>(Q);
      const float m = S * (1.f / 512.f), e = Q * (1.f / 512.f);
      u32 adr = L_JUNK + lane * 4;
      adr = lane == 0 ? L_OWN + w * 4 : adr;
      if (TYPE == 0) adr = (lane == 32 && w < 2) ? L_OWN + 32 + w * 4 : adr;
      *(LAS float*)(L + adr) = -m;
      *(LAS float*)(L + adr + 576) = __builtin_amdgcn_rsqf(fmaxf(e - m * m, 0.f) + 1e-5f);
      SB();
    }
  });
  lds_bar();
  auto slot = [&](auto kc) INL {
    constexpr int k = decltype(kc)::v;
    SB();
    if constexpr (k < NEPI) wdma(IC<6 + k / 2>{}, IC<k % 2>{});
    SB();
  };
  if constexpr (!FFN) {
    const int h = w & 3;
    const f32x4 csq = ldsf4(L, L_CS + (h * 16 + lg * 4) * 4), csk = ldsf4(L, L_CS + (64 + h * 16 + lg * 4) * 4);
    const float csv1 = ldsf(L, L_CS + (128 + h * 16 + lc) * 4);
    f32x4 k4 = acc[0][1], v4 = acc[0][2];
    if (TYPE == 1) { k4 = ldsf4(L, L_KV + h * 2048 + lane * 16); v4 = ldsf4(L, L_KV + h * 2048 + 1024 + lane * 16); }
    slot(IC<0>{});
    wait_vm<7>();
    wread(0); wread(1); wread(2);
    const bool odd = lc & 1;
    const f32x4 csv = splat4(csv1);
    u32x2 Qb[4], Kb[NK], Vb[NK];
    sfor<0, NK>([&](auto ic) INL {
      constexpr int i = decltype(ic)::v, ai = TYPE == 0 ? i : (i > 0 ? i - 1 : 0);
      slot(IC<1 + i>{});
      const f32x4 mc = splat4(mc1[i]);
      Kb[i] = pk4(((i == 0 ? k4 : acc[ai][1]) + mc * csk) * splat4(rc1[i]));
      if constexpr (i > 0) Qb[i - 1] = pk4((acc[ai][0] + mc * csq) * splat4(rc1[i] * (0.25f * 1.4426950408889634f)));
      Vb[i] = pk4(((i == 0 ? v4 : acc[ai][2]) + m4[i] * csv) * r4[i]);
    });
    f32x4 sp[4], sc[4];
#pragma unroll
    for (int q = 0; q < 4; ++q) {
      sp[q] = mfma16k16(Kb[q], Qb[q], q == 0 ? mP0 : mP);
      sc[q] = mfma16k16(Kb[q + 1], Qb[q], mC);
    }
    SB();
    wait_lds();
    wait_vm<6>();
    wread(3); wread(4); wread(5);
    slot(IC<6>{});
    f32x4 a[4];
    float mx[4];
#pragma unroll
    for (int q = 0; q < 4; ++q) {
      a[q] = f32x4{sel(odd, sp[q][1], sp[q][0]), sel(odd, sp[q][3], sp[q][2]), sel(odd, sc[q][1], sc[q][0]), sel(odd, sc[q][3], sc[q][2])};
      mx[q] = vmax(vmax(a[q][0], a[q][1]), vmax(a[q][2], a[q][3]));
    }
    slot(IC<7>{});
    {
      const f32x2 x = swap32(mx[0], mx[1]), y = swap32(mx[2], mx[3]);
      const f32x2 z = swap16(vmax(x[0], x[1]), vmax(y[0], y[1]));
      const float u = vmax(z[0], z[1]);
      const f32x2 p = swap16(u, u);
      const f32x2 p0 = swap32(p[0], p[0]), p1 = swap32(p[1], p[1]);
      mx[0] = p0[0]; mx[1] = p0[1]; mx[2] = p1[0]; mx[3] = p1[1];
    }
    slot(IC<8>{});
#pragma unroll
    for (int q = 0; q < 4; ++q) a[q] -= splat4(mx[q]);
    slot(IC<9>{});
    sfor<0, 4>([&](auto qc) INL {
      constexpr int q = decltype(qc)::v;
#pragma unroll
      for (int r = 0; r < 4; ++r) a[q][r] = __builtin_amdgcn_exp2f(a[q][r]);
      if constexpr (q & 1) slot(IC<10 + q / 2>{});
    });
    wait_lds();
    const f32x4 z4 = {0.f, 0.f, 0.f, 0.f};
    u32x4 pb[4];
    sfor<0, 4>([&](auto qc) INL {
      constexpr int q = decltype(qc)::v;
      const u32x2 u = pk4(a[q]);
      pb[q] = u32x4{__builtin_amdgcn_perm(0, u[0], sel0), __builtin_amdgcn_perm(0, u[0], sel1),
                    __builtin_amdgcn_perm(0, u[1], sel0), __builtin_amdgcn_perm(0, u[1], sel1)};
      if constexpr (q & 1) slot(IC<12 + q / 2>{});
    });
#pragma unroll
    for (int q = 0; q < 4; ++q) {
      sc[q] = mfma16(ones, pb[q], z4);
      sp[q] = mfma16k16(Vb[q], u32x2{pb[q][0], pb[q][1]}, z4);
    }
#pragma unroll
    for (int q = 0; q < 4; ++q) sp[q] = mfma16k16(Vb[q + 1], u32x2{pb[q][2], pb[q][3]}, sp[q]);
    slot(IC<14>{}); slot(IC<15>{}); slot(IC<16>{}); slot(IC<17>{});
    u32x2 Ob[4];
#pragma unroll
    for (int q = 0; q < 4; ++q) Ob[q] = pk4(sp[q] * splat4(__builtin_amdgcn_rcpf(sc[q][0])));
    const int rt = 4 * g + (TYPE == 0 ? 0 : 2), ks = 4 * j + h;
    st128(pair_rows(Ob[0], Ob[1]), rsA2, lane * 16, (rt * NKS + ks) * 1024);
    st128(pair_rows(Ob[2], Ob[3]), rsA2, lane * 16, ((rt + 1) * NKS + ks) * 1024);
  } else {
    f32x4 cs[NC];
#pragma unroll
    for (int c = 0; c < NC; ++c) {
      const int ct = CB + (c + crot >= NC ? c + crot - NC : c + crot);
      cs[c] = ldsf4(L, L_CS + (ct * 16 + lg * 4) * 4);
    }
    wait_vm<6>();
    wread(0); wread(1); wread(2);
    const f32x4 mc[2] = {splat4(mc1[0]), splat4(mc1[1])}, rc[2] = {splat4(rc1[0]), splat4(rc1[1])};
    const int nct = j == 15 ? 9 : 11;
    const int rt = 4 * g + (w & 3), ksb = 32 + 11 * (j - 8);
    const f32x4 C1 = splat4(-2.f * 1.4426950408889634f * 0.7978845608028654f);
    const f32x4 C2 = splat4(-2.f * 1.4426950408889634f * 0.7978845608028654f * 0.044715f);
    const f32x4 ONE = splat4(1.f);
    sfor<0, 3>([&](auto gc) INL {
      constexpr int G = decltype(gc)::v, c0 = 2 * G, n = c0 + 1 < NC ? 4 : 2;
      if constexpr (G == 1) { wait_lds(); wait_vm<8>(); wread(3); wread(4); wread(5); }
      if constexpr (G == 2) wait_lds();
      f32x4 v[4], e[4];
      slot(IC<6 * G>{});
#pragma unroll
      for (int u = 0; u < n; ++u) v[u] = (acc[u & 1][c0 + (u >> 1)] + mc[u & 1] * cs[c0 + (u >> 1)]) * rc[u & 1] + bb[c0 + (u >> 1)];
      slot(IC<6 * G + 1>{});
#pragma unroll
      for (int u = 0; u < n; ++u) e[u] = v[u] * (v[u] * v[u] * C2 + C1);
      slot(IC<6 * G + 2>{});
#pragma unroll
      for (int u = 0; u < n; ++u) {
#pragma unroll
        for (int r = 0; r < 4; ++r) e[u][r] = __builtin_amdgcn_exp2f(e[u][r]);
        if (u == 1) slot(IC<6 * G + 3>{});
      }
#pragma unroll
      for (int u = 0; u < n; ++u) e[u] += ONE;
      slot(IC<6 * G + 4>{});
#pragma unroll
      for (int u = 0; u < n; ++u) {
#pragma unroll
        for (int r = 0; r < 4; ++r) e[u][r] = __builtin_amdgcn_rcpf(e[u][r]);
        if (u == 1) slot(IC<6 * G + 5>{});
      }
#pragma unroll
      for (int cc = 0; cc < n / 2; ++cc) {
        const int c = c0 + cc, ct = CB + (c + crot >= NC ? c + crot - NC : c + crot);
        st128(pair_rows(pk4(v[2 * cc] * e[2 * cc]), pk4(v[2 * cc + 1] * e[2 * cc + 1])), rsA2, lane * 16,
              ct < nct ? (rt * NKS + ksb + ct) * 1024 : A2_BYTES);
      }
      SB();
    });
  }
  wait_vm<0>();
  asm volatile("s_barrier" ::: "memory");
  if (tid == 0) __hip_atomic_store(ctl + 16 * (j + 1), tag, __ATOMIC_RELAXED, __HIP_MEMORY_SCOPE_AGENT);
  sfor<NEPI, 18>([&](auto kc) INL { wdma(IC<6 + decltype(kc)::v / 2>{}, IC<decltype(kc)::v % 2>{}); });
#pragma unroll
  for (int s = 6; s < 9; ++s) wread(s);
  tid = fresh_tid(); lane = tid & 63; lg = lane >> 4; lc = lane & 15;
  const u32 ooff = (u32)((mt * 64 + 4 * w + ((lane >> 3) & 3)) * LDX + (nt * 64 + (lane & 7) * 8) * 2);
  f32x4 xb0, xb1;
#pragma unroll
  for (int s = 0; s < 9; ++s) asm volatile("" : "+v"(wf[s][0]), "+v"(wf[s][1]));
  u64 v0, v1, v2, v3;
  wait_vm<0>();
  {
    const u32 fo = 128 * (lc + 1);
    u32 spins = SPIN_CAP;
    asm volatile(
        "s_cmp_lg_u32 %9, %10\n\ts_cbranch_scc1 2f\n\t"
        "global_load_dwordx2 %0, %5, %6 sc1\n\ts_sleep %8\n\tglobal_load_dwordx2 %1, %5, %6 sc1\n\ts_sleep %8\n\t"
        "global_load_dwordx2 %2, %5, %6 sc1\n\ts_sleep %8\n"
        "1:\n\t"
        "global_load_dwordx2 %3, %5, %6 sc1\n\ts_waitcnt vmcnt(3)\n\tv_cmp_ne_u64 vcc, %7, %0\n\ts_cmp_eq_u64 vcc, 0\n\ts_cbranch_scc1 2f\n\ts_sleep %8\n\t"
        "global_load_dwordx2 %0, %5, %6 sc1\n\ts_waitcnt vmcnt(3)\n\tv_cmp_ne_u64 vcc, %7, %1\n\ts_cmp_eq_u64 vcc, 0\n\ts_cbranch_scc1 2f\n\ts_sleep %8\n\t"
        "global_load_dwordx2 %1, %5, %6 sc1\n\ts_waitcnt vmcnt(3)\n\tv_cmp_ne_u64 vcc, %7, %2\n\ts_cmp_eq_u64 vcc, 0\n\ts_cbranch_scc1 2f\n\ts_sleep %8\n\t"
        "global_load_dwordx2 %2, %5, %6 sc1\n\ts_waitcnt vmcnt(3)\n\tv_cmp_ne_u64 vcc, %7, %3\n\ts_cmp_eq_u64 vcc, 0\n\ts_cbranch_scc1 2f\n\ts_sleep %8\n\t"
        "s_sub_u32 %4, %4, 1\n\ts_cmp_lg_u32 %4, 0\n\ts_cbranch_scc1 1b\n"
        "2:\n\ts_barrier"
        : "=&v"(v0), "=&v"(v1), "=&v"(v2), "=&v"(v3), "+s"(spins)
        : "v"(fo), "s"(ctl), "s"(tag), "i"(PGAP), "s"(w), "i"(7) : "memory", "vcc", "scc");
  }
  u32x4 af[2][15];
  auto lda = [&](int r, int s) INL {
    const int so = (s < nks) ? ((2 * mt + r) * NKS + ks0 + s) * 1024 : A2_BYTES;
    af[r][s] = __builtin_amdgcn_raw_buffer_load_b128(rsA2, lane * 16, so, AUX_SC1);
  };
#pragma unroll
  for (int n = 0; n < WIN; ++n) lda(n / 15, n % 15);
  SB();
  u32 pw[4];
#pragma unroll
  for (int y = 0; y < 4; ++y) pw[y] = ((lane + 4 * y) & 63) * 16 + y * 1024 + 4 * R.wb;
  const int blk = lane & 7;
  const u32 prd = (((lane & 32) + 4 * w + ((lane >> 3) & 3) + 4 * (blk & 3)) & 63) * 16 + (blk & 3) * 1024;
  const u32 prd0 = prd + (blk >> 2) * SLOT;
  u32 prd1 = prd + (blk >> 2) * 4096 + 2 * SLOT, pw1[4], oo1 = lane < 32 ? ooff + 32 * LDX : 2048 * LDX;
#pragma unroll
  for (int y = 0; y < 4; ++y) { pw1[y] = pw[y] + 2 * SLOT + 4 * R.wb; PIN(pw1[y]); }
  PIN(prd1); PIN(oo1);
  const __amdgpu_buffer_rsrc_t rsO = __builtin_amdgcn_make_buffer_rsrc(P.out, 0, 2048 * LDX, 0x00020000);
  auto reduce = [&](int r, f32x4 xb) INL {
    f32x4 p[8];
#pragma unroll
    for (int sw = 0; sw < 8; ++sw) p[sw] = ldsf4(L, r == 0 ? prd0 + sw * 4096 : prd1 + sw * 8192);
    SB();
    const u32x2 o = pk4(((p[0] + p[1]) + (p[2] + p[3])) + ((p[4] + p[5]) + (p[6] + p[7])) + xb);
    auto e0 = __builtin_amdgcn_permlane32_swap(o[0], o[0], false, false), e1 = __builtin_amdgcn_permlane32_swap(o[1], o[1], false, false);
    __builtin_amdgcn_raw_buffer_store_b128(u32x4{e0[0], e1[0], e0[1], e1[1]}, rsO, r == 1 ? oo1 : (lane < 32 ? ooff : 2048 * LDX), 0, AUX_SC0 | AUX_SC1);
  };
  f32x16 pa[2][2];
  auto pwrite = [&](int r, int b8) INL {
    const f32x16& a = pa[r][b8 >> 2];
    const int y = b8 & 3;
    *(LAS f32x4*)(L + (r == 0 ? pw[y] + (b8 >> 2) * SLOT : pw1[y] + (b8 >> 2) * 4096)) =
        f32x4{a[4 * y], a[4 * y + 1], a[4 * y + 2], a[4 * y + 3]};
  };
  sfor<0, 2>([&](auto rc_) INL {
    constexpr int r = decltype(rc_)::v;
#pragma unroll
    for (int i = 0; i < 16; ++i) pa[r][0][i] = pa[r][1][i] = 0.f;
    sfor<0, 15>([&](auto sc_) INL {
      constexpr int s = decltype(sc_)::v;
      pa[r][0] = mfma32(wf[s][0], af[r][s], pa[r][0]);
      PIN(pa[r][0]);
      if constexpr (r == 1 && s >= 1 && s < 5) pwrite(0, 2 * s - 2);
      pa[r][1] = mfma32(wf[s][1], af[r][s], pa[r][1]);
      PIN(pa[r][1]);
      if constexpr (r == 1 && s >= 1 && s < 5) pwrite(0, 2 * s - 1);
      if constexpr (r == 0 && s == 2) asm volatile("" ::"v"(v0), "v"(v1), "v"(v2), "v"(v3));
      if constexpr (r * 15 + s + WIN < 30) lda((r * 15 + s + WIN) / 15, (r * 15 + s + WIN) % 15);
      if constexpr (r == 0 && s >= 7 && s < 13) wread(s + 2);
      if constexpr (r == 0 && s == 3) {
        const f32x4 bv = {bf_lo(bres[0]), bf_hi(bres[0]), bf_lo(bres[1]), bf_hi(bres[1])};
        xb0 = f32x4{bf_lo(xr0[0]), bf_hi(xr0[0]), bf_lo(xr0[1]), bf_hi(xr0[1])} + bv;
        xb1 = f32x4{bf_lo(xr1[0]), bf_hi(xr1[0]), bf_lo(xr1[1]), bf_hi(xr1[1])} + bv;
        PIN(xb0); PIN(xb1);
      }
      if constexpr (r == 1 && s == RMID) { lds_bar(); reduce(0, xb0); }
      SB();
    });
  });
#pragma unroll
  for (int y = 0; y < 8; ++y) pwrite(1, y);
  lds_bar();
  reduce(1, xb1);
  if (j == 0 && tid == 0) __hip_atomic_store(ctl, tag, __ATOMIC_RELAXED, __HIP_MEMORY_SCOPE_AGENT);
  asm volatile(".Lfk_end%c0:" ::"i"(TYPE));
  __builtin_amdgcn_endpgm();
}

__global__ __launch_bounds__(512, 2) void fused(const char*, const char*, const char*, const char*, const u16*, const char*, const char*,
                                                char*, char*) {
  extern __shared__ __attribute__((aligned(16))) char smem[];
  LAS char* L = (LAS char*)smem;
  typedef u64 u64x8 __attribute__((ext_vector_type(8)));
  u64x8 ka;
  u64 kb;
  asm volatile("s_load_dwordx16 %0, %2, 0x0\n\ts_load_dwordx2 %1, %2, 0x40"
               : "=&s"(ka), "=&s"(kb) : "s"(__builtin_amdgcn_kernarg_segment_ptr()) : "memory");
  const int lane = threadIdx.x & 63, w = __builtin_amdgcn_readfirstlane(threadIdx.x >> 6);
  const int b = blockIdx.x, bi = b >> 3;
  const int g = (b & 3) * 4 + (bi & 3), j = ((bi >> 2) & 3) + ((b >> 2) & 1) * 4 + (bi >> 4) * 8;
  const bool ffn = j >= 8;
  if (w == 7) {
    u64 pc;
    u32 len;
    asm volatile("s_getpc_b64 %0\n.Lfk_here:\n\ts_mov_b32 %1, .Lfk_end0-.Lfk_here\n\ts_max_u32 %1, %1, .Lfk_end1-.Lfk_here\n\t"
                 "s_max_u32 %1, %1, .Lfk_end2-.Lfk_here\n\ts_max_u32 %1, %1, .Lfk_end3-.Lfk_here" : "=s"(pc), "=s"(len)::"scc");
    touch((const char*)pc + min((u32)(lane * 128 + (bi & 7) * 8192), len));
  }
  const int rowbase = ffn ? 128 * g : 128 * g - 16;
  Ring R;
  R.ldw = ffn ? LDW1 : LDQKV;
  R.maxch = ffn ? (j == 15 ? 1 : 5) : 7;
  R.wb = w * 1024;
  R.rot = (j + g) & 7;
  R.lanes(lane);
  R.stepB = 64 * R.ldw;
  R.mlo = (!ffn && w == 0) ? 0xFFFFFF00u : 0xFFFFFFFFu;
  R.xcond = ffn ? 2u : (u32)(w - 4);
  const u32 oA0 = (rowbase + 8 * w < 0 ? 8 * w : rowbase + 8 * w) * LDX, oA1 = (rowbase + 8 * (w + 8)) * LDX;
  const u32 oB = 8 * w * R.ldw + (ffn ? (j - 8) * 352 : j * 128), seg = ffn ? 128 : 1024;
  asm volatile("s_waitcnt lgkmcnt(0)" : "+s"(ka), "+s"(kb)::"memory");
  const Ptrs P = {(const char*)(GAS const char*)ka[0], (const char*)(GAS const char*)ka[1], (const char*)(GAS const char*)ka[2],
                  (const char*)(GAS const char*)ka[3], (const u16*)(GAS const u16*)ka[4], (const char*)(GAS const char*)ka[5],
                  (const char*)(GAS const char*)ka[6], (char*)(GAS char*)ka[7], (char*)(GAS char*)kb};
  R.sA0 = P.x + oA0;
  R.sA1 = P.x + oA1;
  R.sB0 = (const char*)((ffn ? ka[3] : ka[1]) + oB);
  R.sB1 = R.sB0 + seg;
  R.sB2 = R.sB1 + seg;
  sfor<0, 6>([&](auto kc) INL { R.template issue<0, decltype(kc)::v>(); });
  if (!ffn && w == 0 && lane < 8) {
    const u32x4 one = {0x3F803F80u, 0x3F803F80u, 0x3F803F80u, 0x3F803F80u};
#pragma unroll
    for (int s = 0; s < 3; ++s) *(LAS u32x4*)(L + s * SLOT + lane * 16) = one;
  }
  if (!ffn) {
    if (w < 4) body<0>(L, P, R, g, j, w);
    else body<1>(L, P, R, g, j, w);
  } else {
    if (w < 4) body<2>(L, P, R, g, j, w);
    else body<3>(L, P, R, g, j, w);
  }
}
}

extern "C" void kernel_launch(void* const* d_in, const int* in_sizes, int n_in, void* d_out, int out_size, void* d_ws,
                              size_t ws_size, hipStream_t stream) {
  (void)hipFuncSetAttribute((const void*)fk::fused, hipFuncAttributeMaxDynamicSharedMemorySize, fk::L_TOTAL);
  fk::fused<<<256, 512, fk::L_TOTAL, stream>>>((const char*)d_in[0], (const char*)d_in[1], (const char*)d_in[2], (const char*)d_in[4],
                                               (const u16*)d_in[5], (const char*)d_in[6], (const char*)d_in[7], (char*)d_out,
                                               (char*)d_ws);
}
```
